```python
import torch

def _softmax(x, axis=-1):
    return torch.softmax(x, dim=axis)
def _gelu(x):
    return torch.nn.functional.gelu(x, approximate="tanh")
def _silu(x):
    return torch.nn.functional.silu(x)
def _sigmoid(x):
    return torch.sigmoid(x)
def _layernorm(x, axis=-1, eps=1e-5):
    m = x.mean(dim=axis, keepdim=True)
    v = x.var(dim=axis, keepdim=True, unbiased=False)
    return (x - m) / torch.sqrt(v + eps)
def _scatter_add(tgt, idx, src, axis=0):
    return tgt.clone().index_add_(axis, idx, src)
def _segment_sum(src, seg, n):
    out = torch.zeros((n,) + src.shape[1:], dtype=src.dtype)
    return out.index_add_(0, seg, src)
def _pad(x, pw):
    flat = tuple(v for pair in reversed(pw) for v in pair)
    return torch.nn.functional.pad(x, flat)

kernel_name = 'standalone__mha_block_cached__near_ridge'

def setup_inputs():

    _w = (lambda a: (lambda t: t.to(torch.bfloat16) if t.is_floating_point() else t)(torch.as_tensor(a)))
    B, Sq, D = 16, 160, 640
    H, Sk, Dh = 32, 512, 128
    return {
        "x": _w((lambda *s: torch.randn(*s, dtype=torch.bfloat16) * 0.8)(B, Sq, D)),
        "k_cache": _w((lambda *s: torch.randn(*s, dtype=torch.bfloat16) * 0.8)(B, H, Sk, Dh)),
        "v_cache": _w((lambda *s: torch.randn(*s, dtype=torch.bfloat16) * 0.05)(B, H, Sk, Dh)),
        "wq": _w((lambda *s: torch.randn(*s, dtype=torch.bfloat16) * 0.8)(D, H * Dh) * (D ** -0.5)),
        "wo": _w((lambda *s: torch.randn(*s, dtype=torch.bfloat16) * 0.05)(H * Dh, D) * ((H * Dh) ** -0.5)),
    }

def reference(x, k_cache, v_cache, wq, wo):
    B, Sq, D = x.shape
    H, Dh = 32, 128
    q = (lambda x, *d: x.permute(*d))((x @ wq).reshape(B, Sq, H, Dh), 0, 2, 1, 3)
    scores = torch.einsum("bhqd,bhkd->bhqk", q, k_cache) * (Dh ** -0.5)
    p = _softmax(scores, axis=-1)
    o = torch.einsum("bhqk,bhkd->bhqd", p, v_cache)
    return (lambda x, *d: x.permute(*d))(o, 0, 2, 1, 3).reshape(B, Sq, H * Dh) @ wo
```

```cpp
#include <hip/hip_runtime.h>
#include <hip/hip_bf16.h>

typedef unsigned short u16;
__device__ __forceinline__ float bf2f(u16 x) { return __uint_as_float(((unsigned)x) << 16); }
__device__ __forceinline__ u16 f2bf(float x) { unsigned u = __float_as_uint(x); u += 0x7fffu + ((u >> 16) & 1u); return (u16)(u >> 16); }

constexpr int B = 16, SQ = 160, D = 640, H = 32, SK = 512, DH = 128;

__global__ void gemm_naive(const u16* __restrict__ A, const u16* __restrict__ W, u16* __restrict__ C, int M, int N, int K) {
    int n = blockIdx.x * 256 + threadIdx.x; int m = blockIdx.y;
    if (n >= N) return;
    float acc = 0.f;
    for (int k = 0; k < K; ++k) acc += bf2f(A[(long)m * K + k]) * bf2f(W[(long)k * N + n]);
    C[(long)m * N + n] = f2bf(acc);
}
__global__ void attn_naive(const u16* __restrict__ Q, const u16* __restrict__ Kc, const u16* __restrict__ Vc, u16* __restrict__ O) {
    __shared__ float q[DH]; __shared__ float p[SK]; __shared__ float red[128];
    int qi = blockIdx.x, h = blockIdx.y, b = blockIdx.z, t = threadIdx.x;
    q[t] = bf2f(Q[((long)(b * SQ + qi)) * (H * DH) + h * DH + t]);
    __syncthreads();
    const u16* Kp = Kc + ((long)(b * H + h)) * SK * DH; const u16* Vp = Vc + ((long)(b * H + h)) * SK * DH;
    float mx = -1e30f;
    for (int j = t; j < SK; j += 128) { float s = 0.f; for (int d = 0; d < DH; ++d) s += q[d] * bf2f(Kp[j * DH + d]); s *= 0.08838834764831845f; p[j] = s; mx = fmaxf(mx, s); }
    red[t] = mx; __syncthreads();
    for (int o = 64; o > 0; o >>= 1) { if (t < o) red[t] = fmaxf(red[t], red[t + o]); __syncthreads(); }
    mx = red[0]; __syncthreads();
    float sm = 0.f;
    for (int j = t; j < SK; j += 128) { float e = __expf(p[j] - mx); p[j] = e; sm += e; }
    red[t] = sm; __syncthreads();
    for (int o = 64; o > 0; o >>= 1) { if (t < o) red[t] += red[t + o]; __syncthreads(); }
    sm = red[0];
    float acc = 0.f;
    for (int j = 0; j < SK; ++j) acc += bf2f(f2bf(p[j])) * bf2f(Vp[j * DH + t]);
    O[((long)(b * SQ + qi)) * (H * DH) + h * DH + t] = f2bf(acc / sm);
}

extern "C" void kernel_launch(void* const* d_in, const int* in_sizes, int n_in, void* d_out, int out_size, void* d_ws, size_t ws_size, hipStream_t stream) {
    const u16* x = (const u16*)d_in[0]; const u16* kc = (const u16*)d_in[1]; const u16* vc = (const u16*)d_in[2];
    const u16* wq = (const u16*)d_in[3]; const u16* wo = (const u16*)d_in[4];
    u16* Q = (u16*)d_ws; u16* O = Q + (long)B * SQ * H * DH;
    gemm_naive<<<dim3(H * DH / 256, B * SQ), 256, 0, stream>>>(x, wq, Q, B * SQ, H * DH, D);
    attn_naive<<<dim3(SQ, H, B), 128, 0, stream>>>(Q, kc, vc, O);
    gemm_naive<<<dim3((D + 255) / 256, B * SQ), 256, 0, stream>>>(O, wo, (u16*)d_out, B * SQ, D, H * DH);
}
```

```cpp
#include <hip/hip_runtime.h>
#include <stdint.h>

#define CLOB "v0", "v1", "v2", "v3", "v4", "v5", "v6", "v7", "v8", "v9", "v10", "v11", "v12", "v13", "v14", "v15", "v16", "v17", "v18", "v19", "v20", "v21", "v22", "v23", "v24", "v25", "v26", "v27", "v28", "v29", "v30", "v31", "v32", "v33", "v34", "v35", "v36", "v37", "v38", "v39", "v40", "v41", "v42", "v43", "v44", "v45", "v46", "v47", "v48", "v49", "v50", "v51", "v52", "v53", "v54", "v55", "v56", "v57", "v58", "v59", "v60", "v61", "v62", "v63", "v64", "v65", "v66", "v67", "v68", "v69", "v70", "v71", "v72", "v73", "v74", "v75", "v76", "v77", "v78", "v79", "v80", "v81", "v82", "v83", "v84", "v85", "v86", "v87", "v88", "v89", "v90", "v91", "v92", "v93", "v94", "v95", "v96", "v97", "v98", "v99", "v100", "v101", "v102", "v103", "v104", "v105", "v106", "v107", "v108", "v109", "v110", "v111", "v112", "v113", "v114", "v115", "v116", "v117", "v118", "v119", "v120", "v121", "v122", "v123", "v124", "v125", "v126", "v127", "v128", "v129", "v130", "v131", "v132", "v133", "v134", "v135", "v136", "v137", "v138", "v139", "v140", "v141", "v142", "v143", "v144", "v145", "v146", "v147", "v148", "v149", "v150", "v151", "v152", "v153", "v154", "v155", "v156", "v157", "v158", "v159", "v160", "v161", "v162", "v163", "v164", "v165", "v166", "v167", "v168", "v169", "v170", "v171", "v172", "v173", "v174", "v175", "v176", "v177", "v178", "v179", "v180", "v181", "v182", "v183", "v184", "v185", "v186", "v187", "v188", "v189", "v190", "v191", "v192", "v193", "v194", "v195", "v196", "v197", "v198", "v199", "v200", "v201", "v202", "v203", "v204", "v205", "v206", "v207", "v208", "v209", "v210", "v211", "v212", "v213", "v214", "v215", "v216", "v217", "v218", "v219", "v220", "v221", "v222", "v223", "v224", "v225", "v226", "v227", "v228", "v229", "v230", "v231", "v232", "v233", "v234", "v235", "v236", "v237", "v238", "v239", "v240", "v241", "v242", "v243", "v244", "v245", "v246", "v247", "v248", "v249", "v250", "v251", "v252", "v253", "v254", "v255", "a0", "a1", "a2", "a3", "a4", "a5", "a6", "a7", "a8", "a9", "a10", "a11", "a12", "a13", "a14", "a15", "a16", "a17", "a18", "a19", "a20", "a21", "a22", "a23", "a24", "a25", "a26", "a27", "a28", "a29", "a30", "a31", "a32", "a33", "a34", "a35", "a36", "a37", "a38", "a39", "a40", "a41", "a42", "a43", "a44", "a45", "a46", "a47", "a48", "a49", "a50", "a51", "a52", "a53", "a54", "a55", "a56", "a57", "a58", "a59", "a60", "a61", "a62", "a63", "a64", "a65", "a66", "a67", "a68", "a69", "a70", "a71", "a72", "a73", "a74", "a75", "a76", "a77", "a78", "a79", "a80", "a81", "a82", "a83", "a84", "a85", "a86", "a87", "a88", "a89", "a90", "a91", "a92", "a93", "a94", "a95", "a96", "a97", "a98", "a99", "a100", "a101", "a102", "a103", "a104", "a105", "a106", "a107", "a108", "a109", "a110", "a111", "a112", "a113", "a114", "a115", "a116", "a117", "a118", "a119", "a120", "a121", "a122", "a123", "a124", "a125", "a126", "a127", "a128", "a129", "a130", "a131", "a132", "a133", "a134", "a135", "a136", "a137", "a138", "a139", "a140", "a141", "a142", "a143", "a144", "a145", "a146", "a147", "a148", "a149", "a150", "a151", "a152", "a153", "a154", "a155", "a156", "a157", "a158", "a159", "a160", "a161", "a162", "a163", "a164", "a165", "a166", "a167", "a168", "a169", "a170", "a171", "a172", "a173", "a174", "a175", "a176", "a177", "a178", "a179", "a180", "a181", "a182", "a183", "a184", "a185", "a186", "a187", "a188", "a189", "a190", "a191", "a192", "a193", "a194", "a195", "a196", "a197", "a198", "a199", "a200", "a201", "a202", "a203", "a204", "a205", "a206", "a207", "a208", "a209", "a210", "a211", "a212", "a213", "a214", "a215", "a216", "a217", "a218", "a219", "a220", "a221", "a222", "a223", "a224", "a225", "a226", "a227", "a228", "a229", "a230", "a231", "a232", "a233", "a234", "a235", "a236", "a237", "a238", "a239", "a240", "a241", "a242", "a243", "a244", "a245", "a246", "a247", "a248", "a249", "a250", "a251", "a252", "a253", "a254", "a255", "s36", "s37", "s38", "s39", "s40", "s41", "s42", "s43", "s44", "s45", "s46", "s47", "s48", "s49", "s50", "s51", "s52", "s53", "s54", "s55", "s56", "s57", "s58", "s59", "s60", "s61", "s62", "s63", "s64", "s65", "s66", "s67", "s68", "s69", "s70", "s71", "s72", "s73", "s74", "s75", "s76", "s77", "s78", "s79", "s80", "s81", "s82", "s83", "s84", "s85", "s86", "s87", "s88", "s89", "s90", "s91", "s92", "s93", "s94", "s95", "s96", "s97", "s98", "s99", "vcc", "scc", "memory"

__global__ __launch_bounds__(256, 1) void mha_fused(const void* x, const void* kc, const void* vc, const void* wq, const void* wo, void* ws, void* out) {
  __shared__ __attribute__((aligned(1024))) char lds[163840];
  const unsigned wave = __builtin_amdgcn_readfirstlane(threadIdx.x >> 6);
  asm volatile(
".macro MM d, a, b, c\n"
"v_mfma_f32_16x16x32_bf16 \\d, \\a, \\b, \\c\n"
".endm\n"
"K1_START:\n"
"s_mov_b32 s65, %7\n"
"s_mov_b32 s60, %8\n"
"s_mov_b32 s77, %9\n"
"s_and_b32 s68, s65, 7\n"
"s_lshr_b32 s69, s65, 3\n"
"s_lshr_b32 s63, s68, 2\n"
"s_lshl_b32 s63, s63, 3\n"
"s_lshr_b32 s64, s69, 2\n"
"s_add_u32 s63, s63, s64\n"
"s_and_b32 s64, s68, 3\n"
"s_lshl_b32 s64, s64, 2\n"
"s_and_b32 s91, s69, 3\n"
"s_add_u32 s64, s64, s91\n"
"s_and_b32 s91, s64, 1\n"
"s_lshl_b32 s86, s63, 1\n"
"s_add_u32 s86, s86, s91\n"
"s_lshr_b32 s87, s64, 1\n"
"s_lshr_b32 s61, s60, 1\n"
"s_and_b32 s62, s60, 1\n"
"s_lshl_b32 s68, s64, 9\n"
"s_mov_b64 s[40:41], %3\n"
"s_add_u32 s40, s40, s68\n"
"s_addc_u32 s41, s41, 0\n"
"s_and_b32 s41, s41, 0xffff\n"
"s_sub_u32 s42, 5242880, s68\n"
"s_mov_b32 s43, 0x20000\n"
"v_mbcnt_lo_u32_b32 v253, -1, 0\n"
"v_mbcnt_hi_u32_b32 v253, -1, v253\n"
"v_lshrrev_b32 v6, 5, v253\n"
"v_bfe_u32 v7, v253, 2, 3\n"
"v_and_b32 v8, 3, v253\n"
"v_lshrrev_b32 v0, 2, v7\n"
"v_and_b32 v1, 3, v7\n"
"v_lshl_add_u32 v1, v0, 3, v1\n"
"v_lshlrev_b32 v1, 13, v1\n"
"v_lshl_add_u32 v1, v6, 6, v1\n"
"v_xor_b32 v0, v0, v8\n"
"v_lshl_add_u32 v17, v0, 4, v1\n"
"s_lshl_b32 s68, s61, 8\n"
"s_lshl_b32 s69, s62, 18\n"
"s_add_u32 s68, s68, s69\n"
"s_lshl_b32 s66, s60, 12\n"
"s_add_u32 s66, s66, s77\n"
"s_add_u32 m0, s66, 49152\n"
"s_add_u32 s69, s68, 0\n"
"buffer_load_dwordx4 v17, s[40:43], s69 offen lds\n"
"s_add_u32 m0, s66, 50176\n"
"s_add_u32 s69, s68, 32768\n"
"buffer_load_dwordx4 v17, s[40:43], s69 offen lds\n"
"s_add_u32 m0, s66, 51200\n"
"s_add_u32 s69, s68, 131072\n"
"buffer_load_dwordx4 v17, s[40:43], s69 offen lds\n"
"s_add_u32 m0, s66, 52224\n"
"s_add_u32 s69, s68, 163840\n"
"buffer_load_dwordx4 v17, s[40:43], s69 offen lds\n"
"s_mul_i32 s68, s63, 204800\n"
"s_mov_b64 s[36:37], %0\n"
"s_add_u32 s36, s36, s68\n"
"s_addc_u32 s37, s37, 0\n"
"s_and_b32 s37, s37, 0xffff\n"
"s_mov_b32 s38, 204800\n"
"s_mov_b32 s39, 0x20000\n"
"v_lshrrev_b32 v0, 3, v253\n"
"v_and_b32 v1, 7, v253\n"
"v_lshrrev_b32 v2, 1, v0\n"
"v_xor_b32 v1, v1, v2\n"
"v_lshlrev_b32 v1, 4, v1\n"
"v_mul_u32_u24 v0, 1280, v0\n"
"v_add_u32 v18, v0, v1\n"
"v_xor_b32 v19, 64, v18\n"
"s_lshl_b32 s68, s62, 6\n"
"v_xor_b32 v2, s68, v18\n"
"s_mul_i32 s68, s60, 10240\n"
"s_lshl_b32 s66, s60, 10\n"
"s_add_u32 s66, s66, s77\n"
"s_add_u32 m0, s66, 114688\n"
"s_add_u32 s69, s68, 0\n"
"buffer_load_dwordx4 v2, s[36:39], s69 offen lds\n"
"s_add_u32 m0, s66, 118784\n"
"s_add_u32 s69, s68, 40960\n"
"buffer_load_dwordx4 v2, s[36:39], s69 offen lds\n"
"s_add_u32 m0, s66, 122880\n"
"s_add_u32 s69, s68, 81920\n"
"buffer_load_dwordx4 v2, s[36:39], s69 offen lds\n"
"s_add_u32 m0, s66, 126976\n"
"s_add_u32 s69, s68, 122880\n"
"buffer_load_dwordx4 v2, s[36:39], s69 offen lds\n"
"s_add_u32 m0, s66, 131072\n"
"s_add_u32 s69, s68, 163840\n"
"buffer_load_dwordx4 v2, s[36:39], s69 offen lds\n"
"s_cmp_eq_u32 s60, 2\n"
"s_cbranch_scc1 K1_ROLE_X2\n"
"s_cmp_eq_u32 s60, 3\n"
"s_cbranch_scc1 K1_ROLE_X3\n"
"s_nop 3\n"
"s_lshl_b32 s68, s60, 8\n"
"v_add_u32 v1, s68, v17\n"
"v_add_u32 v164, 0, v1\n"
"v_add_u32 v165, 32768, v1\n"
"v_add_u32 v166, 131072, v1\n"
"v_add_u32 v167, 163840, v1\n"
"v_add_u32 v168, 262144, v1\n"
"v_add_u32 v169, 294912, v1\n"
"v_add_u32 v170, 393216, v1\n"
"v_add_u32 v171, 425984, v1\n"
"s_lshl_b32 s66, s60, 13\n"
"s_add_u32 s66, s66, s77\n"
"s_add_u32 s66, s66, 49152\n"
"s_mov_b32 s67, 128\n"
"s_add_u32 m0, s66, 16384\n"
"s_nop 0\n"
"buffer_load_dwordx4 v164, s[40:43], s67 offen lds\n"
"s_add_u32 m0, s66, 17408\n"
"s_nop 0\n"
"buffer_load_dwordx4 v165, s[40:43], s67 offen lds\n"
"s_add_u32 m0, s66, 18432\n"
"s_nop 0\n"
"buffer_load_dwordx4 v166, s[40:43], s67 offen lds\n"
"s_add_u32 m0, s66, 19456\n"
"s_nop 0\n"
"buffer_load_dwordx4 v167, s[40:43], s67 offen lds\n"
"s_add_u32 m0, s66, 20480\n"
"s_nop 0\n"
"buffer_load_dwordx4 v168, s[40:43], s67 offen lds\n"
"s_add_u32 m0, s66, 21504\n"
"s_nop 0\n"
"buffer_load_dwordx4 v169, s[40:43], s67 offen lds\n"
"s_add_u32 m0, s66, 22528\n"
"s_nop 0\n"
"buffer_load_dwordx4 v170, s[40:43], s67 offen lds\n"
"s_add_u32 m0, s66, 23552\n"
"s_nop 0\n"
"buffer_load_dwordx4 v171, s[40:43], s67 offen lds\n"
"s_add_u32 s67, s67, 524160\n"
"s_lshl_b32 s68, s63, 4\n"
"s_add_u32 s68, s68, s64\n"
"s_lshl_b32 s68, s68, 1\n"
"s_add_u32 s68, s68, s61\n"
"s_lshl_b32 s68, s68, 17\n"
"s_mov_b64 s[44:45], %1\n"
"s_add_u32 s44, s44, s68\n"
"s_addc_u32 s45, s45, 0\n"
"s_and_b32 s45, s45, 0xffff\n"
"s_mov_b32 s46, 131072\n"
"s_mov_b32 s47, 0x20000\n"
"s_mov_b64 s[48:49], %2\n"
"s_add_u32 s48, s48, s68\n"
"s_addc_u32 s49, s49, 0\n"
"s_and_b32 s49, s49, 0xffff\n"
"s_mov_b32 s50, 131072\n"
"s_mov_b32 s51, 0x20000\n"
"s_mov_b64 s[56:57], %5\n"
"s_and_b32 s57, s57, 0xffff\n"
"s_mov_b32 s58, 43089920\n"
"s_mov_b32 s59, 0x20000\n"
"s_mov_b64 s[92:93], %6\n"
"s_mov_b32 s75, 0\n"
"s_mov_b32 s76, 0x41000000\n"
"s_mov_b32 s78, 0x3e0293ee\n"
"s_mov_b32 s79, s78\n"
"s_mov_b32 s82, 0xffff0000\n"
"s_mov_b32 s83, 0xffff0000\n"
"s_lshl_b32 s70, s60, 12\n"
"s_add_u32 s70, s70, s77\n"
"v_lshrrev_b32 v9, 4, v253\n"
"v_and_b32 v10, 15, v253\n"
"v_bfe_u32 v11, v253, 2, 2\n"
"v_and_b32 v12, 1, v9\n"
"v_lshrrev_b32 v13, 1, v9\n"
"s_lshl_b32 s68, s61, 13\n"
"s_add_u32 s68, s68, s77\n"
"v_lshrrev_b32 v0, 2, v10\n"
"v_sub_u32 v0, 4, v0\n"
"v_and_b32 v0, 3, v0\n"
"v_xor_b32 v0, v0, v9\n"
"v_lshlrev_b32 v0, 4, v0\n"
"v_lshrrev_b32 v1, 2, v10\n"
"v_lshl_add_u32 v0, v1, 10, v0\n"
"v_and_b32 v1, 3, v10\n"
"v_lshl_add_u32 v0, v1, 6, v0\n"
"v_add_u32 v240, s68, v0\n"
"v_add_u32 v241, 0x10000, v240\n"
"v_lshl_add_u32 v3, v12, 2, v11\n"
"v_lshlrev_b32 v3, 6, v3\n"
"v_lshlrev_b32 v0, 6, v11\n"
"v_lshl_add_u32 v0, v8, 3, v0\n"
"v_lshl_add_u32 v0, v12, 5, v0\n"
"v_lshl_add_u32 v0, v9, 10, v0\n"
"v_add_u32 v243, s68, v0\n"
"v_xor_b32 v246, 32, v243\n"
"v_add_u32 v244, 0x10000, v243\n"
"v_add_u32 v247, 0x10000, v246\n"
"v_lshlrev_b32 v15, 8, v11\n"
"v_lshl_add_u32 v15, v9, 6, v15\n"
"s_lshl_b32 s69, s62, 12\n"
"v_add_u32 v15, s69, v15\n"
"v_lshl_add_u32 v249, v8, 4, v15\n"
"v_xor_b32 v250, 16, v249\n"
"v_xor_b32 v251, 32, v249\n"
"v_xor_b32 v252, 48, v249\n"
"s_mul_i32 s94, s61, 8192\n"
"v_lshl_add_u32 v0, v13, 11, v3\n"
"v_xor_b32 v1, v8, v12\n"
"v_lshl_add_u32 v0, v1, 4, v0\n"
"v_and_b32 v1, 1, v8\n"
"v_lshl_add_u32 v0, v1, 3, v0\n"
"v_add_u32 v0, s68, v0\n"
"v_add_u32 v160, 49152, v0\n"
"v_xor_b32 v161, 8, v160\n"
"s_mul_i32 s69, s62, 10240\n"
"s_add_u32 s69, s69, s77\n"
"s_add_u32 s69, s69, 114688\n"
"v_lshrrev_b32 v0, 1, v10\n"
"v_xor_b32 v0, v0, v9\n"
"v_lshlrev_b32 v0, 4, v0\n"
"v_lshl_add_u32 v0, v10, 7, v0\n"
"v_add_u32 v162, s69, v0\n"
"v_xor_b32 v163, 64, v162\n"
"v_mov_b32 v0, 0x3f803f80\n"
"v_accvgpr_write_b32 a244, v0\n"
"v_accvgpr_write_b32 a245, v0\n"
"v_accvgpr_write_b32 a246, v0\n"
"v_accvgpr_write_b32 a247, v0\n"
"v_lshl_add_u32 v197, v253, 4, s70\n"
"v_add_u32 v198, 0x10000, v197\n"
"v_add_u32 v199, 0x20000, v197\n"
"s_add_u32 s68, s77, 155648\n"
"v_lshl_add_u32 v191, v253, 2, s68\n"
"s_lshl_b32 s68, s60, 8\n"
"s_add_u32 s68, s68, s77\n"
"s_add_u32 s68, s68, 156160\n"
"v_lshl_add_u32 v242, v253, 2, s68\n"
"v_mul_u32_u24 v0, 260, v8\n"
"s_add_u32 s68, s77, 156160\n"
"v_add_u32 v245, s68, v0\n"
"v_mov_b32 v0, 0\n"
"ds_write_b32 v242, v0 offset:0\n"
"s_add_u32 m0, s66, 32768\n"
"s_nop 0\n"
"buffer_load_dwordx4 v164, s[40:43], s67 offen lds\n"
"s_add_u32 m0, s66, 33792\n"
"s_nop 0\n"
"buffer_load_dwordx4 v165, s[40:43], s67 offen lds\n"
"s_add_u32 m0, s66, 34816\n"
"s_nop 0\n"
"buffer_load_dwordx4 v166, s[40:43], s67 offen lds\n"
"s_add_u32 m0, s66, 35840\n"
"s_nop 0\n"
"buffer_load_dwordx4 v167, s[40:43], s67 offen lds\n"
"s_add_u32 m0, s66, 36864\n"
"s_nop 0\n"
"buffer_load_dwordx4 v168, s[40:43], s67 offen lds\n"
"s_add_u32 m0, s66, 37888\n"
"s_nop 0\n"
"buffer_load_dwordx4 v169, s[40:43], s67 offen lds\n"
"s_add_u32 m0, s66, 38912\n"
"s_nop 0\n"
"buffer_load_dwordx4 v170, s[40:43], s67 offen lds\n"
"s_add_u32 m0, s66, 39936\n"
"s_nop 0\n"
"buffer_load_dwordx4 v171, s[40:43], s67 offen lds\n"
"s_add_u32 s67, s67, 128\n"
"s_add_u32 m0, s66, 49152\n"
"s_nop 0\n"
"buffer_load_dwordx4 v164, s[40:43], s67 offen lds\n"
"s_add_u32 m0, s66, 50176\n"
"s_nop 0\n"
"buffer_load_dwordx4 v165, s[40:43], s67 offen lds\n"
"s_add_u32 m0, s66, 51200\n"
"s_nop 0\n"
"buffer_load_dwordx4 v166, s[40:43], s67 offen lds\n"
"s_add_u32 m0, s66, 52224\n"
"s_nop 0\n"
"buffer_load_dwordx4 v167, s[40:43], s67 offen lds\n"
"s_add_u32 m0, s66, 53248\n"
"s_nop 0\n"
"buffer_load_dwordx4 v168, s[40:43], s67 offen lds\n"
"s_add_u32 m0, s66, 54272\n"
"s_nop 0\n"
"buffer_load_dwordx4 v169, s[40:43], s67 offen lds\n"
"s_add_u32 m0, s66, 55296\n"
"s_nop 0\n"
"buffer_load_dwordx4 v170, s[40:43], s67 offen lds\n"
"s_add_u32 m0, s66, 56320\n"
"s_nop 0\n"
"buffer_load_dwordx4 v171, s[40:43], s67 offen lds\n"
"s_add_u32 s67, s67, 524160\n"
"s_waitcnt vmcnt(24)\n"
"s_waitcnt lgkmcnt(0)\n"
"s_barrier\n"
"ds_read_b64_tr_b16 a[0:1], v160 offset:0\n"
"ds_read_b64_tr_b16 a[2:3], v160 offset:1024\n"
"ds_read_b64_tr_b16 a[16:17], v160 offset:4096\n"
"ds_read_b64_tr_b16 a[18:19], v160 offset:5120\n"
"ds_read_b128 a[64:67], v162 offset:0\n"
"ds_read_b128 a[84:87], v163 offset:0\n"
"ds_read_b128 a[68:71], v162 offset:2048\n"
"ds_read_b128 a[88:91], v163 offset:2048\n"
"ds_read_b128 a[72:75], v162 offset:4096\n"
"ds_read_b128 a[92:95], v163 offset:4096\n"
"ds_read_b128 a[76:79], v162 offset:6144\n"
"ds_read_b128 a[96:99], v163 offset:6144\n"
"ds_read_b128 a[80:83], v162 offset:8192\n"
"ds_read_b128 a[100:103], v163 offset:8192\n"
"ds_read_b64_tr_b16 a[4:5], v161 offset:0\n"
"ds_read_b64_tr_b16 a[6:7], v161 offset:1024\n"
"ds_read_b64_tr_b16 a[20:21], v161 offset:4096\n"
"ds_read_b64_tr_b16 a[22:23], v161 offset:5120\n"
"ds_read_b64_tr_b16 a[8:9], v160 offset:512\n"
"ds_read_b64_tr_b16 a[10:11], v160 offset:1536\n"
"ds_read_b64_tr_b16 a[24:25], v160 offset:4608\n"
"ds_read_b64_tr_b16 a[26:27], v160 offset:5632\n"
"ds_read_b64_tr_b16 a[12:13], v161 offset:512\n"
"ds_read_b64_tr_b16 a[14:15], v161 offset:1536\n"
"ds_read_b64_tr_b16 a[28:29], v161 offset:4608\n"
"ds_read_b64_tr_b16 a[30:31], v161 offset:5632\n"
"s_waitcnt vmcnt(16)\n"
"s_waitcnt lgkmcnt(0)\n"
"s_barrier\n"
"MM v[0:3], a[0:3], a[64:67], 0\n"
"s_add_u32 m0, s66, 0\n"
"ds_read_b64_tr_b16 a[32:33], v160 offset:16384\n"
"MM v[0:3], a[16:19], a[84:87], v[0:3]\n"
"buffer_load_dwordx4 v164, s[40:43], s67 offen lds\n"
"MM v[4:7], a[0:3], a[68:71], 0\n"
"ds_read_b64_tr_b16 a[34:35], v160 offset:17408\n"
"ds_read_b64_tr_b16 a[48:49], v160 offset:20480\n"
"MM v[4:7], a[16:19], a[88:91], v[4:7]\n"
"ds_read_b64_tr_b16 a[50:51], v160 offset:21504\n"
"s_add_u32 m0, s66, 1024\n"
"MM v[8:11], a[0:3], a[72:75], 0\n"
"ds_read_b64_tr_b16 a[36:37], v161 offset:16384\n"
"ds_read_b64_tr_b16 a[38:39], v161 offset:17408\n"
"MM v[8:11], a[16:19], a[92:95], v[8:11]\n"
"buffer_load_dwordx4 v165, s[40:43], s67 offen lds\n"
"MM v[12:15], a[0:3], a[76:79], 0\n"
"ds_read_b64_tr_b16 a[52:53], v161 offset:20480\n"
"ds_read_b64_tr_b16 a[54:55], v161 offset:21504\n"
"MM v[12:15], a[16:19], a[96:99], v[12:15]\n"
"s_add_u32 m0, s66, 2048\n"
"ds_read_b64_tr_b16 a[40:41], v160 offset:16896\n"
"MM v[16:19], a[0:3], a[80:83], 0\n"
"buffer_load_dwordx4 v166, s[40:43], s67 offen lds\n"
"MM v[16:19], a[16:19], a[100:103], v[16:19]\n"
"ds_read_b64_tr_b16 a[42:43], v160 offset:17920\n"
"MM v[20:23], a[4:7], a[64:67], 0\n"
"ds_read_b64_tr_b16 a[56:57], v160 offset:20992\n"
"MM v[20:23], a[20:23], a[84:87], v[20:23]\n"
"s_add_u32 m0, s66, 3072\n"
"ds_read_b64_tr_b16 a[58:59], v160 offset:22016\n"
"MM v[24:27], a[4:7], a[68:71], 0\n"
"buffer_load_dwordx4 v167, s[40:43], s67 offen lds\n"
"MM v[24:27], a[20:23], a[88:91], v[24:27]\n"
"ds_read_b64_tr_b16 a[44:45], v161 offset:16896\n"
"MM v[28:31], a[4:7], a[72:75], 0\n"
"ds_read_b64_tr_b16 a[46:47], v161 offset:17920\n"
"MM v[28:31], a[20:23], a[92:95], v[28:31]\n"
"MM v[32:35], a[4:7], a[76:79], 0\n"
"s_add_u32 m0, s66, 4096\n"
"ds_read_b64_tr_b16 a[60:61], v161 offset:20992\n"
"MM v[32:35], a[20:23], a[96:99], v[32:35]\n"
"buffer_load_dwordx4 v168, s[40:43], s67 offen lds\n"
"MM v[36:39], a[4:7], a[80:83], 0\n"
"ds_read_b64_tr_b16 a[62:63], v161 offset:22016\n"
"MM v[36:39], a[20:23], a[100:103], v[36:39]\n"
"MM v[40:43], a[8:11], a[64:67], 0\n"
"MM v[40:43], a[24:27], a[84:87], v[40:43]\n"
"s_add_u32 m0, s66, 5120\n"
"MM v[44:47], a[8:11], a[68:71], 0\n"
"buffer_load_dwordx4 v169, s[40:43], s67 offen lds\n"
"MM v[44:47], a[24:27], a[88:91], v[44:47]\n"
"MM v[48:51], a[8:11], a[72:75], 0\n"
"MM v[48:51], a[24:27], a[92:95], v[48:51]\n"
"MM v[52:55], a[8:11], a[76:79], 0\n"
"s_add_u32 m0, s66, 6144\n"
"MM v[52:55], a[24:27], a[96:99], v[52:55]\n"
"buffer_load_dwordx4 v170, s[40:43], s67 offen lds\n"
"MM v[56:59], a[8:11], a[80:83], 0\n"
"MM v[56:59], a[24:27], a[100:103], v[56:59]\n"
"MM v[60:63], a[12:15], a[64:67], 0\n"
"s_add_u32 m0, s66, 7168\n"
"MM v[60:63], a[28:31], a[84:87], v[60:63]\n"
"buffer_load_dwordx4 v171, s[40:43], s67 offen lds\n"
"MM v[64:67], a[12:15], a[68:71], 0\n"
"MM v[64:67], a[28:31], a[88:91], v[64:67]\n"
"MM v[68:71], a[12:15], a[72:75], 0\n"
"MM v[68:71], a[28:31], a[92:95], v[68:71]\n"
"s_add_u32 s67, s67, 128\n"
"MM v[72:75], a[12:15], a[76:79], 0\n"
"MM v[72:75], a[28:31], a[96:99], v[72:75]\n"
"MM v[76:79], a[12:15], a[80:83], 0\n"
"MM v[76:79], a[28:31], a[100:103], v[76:79]\n"
"s_waitcnt vmcnt(16)\n"
"s_waitcnt lgkmcnt(0)\n"
"s_barrier\n"
"MM v[80:83], a[32:35], a[64:67], 0\n"
"s_add_u32 m0, s66, 16384\n"
"ds_read_b64_tr_b16 a[0:1], v160 offset:32768\n"
"MM v[80:83], a[48:51], a[84:87], v[80:83]\n"
"buffer_load_dwordx4 v164, s[40:43], s67 offen lds\n"
"MM v[84:87], a[32:35], a[68:71], 0\n"
"ds_read_b64_tr_b16 a[2:3], v160 offset:33792\n"
"ds_read_b64_tr_b16 a[16:17], v160 offset:36864\n"
"MM v[84:87], a[48:51], a[88:91], v[84:87]\n"
"ds_read_b64_tr_b16 a[18:19], v160 offset:37888\n"
"ds_read_b128 a[104:107], v162 offset:20480\n"
"MM v[88:91], a[32:35], a[72:75], 0\n"
"s_add_u32 m0, s66, 17408\n"
"ds_read_b128 a[124:127], v163 offset:20480\n"
"MM v[88:91], a[48:51], a[92:95], v[88:91]\n"
"buffer_load_dwordx4 v165, s[40:43], s67 offen lds\n"
"MM v[92:95], a[32:35], a[76:79], 0\n"
"ds_read_b128 a[108:111], v162 offset:22528\n"
"ds_read_b128 a[128:131], v163 offset:22528\n"
"MM v[92:95], a[48:51], a[96:99], v[92:95]\n"
"ds_read_b128 a[112:115], v162 offset:24576\n"
"s_add_u32 m0, s66, 18432\n"
"MM v[96:99], a[32:35], a[80:83], 0\n"
"ds_read_b128 a[132:135], v163 offset:24576\n"
"ds_read_b128 a[116:119], v162 offset:26624\n"
"MM v[96:99], a[48:51], a[100:103], v[96:99]\n"
"buffer_load_dwordx4 v166, s[40:43], s67 offen lds\n"
"MM v[100:103], a[36:39], a[64:67], 0\n"
"ds_read_b128 a[136:139], v163 offset:26624\n"
"ds_read_b128 a[120:123], v162 offset:28672\n"
"MM v[100:103], a[52:55], a[84:87], v[100:103]\n"
"s_add_u32 m0, s66, 19456\n"
"ds_read_b128 a[140:143], v163 offset:28672\n"
"MM v[104:107], a[36:39], a[68:71], 0\n"
"buffer_load_dwordx4 v167, s[40:43], s67 offen lds\n"
"MM v[104:107], a[52:55], a[88:91], v[104:107]\n"
"ds_read_b64_tr_b16 a[4:5], v161 offset:32768\n"
"ds_read_b64_tr_b16 a[6:7], v161 offset:33792\n"
"MM v[108:111], a[36:39], a[72:75], 0\n"
"ds_read_b64_tr_b16 a[20:21], v161 offset:36864\n"
"MM v[108:111], a[52:55], a[92:95], v[108:111]\n"
"MM v[112:115], a[36:39], a[76:79], 0\n"
"ds_read_b64_tr_b16 a[22:23], v161 offset:37888\n"
"s_add_u32 m0, s66, 20480\n"
"MM v[112:115], a[52:55], a[96:99], v[112:115]\n"
"buffer_load_dwordx4 v168, s[40:43], s67 offen lds\n"
"MM v[116:119], a[36:39], a[80:83], 0\n"
"ds_read_b64_tr_b16 a[8:9], v160 offset:33280\n"
"ds_read_b64_tr_b16 a[10:11], v160 offset:34304\n"
"MM v[116:119], a[52:55], a[100:103], v[116:119]\n"
"ds_read_b64_tr_b16 a[24:25], v160 offset:37376\n"
"MM v[120:123], a[40:43], a[64:67], 0\n"
"ds_read_b64_tr_b16 a[26:27], v160 offset:38400\n"
"MM v[120:123], a[56:59], a[84:87], v[120:123]\n"
"s_add_u32 m0, s66, 21504\n"
"ds_read_b64_tr_b16 a[12:13], v161 offset:33280\n"
"MM v[124:127], a[40:43], a[68:71], 0\n"
"buffer_load_dwordx4 v169, s[40:43], s67 offen lds\n"
"MM v[124:127], a[56:59], a[88:91], v[124:127]\n"
"ds_read_b64_tr_b16 a[14:15], v161 offset:34304\n"
"MM v[128:131], a[40:43], a[72:75], 0\n"
"ds_read_b64_tr_b16 a[28:29], v161 offset:37376\n"
"MM v[128:131], a[56:59], a[92:95], v[128:131]\n"
"ds_read_b64_tr_b16 a[30:31], v161 offset:38400\n"
"MM v[132:135], a[40:43], a[76:79], 0\n"
"s_add_u32 m0, s66, 22528\n"
"MM v[132:135], a[56:59], a[96:99], v[132:135]\n"
"buffer_load_dwordx4 v170, s[40:43], s67 offen lds\n"
"MM v[136:139], a[40:43], a[80:83], 0\n"
"MM v[136:139], a[56:59], a[100:103], v[136:139]\n"
"MM v[140:143], a[44:47], a[64:67], 0\n"
"s_add_u32 m0, s66, 23552\n"
"MM v[140:143], a[60:63], a[84:87], v[140:143]\n"
"buffer_load_dwordx4 v171, s[40:43], s67 offen lds\n"
"MM v[144:147], a[44:47], a[68:71], 0\n"
"MM v[144:147], a[60:63], a[88:91], v[144:147]\n"
"MM v[148:151], a[44:47], a[72:75], 0\n"
"MM v[148:151], a[60:63], a[92:95], v[148:151]\n"
"s_add_u32 s67, s67, 524160\n"
"MM v[152:155], a[44:47], a[76:79], 0\n"
"MM v[152:155], a[60:63], a[96:99], v[152:155]\n"
"MM v[156:159], a[44:47], a[80:83], 0\n"
"MM v[156:159], a[60:63], a[100:103], v[156:159]\n"
"s_waitcnt vmcnt(16)\n"
"s_waitcnt lgkmcnt(0)\n"
"s_barrier\n"
"MM v[0:3], a[0:3], a[104:107], v[0:3]\n"
"s_add_u32 m0, s66, 32768\n"
"ds_read_b64_tr_b16 a[32:33], v160 offset:49152\n"
"MM v[0:3], a[16:19], a[124:127], v[0:3]\n"
"buffer_load_dwordx4 v164, s[40:43], s67 offen lds\n"
"MM v[4:7], a[0:3], a[108:111], v[4:7]\n"
"ds_read_b64_tr_b16 a[34:35], v160 offset:50176\n"
"ds_read_b64_tr_b16 a[48:49], v160 offset:53248\n"
"MM v[4:7], a[16:19], a[128:131], v[4:7]\n"
"ds_read_b64_tr_b16 a[50:51], v160 offset:54272\n"
"s_add_u32 m0, s66, 33792\n"
"MM v[8:11], a[0:3], a[112:115], v[8:11]\n"
"ds_read_b64_tr_b16 a[36:37], v161 offset:49152\n"
"ds_read_b64_tr_b16 a[38:39], v161 offset:50176\n"
"MM v[8:11], a[16:19], a[132:135], v[8:11]\n"
"buffer_load_dwordx4 v165, s[40:43], s67 offen lds\n"
"MM v[12:15], a[0:3], a[116:119], v[12:15]\n"
"ds_read_b64_tr_b16 a[52:53], v161 offset:53248\n"
"ds_read_b64_tr_b16 a[54:55], v161 offset:54272\n"
"MM v[12:15], a[16:19], a[136:139], v[12:15]\n"
"s_add_u32 m0, s66, 34816\n"
"ds_read_b64_tr_b16 a[40:41], v160 offset:49664\n"
"MM v[16:19], a[0:3], a[120:123], v[16:19]\n"
"buffer_load_dwordx4 v166, s[40:43], s67 offen lds\n"
"MM v[16:19], a[16:19], a[140:143], v[16:19]\n"
"ds_read_b64_tr_b16 a[42:43], v160 offset:50688\n"
"MM v[20:23], a[4:7], a[104:107], v[20:23]\n"
"ds_read_b64_tr_b16 a[56:57], v160 offset:53760\n"
"MM v[20:23], a[20:23], a[124:127], v[20:23]\n"
"s_add_u32 m0, s66, 35840\n"
"ds_read_b64_tr_b16 a[58:59], v160 offset:54784\n"
"MM v[24:27], a[4:7], a[108:111], v[24:27]\n"
"buffer_load_dwordx4 v167, s[40:43], s67 offen lds\n"
"MM v[24:27], a[20:23], a[128:131], v[24:27]\n"
"ds_read_b64_tr_b16 a[44:45], v161 offset:49664\n"
"MM v[28:31], a[4:7], a[112:115], v[28:31]\n"
"ds_read_b64_tr_b16 a[46:47], v161 offset:50688\n"
"MM v[28:31], a[20:23], a[132:135], v[28:31]\n"
"MM v[32:35], a[4:7], a[116:119], v[32:35]\n"
"s_add_u32 m0, s66, 36864\n"
"ds_read_b64_tr_b16 a[60:61], v161 offset:53760\n"
"MM v[32:35], a[20:23], a[136:139], v[32:35]\n"
"buffer_load_dwordx4 v168, s[40:43], s67 offen lds\n"
"MM v[36:39], a[4:7], a[120:123], v[36:39]\n"
"ds_read_b64_tr_b16 a[62:63], v161 offset:54784\n"
"MM v[36:39], a[20:23], a[140:143], v[36:39]\n"
"MM v[40:43], a[8:11], a[104:107], v[40:43]\n"
"MM v[40:43], a[24:27], a[124:127], v[40:43]\n"
"s_add_u32 m0, s66, 37888\n"
"MM v[44:47], a[8:11], a[108:111], v[44:47]\n"
"buffer_load_dwordx4 v169, s[40:43], s67 offen lds\n"
"MM v[44:47], a[24:27], a[128:131], v[44:47]\n"
"MM v[48:51], a[8:11], a[112:115], v[48:51]\n"
"MM v[48:51], a[24:27], a[132:135], v[48:51]\n"
"MM v[52:55], a[8:11], a[116:119], v[52:55]\n"
"s_add_u32 m0, s66, 38912\n"
"MM v[52:55], a[24:27], a[136:139], v[52:55]\n"
"buffer_load_dwordx4 v170, s[40:43], s67 offen lds\n"
"MM v[56:59], a[8:11], a[120:123], v[56:59]\n"
"MM v[56:59], a[24:27], a[140:143], v[56:59]\n"
"MM v[60:63], a[12:15], a[104:107], v[60:63]\n"
"s_add_u32 m0, s66, 39936\n"
"MM v[60:63], a[28:31], a[124:127], v[60:63]\n"
"buffer_load_dwordx4 v171, s[40:43], s67 offen lds\n"
"MM v[64:67], a[12:15], a[108:111], v[64:67]\n"
"MM v[64:67], a[28:31], a[128:131], v[64:67]\n"
"MM v[68:71], a[12:15], a[112:115], v[68:71]\n"
"MM v[68:71], a[28:31], a[132:135], v[68:71]\n"
"s_add_u32 s67, s67, 128\n"
"MM v[72:75], a[12:15], a[116:119], v[72:75]\n"
"MM v[72:75], a[28:31], a[136:139], v[72:75]\n"
"MM v[76:79], a[12:15], a[120:123], v[76:79]\n"
"MM v[76:79], a[28:31], a[140:143], v[76:79]\n"
"s_waitcnt vmcnt(16)\n"
"s_waitcnt lgkmcnt(0)\n"
"s_barrier\n"
"MM v[80:83], a[32:35], a[104:107], v[80:83]\n"
"s_add_u32 m0, s66, 49152\n"
"ds_read_b64_tr_b16 a[0:1], v160 offset:0\n"
"MM v[80:83], a[48:51], a[124:127], v[80:83]\n"
"buffer_load_dwordx4 v164, s[40:43], s67 offen lds\n"
"MM v[84:87], a[32:35], a[108:111], v[84:87]\n"
"ds_read_b64_tr_b16 a[2:3], v160 offset:1024\n"
"ds_read_b64_tr_b16 a[16:17], v160 offset:4096\n"
"MM v[84:87], a[48:51], a[128:131], v[84:87]\n"
"ds_read_b64_tr_b16 a[18:19], v160 offset:5120\n"
"ds_read_b128 a[64:67], v162 offset:0\n"
"MM v[88:91], a[32:35], a[112:115], v[88:91]\n"
"s_add_u32 m0, s66, 50176\n"
"ds_read_b128 a[84:87], v163 offset:0\n"
"MM v[88:91], a[48:51], a[132:135], v[88:91]\n"
"buffer_load_dwordx4 v165, s[40:43], s67 offen lds\n"
"MM v[92:95], a[32:35], a[116:119], v[92:95]\n"
"ds_read_b128 a[68:71], v162 offset:2048\n"
"ds_read_b128 a[88:91], v163 offset:2048\n"
"MM v[92:95], a[48:51], a[136:139], v[92:95]\n"
"ds_read_b128 a[72:75], v162 offset:4096\n"
"s_add_u32 m0, s66, 51200\n"
"MM v[96:99], a[32:35], a[120:123], v[96:99]\n"
"ds_read_b128 a[92:95], v163 offset:4096\n"
"ds_read_b128 a[76:79], v162 offset:6144\n"
"MM v[96:99], a[48:51], a[140:143], v[96:99]\n"
"buffer_load_dwordx4 v166, s[40:43], s67 offen lds\n"
"MM v[100:103], a[36:39], a[104:107], v[100:103]\n"
"ds_read_b128 a[96:99], v163 offset:6144\n"
"ds_read_b128 a[80:83], v162 offset:8192\n"
"MM v[100:103], a[52:55], a[124:127], v[100:103]\n"
"s_add_u32 m0, s66, 52224\n"
"ds_read_b128 a[100:103], v163 offset:8192\n"
"MM v[104:107], a[36:39], a[108:111], v[104:107]\n"
"buffer_load_dwordx4 v167, s[40:43], s67 offen lds\n"
"MM v[104:107], a[52:55], a[128:131], v[104:107]\n"
"ds_read_b64_tr_b16 a[4:5], v161 offset:0\n"
"ds_read_b64_tr_b16 a[6:7], v161 offset:1024\n"
"MM v[108:111], a[36:39], a[112:115], v[108:111]\n"
"ds_read_b64_tr_b16 a[20:21], v161 offset:4096\n"
"MM v[108:111], a[52:55], a[132:135], v[108:111]\n"
"MM v[112:115], a[36:39], a[116:119], v[112:115]\n"
"ds_read_b64_tr_b16 a[22:23], v161 offset:5120\n"
"s_add_u32 m0, s66, 53248\n"
"MM v[112:115], a[52:55], a[136:139], v[112:115]\n"
"buffer_load_dwordx4 v168, s[40:43], s67 offen lds\n"
"MM v[116:119], a[36:39], a[120:123], v[116:119]\n"
"ds_read_b64_tr_b16 a[8:9], v160 offset:512\n"
"ds_read_b64_tr_b16 a[10:11], v160 offset:1536\n"
"MM v[116:119], a[52:55], a[140:143], v[116:119]\n"
"ds_read_b64_tr_b16 a[24:25], v160 offset:4608\n"
"MM v[120:123], a[40:43], a[104:107], v[120:123]\n"
"ds_read_b64_tr_b16 a[26:27], v160 offset:5632\n"
"MM v[120:123], a[56:59], a[124:127], v[120:123]\n"
"s_add_u32 m0, s66, 54272\n"
"ds_read_b64_tr_b16 a[12:13], v161 offset:512\n"
"MM v[124:127], a[40:43], a[108:111], v[124:127]\n"
"buffer_load_dwordx4 v169, s[40:43], s67 offen lds\n"
"MM v[124:127], a[56:59], a[128:131], v[124:127]\n"
"ds_read_b64_tr_b16 a[14:15], v161 offset:1536\n"
"MM v[128:131], a[40:43], a[112:115], v[128:131]\n"
"ds_read_b64_tr_b16 a[28:29], v161 offset:4608\n"
"MM v[128:131], a[56:59], a[132:135], v[128:131]\n"
"ds_read_b64_tr_b16 a[30:31], v161 offset:5632\n"
"MM v[132:135], a[40:43], a[116:119], v[132:135]\n"
"s_add_u32 m0, s66, 55296\n"
"MM v[132:135], a[56:59], a[136:139], v[132:135]\n"
"buffer_load_dwordx4 v170, s[40:43], s67 offen lds\n"
"MM v[136:139], a[40:43], a[120:123], v[136:139]\n"
"MM v[136:139], a[56:59], a[140:143], v[136:139]\n"
"MM v[140:143], a[44:47], a[104:107], v[140:143]\n"
"s_add_u32 m0, s66, 56320\n"
"MM v[140:143], a[60:63], a[124:127], v[140:143]\n"
"buffer_load_dwordx4 v171, s[40:43], s67 offen lds\n"
"MM v[144:147], a[44:47], a[108:111], v[144:147]\n"
"MM v[144:147], a[60:63], a[128:131], v[144:147]\n"
"MM v[148:151], a[44:47], a[112:115], v[148:151]\n"
"MM v[148:151], a[60:63], a[132:135], v[148:151]\n"
"s_add_u32 s67, s67, 524160\n"
"MM v[152:155], a[44:47], a[116:119], v[152:155]\n"
"MM v[152:155], a[60:63], a[136:139], v[152:155]\n"
"MM v[156:159], a[44:47], a[120:123], v[156:159]\n"
"MM v[156:159], a[60:63], a[140:143], v[156:159]\n"
"s_waitcnt vmcnt(16)\n"
"s_waitcnt lgkmcnt(0)\n"
"s_barrier\n"
"MM v[0:3], a[0:3], a[64:67], v[0:3]\n"
"s_add_u32 m0, s66, 0\n"
"ds_read_b64_tr_b16 a[32:33], v160 offset:16384\n"
"MM v[0:3], a[16:19], a[84:87], v[0:3]\n"
"buffer_load_dwordx4 v164, s[40:43], s67 offen lds\n"
"MM v[4:7], a[0:3], a[68:71], v[4:7]\n"
"ds_read_b64_tr_b16 a[34:35], v160 offset:17408\n"
"ds_read_b64_tr_b16 a[48:49], v160 offset:20480\n"
"MM v[4:7], a[16:19], a[88:91], v[4:7]\n"
"ds_read_b64_tr_b16 a[50:51], v160 offset:21504\n"
"s_add_u32 m0, s66, 1024\n"
"MM v[8:11], a[0:3], a[72:75], v[8:11]\n"
"ds_read_b64_tr_b16 a[36:37], v161 offset:16384\n"
"ds_read_b64_tr_b16 a[38:39], v161 offset:17408\n"
"MM v[8:11], a[16:19], a[92:95], v[8:11]\n"
"buffer_load_dwordx4 v165, s[40:43], s67 offen lds\n"
"MM v[12:15], a[0:3], a[76:79], v[12:15]\n"
"ds_read_b64_tr_b16 a[52:53], v161 offset:20480\n"
"ds_read_b64_tr_b16 a[54:55], v161 offset:21504\n"
"MM v[12:15], a[16:19], a[96:99], v[12:15]\n"
"s_add_u32 m0, s66, 2048\n"
"ds_read_b64_tr_b16 a[40:41], v160 offset:16896\n"
"MM v[16:19], a[0:3], a[80:83], v[16:19]\n"
"buffer_load_dwordx4 v166, s[40:43], s67 offen lds\n"
"MM v[16:19], a[16:19], a[100:103], v[16:19]\n"
"ds_read_b64_tr_b16 a[42:43], v160 offset:17920\n"
"MM v[20:23], a[4:7], a[64:67], v[20:23]\n"
"ds_read_b64_tr_b16 a[56:57], v160 offset:20992\n"
"MM v[20:23], a[20:23], a[84:87], v[20:23]\n"
"s_add_u32 m0, s66, 3072\n"
"ds_read_b64_tr_b16 a[58:59], v160 offset:22016\n"
"MM v[24:27], a[4:7], a[68:71], v[24:27]\n"
"buffer_load_dwordx4 v167, s[40:43], s67 offen lds\n"
"MM v[24:27], a[20:23], a[88:91], v[24:27]\n"
"ds_read_b64_tr_b16 a[44:45], v161 offset:16896\n"
"MM v[28:31], a[4:7], a[72:75], v[28:31]\n"
"ds_read_b64_tr_b16 a[46:47], v161 offset:17920\n"
"MM v[28:31], a[20:23], a[92:95], v[28:31]\n"
"MM v[32:35], a[4:7], a[76:79], v[32:35]\n"
"s_add_u32 m0, s66, 4096\n"
"ds_read_b64_tr_b16 a[60:61], v161 offset:20992\n"
"MM v[32:35], a[20:23], a[96:99], v[32:35]\n"
"buffer_load_dwordx4 v168, s[40:43], s67 offen lds\n"
"MM v[36:39], a[4:7], a[80:83], v[36:39]\n"
"ds_read_b64_tr_b16 a[62:63], v161 offset:22016\n"
"MM v[36:39], a[20:23], a[100:103], v[36:39]\n"
"MM v[40:43], a[8:11], a[64:67], v[40:43]\n"
"MM v[40:43], a[24:27], a[84:87], v[40:43]\n"
"s_add_u32 m0, s66, 5120\n"
"MM v[44:47], a[8:11], a[68:71], v[44:47]\n"
"buffer_load_dwordx4 v169, s[40:43], s67 offen lds\n"
"MM v[44:47], a[24:27], a[88:91], v[44:47]\n"
"MM v[48:51], a[8:11], a[72:75], v[48:51]\n"
"MM v[48:51], a[24:27], a[92:95], v[48:51]\n"
"MM v[52:55], a[8:11], a[76:79], v[52:55]\n"
"s_add_u32 m0, s66, 6144\n"
"MM v[52:55], a[24:27], a[96:99], v[52:55]\n"
"buffer_load_dwordx4 v170, s[40:43], s67 offen lds\n"
"MM v[56:59], a[8:11], a[80:83], v[56:59]\n"
"MM v[56:59], a[24:27], a[100:103], v[56:59]\n"
"MM v[60:63], a[12:15], a[64:67], v[60:63]\n"
"s_add_u32 m0, s66, 7168\n"
"MM v[60:63], a[28:31], a[84:87], v[60:63]\n"
"buffer_load_dwordx4 v171, s[40:43], s67 offen lds\n"
"MM v[64:67], a[12:15], a[68:71], v[64:67]\n"
"MM v[64:67], a[28:31], a[88:91], v[64:67]\n"
"MM v[68:71], a[12:15], a[72:75], v[68:71]\n"
"MM v[68:71], a[28:31], a[92:95], v[68:71]\n"
"s_add_u32 s67, s67, 128\n"
"MM v[72:75], a[12:15], a[76:79], v[72:75]\n"
"MM v[72:75], a[28:31], a[96:99], v[72:75]\n"
"MM v[76:79], a[12:15], a[80:83], v[76:79]\n"
"MM v[76:79], a[28:31], a[100:103], v[76:79]\n"
"s_waitcnt vmcnt(16)\n"
"s_waitcnt lgkmcnt(0)\n"
"s_barrier\n"
"MM v[80:83], a[32:35], a[64:67], v[80:83]\n"
"s_add_u32 m0, s66, 16384\n"
"ds_read_b64_tr_b16 a[0:1], v160 offset:32768\n"
"MM v[80:83], a[48:51], a[84:87], v[80:83]\n"
"buffer_load_dwordx4 v164, s[40:43], s67 offen lds\n"
"MM v[84:87], a[32:35], a[68:71], v[84:87]\n"
"ds_read_b64_tr_b16 a[2:3], v160 offset:33792\n"
"ds_read_b64_tr_b16 a[16:17], v160 offset:36864\n"
"MM v[84:87], a[48:51], a[88:91], v[84:87]\n"
"ds_read_b64_tr_b16 a[18:19], v160 offset:37888\n"
"ds_read_b128 a[104:107], v162 offset:20480\n"
"MM v[88:91], a[32:35], a[72:75], v[88:91]\n"
"s_add_u32 m0, s66, 17408\n"
"ds_read_b128 a[124:127], v163 offset:20480\n"
"MM v[88:91], a[48:51], a[92:95], v[88:91]\n"
"buffer_load_dwordx4 v165, s[40:43], s67 offen lds\n"
"MM v[92:95], a[32:35], a[76:79], v[92:95]\n"
"ds_read_b128 a[108:111], v162 offset:22528\n"
"ds_read_b128 a[128:131], v163 offset:22528\n"
"MM v[92:95], a[48:51], a[96:99], v[92:95]\n"
"ds_read_b128 a[112:115], v162 offset:24576\n"
"s_add_u32 m0, s66, 18432\n"
"MM v[96:99], a[32:35], a[80:83], v[96:99]\n"
"ds_read_b128 a[132:135], v163 offset:24576\n"
"ds_read_b128 a[116:119], v162 offset:26624\n"
"MM v[96:99], a[48:51], a[100:103], v[96:99]\n"
"buffer_load_dwordx4 v166, s[40:43], s67 offen lds\n"
"MM v[100:103], a[36:39], a[64:67], v[100:103]\n"
"ds_read_b128 a[136:139], v163 offset:26624\n"
"ds_read_b128 a[120:123], v162 offset:28672\n"
"MM v[100:103], a[52:55], a[84:87], v[100:103]\n"
"s_add_u32 m0, s66, 19456\n"
"ds_read_b128 a[140:143], v163 offset:28672\n"
"MM v[104:107], a[36:39], a[68:71], v[104:107]\n"
"buffer_load_dwordx4 v167, s[40:43], s67 offen lds\n"
"MM v[104:107], a[52:55], a[88:91], v[104:107]\n"
"ds_read_b64_tr_b16 a[4:5], v161 offset:32768\n"
"ds_read_b64_tr_b16 a[6:7], v161 offset:33792\n"
"MM v[108:111], a[36:39], a[72:75], v[108:111]\n"
"ds_read_b64_tr_b16 a[20:21], v161 offset:36864\n"
"MM v[108:111], a[52:55], a[92:95], v[108:111]\n"
"MM v[112:115], a[36:39], a[76:79], v[112:115]\n"
"ds_read_b64_tr_b16 a[22:23], v161 offset:37888\n"
"s_add_u32 m0, s66, 20480\n"
"MM v[112:115], a[52:55], a[96:99], v[112:115]\n"
"buffer_load_dwordx4 v168, s[40:43], s67 offen lds\n"
"MM v[116:119], a[36:39], a[80:83], v[116:119]\n"
"ds_read_b64_tr_b16 a[8:9], v160 offset:33280\n"
"ds_read_b64_tr_b16 a[10:11], v160 offset:34304\n"
"MM v[116:119], a[52:55], a[100:103], v[116:119]\n"
"ds_read_b64_tr_b16 a[24:25], v160 offset:37376\n"
"MM v[120:123], a[40:43], a[64:67], v[120:123]\n"
"ds_read_b64_tr_b16 a[26:27], v160 offset:38400\n"
"MM v[120:123], a[56:59], a[84:87], v[120:123]\n"
"s_add_u32 m0, s66, 21504\n"
"ds_read_b64_tr_b16 a[12:13], v161 offset:33280\n"
"MM v[124:127], a[40:43], a[68:71], v[124:127]\n"
"buffer_load_dwordx4 v169, s[40:43], s67 offen lds\n"
"MM v[124:127], a[56:59], a[88:91], v[124:127]\n"
"ds_read_b64_tr_b16 a[14:15], v161 offset:34304\n"
"MM v[128:131], a[40:43], a[72:75], v[128:131]\n"
"ds_read_b64_tr_b16 a[28:29], v161 offset:37376\n"
"MM v[128:131], a[56:59], a[92:95], v[128:131]\n"
"ds_read_b64_tr_b16 a[30:31], v161 offset:38400\n"
"MM v[132:135], a[40:43], a[76:79], v[132:135]\n"
"s_add_u32 m0, s66, 22528\n"
"MM v[132:135], a[56:59], a[96:99], v[132:135]\n"
"buffer_load_dwordx4 v170, s[40:43], s67 offen lds\n"
"MM v[136:139], a[40:43], a[80:83], v[136:139]\n"
"MM v[136:139], a[56:59], a[100:103], v[136:139]\n"
"MM v[140:143], a[44:47], a[64:67], v[140:143]\n"
"s_add_u32 m0, s66, 23552\n"
"MM v[140:143], a[60:63], a[84:87], v[140:143]\n"
"buffer_load_dwordx4 v171, s[40:43], s67 offen lds\n"
"MM v[144:147], a[44:47], a[68:71], v[144:147]\n"
"MM v[144:147], a[60:63], a[88:91], v[144:147]\n"
"MM v[148:151], a[44:47], a[72:75], v[148:151]\n"
"MM v[148:151], a[60:63], a[92:95], v[148:151]\n"
"s_add_u32 s67, s67, 524160\n"
"MM v[152:155], a[44:47], a[76:79], v[152:155]\n"
"MM v[152:155], a[60:63], a[96:99], v[152:155]\n"
"MM v[156:159], a[44:47], a[80:83], v[156:159]\n"
"MM v[156:159], a[60:63], a[100:103], v[156:159]\n"
"s_waitcnt vmcnt(16)\n"
"s_waitcnt lgkmcnt(0)\n"
"s_barrier\n"
"MM v[0:3], a[0:3], a[104:107], v[0:3]\n"
"s_add_u32 m0, s66, 32768\n"
"ds_read_b64_tr_b16 a[32:33], v160 offset:49152\n"
"MM v[0:3], a[16:19], a[124:127], v[0:3]\n"
"buffer_load_dwordx4 v164, s[40:43], s67 offen lds\n"
"MM v[4:7], a[0:3], a[108:111], v[4:7]\n"
"ds_read_b64_tr_b16 a[34:35], v160 offset:50176\n"
"ds_read_b64_tr_b16 a[48:49], v160 offset:53248\n"
"MM v[4:7], a[16:19], a[128:131], v[4:7]\n"
"ds_read_b64_tr_b16 a[50:51], v160 offset:54272\n"
"s_add_u32 m0, s66, 33792\n"
"MM v[8:11], a[0:3], a[112:115], v[8:11]\n"
"ds_read_b64_tr_b16 a[36:37], v161 offset:49152\n"
"ds_read_b64_tr_b16 a[38:39], v161 offset:50176\n"
"MM v[8:11], a[16:19], a[132:135], v[8:11]\n"
"buffer_load_dwordx4 v165, s[40:43], s67 offen lds\n"
"MM v[12:15], a[0:3], a[116:119], v[12:15]\n"
"ds_read_b64_tr_b16 a[52:53], v161 offset:53248\n"
"ds_read_b64_tr_b16 a[54:55], v161 offset:54272\n"
"MM v[12:15], a[16:19], a[136:139], v[12:15]\n"
"s_add_u32 m0, s66, 34816\n"
"ds_read_b64_tr_b16 a[40:41], v160 offset:49664\n"
"MM v[16:19], a[0:3], a[120:123], v[16:19]\n"
"buffer_load_dwordx4 v166, s[40:43], s67 offen lds\n"
"MM v[16:19], a[16:19], a[140:143], v[16:19]\n"
"ds_read_b64_tr_b16 a[42:43], v160 offset:50688\n"
"MM v[20:23], a[4:7], a[104:107], v[20:23]\n"
"ds_read_b64_tr_b16 a[56:57], v160 offset:53760\n"
"MM v[20:23], a[20:23], a[124:127], v[20:23]\n"
"s_add_u32 m0, s66, 35840\n"
"ds_read_b64_tr_b16 a[58:59], v160 offset:54784\n"
"MM v[24:27], a[4:7], a[108:111], v[24:27]\n"
"buffer_load_dwordx4 v167, s[40:43], s67 offen lds\n"
"MM v[24:27], a[20:23], a[128:131], v[24:27]\n"
"ds_read_b64_tr_b16 a[44:45], v161 offset:49664\n"
"MM v[28:31], a[4:7], a[112:115], v[28:31]\n"
"ds_read_b64_tr_b16 a[46:47], v161 offset:50688\n"
"MM v[28:31], a[20:23], a[132:135], v[28:31]\n"
"MM v[32:35], a[4:7], a[116:119], v[32:35]\n"
"s_add_u32 m0, s66, 36864\n"
"ds_read_b64_tr_b16 a[60:61], v161 offset:53760\n"
"MM v[32:35], a[20:23], a[136:139], v[32:35]\n"
"buffer_load_dwordx4 v168, s[40:43], s67 offen lds\n"
"MM v[36:39], a[4:7], a[120:123], v[36:39]\n"
"ds_read_b64_tr_b16 a[62:63], v161 offset:54784\n"
"MM v[36:39], a[20:23], a[140:143], v[36:39]\n"
"MM v[40:43], a[8:11], a[104:107], v[40:43]\n"
"MM v[40:43], a[24:27], a[124:127], v[40:43]\n"
"s_add_u32 m0, s66, 37888\n"
"MM v[44:47], a[8:11], a[108:111], v[44:47]\n"
"buffer_load_dwordx4 v169, s[40:43], s67 offen lds\n"
"MM v[44:47], a[24:27], a[128:131], v[44:47]\n"
"MM v[48:51], a[8:11], a[112:115], v[48:51]\n"
"MM v[48:51], a[24:27], a[132:135], v[48:51]\n"
"MM v[52:55], a[8:11], a[116:119], v[52:55]\n"
"s_add_u32 m0, s66, 38912\n"
"MM v[52:55], a[24:27], a[136:139], v[52:55]\n"
"buffer_load_dwordx4 v170, s[40:43], s67 offen lds\n"
"MM v[56:59], a[8:11], a[120:123], v[56:59]\n"
"MM v[56:59], a[24:27], a[140:143], v[56:59]\n"
"MM v[60:63], a[12:15], a[104:107], v[60:63]\n"
"s_add_u32 m0, s66, 39936\n"
"MM v[60:63], a[28:31], a[124:127], v[60:63]\n"
"buffer_load_dwordx4 v171, s[40:43], s67 offen lds\n"
"MM v[64:67], a[12:15], a[108:111], v[64:67]\n"
"MM v[64:67], a[28:31], a[128:131], v[64:67]\n"
"MM v[68:71], a[12:15], a[112:115], v[68:71]\n"
"MM v[68:71], a[28:31], a[132:135], v[68:71]\n"
"s_add_u32 s67, s67, 128\n"
"MM v[72:75], a[12:15], a[116:119], v[72:75]\n"
"MM v[72:75], a[28:31], a[136:139], v[72:75]\n"
"MM v[76:79], a[12:15], a[120:123], v[76:79]\n"
"MM v[76:79], a[28:31], a[140:143], v[76:79]\n"
"s_waitcnt vmcnt(16)\n"
"s_waitcnt lgkmcnt(0)\n"
"s_barrier\n"
"MM v[80:83], a[32:35], a[104:107], v[80:83]\n"
"s_add_u32 m0, s66, 49152\n"
"ds_read_b64_tr_b16 a[0:1], v160 offset:0\n"
"MM v[80:83], a[48:51], a[124:127], v[80:83]\n"
"buffer_load_dwordx4 v164, s[40:43], s67 offen lds\n"
"MM v[84:87], a[32:35], a[108:111], v[84:87]\n"
"ds_read_b64_tr_b16 a[2:3], v160 offset:1024\n"
"ds_read_b64_tr_b16 a[16:17], v160 offset:4096\n"
"MM v[84:87], a[48:51], a[128:131], v[84:87]\n"
"ds_read_b64_tr_b16 a[18:19], v160 offset:5120\n"
"ds_read_b128 a[64:67], v162 offset:0\n"
"MM v[88:91], a[32:35], a[112:115], v[88:91]\n"
"s_add_u32 m0, s66, 50176\n"
"ds_read_b128 a[84:87], v163 offset:0\n"
"MM v[88:91], a[48:51], a[132:135], v[88:91]\n"
"buffer_load_dwordx4 v165, s[40:43], s67 offen lds\n"
"MM v[92:95], a[32:35], a[116:119], v[92:95]\n"
"ds_read_b128 a[68:71], v162 offset:2048\n"
"ds_read_b128 a[88:91], v163 offset:2048\n"
"MM v[92:95], a[48:51], a[136:139], v[92:95]\n"
"ds_read_b128 a[72:75], v162 offset:4096\n"
"s_add_u32 m0, s66, 51200\n"
"MM v[96:99], a[32:35], a[120:123], v[96:99]\n"
"ds_read_b128 a[92:95], v163 offset:4096\n"
"ds_read_b128 a[76:79], v162 offset:6144\n"
"MM v[96:99], a[48:51], a[140:143], v[96:99]\n"
"buffer_load_dwordx4 v166, s[40:43], s67 offen lds\n"
"MM v[100:103], a[36:39], a[104:107], v[100:103]\n"
"ds_read_b128 a[96:99], v163 offset:6144\n"
"ds_read_b128 a[80:83], v162 offset:8192\n"
"MM v[100:103], a[52:55], a[124:127], v[100:103]\n"
"s_add_u32 m0, s66, 52224\n"
"ds_read_b128 a[100:103], v163 offset:8192\n"
"MM v[104:107], a[36:39], a[108:111], v[104:107]\n"
"buffer_load_dwordx4 v167, s[40:43], s67 offen lds\n"
"MM v[104:107], a[52:55], a[128:131], v[104:107]\n"
"ds_read_b64_tr_b16 a[4:5], v161 offset:0\n"
"ds_read_b64_tr_b16 a[6:7], v161 offset:1024\n"
"MM v[108:111], a[36:39], a[112:115], v[108:111]\n"
"ds_read_b64_tr_b16 a[20:21], v161 offset:4096\n"
"MM v[108:111], a[52:55], a[132:135], v[108:111]\n"
"MM v[112:115], a[36:39], a[116:119], v[112:115]\n"
"ds_read_b64_tr_b16 a[22:23], v161 offset:5120\n"
"s_add_u32 m0, s66, 53248\n"
"MM v[112:115], a[52:55], a[136:139], v[112:115]\n"
"buffer_load_dwordx4 v168, s[40:43], s67 offen lds\n"
"MM v[116:119], a[36:39], a[120:123], v[116:119]\n"
"ds_read_b64_tr_b16 a[8:9], v160 offset:512\n"
"ds_read_b64_tr_b16 a[10:11], v160 offset:1536\n"
"MM v[116:119], a[52:55], a[140:143], v[116:119]\n"
"ds_read_b64_tr_b16 a[24:25], v160 offset:4608\n"
"MM v[120:123], a[40:43], a[104:107], v[120:123]\n"
"ds_read_b64_tr_b16 a[26:27], v160 offset:5632\n"
"MM v[120:123], a[56:59], a[124:127], v[120:123]\n"
"s_add_u32 m0, s66, 54272\n"
"ds_read_b64_tr_b16 a[12:13], v161 offset:512\n"
"MM v[124:127], a[40:43], a[108:111], v[124:127]\n"
"buffer_load_dwordx4 v169, s[40:43], s67 offen lds\n"
"MM v[124:127], a[56:59], a[128:131], v[124:127]\n"
"ds_read_b64_tr_b16 a[14:15], v161 offset:1536\n"
"MM v[128:131], a[40:43], a[112:115], v[128:131]\n"
"ds_read_b64_tr_b16 a[28:29], v161 offset:4608\n"
"MM v[128:131], a[56:59], a[132:135], v[128:131]\n"
"ds_read_b64_tr_b16 a[30:31], v161 offset:5632\n"
"MM v[132:135], a[40:43], a[116:119], v[132:135]\n"
"s_add_u32 m0, s66, 55296\n"
"MM v[132:135], a[56:59], a[136:139], v[132:135]\n"
"buffer_load_dwordx4 v170, s[40:43], s67 offen lds\n"
"MM v[136:139], a[40:43], a[120:123], v[136:139]\n"
"MM v[136:139], a[56:59], a[140:143], v[136:139]\n"
"MM v[140:143], a[44:47], a[104:107], v[140:143]\n"
"s_add_u32 m0, s66, 56320\n"
"MM v[140:143], a[60:63], a[124:127], v[140:143]\n"
"buffer_load_dwordx4 v171, s[40:43], s67 offen lds\n"
"MM v[144:147], a[44:47], a[108:111], v[144:147]\n"
"MM v[144:147], a[60:63], a[128:131], v[144:147]\n"
"MM v[148:151], a[44:47], a[112:115], v[148:151]\n"
"MM v[148:151], a[60:63], a[132:135], v[148:151]\n"
"s_add_u32 s67, s67, 524160\n"
"MM v[152:155], a[44:47], a[116:119], v[152:155]\n"
"MM v[152:155], a[60:63], a[136:139], v[152:155]\n"
"MM v[156:159], a[44:47], a[120:123], v[156:159]\n"
"MM v[156:159], a[60:63], a[140:143], v[156:159]\n"
"s_waitcnt vmcnt(16)\n"
"s_waitcnt lgkmcnt(0)\n"
"s_barrier\n"
"MM v[0:3], a[0:3], a[64:67], v[0:3]\n"
"s_add_u32 m0, s66, 0\n"
"ds_read_b64_tr_b16 a[32:33], v160 offset:16384\n"
"MM v[0:3], a[16:19], a[84:87], v[0:3]\n"
"buffer_load_dwordx4 v164, s[40:43], s67 offen lds\n"
"MM v[4:7], a[0:3], a[68:71], v[4:7]\n"
"ds_read_b64_tr_b16 a[34:35], v160 offset:17408\n"
"ds_read_b64_tr_b16 a[48:49], v160 offset:20480\n"
"MM v[4:7], a[16:19], a[88:91], v[4:7]\n"
"ds_read_b64_tr_b16 a[50:51], v160 offset:21504\n"
"s_add_u32 m0, s66, 1024\n"
"MM v[8:11], a[0:3], a[72:75], v[8:11]\n"
"ds_read_b64_tr_b16 a[36:37], v161 offset:16384\n"
"ds_read_b64_tr_b16 a[38:39], v161 offset:17408\n"
"MM v[8:11], a[16:19], a[92:95], v[8:11]\n"
"buffer_load_dwordx4 v165, s[40:43], s67 offen lds\n"
"MM v[12:15], a[0:3], a[76:79], v[12:15]\n"
"ds_read_b64_tr_b16 a[52:53], v161 offset:20480\n"
"ds_read_b64_tr_b16 a[54:55], v161 offset:21504\n"
"MM v[12:15], a[16:19], a[96:99], v[12:15]\n"
"s_add_u32 m0, s66, 2048\n"
"ds_read_b64_tr_b16 a[40:41], v160 offset:16896\n"
"MM v[16:19], a[0:3], a[80:83], v[16:19]\n"
"buffer_load_dwordx4 v166, s[40:43], s67 offen lds\n"
"MM v[16:19], a[16:19], a[100:103], v[16:19]\n"
"ds_read_b64_tr_b16 a[42:43], v160 offset:17920\n"
"MM v[20:23], a[4:7], a[64:67], v[20:23]\n"
"ds_read_b64_tr_b16 a[56:57], v160 offset:20992\n"
"MM v[20:23], a[20:23], a[84:87], v[20:23]\n"
"s_add_u32 m0, s66, 3072\n"
"ds_read_b64_tr_b16 a[58:59], v160 offset:22016\n"
"MM v[24:27], a[4:7], a[68:71], v[24:27]\n"
"buffer_load_dwordx4 v167, s[40:43], s67 offen lds\n"
"MM v[24:27], a[20:23], a[88:91], v[24:27]\n"
"ds_read_b64_tr_b16 a[44:45], v161 offset:16896\n"
"MM v[28:31], a[4:7], a[72:75], v[28:31]\n"
"ds_read_b64_tr_b16 a[46:47], v161 offset:17920\n"
"MM v[28:31], a[20:23], a[92:95], v[28:31]\n"
"s_add_u32 m0, s66, 4096\n"
"MM v[32:35], a[4:7], a[76:79], v[32:35]\n"
"ds_read_b64_tr_b16 a[60:61], v161 offset:20992\n"
"MM v[32:35], a[20:23], a[96:99], v[32:35]\n"
"buffer_load_dwordx4 v168, s[40:43], s67 offen lds\n"
"MM v[36:39], a[4:7], a[80:83], v[36:39]\n"
"ds_read_b64_tr_b16 a[62:63], v161 offset:22016\n"
"MM v[36:39], a[20:23], a[100:103], v[36:39]\n"
"MM v[40:43], a[8:11], a[64:67], v[40:43]\n"
"s_add_u32 m0, s66, 5120\n"
"MM v[40:43], a[24:27], a[84:87], v[40:43]\n"
"buffer_load_dwordx4 v169, s[40:43], s67 offen lds\n"
"MM v[44:47], a[8:11], a[68:71], v[44:47]\n"
"MM v[44:47], a[24:27], a[88:91], v[44:47]\n"
"MM v[48:51], a[8:11], a[72:75], v[48:51]\n"
"MM v[48:51], a[24:27], a[92:95], v[48:51]\n"
"s_add_u32 m0, s66, 6144\n"
"MM v[52:55], a[8:11], a[76:79], v[52:55]\n"
"buffer_load_dwordx4 v170, s[40:43], s67 offen lds\n"
"MM v[52:55], a[24:27], a[96:99], v[52:55]\n"
"MM v[56:59], a[8:11], a[80:83], v[56:59]\n"
"MM v[56:59], a[24:27], a[100:103], v[56:59]\n"
"MM v[60:63], a[12:15], a[64:67], v[60:63]\n"
"s_add_u32 m0, s66, 7168\n"
"MM v[60:63], a[28:31], a[84:87], v[60:63]\n"
"buffer_load_dwordx4 v171, s[40:43], s67 offen lds\n"
"MM v[64:67], a[12:15], a[68:71], v[64:67]\n"
"MM v[64:67], a[28:31], a[88:91], v[64:67]\n"
"MM v[68:71], a[12:15], a[72:75], v[68:71]\n"
"s_add_u32 s67, s67, 128\n"
"MM v[68:71], a[28:31], a[92:95], v[68:71]\n"
"ds_read_b32 v254, v191 offset:0\n"
"MM v[72:75], a[12:15], a[76:79], v[72:75]\n"
"MM v[72:75], a[28:31], a[96:99], v[72:75]\n"
"MM v[76:79], a[12:15], a[80:83], v[76:79]\n"
"MM v[76:79], a[28:31], a[100:103], v[76:79]\n"
"s_waitcnt vmcnt(16)\n"
"s_waitcnt lgkmcnt(0)\n"
"s_barrier\n"
"MM v[80:83], a[32:35], a[64:67], v[80:83]\n"
"s_add_u32 m0, s66, 16384\n"
"ds_read_b64_tr_b16 a[0:1], v160 offset:32768\n"
"MM v[80:83], a[48:51], a[84:87], v[80:83]\n"
"buffer_load_dwordx4 v164, s[40:43], s67 offen lds\n"
"MM v[84:87], a[32:35], a[68:71], v[84:87]\n"
"ds_read_b64_tr_b16 a[2:3], v160 offset:33792\n"
"ds_read_b64_tr_b16 a[16:17], v160 offset:36864\n"
"MM v[84:87], a[48:51], a[88:91], v[84:87]\n"
"ds_read_b64_tr_b16 a[18:19], v160 offset:37888\n"
"ds_read_b128 a[104:107], v162 offset:20480\n"
"MM v[88:91], a[32:35], a[72:75], v[88:91]\n"
"s_add_u32 m0, s66, 17408\n"
"ds_read_b128 a[124:127], v163 offset:20480\n"
"MM v[88:91], a[48:51], a[92:95], v[88:91]\n"
"buffer_load_dwordx4 v165, s[40:43], s67 offen lds\n"
"MM v[92:95], a[32:35], a[76:79], v[92:95]\n"
"ds_read_b128 a[108:111], v162 offset:22528\n"
"ds_read_b128 a[128:131], v163 offset:22528\n"
"MM v[92:95], a[48:51], a[96:99], v[92:95]\n"
"ds_read_b128 a[112:115], v162 offset:24576\n"
"s_add_u32 m0, s66, 18432\n"
"MM v[96:99], a[32:35], a[80:83], v[96:99]\n"
"ds_read_b128 a[132:135], v163 offset:24576\n"
"ds_read_b128 a[116:119], v162 offset:26624\n"
"MM v[96:99], a[48:51], a[100:103], v[96:99]\n"
"buffer_load_dwordx4 v166, s[40:43], s67 offen lds\n"
"MM v[100:103], a[36:39], a[64:67], v[100:103]\n"
"ds_read_b128 a[136:139], v163 offset:26624\n"
"ds_read_b128 a[120:123], v162 offset:28672\n"
"MM v[100:103], a[52:55], a[84:87], v[100:103]\n"
"s_add_u32 m0, s66, 19456\n"
"ds_read_b128 a[140:143], v163 offset:28672\n"
"MM v[104:107], a[36:39], a[68:71], v[104:107]\n"
"buffer_load_dwordx4 v167, s[40:43], s67 offen lds\n"
"MM v[104:107], a[52:55], a[88:91], v[104:107]\n"
"ds_read_b64_tr_b16 a[4:5], v161 offset:32768\n"
"ds_read_b64_tr_b16 a[6:7], v161 offset:33792\n"
"MM v[108:111], a[36:39], a[72:75], v[108:111]\n"
"ds_read_b64_tr_b16 a[20:21], v161 offset:36864\n"
"MM v[108:111], a[52:55], a[92:95], v[108:111]\n"
"MM v[112:115], a[36:39], a[76:79], v[112:115]\n"
"ds_read_b64_tr_b16 a[22:23], v161 offset:37888\n"
"s_add_u32 m0, s66, 20480\n"
"MM v[112:115], a[52:55], a[96:99], v[112:115]\n"
"buffer_load_dwordx4 v168, s[40:43], s67 offen lds\n"
"MM v[116:119], a[36:39], a[80:83], v[116:119]\n"
"ds_read_b64_tr_b16 a[8:9], v160 offset:33280\n"
"ds_read_b64_tr_b16 a[10:11], v160 offset:34304\n"
"MM v[116:119], a[52:55], a[100:103], v[116:119]\n"
"ds_read_b64_tr_b16 a[24:25], v160 offset:37376\n"
"MM v[120:123], a[40:43], a[64:67], v[120:123]\n"
"ds_read_b64_tr_b16 a[26:27], v160 offset:38400\n"
"MM v[120:123], a[56:59], a[84:87], v[120:123]\n"
"s_add_u32 m0, s66, 21504\n"
"ds_read_b64_tr_b16 a[12:13], v161 offset:33280\n"
"MM v[124:127], a[40:43], a[68:71], v[124:127]\n"
"buffer_load_dwordx4 v169, s[40:43], s67 offen lds\n"
"MM v[124:127], a[56:59], a[88:91], v[124:127]\n"
"ds_read_b64_tr_b16 a[14:15], v161 offset:34304\n"
"MM v[128:131], a[40:43], a[72:75], v[128:131]\n"
"ds_read_b64_tr_b16 a[28:29], v161 offset:37376\n"
"MM v[128:131], a[56:59], a[92:95], v[128:131]\n"
"ds_read_b64_tr_b16 a[30:31], v161 offset:38400\n"
"MM v[132:135], a[40:43], a[76:79], v[132:135]\n"
"s_add_u32 m0, s66, 22528\n"
"MM v[132:135], a[56:59], a[96:99], v[132:135]\n"
"buffer_load_dwordx4 v170, s[40:43], s67 offen lds\n"
"MM v[136:139], a[40:43], a[80:83], v[136:139]\n"
"MM v[136:139], a[56:59], a[100:103], v[136:139]\n"
"MM v[140:143], a[44:47], a[64:67], v[140:143]\n"
"s_add_u32 m0, s66, 23552\n"
"MM v[140:143], a[60:63], a[84:87], v[140:143]\n"
"buffer_load_dwordx4 v171, s[40:43], s67 offen lds\n"
"MM v[144:147], a[44:47], a[68:71], v[144:147]\n"
"MM v[144:147], a[60:63], a[88:91], v[144:147]\n"
"MM v[148:151], a[44:47], a[72:75], v[148:151]\n"
"MM v[148:151], a[60:63], a[92:95], v[148:151]\n"
"s_add_u32 s67, s67, 524160\n"
"MM v[152:155], a[44:47], a[76:79], v[152:155]\n"
"MM v[152:155], a[60:63], a[96:99], v[152:155]\n"
"MM v[156:159], a[44:47], a[80:83], v[156:159]\n"
"MM v[156:159], a[60:63], a[100:103], v[156:159]\n"
"s_branch K1_EW\n"
"K1_ROLE_X2:\n"
"s_nop 3\n"
"v_add_u32 v164, 0, v18\n"
"v_add_u32 v165, 10240, v19\n"
"v_add_u32 v166, 20480, v18\n"
"v_add_u32 v167, 30720, v19\n"
"v_add_u32 v168, 40960, v18\n"
"v_add_u32 v169, 51200, v19\n"
"v_add_u32 v170, 61440, v18\n"
"v_add_u32 v171, 71680, v19\n"
"v_add_u32 v172, 81920, v18\n"
"v_add_u32 v173, 92160, v19\n"
"s_add_u32 s66, s77, 114688\n"
"s_mov_b32 s67, 128\n"
"s_add_u32 m0, s66, 20480\n"
"s_nop 0\n"
"buffer_load_dwordx4 v164, s[36:39], s67 offen lds\n"
"s_add_u32 m0, s66, 21504\n"
"s_nop 0\n"
"buffer_load_dwordx4 v165, s[36:39], s67 offen lds\n"
"s_add_u32 m0, s66, 22528\n"
"s_nop 0\n"
"buffer_load_dwordx4 v166, s[36:39], s67 offen lds\n"
"s_add_u32 m0, s66, 23552\n"
"s_nop 0\n"
"buffer_load_dwordx4 v167, s[36:39], s67 offen lds\n"
"s_add_u32 m0, s66, 24576\n"
"s_nop 0\n"
"buffer_load_dwordx4 v168, s[36:39], s67 offen lds\n"
"s_add_u32 m0, s66, 25600\n"
"s_nop 0\n"
"buffer_load_dwordx4 v169, s[36:39], s67 offen lds\n"
"s_add_u32 m0, s66, 26624\n"
"s_nop 0\n"
"buffer_load_dwordx4 v170, s[36:39], s67 offen lds\n"
"s_add_u32 m0, s66, 27648\n"
"s_nop 0\n"
"buffer_load_dwordx4 v171, s[36:39], s67 offen lds\n"
"s_add_u32 m0, s66, 28672\n"
"s_nop 0\n"
"buffer_load_dwordx4 v172, s[36:39], s67 offen lds\n"
"s_add_u32 m0, s66, 29696\n"
"s_nop 0\n"
"buffer_load_dwordx4 v173, s[36:39], s67 offen lds\n"
"s_add_u32 s67, s67, 128\n"
"s_lshl_b32 s68, s63, 4\n"
"s_add_u32 s68, s68, s64\n"
"s_lshl_b32 s68, s68, 1\n"
"s_add_u32 s68, s68, s61\n"
"s_lshl_b32 s68, s68, 17\n"
"s_mov_b64 s[44:45], %1\n"
"s_add_u32 s44, s44, s68\n"
"s_addc_u32 s45, s45, 0\n"
"s_and_b32 s45, s45, 0xffff\n"
"s_mov_b32 s46, 131072\n"
"s_mov_b32 s47, 0x20000\n"
"s_mov_b64 s[48:49], %2\n"
"s_add_u32 s48, s48, s68\n"
"s_addc_u32 s49, s49, 0\n"
"s_and_b32 s49, s49, 0xffff\n"
"s_mov_b32 s50, 131072\n"
"s_mov_b32 s51, 0x20000\n"
"s_mov_b64 s[56:57], %5\n"
"s_and_b32 s57, s57, 0xffff\n"
"s_mov_b32 s58, 43089920\n"
"s_mov_b32 s59, 0x20000\n"
"s_mov_b64 s[92:93], %6\n"
"s_mov_b32 s75, 0\n"
"s_mov_b32 s76, 0x41000000\n"
"s_mov_b32 s78, 0x3e0293ee\n"
"s_mov_b32 s79, s78\n"
"s_mov_b32 s82, 0xffff0000\n"
"s_mov_b32 s83, 0xffff0000\n"
"s_lshl_b32 s70, s60, 12\n"
"s_add_u32 s70, s70, s77\n"
"v_lshrrev_b32 v9, 4, v253\n"
"v_and_b32 v10, 15, v253\n"
"v_bfe_u32 v11, v253, 2, 2\n"
"v_and_b32 v12, 1, v9\n"
"v_lshrrev_b32 v13, 1, v9\n"
"s_lshl_b32 s68, s61, 13\n"
"s_add_u32 s68, s68, s77\n"
"v_lshrrev_b32 v0, 2, v10\n"
"v_sub_u32 v0, 4, v0\n"
"v_and_b32 v0, 3, v0\n"
"v_xor_b32 v0, v0, v9\n"
"v_lshlrev_b32 v0, 4, v0\n"
"v_lshrrev_b32 v1, 2, v10\n"
"v_lshl_add_u32 v0, v1, 10, v0\n"
"v_and_b32 v1, 3, v10\n"
"v_lshl_add_u32 v0, v1, 6, v0\n"
"v_add_u32 v240, s68, v0\n"
"v_add_u32 v241, 0x10000, v240\n"
"v_lshl_add_u32 v3, v12, 2, v11\n"
"v_lshlrev_b32 v3, 6, v3\n"
"v_lshlrev_b32 v0, 6, v11\n"
"v_lshl_add_u32 v0, v8, 3, v0\n"
"v_lshl_add_u32 v0, v12, 5, v0\n"
"v_lshl_add_u32 v0, v9, 10, v0\n"
"v_add_u32 v243, s68, v0\n"
"v_xor_b32 v246, 32, v243\n"
"v_add_u32 v244, 0x10000, v243\n"
"v_add_u32 v247, 0x10000, v246\n"
"v_lshlrev_b32 v15, 8, v11\n"
"v_lshl_add_u32 v15, v9, 6, v15\n"
"s_lshl_b32 s69, s62, 12\n"
"v_add_u32 v15, s69, v15\n"
"v_lshl_add_u32 v249, v8, 4, v15\n"
"v_xor_b32 v250, 16, v249\n"
"v_xor_b32 v251, 32, v249\n"
"v_xor_b32 v252, 48, v249\n"
"s_mul_i32 s94, s61, 8192\n"
"v_lshl_add_u32 v0, v13, 11, v3\n"
"v_xor_b32 v1, v8, v12\n"
"v_lshl_add_u32 v0, v1, 4, v0\n"
"v_and_b32 v1, 1, v8\n"
"v_lshl_add_u32 v0, v1, 3, v0\n"
"v_add_u32 v0, s68, v0\n"
"v_add_u32 v160, 49152, v0\n"
"v_xor_b32 v161, 8, v160\n"
"s_mul_i32 s69, s62, 10240\n"
"s_add_u32 s69, s69, s77\n"
"s_add_u32 s69, s69, 114688\n"
"v_lshrrev_b32 v0, 1, v10\n"
"v_xor_b32 v0, v0, v9\n"
"v_lshlrev_b32 v0, 4, v0\n"
"v_lshl_add_u32 v0, v10, 7, v0\n"
"v_add_u32 v162, s69, v0\n"
"v_xor_b32 v163, 64, v162\n"
"v_mov_b32 v0, 0x3f803f80\n"
"v_accvgpr_write_b32 a244, v0\n"
"v_accvgpr_write_b32 a245, v0\n"
"v_accvgpr_write_b32 a246, v0\n"
"v_accvgpr_write_b32 a247, v0\n"
"v_lshl_add_u32 v197, v253, 4, s70\n"
"v_add_u32 v198, 0x10000, v197\n"
"v_add_u32 v199, 0x20000, v197\n"
"s_add_u32 s68, s77, 155648\n"
"v_lshl_add_u32 v191, v253, 2, s68\n"
"s_lshl_b32 s68, s60, 8\n"
"s_add_u32 s68, s68, s77\n"
"s_add_u32 s68, s68, 156160\n"
"v_lshl_add_u32 v242, v253, 2, s68\n"
"v_mul_u32_u24 v0, 260, v8\n"
"s_add_u32 s68, s77, 156160\n"
"v_add_u32 v245, s68, v0\n"
"v_mov_b32 v0, 0\n"
"ds_write_b32 v242, v0 offset:0\n"
"s_nop 1\n"
"v_lshlrev_b32 v0, 4, v253\n"
"s_mov_b64 s[52:53], %0\n"
"s_and_b32 s53, s53, 0xffff\n"
"s_mov_b32 s54, 3276800\n"
"s_mov_b32 s55, 0x20000\n"
"s_mul_i32 s68, s65, 12800\n"
"buffer_load_dwordx4 a[184:187], v0, s[52:55], s68 offen\n"
"buffer_load_dwordx4 a[188:191], v0, s[52:55], s68 offen offset:1024\n"
"buffer_load_dwordx4 a[192:195], v0, s[52:55], s68 offen offset:2048\n"
"buffer_load_dwordx4 a[196:199], v0, s[52:55], s68 offen offset:3072\n"
"s_add_u32 s68, s68, 4096\n"
"buffer_load_dwordx4 a[200:203], v0, s[52:55], s68 offen\n"
"buffer_load_dwordx4 a[204:207], v0, s[52:55], s68 offen offset:1024\n"
"buffer_load_dwordx4 a[208:211], v0, s[52:55], s68 offen offset:2048\n"
"buffer_load_dwordx4 a[212:215], v0, s[52:55], s68 offen offset:3072\n"
"s_add_u32 s68, s68, 4096\n"
"buffer_load_dwordx4 a[216:219], v0, s[52:55], s68 offen\n"
"buffer_load_dwordx4 a[220:223], v0, s[52:55], s68 offen offset:1024\n"
"buffer_load_dwordx4 a[224:227], v0, s[52:55], s68 offen offset:2048\n"
"buffer_load_dwordx4 a[228:231], v0, s[52:55], s68 offen offset:3072\n"
"s_add_u32 s68, s68, 4096\n"
"buffer_load_dwordx4 a[184:187], v0, s[52:55], s68 offen\n"
"s_mov_b64 s[52:53], %3\n"
"s_and_b32 s53, s53, 0xffff\n"
"s_mov_b32 s54, 5242880\n"
"s_mov_b32 s55, 0x20000\n"
"s_mul_i32 s68, s65, 16384\n"
"s_add_u32 s68, s68, 1063936\n"
"buffer_load_dwordx4 a[184:187], v0, s[52:55], s68 offen\n"
"v_lshlrev_b32 v193, 4, v253\n"
"s_mov_b64 s[52:53], %4\n"
"s_and_b32 s53, s53, 0xffff\n"
"s_mov_b32 s54, 5242880\n"
"s_mov_b32 s55, 0x20000\n"
"v_lshrrev_b32 v0, 2, v253\n"
"v_add_u32 v0, 0, v0\n"
"v_mul_u32_u24 v1, 43, v0\n"
"v_lshrrev_b32 v1, 7, v1\n"
"v_mul_u32_u24 v2, 3, v1\n"
"v_sub_u32 v2, v0, v2\n"
"v_mul_u32_u24 v1, 1280, v1\n"
"v_lshl_add_u32 v1, v2, 6, v1\n"
"v_lshl_add_u32 v194, v8, 4, v1\n"
"v_lshrrev_b32 v0, 2, v253\n"
"v_add_u32 v0, 16, v0\n"
"v_mul_u32_u24 v1, 43, v0\n"
"v_lshrrev_b32 v1, 7, v1\n"
"v_mul_u32_u24 v2, 3, v1\n"
"v_sub_u32 v2, v0, v2\n"
"v_mul_u32_u24 v1, 1280, v1\n"
"v_lshl_add_u32 v1, v2, 6, v1\n"
"v_lshl_add_u32 v195, v8, 4, v1\n"
"v_lshrrev_b32 v0, 2, v253\n"
"v_add_u32 v0, 32, v0\n"
"v_mul_u32_u24 v1, 43, v0\n"
"v_lshrrev_b32 v1, 7, v1\n"
"v_mul_u32_u24 v2, 3, v1\n"
"v_sub_u32 v2, v0, v2\n"
"v_mul_u32_u24 v1, 1280, v1\n"
"v_lshl_add_u32 v1, v2, 6, v1\n"
"v_lshl_add_u32 v196, v8, 4, v1\n"
"s_mul_i32 s98, s86, 163840\n"
"s_mul_i32 s68, s87, 160\n"
"s_add_u32 s98, s98, s68\n"
"s_and_b32 s69, s87, 1\n"
"s_lshl_b32 s69, s69, 5\n"
"s_sub_u32 s98, s98, s69\n"
"v_lshrrev_b32 v0, 2, v253\n"
"v_mul_u32_u24 v0, 192, v0\n"
"v_lshl_add_u32 v0, v8, 3, v0\n"
"s_add_u32 s69, s69, s77\n"
"v_add_u32 v0, s69, v0\n"
"v_add_u32 v192, 16384, v0\n"
"s_mul_i32 s99, s87, 5120\n"
"s_mul_i32 s69, s86, 163840\n"
"s_add_u32 s99, s99, s69\n"
"s_add_u32 s99, s99, 10485760\n"
"s_waitcnt vmcnt(24)\n"
"s_waitcnt lgkmcnt(0)\n"
"s_barrier\n"
"ds_read_b64_tr_b16 a[0:1], v160 offset:0\n"
"ds_read_b64_tr_b16 a[2:3], v160 offset:1024\n"
"ds_read_b64_tr_b16 a[16:17], v160 offset:4096\n"
"ds_read_b64_tr_b16 a[18:19], v160 offset:5120\n"
"ds_read_b128 a[64:67], v162 offset:0\n"
"ds_read_b128 a[84:87], v163 offset:0\n"
"ds_read_b128 a[68:71], v162 offset:2048\n"
"ds_read_b128 a[88:91], v163 offset:2048\n"
"ds_read_b128 a[72:75], v162 offset:4096\n"
"ds_read_b128 a[92:95], v163 offset:4096\n"
"ds_read_b128 a[76:79], v162 offset:6144\n"
"ds_read_b128 a[96:99], v163 offset:6144\n"
"ds_read_b128 a[80:83], v162 offset:8192\n"
"ds_read_b128 a[100:103], v163 offset:8192\n"
"ds_read_b64_tr_b16 a[4:5], v161 offset:0\n"
"ds_read_b64_tr_b16 a[6:7], v161 offset:1024\n"
"ds_read_b64_tr_b16 a[20:21], v161 offset:4096\n"
"ds_read_b64_tr_b16 a[22:23], v161 offset:5120\n"
"ds_read_b64_tr_b16 a[8:9], v160 offset:512\n"
"ds_read_b64_tr_b16 a[10:11], v160 offset:1536\n"
"ds_read_b64_tr_b16 a[24:25], v160 offset:4608\n"
"ds_read_b64_tr_b16 a[26:27], v160 offset:5632\n"
"ds_read_b64_tr_b16 a[12:13], v161 offset:512\n"
"ds_read_b64_tr_b16 a[14:15], v161 offset:1536\n"
"ds_read_b64_tr_b16 a[28:29], v161 offset:4608\n"
"ds_read_b64_tr_b16 a[30:31], v161 offset:5632\n"
"s_waitcnt lgkmcnt(0)\n"
"s_barrier\n"
"MM v[0:3], a[0:3], a[64:67], 0\n"
"ds_read_b64_tr_b16 a[32:33], v160 offset:16384\n"
"ds_read_b64_tr_b16 a[34:35], v160 offset:17408\n"
"MM v[0:3], a[16:19], a[84:87], v[0:3]\n"
"s_add_u32 m0, s66, 0\n"
"ds_read_b64_tr_b16 a[48:49], v160 offset:20480\n"
"MM v[4:7], a[0:3], a[68:71], 0\n"
"buffer_load_dwordx4 v164, s[36:39], s67 offen lds\n"
"MM v[4:7], a[16:19], a[88:91], v[4:7]\n"
"ds_read_b64_tr_b16 a[50:51], v160 offset:21504\n"
"s_add_u32 m0, s66, 1024\n"
"MM v[8:11], a[0:3], a[72:75], 0\n"
"ds_read_b64_tr_b16 a[36:37], v161 offset:16384\n"
"ds_read_b64_tr_b16 a[38:39], v161 offset:17408\n"
"MM v[8:11], a[16:19], a[92:95], v[8:11]\n"
"buffer_load_dwordx4 v165, s[36:39], s67 offen lds\n"
"MM v[12:15], a[0:3], a[76:79], 0\n"
"ds_read_b64_tr_b16 a[52:53], v161 offset:20480\n"
"s_add_u32 m0, s66, 2048\n"
"MM v[12:15], a[16:19], a[96:99], v[12:15]\n"
"ds_read_b64_tr_b16 a[54:55], v161 offset:21504\n"
"ds_read_b64_tr_b16 a[40:41], v160 offset:16896\n"
"MM v[16:19], a[0:3], a[80:83], 0\n"
"buffer_load_dwordx4 v166, s[36:39], s67 offen lds\n"
"MM v[16:19], a[16:19], a[100:103], v[16:19]\n"
"ds_read_b64_tr_b16 a[42:43], v160 offset:17920\n"
"MM v[20:23], a[4:7], a[64:67], 0\n"
"s_add_u32 m0, s66, 3072\n"
"ds_read_b64_tr_b16 a[56:57], v160 offset:20992\n"
"MM v[20:23], a[20:23], a[84:87], v[20:23]\n"
"buffer_load_dwordx4 v167, s[36:39], s67 offen lds\n"
"MM v[24:27], a[4:7], a[68:71], 0\n"
"ds_read_b64_tr_b16 a[58:59], v160 offset:22016\n"
"MM v[24:27], a[20:23], a[88:91], v[24:27]\n"
"ds_read_b64_tr_b16 a[44:45], v161 offset:16896\n"
"s_add_u32 m0, s66, 4096\n"
"MM v[28:31], a[4:7], a[72:75], 0\n"
"buffer_load_dwordx4 v168, s[36:39], s67 offen lds\n"
"MM v[28:31], a[20:23], a[92:95], v[28:31]\n"
"ds_read_b64_tr_b16 a[46:47], v161 offset:17920\n"
"MM v[32:35], a[4:7], a[76:79], 0\n"
"ds_read_b64_tr_b16 a[60:61], v161 offset:20992\n"
"MM v[32:35], a[20:23], a[96:99], v[32:35]\n"
"s_add_u32 m0, s66, 5120\n"
"ds_read_b64_tr_b16 a[62:63], v161 offset:22016\n"
"MM v[36:39], a[4:7], a[80:83], 0\n"
"buffer_load_dwordx4 v169, s[36:39], s67 offen lds\n"
"MM v[36:39], a[20:23], a[100:103], v[36:39]\n"
"MM v[40:43], a[8:11], a[64:67], 0\n"
"MM v[40:43], a[24:27], a[84:87], v[40:43]\n"
"s_add_u32 m0, s66, 6144\n"
"MM v[44:47], a[8:11], a[68:71], 0\n"
"buffer_load_dwordx4 v170, s[36:39], s67 offen lds\n"
"MM v[44:47], a[24:27], a[88:91], v[44:47]\n"
"MM v[48:51], a[8:11], a[72:75], 0\n"
"MM v[48:51], a[24:27], a[92:95], v[48:51]\n"
"s_add_u32 m0, s66, 7168\n"
"MM v[52:55], a[8:11], a[76:79], 0\n"
"buffer_load_dwordx4 v171, s[36:39], s67 offen lds\n"
"MM v[52:55], a[24:27], a[96:99], v[52:55]\n"
"MM v[56:59], a[8:11], a[80:83], 0\n"
"s_add_u32 m0, s66, 8192\n"
"MM v[56:59], a[24:27], a[100:103], v[56:59]\n"
"buffer_load_dwordx4 v172, s[36:39], s67 offen lds\n"
"MM v[60:63], a[12:15], a[64:67], 0\n"
"MM v[60:63], a[28:31], a[84:87], v[60:63]\n"
"MM v[64:67], a[12:15], a[68:71], 0\n"
"s_add_u32 m0, s66, 9216\n"
"MM v[64:67], a[28:31], a[88:91], v[64:67]\n"
"buffer_load_dwordx4 v173, s[36:39], s67 offen lds\n"
"MM v[68:71], a[12:15], a[72:75], 0\n"
"MM v[68:71], a[28:31], a[92:95], v[68:71]\n"
"MM v[72:75], a[12:15], a[76:79], 0\n"
"s_add_u32 s67, s67, 128\n"
"MM v[72:75], a[28:31], a[96:99], v[72:75]\n"
"MM v[76:79], a[12:15], a[80:83], 0\n"
"MM v[76:79], a[28:31], a[100:103], v[76:79]\n"
"s_waitcnt vmcnt(24)\n"
"s_waitcnt lgkmcnt(0)\n"
"s_barrier\n"
"MM v[80:83], a[32:35], a[64:67], 0\n"
"ds_read_b64_tr_b16 a[0:1], v160 offset:32768\n"
"ds_read_b64_tr_b16 a[2:3], v160 offset:33792\n"
"MM v[80:83], a[48:51], a[84:87], v[80:83]\n"
"s_add_u32 m0, s77, 16384\n"
"ds_read_b64_tr_b16 a[16:17], v160 offset:36864\n"
"MM v[84:87], a[32:35], a[68:71], 0\n"
"buffer_load_dwordx4 v194, s[52:55], s98 offen lds\n"
"MM v[84:87], a[48:51], a[88:91], v[84:87]\n"
"ds_read_b64_tr_b16 a[18:19], v160 offset:37888\n"
"s_add_u32 m0, s77, 17408\n"
"MM v[88:91], a[32:35], a[72:75], 0\n"
"buffer_load_dwordx4 v195, s[52:55], s98 offen lds\n"
"MM v[88:91], a[48:51], a[92:95], v[88:91]\n"
"ds_read_b128 a[104:107], v162 offset:20480\n"
"ds_read_b128 a[124:127], v163 offset:20480\n"
"MM v[92:95], a[32:35], a[76:79], 0\n"
"ds_read_b128 a[108:111], v162 offset:22528\n"
"s_add_u32 m0, s77, 18432\n"
"MM v[92:95], a[48:51], a[96:99], v[92:95]\n"
"buffer_load_dwordx4 v196, s[52:55], s98 offen lds\n"
"MM v[96:99], a[32:35], a[80:83], 0\n"
"ds_read_b128 a[128:131], v163 offset:22528\n"
"ds_read_b128 a[112:115], v162 offset:24576\n"
"MM v[96:99], a[48:51], a[100:103], v[96:99]\n"
"s_add_u32 s98, s98, 20480\n"
"ds_read_b128 a[132:135], v163 offset:24576\n"
"MM v[100:103], a[36:39], a[64:67], 0\n"
"s_add_u32 m0, s77, 19456\n"
"ds_read_b128 a[116:119], v162 offset:26624\n"
"MM v[100:103], a[52:55], a[84:87], v[100:103]\n"
"buffer_load_dwordx4 v194, s[52:55], s98 offen lds\n"
"MM v[104:107], a[36:39], a[68:71], 0\n"
"ds_read_b128 a[136:139], v163 offset:26624\n"
"ds_read_b128 a[120:123], v162 offset:28672\n"
"MM v[104:107], a[52:55], a[88:91], v[104:107]\n"
"s_add_u32 m0, s77, 20480\n"
"ds_read_b128 a[140:143], v163 offset:28672\n"
"MM v[108:111], a[36:39], a[72:75], 0\n"
"buffer_load_dwordx4 v195, s[52:55], s98 offen lds\n"
"MM v[108:111], a[52:55], a[92:95], v[108:111]\n"
"ds_read_b64_tr_b16 a[4:5], v161 offset:32768\n"
"s_add_u32 m0, s77, 21504\n"
"MM v[112:115], a[36:39], a[76:79], 0\n"
"ds_read_b64_tr_b16 a[6:7], v161 offset:33792\n"
"ds_read_b64_tr_b16 a[20:21], v161 offset:36864\n"
"MM v[112:115], a[52:55], a[96:99], v[112:115]\n"
"buffer_load_dwordx4 v196, s[52:55], s98 offen lds\n"
"MM v[116:119], a[36:39], a[80:83], 0\n"
"ds_read_b64_tr_b16 a[22:23], v161 offset:37888\n"
"s_add_u32 s98, s98, 20480\n"
"MM v[116:119], a[52:55], a[100:103], v[116:119]\n"
"s_add_u32 m0, s77, 22528\n"
"ds_read_b64_tr_b16 a[8:9], v160 offset:33280\n"
"MM v[120:123], a[40:43], a[64:67], 0\n"
"buffer_load_dwordx4 v194, s[52:55], s98 offen lds\n"
"MM v[120:123], a[56:59], a[84:87], v[120:123]\n"
"ds_read_b64_tr_b16 a[10:11], v160 offset:34304\n"
"ds_read_b64_tr_b16 a[24:25], v160 offset:37376\n"
"MM v[124:127], a[40:43], a[68:71], 0\n"
"s_add_u32 m0, s77, 23552\n"
"ds_read_b64_tr_b16 a[26:27], v160 offset:38400\n"
"MM v[124:127], a[56:59], a[88:91], v[124:127]\n"
"buffer_load_dwordx4 v195, s[52:55], s98 offen lds\n"
"MM v[128:131], a[40:43], a[72:75], 0\n"
"ds_read_b64_tr_b16 a[12:13], v161 offset:33280\n"
"ds_read_b64_tr_b16 a[14:15], v161 offset:34304\n"
"MM v[128:131], a[56:59], a[92:95], v[128:131]\n"
"s_add_u32 m0, s77, 24576\n"
"ds_read_b64_tr_b16 a[28:29], v161 offset:37376\n"
"MM v[132:135], a[40:43], a[76:79], 0\n"
"buffer_load_dwordx4 v196, s[52:55], s98 offen lds\n"
"MM v[132:135], a[56:59], a[96:99], v[132:135]\n"
"ds_read_b64_tr_b16 a[30:31], v161 offset:38400\n"
"s_add_u32 s98, s98, 20480\n"
"MM v[136:139], a[40:43], a[80:83], 0\n"
"s_add_u32 m0, s77, 25600\n"
"MM v[136:139], a[56:59], a[100:103], v[136:139]\n"
"buffer_load_dwordx4 v194, s[52:55], s98 offen lds\n"
"MM v[140:143], a[44:47], a[64:67], 0\n"
"MM v[140:143], a[60:63], a[84:87], v[140:143]\n"
"s_add_u32 m0, s77, 26624\n"
"MM v[144:147], a[44:47], a[68:71], 0\n"
"buffer_load_dwordx4 v195, s[52:55], s98 offen lds\n"
"MM v[144:147], a[60:63], a[88:91], v[144:147]\n"
"MM v[148:151], a[44:47], a[72:75], 0\n"
"s_add_u32 m0, s77, 27648\n"
"MM v[148:151], a[60:63], a[92:95], v[148:151]\n"
"buffer_load_dwordx4 v196, s[52:55], s98 offen lds\n"
"MM v[152:155], a[44:47], a[76:79], 0\n"
"MM v[152:155], a[60:63], a[96:99], v[152:155]\n"
"s_add_u32 s98, s98, 20480\n"
"MM v[156:159], a[44:47], a[80:83], 0\n"
"MM v[156:159], a[60:63], a[100:103], v[156:159]\n"
"s_waitcnt lgkmcnt(0)\n"
"s_barrier\n"
"MM v[0:3], a[0:3], a[104:107], v[0:3]\n"
"ds_read_b64_tr_b16 a[32:33], v160 offset:49152\n"
"ds_read_b64_tr_b16 a[34:35], v160 offset:50176\n"
"MM v[0:3], a[16:19], a[124:127], v[0:3]\n"
"s_add_u32 m0, s66, 20480\n"
"ds_read_b64_tr_b16 a[48:49], v160 offset:53248\n"
"MM v[4:7], a[0:3], a[108:111], v[4:7]\n"
"buffer_load_dwordx4 v164, s[36:39], s67 offen lds\n"
"MM v[4:7], a[16:19], a[128:131], v[4:7]\n"
"ds_read_b64_tr_b16 a[50:51], v160 offset:54272\n"
"s_add_u32 m0, s66, 21504\n"
"MM v[8:11], a[0:3], a[112:115], v[8:11]\n"
"ds_read_b64_tr_b16 a[36:37], v161 offset:49152\n"
"ds_read_b64_tr_b16 a[38:39], v161 offset:50176\n"
"MM v[8:11], a[16:19], a[132:135], v[8:11]\n"
"buffer_load_dwordx4 v165, s[36:39], s67 offen lds\n"
"MM v[12:15], a[0:3], a[116:119], v[12:15]\n"
"ds_read_b64_tr_b16 a[52:53], v161 offset:53248\n"
"s_add_u32 m0, s66, 22528\n"
"MM v[12:15], a[16:19], a[136:139], v[12:15]\n"
"ds_read_b64_tr_b16 a[54:55], v161 offset:54272\n"
"ds_read_b64_tr_b16 a[40:41], v160 offset:49664\n"
"MM v[16:19], a[0:3], a[120:123], v[16:19]\n"
"buffer_load_dwordx4 v166, s[36:39], s67 offen lds\n"
"MM v[16:19], a[16:19], a[140:143], v[16:19]\n"
"ds_read_b64_tr_b16 a[42:43], v160 offset:50688\n"
"MM v[20:23], a[4:7], a[104:107], v[20:23]\n"
"s_add_u32 m0, s66, 23552\n"
"ds_read_b64_tr_b16 a[56:57], v160 offset:53760\n"
"MM v[20:23], a[20:23], a[124:127], v[20:23]\n"
"buffer_load_dwordx4 v167, s[36:39], s67 offen lds\n"
"MM v[24:27], a[4:7], a[108:111], v[24:27]\n"
"ds_read_b64_tr_b16 a[58:59], v160 offset:54784\n"
"MM v[24:27], a[20:23], a[128:131], v[24:27]\n"
"ds_read_b64_tr_b16 a[44:45], v161 offset:49664\n"
"s_add_u32 m0, s66, 24576\n"
"MM v[28:31], a[4:7], a[112:115], v[28:31]\n"
"buffer_load_dwordx4 v168, s[36:39], s67 offen lds\n"
"MM v[28:31], a[20:23], a[132:135], v[28:31]\n"
"ds_read_b64_tr_b16 a[46:47], v161 offset:50688\n"
"MM v[32:35], a[4:7], a[116:119], v[32:35]\n"
"ds_read_b64_tr_b16 a[60:61], v161 offset:53760\n"
"MM v[32:35], a[20:23], a[136:139], v[32:35]\n"
"s_add_u32 m0, s66, 25600\n"
"ds_read_b64_tr_b16 a[62:63], v161 offset:54784\n"
"MM v[36:39], a[4:7], a[120:123], v[36:39]\n"
"buffer_load_dwordx4 v169, s[36:39], s67 offen lds\n"
"MM v[36:39], a[20:23], a[140:143], v[36:39]\n"
"MM v[40:43], a[8:11], a[104:107], v[40:43]\n"
"MM v[40:43], a[24:27], a[124:127], v[40:43]\n"
"s_add_u32 m0, s66, 26624\n"
"MM v[44:47], a[8:11], a[108:111], v[44:47]\n"
"buffer_load_dwordx4 v170, s[36:39], s67 offen lds\n"
"MM v[44:47], a[24:27], a[128:131], v[44:47]\n"
"MM v[48:51], a[8:11], a[112:115], v[48:51]\n"
"MM v[48:51], a[24:27], a[132:135], v[48:51]\n"
"s_add_u32 m0, s66, 27648\n"
"MM v[52:55], a[8:11], a[116:119], v[52:55]\n"
"buffer_load_dwordx4 v171, s[36:39], s67 offen lds\n"
"MM v[52:55], a[24:27], a[136:139], v[52:55]\n"
"MM v[56:59], a[8:11], a[120:123], v[56:59]\n"
"s_add_u32 m0, s66, 28672\n"
"MM v[56:59], a[24:27], a[140:143], v[56:59]\n"
"buffer_load_dwordx4 v172, s[36:39], s67 offen lds\n"
"MM v[60:63], a[12:15], a[104:107], v[60:63]\n"
"MM v[60:63], a[28:31], a[124:127], v[60:63]\n"
"MM v[64:67], a[12:15], a[108:111], v[64:67]\n"
"s_add_u32 m0, s66, 29696\n"
"MM v[64:67], a[28:31], a[128:131], v[64:67]\n"
"buffer_load_dwordx4 v173, s[36:39], s67 offen lds\n"
"MM v[68:71], a[12:15], a[112:115], v[68:71]\n"
"MM v[68:71], a[28:31], a[132:135], v[68:71]\n"
"MM v[72:75], a[12:15], a[116:119], v[72:75]\n"
"s_add_u32 s67, s67, 128\n"
"MM v[72:75], a[28:31], a[136:139], v[72:75]\n"
"MM v[76:79], a[12:15], a[120:123], v[76:79]\n"
"MM v[76:79], a[28:31], a[140:143], v[76:79]\n"
"s_waitcnt vmcnt(22)\n"
"s_waitcnt lgkmcnt(0)\n"
"s_barrier\n"
"MM v[80:83], a[32:35], a[104:107], v[80:83]\n"
"ds_read_b64_tr_b16 a[0:1], v160 offset:0\n"
"ds_read_b64_tr_b16 a[2:3], v160 offset:1024\n"
"MM v[80:83], a[48:51], a[124:127], v[80:83]\n"
"s_add_u32 m0, s77, 28672\n"
"ds_read_b64_tr_b16 a[16:17], v160 offset:4096\n"
"MM v[84:87], a[32:35], a[108:111], v[84:87]\n"
"buffer_load_dwordx4 v194, s[52:55], s98 offen lds\n"
"MM v[84:87], a[48:51], a[128:131], v[84:87]\n"
"ds_read_b64_tr_b16 a[18:19], v160 offset:5120\n"
"s_add_u32 m0, s77, 29696\n"
"MM v[88:91], a[32:35], a[112:115], v[88:91]\n"
"buffer_load_dwordx4 v195, s[52:55], s98 offen lds\n"
"MM v[88:91], a[48:51], a[132:135], v[88:91]\n"
"ds_read_b128 a[64:67], v162 offset:0\n"
"ds_read_b128 a[84:87], v163 offset:0\n"
"MM v[92:95], a[32:35], a[116:119], v[92:95]\n"
"ds_read_b128 a[68:71], v162 offset:2048\n"
"s_add_u32 m0, s77, 30720\n"
"MM v[92:95], a[48:51], a[136:139], v[92:95]\n"
"buffer_load_dwordx4 v196, s[52:55], s98 offen lds\n"
"MM v[96:99], a[32:35], a[120:123], v[96:99]\n"
"ds_read_b128 a[88:91], v163 offset:2048\n"
"ds_read_b128 a[72:75], v162 offset:4096\n"
"MM v[96:99], a[48:51], a[140:143], v[96:99]\n"
"s_add_u32 s98, s98, 20480\n"
"ds_read_b128 a[92:95], v163 offset:4096\n"
"MM v[100:103], a[36:39], a[104:107], v[100:103]\n"
"s_add_u32 m0, s77, 31744\n"
"ds_read_b128 a[76:79], v162 offset:6144\n"
"MM v[100:103], a[52:55], a[124:127], v[100:103]\n"
"buffer_load_dwordx4 v194, s[52:55], s98 offen lds\n"
"MM v[104:107], a[36:39], a[108:111], v[104:107]\n"
"ds_read_b128 a[96:99], v163 offset:6144\n"
"ds_read_b128 a[80:83], v162 offset:8192\n"
"MM v[104:107], a[52:55], a[128:131], v[104:107]\n"
"s_add_u32 m0, s77, 32768\n"
"ds_read_b128 a[100:103], v163 offset:8192\n"
"MM v[108:111], a[36:39], a[112:115], v[108:111]\n"
"buffer_load_dwordx4 v195, s[52:55], s98 offen lds\n"
"MM v[108:111], a[52:55], a[132:135], v[108:111]\n"
"ds_read_b64_tr_b16 a[4:5], v161 offset:0\n"
"s_add_u32 m0, s77, 33792\n"
"MM v[112:115], a[36:39], a[116:119], v[112:115]\n"
"ds_read_b64_tr_b16 a[6:7], v161 offset:1024\n"
"ds_read_b64_tr_b16 a[20:21], v161 offset:4096\n"
"MM v[112:115], a[52:55], a[136:139], v[112:115]\n"
"buffer_load_dwordx4 v196, s[52:55], s98 offen lds\n"
"MM v[116:119], a[36:39], a[120:123], v[116:119]\n"
"ds_read_b64_tr_b16 a[22:23], v161 offset:5120\n"
"s_add_u32 s98, s98, 20480\n"
"MM v[116:119], a[52:55], a[140:143], v[116:119]\n"
"s_add_u32 m0, s77, 34816\n"
"ds_read_b64_tr_b16 a[8:9], v160 offset:512\n"
"MM v[120:123], a[40:43], a[104:107], v[120:123]\n"
"buffer_load_dwordx4 v194, s[52:55], s98 offen lds\n"
"MM v[120:123], a[56:59], a[124:127], v[120:123]\n"
"ds_read_b64_tr_b16 a[10:11], v160 offset:1536\n"
"ds_read_b64_tr_b16 a[24:25], v160 offset:4608\n"
"MM v[124:127], a[40:43], a[108:111], v[124:127]\n"
"s_add_u32 m0, s77, 35840\n"
"ds_read_b64_tr_b16 a[26:27], v160 offset:5632\n"
"MM v[124:127], a[56:59], a[128:131], v[124:127]\n"
"buffer_load_dwordx4 v195, s[52:55], s98 offen lds\n"
"MM v[128:131], a[40:43], a[112:115], v[128:131]\n"
"ds_read_b64_tr_b16 a[12:13], v161 offset:512\n"
"ds_read_b64_tr_b16 a[14:15], v161 offset:1536\n"
"MM v[128:131], a[56:59], a[132:135], v[128:131]\n"
"s_add_u32 m0, s77, 36864\n"
"ds_read_b64_tr_b16 a[28:29], v161 offset:4608\n"
"MM v[132:135], a[40:43], a[116:119], v[132:135]\n"
"buffer_load_dwordx4 v196, s[52:55], s98 offen lds\n"
"MM v[132:135], a[56:59], a[136:139], v[132:135]\n"
"ds_read_b64_tr_b16 a[30:31], v161 offset:5632\n"
"s_add_u32 s98, s98, 20480\n"
"MM v[136:139], a[40:43], a[120:123], v[136:139]\n"
"s_add_u32 m0, s77, 37888\n"
"MM v[136:139], a[56:59], a[140:143], v[136:139]\n"
"buffer_load_dwordx4 v194, s[52:55], s98 offen lds\n"
"MM v[140:143], a[44:47], a[104:107], v[140:143]\n"
"MM v[140:143], a[60:63], a[124:127], v[140:143]\n"
"s_add_u32 m0, s77, 38912\n"
"MM v[144:147], a[44:47], a[108:111], v[144:147]\n"
"buffer_load_dwordx4 v195, s[52:55], s98 offen lds\n"
"MM v[144:147], a[60:63], a[128:131], v[144:147]\n"
"MM v[148:151], a[44:47], a[112:115], v[148:151]\n"
"s_add_u32 m0, s77, 39936\n"
"MM v[148:151], a[60:63], a[132:135], v[148:151]\n"
"buffer_load_dwordx4 v196, s[52:55], s98 offen lds\n"
"MM v[152:155], a[44:47], a[116:119], v[152:155]\n"
"MM v[152:155], a[60:63], a[136:139], v[152:155]\n"
"s_add_u32 s98, s98, 20480\n"
"MM v[156:159], a[44:47], a[120:123], v[156:159]\n"
"MM v[156:159], a[60:63], a[140:143], v[156:159]\n"
"s_waitcnt lgkmcnt(0)\n"
"s_barrier\n"
"MM v[0:3], a[0:3], a[64:67], v[0:3]\n"
"ds_read_b64_tr_b16 a[32:33], v160 offset:16384\n"
"ds_read_b64_tr_b16 a[34:35], v160 offset:17408\n"
"MM v[0:3], a[16:19], a[84:87], v[0:3]\n"
"s_add_u32 m0, s66, 0\n"
"ds_read_b64_tr_b16 a[48:49], v160 offset:20480\n"
"MM v[4:7], a[0:3], a[68:71], v[4:7]\n"
"buffer_load_dwordx4 v164, s[36:39], s67 offen lds\n"
"MM v[4:7], a[16:19], a[88:91], v[4:7]\n"
"ds_read_b64_tr_b16 a[50:51], v160 offset:21504\n"
"s_add_u32 m0, s66, 1024\n"
"MM v[8:11], a[0:3], a[72:75], v[8:11]\n"
"ds_read_b64_tr_b16 a[36:37], v161 offset:16384\n"
"ds_read_b64_tr_b16 a[38:39], v161 offset:17408\n"
"MM v[8:11], a[16:19], a[92:95], v[8:11]\n"
"buffer_load_dwordx4 v165, s[36:39], s67 offen lds\n"
"MM v[12:15], a[0:3], a[76:79], v[12:15]\n"
"ds_read_b64_tr_b16 a[52:53], v161 offset:20480\n"
"s_add_u32 m0, s66, 2048\n"
"MM v[12:15], a[16:19], a[96:99], v[12:15]\n"
"ds_read_b64_tr_b16 a[54:55], v161 offset:21504\n"
"ds_read_b64_tr_b16 a[40:41], v160 offset:16896\n"
"MM v[16:19], a[0:3], a[80:83], v[16:19]\n"
"buffer_load_dwordx4 v166, s[36:39], s67 offen lds\n"
"MM v[16:19], a[16:19], a[100:103], v[16:19]\n"
"ds_read_b64_tr_b16 a[42:43], v160 offset:17920\n"
"MM v[20:23], a[4:7], a[64:67], v[20:23]\n"
"s_add_u32 m0, s66, 3072\n"
"ds_read_b64_tr_b16 a[56:57], v160 offset:20992\n"
"MM v[20:23], a[20:23], a[84:87], v[20:23]\n"
"buffer_load_dwordx4 v167, s[36:39], s67 offen lds\n"
"MM v[24:27], a[4:7], a[68:71], v[24:27]\n"
"ds_read_b64_tr_b16 a[58:59], v160 offset:22016\n"
"MM v[24:27], a[20:23], a[88:91], v[24:27]\n"
"ds_read_b64_tr_b16 a[44:45], v161 offset:16896\n"
"s_add_u32 m0, s66, 4096\n"
"MM v[28:31], a[4:7], a[72:75], v[28:31]\n"
"buffer_load_dwordx4 v168, s[36:39], s67 offen lds\n"
"MM v[28:31], a[20:23], a[92:95], v[28:31]\n"
"ds_read_b64_tr_b16 a[46:47], v161 offset:17920\n"
"MM v[32:35], a[4:7], a[76:79], v[32:35]\n"
"ds_read_b64_tr_b16 a[60:61], v161 offset:20992\n"
"MM v[32:35], a[20:23], a[96:99], v[32:35]\n"
"s_add_u32 m0, s66, 5120\n"
"ds_read_b64_tr_b16 a[62:63], v161 offset:22016\n"
"MM v[36:39], a[4:7], a[80:83], v[36:39]\n"
"buffer_load_dwordx4 v169, s[36:39], s67 offen lds\n"
"MM v[36:39], a[20:23], a[100:103], v[36:39]\n"
"MM v[40:43], a[8:11], a[64:67], v[40:43]\n"
"MM v[40:43], a[24:27], a[84:87], v[40:43]\n"
"s_add_u32 m0, s66, 6144\n"
"MM v[44:47], a[8:11], a[68:71], v[44:47]\n"
"buffer_load_dwordx4 v170, s[36:39], s67 offen lds\n"
"MM v[44:47], a[24:27], a[88:91], v[44:47]\n"
"MM v[48:51], a[8:11], a[72:75], v[48:51]\n"
"MM v[48:51], a[24:27], a[92:95], v[48:51]\n"
"s_add_u32 m0, s66, 7168\n"
"MM v[52:55], a[8:11], a[76:79], v[52:55]\n"
"buffer_load_dwordx4 v171, s[36:39], s67 offen lds\n"
"MM v[52:55], a[24:27], a[96:99], v[52:55]\n"
"MM v[56:59], a[8:11], a[80:83], v[56:59]\n"
"s_add_u32 m0, s66, 8192\n"
"MM v[56:59], a[24:27], a[100:103], v[56:59]\n"
"buffer_load_dwordx4 v172, s[36:39], s67 offen lds\n"
"MM v[60:63], a[12:15], a[64:67], v[60:63]\n"
"MM v[60:63], a[28:31], a[84:87], v[60:63]\n"
"MM v[64:67], a[12:15], a[68:71], v[64:67]\n"
"s_add_u32 m0, s66, 9216\n"
"MM v[64:67], a[28:31], a[88:91], v[64:67]\n"
"buffer_load_dwordx4 v173, s[36:39], s67 offen lds\n"
"MM v[68:71], a[12:15], a[72:75], v[68:71]\n"
"MM v[68:71], a[28:31], a[92:95], v[68:71]\n"
"MM v[72:75], a[12:15], a[76:79], v[72:75]\n"
"s_add_u32 s67, s67, 128\n"
"MM v[72:75], a[28:31], a[96:99], v[72:75]\n"
"MM v[76:79], a[12:15], a[80:83], v[76:79]\n"
"MM v[76:79], a[28:31], a[100:103], v[76:79]\n"
"s_waitcnt vmcnt(22)\n"
"s_waitcnt lgkmcnt(0)\n"
"s_barrier\n"
"MM v[80:83], a[32:35], a[64:67], v[80:83]\n"
"ds_read_b64_tr_b16 a[0:1], v160 offset:32768\n"
"ds_read_b64_tr_b16 a[2:3], v160 offset:33792\n"
"MM v[80:83], a[48:51], a[84:87], v[80:83]\n"
"ds_read_b64_tr_b16 a[16:17], v160 offset:36864\n"
"ds_read_b64_tr_b16 a[18:19], v160 offset:37888\n"
"MM v[84:87], a[32:35], a[68:71], v[84:87]\n"
"ds_read_b128 a[104:107], v162 offset:20480\n"
"ds_read_b128 a[124:127], v163 offset:20480\n"
"MM v[84:87], a[48:51], a[88:91], v[84:87]\n"
"ds_read_b128 a[108:111], v162 offset:22528\n"
"MM v[88:91], a[32:35], a[72:75], v[88:91]\n"
"ds_read_b128 a[128:131], v163 offset:22528\n"
"MM v[88:91], a[48:51], a[92:95], v[88:91]\n"
"ds_read_b128 a[112:115], v162 offset:24576\n"
"MM v[92:95], a[32:35], a[76:79], v[92:95]\n"
"ds_read_b128 a[132:135], v163 offset:24576\n"
"MM v[92:95], a[48:51], a[96:99], v[92:95]\n"
"MM v[96:99], a[32:35], a[80:83], v[96:99]\n"
"ds_read_b128 a[116:119], v162 offset:26624\n"
"MM v[96:99], a[48:51], a[100:103], v[96:99]\n"
"ds_read_b128 a[136:139], v163 offset:26624\n"
"MM v[100:103], a[36:39], a[64:67], v[100:103]\n"
"ds_read_b128 a[120:123], v162 offset:28672\n"
"MM v[100:103], a[52:55], a[84:87], v[100:103]\n"
"ds_read_b128 a[140:143], v163 offset:28672\n"
"MM v[104:107], a[36:39], a[68:71], v[104:107]\n"
"ds_read_b64_tr_b16 a[4:5], v161 offset:32768\n"
"MM v[104:107], a[52:55], a[88:91], v[104:107]\n"
"ds_read_b64_tr_b16 a[6:7], v161 offset:33792\n"
"MM v[108:111], a[36:39], a[72:75], v[108:111]\n"
"ds_read_b64_tr_b16 a[20:21], v161 offset:36864\n"
"MM v[108:111], a[52:55], a[92:95], v[108:111]\n"
"MM v[112:115], a[36:39], a[76:79], v[112:115]\n"
"ds_read_b64_tr_b16 a[22:23], v161 offset:37888\n"
"MM v[112:115], a[52:55], a[96:99], v[112:115]\n"
"ds_read_b64_tr_b16 a[8:9], v160 offset:33280\n"
"MM v[116:119], a[36:39], a[80:83], v[116:119]\n"
"ds_read_b64_tr_b16 a[10:11], v160 offset:34304\n"
"MM v[116:119], a[52:55], a[100:103], v[116:119]\n"
"ds_read_b64_tr_b16 a[24:25], v160 offset:37376\n"
"MM v[120:123], a[40:43], a[64:67], v[120:123]\n"
"ds_read_b64_tr_b16 a[26:27], v160 offset:38400\n"
"MM v[120:123], a[56:59], a[84:87], v[120:123]\n"
"ds_read_b64_tr_b16 a[12:13], v161 offset:33280\n"
"MM v[124:127], a[40:43], a[68:71], v[124:127]\n"
"MM v[124:127], a[56:59], a[88:91], v[124:127]\n"
"ds_read_b64_tr_b16 a[14:15], v161 offset:34304\n"
"MM v[128:131], a[40:43], a[72:75], v[128:131]\n"
"ds_read_b64_tr_b16 a[28:29], v161 offset:37376\n"
"MM v[128:131], a[56:59], a[92:95], v[128:131]\n"
"ds_read_b64_tr_b16 a[30:31], v161 offset:38400\n"
"MM v[132:135], a[40:43], a[76:79], v[132:135]\n"
"MM v[132:135], a[56:59], a[96:99], v[132:135]\n"
"MM v[136:139], a[40:43], a[80:83], v[136:139]\n"
"MM v[136:139], a[56:59], a[100:103], v[136:139]\n"
"MM v[140:143], a[44:47], a[64:67], v[140:143]\n"
"MM v[140:143], a[60:63], a[84:87], v[140:143]\n"
"MM v[144:147], a[44:47], a[68:71], v[144:147]\n"
"MM v[144:147], a[60:63], a[88:91], v[144:147]\n"
"MM v[148:151], a[44:47], a[72:75], v[148:151]\n"
"MM v[148:151], a[60:63], a[92:95], v[148:151]\n"
"MM v[152:155], a[44:47], a[76:79], v[152:155]\n"
"MM v[152:155], a[60:63], a[96:99], v[152:155]\n"
"MM v[156:159], a[44:47], a[80:83], v[156:159]\n"
"MM v[156:159], a[60:63], a[100:103], v[156:159]\n"
"s_waitcnt lgkmcnt(0)\n"
"s_barrier\n"
"MM v[0:3], a[0:3], a[104:107], v[0:3]\n"
"ds_read_b64_tr_b16 a[32:33], v160 offset:49152\n"
"ds_read_b64_tr_b16 a[34:35], v160 offset:50176\n"
"MM v[0:3], a[16:19], a[124:127], v[0:3]\n"
"s_add_u32 m0, s66, 20480\n"
"ds_read_b64_tr_b16 a[48:49], v160 offset:53248\n"
"MM v[4:7], a[0:3], a[108:111], v[4:7]\n"
"buffer_load_dwordx4 v164, s[36:39], s67 offen lds\n"
"MM v[4:7], a[16:19], a[128:131], v[4:7]\n"
"ds_read_b64_tr_b16 a[50:51], v160 offset:54272\n"
"s_add_u32 m0, s66, 21504\n"
"MM v[8:11], a[0:3], a[112:115], v[8:11]\n"
"ds_read_b64_tr_b16 a[36:37], v161 offset:49152\n"
"ds_read_b64_tr_b16 a[38:39], v161 offset:50176\n"
"MM v[8:11], a[16:19], a[132:135], v[8:11]\n"
"buffer_load_dwordx4 v165, s[36:39], s67 offen lds\n"
"MM v[12:15], a[0:3], a[116:119], v[12:15]\n"
"ds_read_b64_tr_b16 a[52:53], v161 offset:53248\n"
"s_add_u32 m0, s66, 22528\n"
"MM v[12:15], a[16:19], a[136:139], v[12:15]\n"
"ds_read_b64_tr_b16 a[54:55], v161 offset:54272\n"
"ds_read_b64_tr_b16 a[40:41], v160 offset:49664\n"
"MM v[16:19], a[0:3], a[120:123], v[16:19]\n"
"buffer_load_dwordx4 v166, s[36:39], s67 offen lds\n"
"MM v[16:19], a[16:19], a[140:143], v[16:19]\n"
"ds_read_b64_tr_b16 a[42:43], v160 offset:50688\n"
"MM v[20:23], a[4:7], a[104:107], v[20:23]\n"
"s_add_u32 m0, s66, 23552\n"
"ds_read_b64_tr_b16 a[56:57], v160 offset:53760\n"
"MM v[20:23], a[20:23], a[124:127], v[20:23]\n"
"buffer_load_dwordx4 v167, s[36:39], s67 offen lds\n"
"MM v[24:27], a[4:7], a[108:111], v[24:27]\n"
"ds_read_b64_tr_b16 a[58:59], v160 offset:54784\n"
"MM v[24:27], a[20:23], a[128:131], v[24:27]\n"
"ds_read_b64_tr_b16 a[44:45], v161 offset:49664\n"
"s_add_u32 m0, s66, 24576\n"
"MM v[28:31], a[4:7], a[112:115], v[28:31]\n"
"buffer_load_dwordx4 v168, s[36:39], s67 offen lds\n"
"MM v[28:31], a[20:23], a[132:135], v[28:31]\n"
"ds_read_b64_tr_b16 a[46:47], v161 offset:50688\n"
"MM v[32:35], a[4:7], a[116:119], v[32:35]\n"
"ds_read_b64_tr_b16 a[60:61], v161 offset:53760\n"
"MM v[32:35], a[20:23], a[136:139], v[32:35]\n"
"s_add_u32 m0, s66, 25600\n"
"ds_read_b64_tr_b16 a[62:63], v161 offset:54784\n"
"MM v[36:39], a[4:7], a[120:123], v[36:39]\n"
"buffer_load_dwordx4 v169, s[36:39], s67 offen lds\n"
"MM v[36:39], a[20:23], a[140:143], v[36:39]\n"
"MM v[40:43], a[8:11], a[104:107], v[40:43]\n"
"MM v[40:43], a[24:27], a[124:127], v[40:43]\n"
"s_add_u32 m0, s66, 26624\n"
"MM v[44:47], a[8:11], a[108:111], v[44:47]\n"
"buffer_load_dwordx4 v170, s[36:39], s67 offen lds\n"
"MM v[44:47], a[24:27], a[128:131], v[44:47]\n"
"MM v[48:51], a[8:11], a[112:115], v[48:51]\n"
"MM v[48:51], a[24:27], a[132:135], v[48:51]\n"
"s_add_u32 m0, s66, 27648\n"
"MM v[52:55], a[8:11], a[116:119], v[52:55]\n"
"buffer_load_dwordx4 v171, s[36:39], s67 offen lds\n"
"MM v[52:55], a[24:27], a[136:139], v[52:55]\n"
"MM v[56:59], a[8:11], a[120:123], v[56:59]\n"
"s_add_u32 m0, s66, 28672\n"
"MM v[56:59], a[24:27], a[140:143], v[56:59]\n"
"buffer_load_dwordx4 v172, s[36:39], s67 offen lds\n"
"MM v[60:63], a[12:15], a[104:107], v[60:63]\n"
"MM v[60:63], a[28:31], a[124:127], v[60:63]\n"
"MM v[64:67], a[12:15], a[108:111], v[64:67]\n"
"s_add_u32 m0, s66, 29696\n"
"MM v[64:67], a[28:31], a[128:131], v[64:67]\n"
"buffer_load_dwordx4 v173, s[36:39], s67 offen lds\n"
"MM v[68:71], a[12:15], a[112:115], v[68:71]\n"
"MM v[68:71], a[28:31], a[132:135], v[68:71]\n"
"MM v[72:75], a[12:15], a[116:119], v[72:75]\n"
"s_add_u32 s67, s67, 128\n"
"MM v[72:75], a[28:31], a[136:139], v[72:75]\n"
"MM v[76:79], a[12:15], a[120:123], v[76:79]\n"
"MM v[76:79], a[28:31], a[140:143], v[76:79]\n"
"s_waitcnt vmcnt(10)\n"
"s_waitcnt lgkmcnt(0)\n"
"s_barrier\n"
"MM v[80:83], a[32:35], a[104:107], v[80:83]\n"
"ds_read_b64_tr_b16 a[0:1], v160 offset:0\n"
"ds_read_b64_tr_b16 a[2:3], v160 offset:1024\n"
"MM v[80:83], a[48:51], a[124:127], v[80:83]\n"
"ds_read_b64_tr_b16 a[144:145], v192 offset:0\n"
"ds_read_b64_tr_b16 a[146:147], v192 offset:3072\n"
"MM v[84:87], a[32:35], a[108:111], v[84:87]\n"
"ds_read_b64_tr_b16 a[16:17], v160 offset:4096\n"
"ds_read_b64_tr_b16 a[148:149], v192 offset:32\n"
"MM v[84:87], a[48:51], a[128:131], v[84:87]\n"
"ds_read_b64_tr_b16 a[18:19], v160 offset:5120\n"
"ds_read_b64_tr_b16 a[150:151], v192 offset:3104\n"
"MM v[88:91], a[32:35], a[112:115], v[88:91]\n"
"ds_read_b128 a[64:67], v162 offset:0\n"
"ds_read_b128 a[84:87], v163 offset:0\n"
"MM v[88:91], a[48:51], a[132:135], v[88:91]\n"
"ds_read_b64_tr_b16 a[152:153], v192 offset:64\n"
"ds_read_b128 a[68:71], v162 offset:2048\n"
"MM v[92:95], a[32:35], a[116:119], v[92:95]\n"
"ds_read_b64_tr_b16 a[154:155], v192 offset:3136\n"
"ds_read_b128 a[88:91], v163 offset:2048\n"
"MM v[92:95], a[48:51], a[136:139], v[92:95]\n"
"ds_read_b64_tr_b16 a[156:157], v192 offset:96\n"
"ds_read_b128 a[72:75], v162 offset:4096\n"
"MM v[96:99], a[32:35], a[120:123], v[96:99]\n"
"ds_read_b64_tr_b16 a[158:159], v192 offset:3168\n"
"ds_read_b128 a[92:95], v163 offset:4096\n"
"MM v[96:99], a[48:51], a[140:143], v[96:99]\n"
"ds_read_b64_tr_b16 a[160:161], v192 offset:128\n"
"ds_read_b128 a[76:79], v162 offset:6144\n"
"MM v[100:103], a[36:39], a[104:107], v[100:103]\n"
"ds_read_b64_tr_b16 a[162:163], v192 offset:3200\n"
"ds_read_b128 a[96:99], v163 offset:6144\n"
"MM v[100:103], a[52:55], a[124:127], v[100:103]\n"
"ds_read_b128 a[80:83], v162 offset:8192\n"
"ds_read_b128 a[100:103], v163 offset:8192\n"
"MM v[104:107], a[36:39], a[108:111], v[104:107]\n"
"ds_read_b64_tr_b16 a[4:5], v161 offset:0\n"
"MM v[104:107], a[52:55], a[128:131], v[104:107]\n"
"ds_read_b64_tr_b16 a[6:7], v161 offset:1024\n"
"MM v[108:111], a[36:39], a[112:115], v[108:111]\n"
"s_add_u32 s68, s99, 0\n"
"ds_read_b64_tr_b16 a[20:21], v161 offset:4096\n"
"MM v[108:111], a[52:55], a[132:135], v[108:111]\n"
"s_waitcnt lgkmcnt(15)\n"
"buffer_store_dwordx4 a[144:147], v193, s[56:59], s68 offen sc1\n"
"MM v[112:115], a[36:39], a[116:119], v[112:115]\n"
"buffer_store_dwordx4 a[148:151], v193, s[56:59], s68 offen offset:1024 sc1\n"
"MM v[112:115], a[52:55], a[136:139], v[112:115]\n"
"ds_read_b64_tr_b16 a[22:23], v161 offset:5120\n"
"ds_read_b64_tr_b16 a[8:9], v160 offset:512\n"
"MM v[116:119], a[36:39], a[120:123], v[116:119]\n"
"ds_read_b64_tr_b16 a[10:11], v160 offset:1536\n"
"MM v[116:119], a[52:55], a[140:143], v[116:119]\n"
"ds_read_b64_tr_b16 a[24:25], v160 offset:4608\n"
"MM v[120:123], a[40:43], a[104:107], v[120:123]\n"
"s_waitcnt lgkmcnt(15)\n"
"buffer_store_dwordx4 a[152:155], v193, s[56:59], s68 offen offset:2048 sc1\n"
"MM v[120:123], a[56:59], a[124:127], v[120:123]\n"
"ds_read_b64_tr_b16 a[26:27], v160 offset:5632\n"
"ds_read_b64_tr_b16 a[12:13], v161 offset:512\n"
"MM v[124:127], a[40:43], a[108:111], v[124:127]\n"
"MM v[124:127], a[56:59], a[128:131], v[124:127]\n"
"ds_read_b64_tr_b16 a[14:15], v161 offset:1536\n"
"MM v[128:131], a[40:43], a[112:115], v[128:131]\n"
"ds_read_b64_tr_b16 a[28:29], v161 offset:4608\n"
"MM v[128:131], a[56:59], a[132:135], v[128:131]\n"
"s_waitcnt lgkmcnt(15)\n"
"buffer_store_dwordx4 a[156:159], v193, s[56:59], s68 offen offset:3072 sc1\n"
"MM v[132:135], a[40:43], a[116:119], v[132:135]\n"
"ds_read_b64_tr_b16 a[30:31], v161 offset:5632\n"
"MM v[132:135], a[56:59], a[136:139], v[132:135]\n"
"MM v[136:139], a[40:43], a[120:123], v[136:139]\n"
"MM v[136:139], a[56:59], a[140:143], v[136:139]\n"
"s_add_u32 s68, s99, 4096\n"
"MM v[140:143], a[44:47], a[104:107], v[140:143]\n"
"MM v[140:143], a[60:63], a[124:127], v[140:143]\n"
"s_waitcnt lgkmcnt(15)\n"
"buffer_store_dwordx4 a[160:163], v193, s[56:59], s68 offen sc1\n"
"MM v[144:147], a[44:47], a[108:111], v[144:147]\n"
"MM v[144:147], a[60:63], a[128:131], v[144:147]\n"
"MM v[148:151], a[44:47], a[112:115], v[148:151]\n"
"MM v[148:151], a[60:63], a[132:135], v[148:151]\n"
"MM v[152:155], a[44:47], a[116:119], v[152:155]\n"
"MM v[152:155], a[60:63], a[136:139], v[152:155]\n"
"MM v[156:159], a[44:47], a[120:123], v[156:159]\n"
"MM v[156:159], a[60:63], a[140:143], v[156:159]\n"
"s_waitcnt lgkmcnt(0)\n"
"s_barrier\n"
"MM v[0:3], a[0:3], a[64:67], v[0:3]\n"
"ds_read_b64_tr_b16 a[32:33], v160 offset:16384\n"
"ds_read_b64_tr_b16 a[34:35], v160 offset:17408\n"
"MM v[0:3], a[16:19], a[84:87], v[0:3]\n"
"s_add_u32 m0, s66, 0\n"
"ds_read_b64_tr_b16 a[48:49], v160 offset:20480\n"
"MM v[4:7], a[0:3], a[68:71], v[4:7]\n"
"buffer_load_dwordx4 v164, s[36:39], s67 offen lds\n"
"MM v[4:7], a[16:19], a[88:91], v[4:7]\n"
"ds_read_b64_tr_b16 a[50:51], v160 offset:21504\n"
"s_add_u32 m0, s66, 1024\n"
"MM v[8:11], a[0:3], a[72:75], v[8:11]\n"
"buffer_load_dwordx4 v165, s[36:39], s67 offen lds\n"
"MM v[8:11], a[16:19], a[92:95], v[8:11]\n"
"ds_read_b64_tr_b16 a[36:37], v161 offset:16384\n"
"ds_read_b64_tr_b16 a[38:39], v161 offset:17408\n"
"MM v[12:15], a[0:3], a[76:79], v[12:15]\n"
"ds_read_b64_tr_b16 a[52:53], v161 offset:20480\n"
"s_add_u32 m0, s66, 2048\n"
"MM v[12:15], a[16:19], a[96:99], v[12:15]\n"
"buffer_load_dwordx4 v166, s[36:39], s67 offen lds\n"
"MM v[16:19], a[0:3], a[80:83], v[16:19]\n"
"ds_read_b64_tr_b16 a[54:55], v161 offset:21504\n"
"ds_read_b64_tr_b16 a[40:41], v160 offset:16896\n"
"MM v[16:19], a[16:19], a[100:103], v[16:19]\n"
"ds_read_b64_tr_b16 a[42:43], v160 offset:17920\n"
"s_add_u32 m0, s66, 3072\n"
"MM v[20:23], a[4:7], a[64:67], v[20:23]\n"
"buffer_load_dwordx4 v167, s[36:39], s67 offen lds\n"
"MM v[20:23], a[20:23], a[84:87], v[20:23]\n"
"ds_read_b64_tr_b16 a[56:57], v160 offset:20992\n"
"ds_read_b64_tr_b16 a[58:59], v160 offset:22016\n"
"MM v[24:27], a[4:7], a[68:71], v[24:27]\n"
"MM v[24:27], a[20:23], a[88:91], v[24:27]\n"
"s_add_u32 m0, s66, 4096\n"
"ds_read_b64_tr_b16 a[44:45], v161 offset:16896\n"
"MM v[28:31], a[4:7], a[72:75], v[28:31]\n"
"buffer_load_dwordx4 v168, s[36:39], s67 offen lds\n"
"MM v[28:31], a[20:23], a[92:95], v[28:31]\n"
"ds_read_b64_tr_b16 a[46:47], v161 offset:17920\n"
"MM v[32:35], a[4:7], a[76:79], v[32:35]\n"
"ds_read_b64_tr_b16 a[60:61], v161 offset:20992\n"
"MM v[32:35], a[20:23], a[96:99], v[32:35]\n"
"s_add_u32 m0, s66, 5120\n"
"ds_read_b64_tr_b16 a[62:63], v161 offset:22016\n"
"MM v[36:39], a[4:7], a[80:83], v[36:39]\n"
"buffer_load_dwordx4 v169, s[36:39], s67 offen lds\n"
"MM v[36:39], a[20:23], a[100:103], v[36:39]\n"
"MM v[40:43], a[8:11], a[64:67], v[40:43]\n"
"s_add_u32 m0, s66, 6144\n"
"MM v[40:43], a[24:27], a[84:87], v[40:43]\n"
"buffer_load_dwordx4 v170, s[36:39], s67 offen lds\n"
"MM v[44:47], a[8:11], a[68:71], v[44:47]\n"
"MM v[44:47], a[24:27], a[88:91], v[44:47]\n"
"MM v[48:51], a[8:11], a[72:75], v[48:51]\n"
"s_add_u32 m0, s66, 7168\n"
"MM v[48:51], a[24:27], a[92:95], v[48:51]\n"
"buffer_load_dwordx4 v171, s[36:39], s67 offen lds\n"
"MM v[52:55], a[8:11], a[76:79], v[52:55]\n"
"MM v[52:55], a[24:27], a[96:99], v[52:55]\n"
"MM v[56:59], a[8:11], a[80:83], v[56:59]\n"
"s_add_u32 m0, s66, 8192\n"
"MM v[56:59], a[24:27], a[100:103], v[56:59]\n"
"buffer_load_dwordx4 v172, s[36:39], s67 offen lds\n"
"MM v[60:63], a[12:15], a[64:67], v[60:63]\n"
"MM v[60:63], a[28:31], a[84:87], v[60:63]\n"
"MM v[64:67], a[12:15], a[68:71], v[64:67]\n"
"s_add_u32 m0, s66, 9216\n"
"MM v[64:67], a[28:31], a[88:91], v[64:67]\n"
"buffer_load_dwordx4 v173, s[36:39], s67 offen lds\n"
"MM v[68:71], a[12:15], a[72:75], v[68:71]\n"
"MM v[68:71], a[28:31], a[92:95], v[68:71]\n"
"s_add_u32 s67, s67, 128\n"
"MM v[72:75], a[12:15], a[76:79], v[72:75]\n"
"ds_read_b32 v254, v191 offset:0\n"
"MM v[72:75], a[28:31], a[96:99], v[72:75]\n"
"MM v[76:79], a[12:15], a[80:83], v[76:79]\n"
"MM v[76:79], a[28:31], a[100:103], v[76:79]\n"
"s_waitcnt vmcnt(15)\n"
"s_waitcnt lgkmcnt(0)\n"
"s_barrier\n"
"MM v[80:83], a[32:35], a[64:67], v[80:83]\n"
"ds_read_b64_tr_b16 a[0:1], v160 offset:32768\n"
"ds_read_b64_tr_b16 a[2:3], v160 offset:33792\n"
"MM v[80:83], a[48:51], a[84:87], v[80:83]\n"
"ds_read_b64_tr_b16 a[144:145], v192 offset:12288\n"
"ds_read_b64_tr_b16 a[146:147], v192 offset:15360\n"
"MM v[84:87], a[32:35], a[68:71], v[84:87]\n"
"ds_read_b64_tr_b16 a[16:17], v160 offset:36864\n"
"ds_read_b64_tr_b16 a[148:149], v192 offset:12320\n"
"MM v[84:87], a[48:51], a[88:91], v[84:87]\n"
"ds_read_b64_tr_b16 a[18:19], v160 offset:37888\n"
"ds_read_b64_tr_b16 a[150:151], v192 offset:15392\n"
"MM v[88:91], a[32:35], a[72:75], v[88:91]\n"
"ds_read_b128 a[104:107], v162 offset:20480\n"
"ds_read_b128 a[124:127], v163 offset:20480\n"
"MM v[88:91], a[48:51], a[92:95], v[88:91]\n"
"ds_read_b64_tr_b16 a[152:153], v192 offset:12352\n"
"ds_read_b128 a[108:111], v162 offset:22528\n"
"MM v[92:95], a[32:35], a[76:79], v[92:95]\n"
"ds_read_b64_tr_b16 a[154:155], v192 offset:15424\n"
"ds_read_b128 a[128:131], v163 offset:22528\n"
"MM v[92:95], a[48:51], a[96:99], v[92:95]\n"
"ds_read_b64_tr_b16 a[156:157], v192 offset:12384\n"
"ds_read_b128 a[112:115], v162 offset:24576\n"
"MM v[96:99], a[32:35], a[80:83], v[96:99]\n"
"ds_read_b64_tr_b16 a[158:159], v192 offset:15456\n"
"ds_read_b128 a[132:135], v163 offset:24576\n"
"MM v[96:99], a[48:51], a[100:103], v[96:99]\n"
"ds_read_b64_tr_b16 a[160:161], v192 offset:12416\n"
"ds_read_b128 a[116:119], v162 offset:26624\n"
"MM v[100:103], a[36:39], a[64:67], v[100:103]\n"
"ds_read_b64_tr_b16 a[162:163], v192 offset:15488\n"
"ds_read_b128 a[136:139], v163 offset:26624\n"
"MM v[100:103], a[52:55], a[84:87], v[100:103]\n"
"ds_read_b128 a[120:123], v162 offset:28672\n"
"ds_read_b128 a[140:143], v163 offset:28672\n"
"MM v[104:107], a[36:39], a[68:71], v[104:107]\n"
"ds_read_b64_tr_b16 a[4:5], v161 offset:32768\n"
"MM v[104:107], a[52:55], a[88:91], v[104:107]\n"
"ds_read_b64_tr_b16 a[6:7], v161 offset:33792\n"
"MM v[108:111], a[36:39], a[72:75], v[108:111]\n"
"s_add_u32 s68, s99, 81920\n"
"ds_read_b64_tr_b16 a[20:21], v161 offset:36864\n"
"MM v[108:111], a[52:55], a[92:95], v[108:111]\n"
"s_waitcnt lgkmcnt(15)\n"
"buffer_store_dwordx4 a[144:147], v193, s[56:59], s68 offen sc1\n"
"MM v[112:115], a[36:39], a[76:79], v[112:115]\n"
"buffer_store_dwordx4 a[148:151], v193, s[56:59], s68 offen offset:1024 sc1\n"
"MM v[112:115], a[52:55], a[96:99], v[112:115]\n"
"ds_read_b64_tr_b16 a[22:23], v161 offset:37888\n"
"ds_read_b64_tr_b16 a[8:9], v160 offset:33280\n"
"MM v[116:119], a[36:39], a[80:83], v[116:119]\n"
"ds_read_b64_tr_b16 a[10:11], v160 offset:34304\n"
"MM v[116:119], a[52:55], a[100:103], v[116:119]\n"
"ds_read_b64_tr_b16 a[24:25], v160 offset:37376\n"
"MM v[120:123], a[40:43], a[64:67], v[120:123]\n"
"s_waitcnt lgkmcnt(15)\n"
"buffer_store_dwordx4 a[152:155], v193, s[56:59], s68 offen offset:2048 sc1\n"
"MM v[120:123], a[56:59], a[84:87], v[120:123]\n"
"ds_read_b64_tr_b16 a[26:27], v160 offset:38400\n"
"ds_read_b64_tr_b16 a[12:13], v161 offset:33280\n"
"MM v[124:127], a[40:43], a[68:71], v[124:127]\n"
"MM v[124:127], a[56:59], a[88:91], v[124:127]\n"
"ds_read_b64_tr_b16 a[14:15], v161 offset:34304\n"
"MM v[128:131], a[40:43], a[72:75], v[128:131]\n"
"ds_read_b64_tr_b16 a[28:29], v161 offset:37376\n"
"MM v[128:131], a[56:59], a[92:95], v[128:131]\n"
"s_waitcnt lgkmcnt(15)\n"
"buffer_store_dwordx4 a[156:159], v193, s[56:59], s68 offen offset:3072 sc1\n"
"MM v[132:135], a[40:43], a[76:79], v[132:135]\n"
"ds_read_b64_tr_b16 a[30:31], v161 offset:38400\n"
"MM v[132:135], a[56:59], a[96:99], v[132:135]\n"
"MM v[136:139], a[40:43], a[80:83], v[136:139]\n"
"MM v[136:139], a[56:59], a[100:103], v[136:139]\n"
"s_add_u32 s68, s99, 86016\n"
"MM v[140:143], a[44:47], a[64:67], v[140:143]\n"
"MM v[140:143], a[60:63], a[84:87], v[140:143]\n"
"s_waitcnt lgkmcnt(15)\n"
"buffer_store_dwordx4 a[160:163], v193, s[56:59], s68 offen sc1\n"
"MM v[144:147], a[44:47], a[68:71], v[144:147]\n"
"MM v[144:147], a[60:63], a[88:91], v[144:147]\n"
"MM v[148:151], a[44:47], a[72:75], v[148:151]\n"
"MM v[148:151], a[60:63], a[92:95], v[148:151]\n"
"MM v[152:155], a[44:47], a[76:79], v[152:155]\n"
"MM v[152:155], a[60:63], a[96:99], v[152:155]\n"
"MM v[156:159], a[44:47], a[80:83], v[156:159]\n"
"MM v[156:159], a[60:63], a[100:103], v[156:159]\n"
"s_branch K1_EX2\n"
"K1_ROLE_X3:\n"
"s_nop 3\n"
"s_getpc_b64 s[52:53]\n"
"K1_PC:\n"
"s_sub_u32 s52, s52, K1_PC-K1_START\n"
"s_subb_u32 s53, s53, 0\n"
"s_and_b32 s53, s53, 0xffff\n"
"s_mov_b32 s54, K1_CODE_END-K1_START\n"
"s_mov_b32 s55, 0x20000\n"
"s_lshr_b32 s68, s65, 3\n"
"s_mul_i32 s68, s68, 6144\n"
"v_lshlrev_b32 v193, 4, v253\n"
"buffer_load_dwordx4 v[200:203], v193, s[52:55], s68 offen\n"
"buffer_load_dwordx4 v[204:207], v193, s[52:55], s68 offen offset:1024\n"
"buffer_load_dwordx4 v[208:211], v193, s[52:55], s68 offen offset:2048\n"
"buffer_load_dwordx4 v[212:215], v193, s[52:55], s68 offen offset:3072\n"
"s_add_u32 s68, s68, 4096\n"
"buffer_load_dwordx4 v[216:219], v193, s[52:55], s68 offen\n"
"buffer_load_dwordx4 v[220:223], v193, s[52:55], s68 offen offset:1024\n"
"v_add_u32 v164, 102400, v18\n"
"v_add_u32 v165, 112640, v19\n"
"v_add_u32 v166, 122880, v18\n"
"v_add_u32 v167, 133120, v19\n"
"v_add_u32 v168, 143360, v18\n"
"v_add_u32 v169, 153600, v19\n"
"v_add_u32 v170, 163840, v18\n"
"v_add_u32 v171, 174080, v19\n"
"v_add_u32 v172, 184320, v18\n"
"v_add_u32 v173, 194560, v19\n"
"s_add_u32 s66, s77, 114688\n"
"s_mov_b32 s67, 128\n"
"s_add_u32 m0, s66, 30720\n"
"s_nop 0\n"
"buffer_load_dwordx4 v164, s[36:39], s67 offen lds\n"
"s_add_u32 m0, s66, 31744\n"
"s_nop 0\n"
"buffer_load_dwordx4 v165, s[36:39], s67 offen lds\n"
"s_add_u32 m0, s66, 32768\n"
"s_nop 0\n"
"buffer_load_dwordx4 v166, s[36:39], s67 offen lds\n"
"s_add_u32 m0, s66, 33792\n"
"s_nop 0\n"
"buffer_load_dwordx4 v167, s[36:39], s67 offen lds\n"
"s_add_u32 m0, s66, 34816\n"
"s_nop 0\n"
"buffer_load_dwordx4 v168, s[36:39], s67 offen lds\n"
"s_add_u32 m0, s66, 35840\n"
"s_nop 0\n"
"buffer_load_dwordx4 v169, s[36:39], s67 offen lds\n"
"s_add_u32 m0, s66, 36864\n"
"s_nop 0\n"
"buffer_load_dwordx4 v170, s[36:39], s67 offen lds\n"
"s_add_u32 m0, s66, 37888\n"
"s_nop 0\n"
"buffer_load_dwordx4 v171, s[36:39], s67 offen lds\n"
"s_add_u32 m0, s66, 38912\n"
"s_nop 0\n"
"buffer_load_dwordx4 v172, s[36:39], s67 offen lds\n"
"s_add_u32 m0, s66, 39936\n"
"s_nop 0\n"
"buffer_load_dwordx4 v173, s[36:39], s67 offen lds\n"
"s_add_u32 s67, s67, 128\n"
"s_lshl_b32 s68, s63, 4\n"
"s_add_u32 s68, s68, s64\n"
"s_lshl_b32 s68, s68, 1\n"
"s_add_u32 s68, s68, s61\n"
"s_lshl_b32 s68, s68, 17\n"
"s_mov_b64 s[44:45], %1\n"
"s_add_u32 s44, s44, s68\n"
"s_addc_u32 s45, s45, 0\n"
"s_and_b32 s45, s45, 0xffff\n"
"s_mov_b32 s46, 131072\n"
"s_mov_b32 s47, 0x20000\n"
"s_mov_b64 s[48:49], %2\n"
"s_add_u32 s48, s48, s68\n"
"s_addc_u32 s49, s49, 0\n"
"s_and_b32 s49, s49, 0xffff\n"
"s_mov_b32 s50, 131072\n"
"s_mov_b32 s51, 0x20000\n"
"s_mov_b64 s[56:57], %5\n"
"s_and_b32 s57, s57, 0xffff\n"
"s_mov_b32 s58, 43089920\n"
"s_mov_b32 s59, 0x20000\n"
"s_mov_b64 s[92:93], %6\n"
"s_mov_b32 s75, 0\n"
"s_mov_b32 s76, 0x41000000\n"
"s_mov_b32 s78, 0x3e0293ee\n"
"s_mov_b32 s79, s78\n"
"s_mov_b32 s82, 0xffff0000\n"
"s_mov_b32 s83, 0xffff0000\n"
"s_lshl_b32 s70, s60, 12\n"
"s_add_u32 s70, s70, s77\n"
"v_lshrrev_b32 v9, 4, v253\n"
"v_and_b32 v10, 15, v253\n"
"v_bfe_u32 v11, v253, 2, 2\n"
"v_and_b32 v12, 1, v9\n"
"v_lshrrev_b32 v13, 1, v9\n"
"s_lshl_b32 s68, s61, 13\n"
"s_add_u32 s68, s68, s77\n"
"v_lshrrev_b32 v0, 2, v10\n"
"v_sub_u32 v0, 4, v0\n"
"v_and_b32 v0, 3, v0\n"
"v_xor_b32 v0, v0, v9\n"
"v_lshlrev_b32 v0, 4, v0\n"
"v_lshrrev_b32 v1, 2, v10\n"
"v_lshl_add_u32 v0, v1, 10, v0\n"
"v_and_b32 v1, 3, v10\n"
"v_lshl_add_u32 v0, v1, 6, v0\n"
"v_add_u32 v240, s68, v0\n"
"v_add_u32 v241, 0x10000, v240\n"
"v_lshl_add_u32 v3, v12, 2, v11\n"
"v_lshlrev_b32 v3, 6, v3\n"
"v_lshlrev_b32 v0, 6, v11\n"
"v_lshl_add_u32 v0, v8, 3, v0\n"
"v_lshl_add_u32 v0, v12, 5, v0\n"
"v_lshl_add_u32 v0, v9, 10, v0\n"
"v_add_u32 v243, s68, v0\n"
"v_xor_b32 v246, 32, v243\n"
"v_add_u32 v244, 0x10000, v243\n"
"v_add_u32 v247, 0x10000, v246\n"
"v_lshlrev_b32 v15, 8, v11\n"
"v_lshl_add_u32 v15, v9, 6, v15\n"
"s_lshl_b32 s69, s62, 12\n"
"v_add_u32 v15, s69, v15\n"
"v_lshl_add_u32 v249, v8, 4, v15\n"
"v_xor_b32 v250, 16, v249\n"
"v_xor_b32 v251, 32, v249\n"
"v_xor_b32 v252, 48, v249\n"
"s_mul_i32 s94, s61, 8192\n"
"v_lshl_add_u32 v0, v13, 11, v3\n"
"v_xor_b32 v1, v8, v12\n"
"v_lshl_add_u32 v0, v1, 4, v0\n"
"v_and_b32 v1, 1, v8\n"
"v_lshl_add_u32 v0, v1, 3, v0\n"
"v_add_u32 v0, s68, v0\n"
"v_add_u32 v160, 49152, v0\n"
"v_xor_b32 v161, 8, v160\n"
"s_mul_i32 s69, s62, 10240\n"
"s_add_u32 s69, s69, s77\n"
"s_add_u32 s69, s69, 114688\n"
"v_lshrrev_b32 v0, 1, v10\n"
"v_xor_b32 v0, v0, v9\n"
"v_lshlrev_b32 v0, 4, v0\n"
"v_lshl_add_u32 v0, v10, 7, v0\n"
"v_add_u32 v162, s69, v0\n"
"v_xor_b32 v163, 64, v162\n"
"v_mov_b32 v0, 0x3f803f80\n"
"v_accvgpr_write_b32 a244, v0\n"
"v_accvgpr_write_b32 a245, v0\n"
"v_accvgpr_write_b32 a246, v0\n"
"v_accvgpr_write_b32 a247, v0\n"
"v_lshl_add_u32 v197, v253, 4, s70\n"
"v_add_u32 v198, 0x10000, v197\n"
"v_add_u32 v199, 0x20000, v197\n"
"s_add_u32 s68, s77, 155648\n"
"v_lshl_add_u32 v191, v253, 2, s68\n"
"s_lshl_b32 s68, s60, 8\n"
"s_add_u32 s68, s68, s77\n"
"s_add_u32 s68, s68, 156160\n"
"v_lshl_add_u32 v242, v253, 2, s68\n"
"v_mul_u32_u24 v0, 260, v8\n"
"s_add_u32 s68, s77, 156160\n"
"v_add_u32 v245, s68, v0\n"
"v_mov_b32 v0, 0\n"
"ds_write_b32 v242, v0 offset:0\n"
"v_subrev_u32 v186, 16, v253\n"
"v_bfe_u32 v184, v186, 4, 1\n"
"s_lshl_b32 s68, s87, 1\n"
"v_add_u32 v184, s68, v184\n"
"v_and_b32 v185, 7, v186\n"
"v_bfe_u32 v181, v186, 3, 1\n"
"v_lshl_add_u32 v185, v185, 1, v181\n"
"v_cmp_gt_u32 vcc, 32, v186\n"
"v_mov_b32 v181, s63\n"
"s_nop 0\n"
"v_cndmask_b32 v184, v181, v184, vcc\n"
"v_mov_b32 v181, s64\n"
"v_cndmask_b32 v185, v181, v185, vcc\n"
"s_and_b32 s68, s64, 1\n"
"v_and_b32 v181, 7, v253\n"
"v_lshl_add_u32 v181, v181, 1, s68\n"
"s_mov_b32 vcc_lo, 0xff\n"
"s_mov_b32 vcc_hi, 0xff000000\n"
"v_cndmask_b32 v185, v185, v181, vcc\n"
"s_xor_b32 s68, s64, 1\n"
"v_mov_b32 v181, s68\n"
"v_cmp_eq_u32 vcc, 8, v253\n"
"s_nop 1\n"
"v_cndmask_b32 v185, v185, v181, vcc\n"
"v_and_b32 v181, 7, v184\n"
"v_lshlrev_b32 v181, 5, v181\n"
"v_and_b32 v186, 3, v185\n"
"v_lshl_add_u32 v181, v186, 3, v181\n"
"v_lshrrev_b32 v186, 3, v184\n"
"v_lshl_add_u32 v181, v186, 2, v181\n"
"v_lshrrev_b32 v186, 2, v185\n"
"v_add_u32 v181, v181, v186\n"
"v_and_b32 v184, 3, v253\n"
"v_lshlrev_b32 v184, 5, v184\n"
"v_mov_b32 v180, 0\n"
"v_cmp_gt_u32 vcc, 48, v253\n"
"s_nop 1\n"
"v_cndmask_b32 v184, v184, v180, vcc\n"
"v_mov_b32 v180, 32\n"
"v_cmp_eq_u32 vcc, 8, v253\n"
"s_nop 1\n"
"v_cndmask_b32 v184, v184, v180, vcc\n"
"v_mov_b32 v180, 64\n"
"v_cmp_gt_u32 vcc, 8, v253\n"
"s_nop 1\n"
"v_cndmask_b32 v184, v184, v180, vcc\n"
"v_mov_b32 v180, 96\n"
"v_cmp_le_u32 vcc, 56, v253\n"
"s_nop 1\n"
"v_cndmask_b32 v184, v184, v180, vcc\n"
"v_lshl_add_u32 v180, v181, 7, v184\n"
"v_add_u32 v180, 42991616, v180\n"
"buffer_load_dword v254, v180, s[56:59], 0 offen sc1\n"
"v_lshlrev_b32 v0, 4, v253\n"
"s_mov_b64 s[52:53], %3\n"
"s_and_b32 s53, s53, 0xffff\n"
"s_mov_b32 s54, 5242880\n"
"s_mov_b32 s55, 0x20000\n"
"s_mul_i32 s68, s65, 16384\n"
"s_add_u32 s68, s68, 1048576\n"
"buffer_load_dwordx4 a[184:187], v0, s[52:55], s68 offen\n"
"buffer_load_dwordx4 a[188:191], v0, s[52:55], s68 offen offset:1024\n"
"buffer_load_dwordx4 a[192:195], v0, s[52:55], s68 offen offset:2048\n"
"buffer_load_dwordx4 a[196:199], v0, s[52:55], s68 offen offset:3072\n"
"s_add_u32 s68, s68, 4096\n"
"buffer_load_dwordx4 a[200:203], v0, s[52:55], s68 offen\n"
"buffer_load_dwordx4 a[204:207], v0, s[52:55], s68 offen offset:1024\n"
"buffer_load_dwordx4 a[208:211], v0, s[52:55], s68 offen offset:2048\n"
"buffer_load_dwordx4 a[212:215], v0, s[52:55], s68 offen offset:3072\n"
"s_add_u32 s68, s68, 4096\n"
"buffer_load_dwordx4 a[216:219], v0, s[52:55], s68 offen\n"
"buffer_load_dwordx4 a[220:223], v0, s[52:55], s68 offen offset:1024\n"
"buffer_load_dwordx4 a[224:227], v0, s[52:55], s68 offen offset:2048\n"
"buffer_load_dwordx4 a[228:231], v0, s[52:55], s68 offen offset:3072\n"
"s_add_u32 s68, s68, 4096\n"
"buffer_load_dwordx4 a[184:187], v0, s[52:55], s68 offen\n"
"buffer_load_dwordx4 a[188:191], v0, s[52:55], s68 offen offset:1024\n"
"buffer_load_dwordx4 a[192:195], v0, s[52:55], s68 offen offset:2048\n"
"s_and_b32 s69, s87, 1\n"
"s_lshl_b32 s69, s69, 5\n"
"v_lshrrev_b32 v0, 2, v253\n"
"v_mul_u32_u24 v0, 192, v0\n"
"v_lshl_add_u32 v0, v8, 3, v0\n"
"s_add_u32 s69, s69, s77\n"
"v_add_u32 v0, s69, v0\n"
"v_add_u32 v192, 16384, v0\n"
"s_mul_i32 s99, s87, 5120\n"
"s_mul_i32 s69, s86, 163840\n"
"s_add_u32 s99, s99, s69\n"
"s_add_u32 s99, s99, 10485760\n"
"s_add_u32 s69, s86, 16\n"
"v_mov_b32 v0, s69\n"
"v_mov_b32 v1, 8\n"
"v_cmp_eq_u32 vcc, 8, v253\n"
"s_nop 1\n"
"v_cndmask_b32 v0, v0, v1, vcc\n"
"v_mov_b32 v1, s87\n"
"v_cmp_gt_u32 vcc, 8, v253\n"
"s_nop 1\n"
"v_cndmask_b32 v0, v0, v1, vcc\n"
"v_add_u32 v1, 56, v1\n"
"v_cmp_le_u32 vcc, 56, v253\n"
"s_nop 1\n"
"v_cndmask_b32 v0, v0, v1, vcc\n"
"v_lshlrev_b32 v0, 2, v0\n"
"v_lshl_add_u32 v0, v181, 8, v0\n"
"v_add_u32 v0, 43024384, v0\n"
"v_mov_b32 v1, 2147483392\n"
"s_mov_b32 s96, 0xffff01ff\n"
"s_mov_b32 s97, 0xff00ffff\n"
"v_cndmask_b32 v182, v1, v0, s[96:97]\n"
"s_waitcnt vmcnt(32)\n"
"s_waitcnt lgkmcnt(0)\n"
"s_barrier\n"
"ds_read_b64_tr_b16 a[0:1], v160 offset:0\n"
"ds_read_b64_tr_b16 a[2:3], v160 offset:1024\n"
"ds_read_b64_tr_b16 a[16:17], v160 offset:4096\n"
"ds_read_b64_tr_b16 a[18:19], v160 offset:5120\n"
"ds_read_b128 a[64:67], v162 offset:0\n"
"ds_read_b128 a[84:87], v163 offset:0\n"
"ds_read_b128 a[68:71], v162 offset:2048\n"
"ds_read_b128 a[88:91], v163 offset:2048\n"
"ds_read_b128 a[72:75], v162 offset:4096\n"
"ds_read_b128 a[92:95], v163 offset:4096\n"
"ds_read_b128 a[76:79], v162 offset:6144\n"
"ds_read_b128 a[96:99], v163 offset:6144\n"
"ds_read_b128 a[80:83], v162 offset:8192\n"
"ds_read_b128 a[100:103], v163 offset:8192\n"
"ds_read_b64_tr_b16 a[4:5], v161 offset:0\n"
"ds_read_b64_tr_b16 a[6:7], v161 offset:1024\n"
"ds_read_b64_tr_b16 a[20:21], v161 offset:4096\n"
"ds_read_b64_tr_b16 a[22:23], v161 offset:5120\n"
"ds_read_b64_tr_b16 a[8:9], v160 offset:512\n"
"ds_read_b64_tr_b16 a[10:11], v160 offset:1536\n"
"ds_read_b64_tr_b16 a[24:25], v160 offset:4608\n"
"ds_read_b64_tr_b16 a[26:27], v160 offset:5632\n"
"ds_read_b64_tr_b16 a[12:13], v161 offset:512\n"
"ds_read_b64_tr_b16 a[14:15], v161 offset:1536\n"
"ds_read_b64_tr_b16 a[28:29], v161 offset:4608\n"
"ds_read_b64_tr_b16 a[30:31], v161 offset:5632\n"
"s_waitcnt lgkmcnt(0)\n"
"s_barrier\n"
"MM v[0:3], a[0:3], a[64:67], 0\n"
"ds_read_b64_tr_b16 a[32:33], v160 offset:16384\n"
"ds_read_b64_tr_b16 a[34:35], v160 offset:17408\n"
"MM v[0:3], a[16:19], a[84:87], v[0:3]\n"
"ds_read_b64_tr_b16 a[48:49], v160 offset:20480\n"
"ds_read_b64_tr_b16 a[50:51], v160 offset:21504\n"
"MM v[4:7], a[0:3], a[68:71], 0\n"
"s_add_u32 m0, s66, 10240\n"
"ds_read_b64_tr_b16 a[36:37], v161 offset:16384\n"
"MM v[4:7], a[16:19], a[88:91], v[4:7]\n"
"buffer_load_dwordx4 v164, s[36:39], s67 offen lds\n"
"MM v[8:11], a[0:3], a[72:75], 0\n"
"s_add_u32 m0, s66, 11264\n"
"ds_read_b64_tr_b16 a[38:39], v161 offset:17408\n"
"MM v[8:11], a[16:19], a[92:95], v[8:11]\n"
"buffer_load_dwordx4 v165, s[36:39], s67 offen lds\n"
"MM v[12:15], a[0:3], a[76:79], 0\n"
"ds_read_b64_tr_b16 a[52:53], v161 offset:20480\n"
"ds_read_b64_tr_b16 a[54:55], v161 offset:21504\n"
"MM v[12:15], a[16:19], a[96:99], v[12:15]\n"
"s_add_u32 m0, s66, 12288\n"
"ds_read_b64_tr_b16 a[40:41], v160 offset:16896\n"
"MM v[16:19], a[0:3], a[80:83], 0\n"
"buffer_load_dwordx4 v166, s[36:39], s67 offen lds\n"
"MM v[16:19], a[16:19], a[100:103], v[16:19]\n"
"ds_read_b64_tr_b16 a[42:43], v160 offset:17920\n"
"MM v[20:23], a[4:7], a[64:67], 0\n"
"ds_read_b64_tr_b16 a[56:57], v160 offset:20992\n"
"s_add_u32 m0, s66, 13312\n"
"MM v[20:23], a[20:23], a[84:87], v[20:23]\n"
"buffer_load_dwordx4 v167, s[36:39], s67 offen lds\n"
"MM v[24:27], a[4:7], a[68:71], 0\n"
"ds_read_b64_tr_b16 a[58:59], v160 offset:22016\n"
"MM v[24:27], a[20:23], a[88:91], v[24:27]\n"
"ds_read_b64_tr_b16 a[44:45], v161 offset:16896\n"
"MM v[28:31], a[4:7], a[72:75], 0\n"
"s_add_u32 m0, s66, 14336\n"
"ds_read_b64_tr_b16 a[46:47], v161 offset:17920\n"
"MM v[28:31], a[20:23], a[92:95], v[28:31]\n"
"buffer_load_dwordx4 v168, s[36:39], s67 offen lds\n"
"MM v[32:35], a[4:7], a[76:79], 0\n"
"ds_read_b64_tr_b16 a[60:61], v161 offset:20992\n"
"MM v[32:35], a[20:23], a[96:99], v[32:35]\n"
"ds_read_b64_tr_b16 a[62:63], v161 offset:22016\n"
"MM v[36:39], a[4:7], a[80:83], 0\n"
"s_add_u32 m0, s66, 15360\n"
"MM v[36:39], a[20:23], a[100:103], v[36:39]\n"
"buffer_load_dwordx4 v169, s[36:39], s67 offen lds\n"
"MM v[40:43], a[8:11], a[64:67], 0\n"
"MM v[40:43], a[24:27], a[84:87], v[40:43]\n"
"s_add_u32 m0, s66, 16384\n"
"MM v[44:47], a[8:11], a[68:71], 0\n"
"buffer_load_dwordx4 v170, s[36:39], s67 offen lds\n"
"MM v[44:47], a[24:27], a[88:91], v[44:47]\n"
"MM v[48:51], a[8:11], a[72:75], 0\n"
"MM v[48:51], a[24:27], a[92:95], v[48:51]\n"
"s_add_u32 m0, s66, 17408\n"
"MM v[52:55], a[8:11], a[76:79], 0\n"
"buffer_load_dwordx4 v171, s[36:39], s67 offen lds\n"
"MM v[52:55], a[24:27], a[96:99], v[52:55]\n"
"MM v[56:59], a[8:11], a[80:83], 0\n"
"MM v[56:59], a[24:27], a[100:103], v[56:59]\n"
"s_add_u32 m0, s66, 18432\n"
"MM v[60:63], a[12:15], a[64:67], 0\n"
"buffer_load_dwordx4 v172, s[36:39], s67 offen lds\n"
"MM v[60:63], a[28:31], a[84:87], v[60:63]\n"
"MM v[64:67], a[12:15], a[68:71], 0\n"
"s_add_u32 m0, s66, 19456\n"
"MM v[64:67], a[28:31], a[88:91], v[64:67]\n"
"buffer_load_dwordx4 v173, s[36:39], s67 offen lds\n"
"MM v[68:71], a[12:15], a[72:75], 0\n"
"MM v[68:71], a[28:31], a[92:95], v[68:71]\n"
"MM v[72:75], a[12:15], a[76:79], 0\n"
"s_add_u32 s67, s67, 128\n"
"MM v[72:75], a[28:31], a[96:99], v[72:75]\n"
"MM v[76:79], a[12:15], a[80:83], 0\n"
"MM v[76:79], a[28:31], a[100:103], v[76:79]\n"
"s_waitcnt vmcnt(26)\n"
"s_waitcnt lgkmcnt(0)\n"
"s_barrier\n"
"MM v[80:83], a[32:35], a[64:67], 0\n"
"ds_read_b64_tr_b16 a[0:1], v160 offset:32768\n"
"ds_read_b64_tr_b16 a[2:3], v160 offset:33792\n"
"MM v[80:83], a[48:51], a[84:87], v[80:83]\n"
"ds_read_b64_tr_b16 a[16:17], v160 offset:36864\n"
"ds_read_b64_tr_b16 a[18:19], v160 offset:37888\n"
"MM v[84:87], a[32:35], a[68:71], 0\n"
"ds_read_b128 a[104:107], v162 offset:20480\n"
"ds_read_b128 a[124:127], v163 offset:20480\n"
"MM v[84:87], a[48:51], a[88:91], v[84:87]\n"
"ds_read_b128 a[108:111], v162 offset:22528\n"
"MM v[88:91], a[32:35], a[72:75], 0\n"
"ds_read_b128 a[128:131], v163 offset:22528\n"
"MM v[88:91], a[48:51], a[92:95], v[88:91]\n"
"ds_read_b128 a[112:115], v162 offset:24576\n"
"MM v[92:95], a[32:35], a[76:79], 0\n"
"ds_read_b128 a[132:135], v163 offset:24576\n"
"MM v[92:95], a[48:51], a[96:99], v[92:95]\n"
"MM v[96:99], a[32:35], a[80:83], 0\n"
"ds_read_b128 a[116:119], v162 offset:26624\n"
"MM v[96:99], a[48:51], a[100:103], v[96:99]\n"
"ds_read_b128 a[136:139], v163 offset:26624\n"
"MM v[100:103], a[36:39], a[64:67], 0\n"
"ds_read_b128 a[120:123], v162 offset:28672\n"
"MM v[100:103], a[52:55], a[84:87], v[100:103]\n"
"ds_read_b128 a[140:143], v163 offset:28672\n"
"MM v[104:107], a[36:39], a[68:71], 0\n"
"ds_read_b64_tr_b16 a[4:5], v161 offset:32768\n"
"MM v[104:107], a[52:55], a[88:91], v[104:107]\n"
"ds_read_b64_tr_b16 a[6:7], v161 offset:33792\n"
"MM v[108:111], a[36:39], a[72:75], 0\n"
"ds_read_b64_tr_b16 a[20:21], v161 offset:36864\n"
"MM v[108:111], a[52:55], a[92:95], v[108:111]\n"
"MM v[112:115], a[36:39], a[76:79], 0\n"
"ds_read_b64_tr_b16 a[22:23], v161 offset:37888\n"
"MM v[112:115], a[52:55], a[96:99], v[112:115]\n"
"ds_read_b64_tr_b16 a[8:9], v160 offset:33280\n"
"MM v[116:119], a[36:39], a[80:83], 0\n"
"ds_read_b64_tr_b16 a[10:11], v160 offset:34304\n"
"MM v[116:119], a[52:55], a[100:103], v[116:119]\n"
"ds_read_b64_tr_b16 a[24:25], v160 offset:37376\n"
"MM v[120:123], a[40:43], a[64:67], 0\n"
"ds_read_b64_tr_b16 a[26:27], v160 offset:38400\n"
"MM v[120:123], a[56:59], a[84:87], v[120:123]\n"
"ds_read_b64_tr_b16 a[12:13], v161 offset:33280\n"
"MM v[124:127], a[40:43], a[68:71], 0\n"
"MM v[124:127], a[56:59], a[88:91], v[124:127]\n"
"ds_read_b64_tr_b16 a[14:15], v161 offset:34304\n"
"MM v[128:131], a[40:43], a[72:75], 0\n"
"ds_read_b64_tr_b16 a[28:29], v161 offset:37376\n"
"MM v[128:131], a[56:59], a[92:95], v[128:131]\n"
"ds_read_b64_tr_b16 a[30:31], v161 offset:38400\n"
"MM v[132:135], a[40:43], a[76:79], 0\n"
"MM v[132:135], a[56:59], a[96:99], v[132:135]\n"
"MM v[136:139], a[40:43], a[80:83], 0\n"
"MM v[136:139], a[56:59], a[100:103], v[136:139]\n"
"MM v[140:143], a[44:47], a[64:67], 0\n"
"MM v[140:143], a[60:63], a[84:87], v[140:143]\n"
"MM v[144:147], a[44:47], a[68:71], 0\n"
"MM v[144:147], a[60:63], a[88:91], v[144:147]\n"
"MM v[148:151], a[44:47], a[72:75], 0\n"
"MM v[148:151], a[60:63], a[92:95], v[148:151]\n"
"MM v[152:155], a[44:47], a[76:79], 0\n"
"MM v[152:155], a[60:63], a[96:99], v[152:155]\n"
"MM v[156:159], a[44:47], a[80:83], 0\n"
"MM v[156:159], a[60:63], a[100:103], v[156:159]\n"
"s_waitcnt lgkmcnt(0)\n"
"s_barrier\n"
"MM v[0:3], a[0:3], a[104:107], v[0:3]\n"
"ds_read_b64_tr_b16 a[32:33], v160 offset:49152\n"
"ds_read_b64_tr_b16 a[34:35], v160 offset:50176\n"
"MM v[0:3], a[16:19], a[124:127], v[0:3]\n"
"ds_read_b64_tr_b16 a[48:49], v160 offset:53248\n"
"ds_read_b64_tr_b16 a[50:51], v160 offset:54272\n"
"MM v[4:7], a[0:3], a[108:111], v[4:7]\n"
"s_add_u32 m0, s66, 30720\n"
"ds_read_b64_tr_b16 a[36:37], v161 offset:49152\n"
"MM v[4:7], a[16:19], a[128:131], v[4:7]\n"
"buffer_load_dwordx4 v164, s[36:39], s67 offen lds\n"
"MM v[8:11], a[0:3], a[112:115], v[8:11]\n"
"s_add_u32 m0, s66, 31744\n"
"ds_read_b64_tr_b16 a[38:39], v161 offset:50176\n"
"MM v[8:11], a[16:19], a[132:135], v[8:11]\n"
"buffer_load_dwordx4 v165, s[36:39], s67 offen lds\n"
"MM v[12:15], a[0:3], a[116:119], v[12:15]\n"
"ds_read_b64_tr_b16 a[52:53], v161 offset:53248\n"
"ds_read_b64_tr_b16 a[54:55], v161 offset:54272\n"
"MM v[12:15], a[16:19], a[136:139], v[12:15]\n"
"s_add_u32 m0, s66, 32768\n"
"ds_read_b64_tr_b16 a[40:41], v160 offset:49664\n"
"MM v[16:19], a[0:3], a[120:123], v[16:19]\n"
"buffer_load_dwordx4 v166, s[36:39], s67 offen lds\n"
"MM v[16:19], a[16:19], a[140:143], v[16:19]\n"
"ds_read_b64_tr_b16 a[42:43], v160 offset:50688\n"
"MM v[20:23], a[4:7], a[104:107], v[20:23]\n"
"ds_read_b64_tr_b16 a[56:57], v160 offset:53760\n"
"s_add_u32 m0, s66, 33792\n"
"MM v[20:23], a[20:23], a[124:127], v[20:23]\n"
"buffer_load_dwordx4 v167, s[36:39], s67 offen lds\n"
"MM v[24:27], a[4:7], a[108:111], v[24:27]\n"
"ds_read_b64_tr_b16 a[58:59], v160 offset:54784\n"
"MM v[24:27], a[20:23], a[128:131], v[24:27]\n"
"ds_read_b64_tr_b16 a[44:45], v161 offset:49664\n"
"MM v[28:31], a[4:7], a[112:115], v[28:31]\n"
"s_add_u32 m0, s66, 34816\n"
"ds_read_b64_tr_b16 a[46:47], v161 offset:50688\n"
"MM v[28:31], a[20:23], a[132:135], v[28:31]\n"
"buffer_load_dwordx4 v168, s[36:39], s67 offen lds\n"
"MM v[32:35], a[4:7], a[116:119], v[32:35]\n"
"ds_read_b64_tr_b16 a[60:61], v161 offset:53760\n"
"MM v[32:35], a[20:23], a[136:139], v[32:35]\n"
"ds_read_b64_tr_b16 a[62:63], v161 offset:54784\n"
"MM v[36:39], a[4:7], a[120:123], v[36:39]\n"
"s_add_u32 m0, s66, 35840\n"
"MM v[36:39], a[20:23], a[140:143], v[36:39]\n"
"buffer_load_dwordx4 v169, s[36:39], s67 offen lds\n"
"MM v[40:43], a[8:11], a[104:107], v[40:43]\n"
"MM v[40:43], a[24:27], a[124:127], v[40:43]\n"
"s_add_u32 m0, s66, 36864\n"
"MM v[44:47], a[8:11], a[108:111], v[44:47]\n"
"buffer_load_dwordx4 v170, s[36:39], s67 offen lds\n"
"MM v[44:47], a[24:27], a[128:131], v[44:47]\n"
"MM v[48:51], a[8:11], a[112:115], v[48:51]\n"
"MM v[48:51], a[24:27], a[132:135], v[48:51]\n"
"s_add_u32 m0, s66, 37888\n"
"MM v[52:55], a[8:11], a[116:119], v[52:55]\n"
"buffer_load_dwordx4 v171, s[36:39], s67 offen lds\n"
"MM v[52:55], a[24:27], a[136:139], v[52:55]\n"
"MM v[56:59], a[8:11], a[120:123], v[56:59]\n"
"MM v[56:59], a[24:27], a[140:143], v[56:59]\n"
"s_add_u32 m0, s66, 38912\n"
"MM v[60:63], a[12:15], a[104:107], v[60:63]\n"
"buffer_load_dwordx4 v172, s[36:39], s67 offen lds\n"
"MM v[60:63], a[28:31], a[124:127], v[60:63]\n"
"MM v[64:67], a[12:15], a[108:111], v[64:67]\n"
"s_add_u32 m0, s66, 39936\n"
"MM v[64:67], a[28:31], a[128:131], v[64:67]\n"
"buffer_load_dwordx4 v173, s[36:39], s67 offen lds\n"
"MM v[68:71], a[12:15], a[112:115], v[68:71]\n"
"MM v[68:71], a[28:31], a[132:135], v[68:71]\n"
"MM v[72:75], a[12:15], a[116:119], v[72:75]\n"
"s_add_u32 s67, s67, 128\n"
"MM v[72:75], a[28:31], a[136:139], v[72:75]\n"
"MM v[76:79], a[12:15], a[120:123], v[76:79]\n"
"MM v[76:79], a[28:31], a[140:143], v[76:79]\n"
"s_waitcnt vmcnt(10)\n"
"s_waitcnt lgkmcnt(0)\n"
"s_barrier\n"
"MM v[80:83], a[32:35], a[104:107], v[80:83]\n"
"ds_read_b64_tr_b16 a[0:1], v160 offset:0\n"
"ds_read_b64_tr_b16 a[2:3], v160 offset:1024\n"
"MM v[80:83], a[48:51], a[124:127], v[80:83]\n"
"ds_read_b64_tr_b16 a[16:17], v160 offset:4096\n"
"ds_read_b64_tr_b16 a[18:19], v160 offset:5120\n"
"MM v[84:87], a[32:35], a[108:111], v[84:87]\n"
"v_xor_b32 v184, 0x5a3c96e1, v254\n"
"ds_read_b128 a[64:67], v162 offset:0\n"
"MM v[84:87], a[48:51], a[128:131], v[84:87]\n"
"buffer_store_dword v184, v182, s[56:59], 0 offen sc1\n"
"MM v[88:91], a[32:35], a[112:115], v[88:91]\n"
"ds_read_b128 a[84:87], v163 offset:0\n"
"ds_read_b128 a[68:71], v162 offset:2048\n"
"MM v[88:91], a[48:51], a[132:135], v[88:91]\n"
"ds_read_b128 a[88:91], v163 offset:2048\n"
"ds_read_b128 a[72:75], v162 offset:4096\n"
"MM v[92:95], a[32:35], a[116:119], v[92:95]\n"
"ds_read_b128 a[92:95], v163 offset:4096\n"
"MM v[92:95], a[48:51], a[136:139], v[92:95]\n"
"MM v[96:99], a[32:35], a[120:123], v[96:99]\n"
"ds_read_b128 a[76:79], v162 offset:6144\n"
"MM v[96:99], a[48:51], a[140:143], v[96:99]\n"
"ds_read_b128 a[96:99], v163 offset:6144\n"
"MM v[100:103], a[36:39], a[104:107], v[100:103]\n"
"ds_read_b128 a[80:83], v162 offset:8192\n"
"v_readlane_b32 s89, v254, 48\n"
"MM v[100:103], a[52:55], a[124:127], v[100:103]\n"
"ds_read_b128 a[100:103], v163 offset:8192\n"
"MM v[104:107], a[36:39], a[108:111], v[104:107]\n"
"ds_read_b64_tr_b16 a[4:5], v161 offset:0\n"
"MM v[104:107], a[52:55], a[128:131], v[104:107]\n"
"ds_read_b64_tr_b16 a[6:7], v161 offset:1024\n"
"MM v[108:111], a[36:39], a[112:115], v[108:111]\n"
"v_readlane_b32 s74, v254, 49\n"
"ds_read_b64_tr_b16 a[20:21], v161 offset:4096\n"
"MM v[108:111], a[52:55], a[132:135], v[108:111]\n"
"MM v[112:115], a[36:39], a[116:119], v[112:115]\n"
"ds_read_b64_tr_b16 a[22:23], v161 offset:5120\n"
"MM v[112:115], a[52:55], a[136:139], v[112:115]\n"
"ds_read_b64_tr_b16 a[8:9], v160 offset:512\n"
"MM v[116:119], a[36:39], a[120:123], v[116:119]\n"
"ds_read_b64_tr_b16 a[10:11], v160 offset:1536\n"
"v_readlane_b32 s88, v254, 50\n"
"MM v[116:119], a[52:55], a[140:143], v[116:119]\n"
"ds_read_b64_tr_b16 a[24:25], v160 offset:4608\n"
"MM v[120:123], a[40:43], a[104:107], v[120:123]\n"
"ds_read_b64_tr_b16 a[26:27], v160 offset:5632\n"
"MM v[120:123], a[56:59], a[124:127], v[120:123]\n"
"ds_read_b64_tr_b16 a[12:13], v161 offset:512\n"
"MM v[124:127], a[40:43], a[108:111], v[124:127]\n"
"v_readlane_b32 s73, v254, 51\n"
"MM v[124:127], a[56:59], a[128:131], v[124:127]\n"
"ds_read_b64_tr_b16 a[14:15], v161 offset:1536\n"
"MM v[128:131], a[40:43], a[112:115], v[128:131]\n"
"ds_read_b64_tr_b16 a[28:29], v161 offset:4608\n"
"MM v[128:131], a[56:59], a[132:135], v[128:131]\n"
"ds_read_b64_tr_b16 a[30:31], v161 offset:5632\n"
"MM v[132:135], a[40:43], a[116:119], v[132:135]\n"
"MM v[132:135], a[56:59], a[136:139], v[132:135]\n"
"MM v[136:139], a[40:43], a[120:123], v[136:139]\n"
"MM v[136:139], a[56:59], a[140:143], v[136:139]\n"
"MM v[140:143], a[44:47], a[104:107], v[140:143]\n"
"MM v[140:143], a[60:63], a[124:127], v[140:143]\n"
"MM v[144:147], a[44:47], a[108:111], v[144:147]\n"
"MM v[144:147], a[60:63], a[128:131], v[144:147]\n"
"MM v[148:151], a[44:47], a[112:115], v[148:151]\n"
"MM v[148:151], a[60:63], a[132:135], v[148:151]\n"
"MM v[152:155], a[44:47], a[116:119], v[152:155]\n"
"MM v[152:155], a[60:63], a[136:139], v[152:155]\n"
"MM v[156:159], a[44:47], a[120:123], v[156:159]\n"
"MM v[156:159], a[60:63], a[140:143], v[156:159]\n"
"s_waitcnt lgkmcnt(0)\n"
"s_barrier\n"
"MM v[0:3], a[0:3], a[64:67], v[0:3]\n"
"ds_read_b64_tr_b16 a[32:33], v160 offset:16384\n"
"ds_read_b64_tr_b16 a[34:35], v160 offset:17408\n"
"MM v[0:3], a[16:19], a[84:87], v[0:3]\n"
"ds_read_b64_tr_b16 a[48:49], v160 offset:20480\n"
"ds_read_b64_tr_b16 a[50:51], v160 offset:21504\n"
"MM v[4:7], a[0:3], a[68:71], v[4:7]\n"
"s_add_u32 m0, s66, 10240\n"
"ds_read_b64_tr_b16 a[36:37], v161 offset:16384\n"
"MM v[4:7], a[16:19], a[88:91], v[4:7]\n"
"buffer_load_dwordx4 v164, s[36:39], s67 offen lds\n"
"MM v[8:11], a[0:3], a[72:75], v[8:11]\n"
"s_add_u32 m0, s66, 11264\n"
"ds_read_b64_tr_b16 a[38:39], v161 offset:17408\n"
"MM v[8:11], a[16:19], a[92:95], v[8:11]\n"
"buffer_load_dwordx4 v165, s[36:39], s67 offen lds\n"
"MM v[12:15], a[0:3], a[76:79], v[12:15]\n"
"ds_read_b64_tr_b16 a[52:53], v161 offset:20480\n"
"ds_read_b64_tr_b16 a[54:55], v161 offset:21504\n"
"MM v[12:15], a[16:19], a[96:99], v[12:15]\n"
"s_add_u32 m0, s66, 12288\n"
"ds_read_b64_tr_b16 a[40:41], v160 offset:16896\n"
"MM v[16:19], a[0:3], a[80:83], v[16:19]\n"
"buffer_load_dwordx4 v166, s[36:39], s67 offen lds\n"
"MM v[16:19], a[16:19], a[100:103], v[16:19]\n"
"ds_read_b64_tr_b16 a[42:43], v160 offset:17920\n"
"MM v[20:23], a[4:7], a[64:67], v[20:23]\n"
"ds_read_b64_tr_b16 a[56:57], v160 offset:20992\n"
"s_add_u32 m0, s66, 13312\n"
"MM v[20:23], a[20:23], a[84:87], v[20:23]\n"
"buffer_load_dwordx4 v167, s[36:39], s67 offen lds\n"
"MM v[24:27], a[4:7], a[68:71], v[24:27]\n"
"ds_read_b64_tr_b16 a[58:59], v160 offset:22016\n"
"MM v[24:27], a[20:23], a[88:91], v[24:27]\n"
"ds_read_b64_tr_b16 a[44:45], v161 offset:16896\n"
"MM v[28:31], a[4:7], a[72:75], v[28:31]\n"
"s_add_u32 m0, s66, 14336\n"
"ds_read_b64_tr_b16 a[46:47], v161 offset:17920\n"
"MM v[28:31], a[20:23], a[92:95], v[28:31]\n"
"buffer_load_dwordx4 v168, s[36:39], s67 offen lds\n"
"MM v[32:35], a[4:7], a[76:79], v[32:35]\n"
"ds_read_b64_tr_b16 a[60:61], v161 offset:20992\n"
"MM v[32:35], a[20:23], a[96:99], v[32:35]\n"
"ds_read_b64_tr_b16 a[62:63], v161 offset:22016\n"
"MM v[36:39], a[4:7], a[80:83], v[36:39]\n"
"s_add_u32 m0, s66, 15360\n"
"MM v[36:39], a[20:23], a[100:103], v[36:39]\n"
"buffer_load_dwordx4 v169, s[36:39], s67 offen lds\n"
"MM v[40:43], a[8:11], a[64:67], v[40:43]\n"
"MM v[40:43], a[24:27], a[84:87], v[40:43]\n"
"s_add_u32 m0, s66, 16384\n"
"MM v[44:47], a[8:11], a[68:71], v[44:47]\n"
"buffer_load_dwordx4 v170, s[36:39], s67 offen lds\n"
"MM v[44:47], a[24:27], a[88:91], v[44:47]\n"
"MM v[48:51], a[8:11], a[72:75], v[48:51]\n"
"MM v[48:51], a[24:27], a[92:95], v[48:51]\n"
"s_add_u32 m0, s66, 17408\n"
"MM v[52:55], a[8:11], a[76:79], v[52:55]\n"
"buffer_load_dwordx4 v171, s[36:39], s67 offen lds\n"
"MM v[52:55], a[24:27], a[96:99], v[52:55]\n"
"MM v[56:59], a[8:11], a[80:83], v[56:59]\n"
"MM v[56:59], a[24:27], a[100:103], v[56:59]\n"
"s_add_u32 m0, s66, 18432\n"
"MM v[60:63], a[12:15], a[64:67], v[60:63]\n"
"buffer_load_dwordx4 v172, s[36:39], s67 offen lds\n"
"MM v[60:63], a[28:31], a[84:87], v[60:63]\n"
"MM v[64:67], a[12:15], a[68:71], v[64:67]\n"
"s_add_u32 m0, s66, 19456\n"
"MM v[64:67], a[28:31], a[88:91], v[64:67]\n"
"buffer_load_dwordx4 v173, s[36:39], s67 offen lds\n"
"MM v[68:71], a[12:15], a[72:75], v[68:71]\n"
"MM v[68:71], a[28:31], a[92:95], v[68:71]\n"
"MM v[72:75], a[12:15], a[76:79], v[72:75]\n"
"s_add_u32 s67, s67, 128\n"
"MM v[72:75], a[28:31], a[96:99], v[72:75]\n"
"MM v[76:79], a[12:15], a[80:83], v[76:79]\n"
"MM v[76:79], a[28:31], a[100:103], v[76:79]\n"
"s_waitcnt vmcnt(11)\n"
"s_waitcnt lgkmcnt(0)\n"
"s_barrier\n"
"MM v[80:83], a[32:35], a[64:67], v[80:83]\n"
"ds_read_b64_tr_b16 a[0:1], v160 offset:32768\n"
"ds_read_b64_tr_b16 a[2:3], v160 offset:33792\n"
"MM v[80:83], a[48:51], a[84:87], v[80:83]\n"
"ds_read_b64_tr_b16 a[16:17], v160 offset:36864\n"
"ds_read_b64_tr_b16 a[18:19], v160 offset:37888\n"
"MM v[84:87], a[32:35], a[68:71], v[84:87]\n"
"ds_write_b32 v191, v254 offset:0\n"
"ds_read_b128 a[104:107], v162 offset:20480\n"
"MM v[84:87], a[48:51], a[88:91], v[84:87]\n"
"ds_read_b128 a[124:127], v163 offset:20480\n"
"ds_read_b128 a[108:111], v162 offset:22528\n"
"MM v[88:91], a[32:35], a[72:75], v[88:91]\n"
"ds_read_b128 a[128:131], v163 offset:22528\n"
"MM v[88:91], a[48:51], a[92:95], v[88:91]\n"
"ds_read_b128 a[112:115], v162 offset:24576\n"
"MM v[92:95], a[32:35], a[76:79], v[92:95]\n"
"ds_read_b128 a[132:135], v163 offset:24576\n"
"MM v[92:95], a[48:51], a[96:99], v[92:95]\n"
"MM v[96:99], a[32:35], a[80:83], v[96:99]\n"
"ds_read_b128 a[116:119], v162 offset:26624\n"
"MM v[96:99], a[48:51], a[100:103], v[96:99]\n"
"ds_read_b128 a[136:139], v163 offset:26624\n"
"MM v[100:103], a[36:39], a[64:67], v[100:103]\n"
"ds_read_b128 a[120:123], v162 offset:28672\n"
"MM v[100:103], a[52:55], a[84:87], v[100:103]\n"
"ds_read_b128 a[140:143], v163 offset:28672\n"
"MM v[104:107], a[36:39], a[68:71], v[104:107]\n"
"ds_read_b64_tr_b16 a[4:5], v161 offset:32768\n"
"MM v[104:107], a[52:55], a[88:91], v[104:107]\n"
"ds_read_b64_tr_b16 a[6:7], v161 offset:33792\n"
"MM v[108:111], a[36:39], a[72:75], v[108:111]\n"
"ds_read_b64_tr_b16 a[20:21], v161 offset:36864\n"
"MM v[108:111], a[52:55], a[92:95], v[108:111]\n"
"MM v[112:115], a[36:39], a[76:79], v[112:115]\n"
"ds_read_b64_tr_b16 a[22:23], v161 offset:37888\n"
"MM v[112:115], a[52:55], a[96:99], v[112:115]\n"
"ds_read_b64_tr_b16 a[8:9], v160 offset:33280\n"
"MM v[116:119], a[36:39], a[80:83], v[116:119]\n"
"ds_read_b64_tr_b16 a[10:11], v160 offset:34304\n"
"MM v[116:119], a[52:55], a[100:103], v[116:119]\n"
"ds_read_b64_tr_b16 a[24:25], v160 offset:37376\n"
"MM v[120:123], a[40:43], a[64:67], v[120:123]\n"
"ds_read_b64_tr_b16 a[26:27], v160 offset:38400\n"
"MM v[120:123], a[56:59], a[84:87], v[120:123]\n"
"ds_read_b64_tr_b16 a[12:13], v161 offset:33280\n"
"MM v[124:127], a[40:43], a[68:71], v[124:127]\n"
"MM v[124:127], a[56:59], a[88:91], v[124:127]\n"
"ds_read_b64_tr_b16 a[14:15], v161 offset:34304\n"
"MM v[128:131], a[40:43], a[72:75], v[128:131]\n"
"ds_read_b64_tr_b16 a[28:29], v161 offset:37376\n"
"MM v[128:131], a[56:59], a[92:95], v[128:131]\n"
"ds_read_b64_tr_b16 a[30:31], v161 offset:38400\n"
"MM v[132:135], a[40:43], a[76:79], v[132:135]\n"
"MM v[132:135], a[56:59], a[96:99], v[132:135]\n"
"MM v[136:139], a[40:43], a[80:83], v[136:139]\n"
"MM v[136:139], a[56:59], a[100:103], v[136:139]\n"
"MM v[140:143], a[44:47], a[64:67], v[140:143]\n"
"MM v[140:143], a[60:63], a[84:87], v[140:143]\n"
"MM v[144:147], a[44:47], a[68:71], v[144:147]\n"
"MM v[144:147], a[60:63], a[88:91], v[144:147]\n"
"MM v[148:151], a[44:47], a[72:75], v[148:151]\n"
"MM v[148:151], a[60:63], a[92:95], v[148:151]\n"
"MM v[152:155], a[44:47], a[76:79], v[152:155]\n"
"MM v[152:155], a[60:63], a[96:99], v[152:155]\n"
"MM v[156:159], a[44:47], a[80:83], v[156:159]\n"
"MM v[156:159], a[60:63], a[100:103], v[156:159]\n"
"s_waitcnt lgkmcnt(0)\n"
"s_barrier\n"
"MM v[0:3], a[0:3], a[104:107], v[0:3]\n"
"ds_read_b64_tr_b16 a[32:33], v160 offset:49152\n"
"ds_read_b64_tr_b16 a[34:35], v160 offset:50176\n"
"MM v[0:3], a[16:19], a[124:127], v[0:3]\n"
"ds_read_b64_tr_b16 a[48:49], v160 offset:53248\n"
"ds_read_b64_tr_b16 a[50:51], v160 offset:54272\n"
"MM v[4:7], a[0:3], a[108:111], v[4:7]\n"
"s_add_u32 m0, s66, 30720\n"
"ds_read_b64_tr_b16 a[36:37], v161 offset:49152\n"
"MM v[4:7], a[16:19], a[128:131], v[4:7]\n"
"buffer_load_dwordx4 v164, s[36:39], s67 offen lds\n"
"MM v[8:11], a[0:3], a[112:115], v[8:11]\n"
"s_add_u32 m0, s66, 31744\n"
"ds_read_b64_tr_b16 a[38:39], v161 offset:50176\n"
"MM v[8:11], a[16:19], a[132:135], v[8:11]\n"
"buffer_load_dwordx4 v165, s[36:39], s67 offen lds\n"
"MM v[12:15], a[0:3], a[116:119], v[12:15]\n"
"ds_read_b64_tr_b16 a[52:53], v161 offset:53248\n"
"ds_read_b64_tr_b16 a[54:55], v161 offset:54272\n"
"MM v[12:15], a[16:19], a[136:139], v[12:15]\n"
"s_add_u32 m0, s66, 32768\n"
"ds_read_b64_tr_b16 a[40:41], v160 offset:49664\n"
"MM v[16:19], a[0:3], a[120:123], v[16:19]\n"
"buffer_load_dwordx4 v166, s[36:39], s67 offen lds\n"
"MM v[16:19], a[16:19], a[140:143], v[16:19]\n"
"ds_read_b64_tr_b16 a[42:43], v160 offset:50688\n"
"MM v[20:23], a[4:7], a[104:107], v[20:23]\n"
"ds_read_b64_tr_b16 a[56:57], v160 offset:53760\n"
"s_add_u32 m0, s66, 33792\n"
"MM v[20:23], a[20:23], a[124:127], v[20:23]\n"
"buffer_load_dwordx4 v167, s[36:39], s67 offen lds\n"
"MM v[24:27], a[4:7], a[108:111], v[24:27]\n"
"ds_read_b64_tr_b16 a[58:59], v160 offset:54784\n"
"MM v[24:27], a[20:23], a[128:131], v[24:27]\n"
"ds_read_b64_tr_b16 a[44:45], v161 offset:49664\n"
"MM v[28:31], a[4:7], a[112:115], v[28:31]\n"
"s_add_u32 m0, s66, 34816\n"
"ds_read_b64_tr_b16 a[46:47], v161 offset:50688\n"
"MM v[28:31], a[20:23], a[132:135], v[28:31]\n"
"buffer_load_dwordx4 v168, s[36:39], s67 offen lds\n"
"MM v[32:35], a[4:7], a[116:119], v[32:35]\n"
"ds_read_b64_tr_b16 a[60:61], v161 offset:53760\n"
"MM v[32:35], a[20:23], a[136:139], v[32:35]\n"
"ds_read_b64_tr_b16 a[62:63], v161 offset:54784\n"
"MM v[36:39], a[4:7], a[120:123], v[36:39]\n"
"s_add_u32 m0, s66, 35840\n"
"MM v[36:39], a[20:23], a[140:143], v[36:39]\n"
"buffer_load_dwordx4 v169, s[36:39], s67 offen lds\n"
"MM v[40:43], a[8:11], a[104:107], v[40:43]\n"
"MM v[40:43], a[24:27], a[124:127], v[40:43]\n"
"s_add_u32 m0, s66, 36864\n"
"MM v[44:47], a[8:11], a[108:111], v[44:47]\n"
"buffer_load_dwordx4 v170, s[36:39], s67 offen lds\n"
"MM v[44:47], a[24:27], a[128:131], v[44:47]\n"
"MM v[48:51], a[8:11], a[112:115], v[48:51]\n"
"MM v[48:51], a[24:27], a[132:135], v[48:51]\n"
"s_add_u32 m0, s66, 37888\n"
"MM v[52:55], a[8:11], a[116:119], v[52:55]\n"
"buffer_load_dwordx4 v171, s[36:39], s67 offen lds\n"
"MM v[52:55], a[24:27], a[136:139], v[52:55]\n"
"MM v[56:59], a[8:11], a[120:123], v[56:59]\n"
"MM v[56:59], a[24:27], a[140:143], v[56:59]\n"
"s_add_u32 m0, s66, 38912\n"
"MM v[60:63], a[12:15], a[104:107], v[60:63]\n"
"buffer_load_dwordx4 v172, s[36:39], s67 offen lds\n"
"MM v[60:63], a[28:31], a[124:127], v[60:63]\n"
"MM v[64:67], a[12:15], a[108:111], v[64:67]\n"
"s_add_u32 m0, s66, 39936\n"
"MM v[64:67], a[28:31], a[128:131], v[64:67]\n"
"buffer_load_dwordx4 v173, s[36:39], s67 offen lds\n"
"MM v[68:71], a[12:15], a[112:115], v[68:71]\n"
"MM v[68:71], a[28:31], a[132:135], v[68:71]\n"
"MM v[72:75], a[12:15], a[116:119], v[72:75]\n"
"s_add_u32 s67, s67, 128\n"
"MM v[72:75], a[28:31], a[136:139], v[72:75]\n"
"MM v[76:79], a[12:15], a[120:123], v[76:79]\n"
"MM v[76:79], a[28:31], a[140:143], v[76:79]\n"
"s_waitcnt vmcnt(10)\n"
"s_waitcnt lgkmcnt(0)\n"
"s_barrier\n"
"MM v[80:83], a[32:35], a[104:107], v[80:83]\n"
"ds_read_b64_tr_b16 a[0:1], v160 offset:0\n"
"ds_read_b64_tr_b16 a[2:3], v160 offset:1024\n"
"MM v[80:83], a[48:51], a[124:127], v[80:83]\n"
"ds_read_b64_tr_b16 a[16:17], v160 offset:4096\n"
"ds_read_b64_tr_b16 a[18:19], v160 offset:5120\n"
"MM v[84:87], a[32:35], a[108:111], v[84:87]\n"
"ds_read_b64_tr_b16 a[164:165], v192 offset:6144\n"
"ds_read_b64_tr_b16 a[166:167], v192 offset:9216\n"
"MM v[84:87], a[48:51], a[128:131], v[84:87]\n"
"ds_read_b64_tr_b16 a[168:169], v192 offset:6176\n"
"ds_read_b128 a[64:67], v162 offset:0\n"
"MM v[88:91], a[32:35], a[112:115], v[88:91]\n"
"ds_read_b64_tr_b16 a[170:171], v192 offset:9248\n"
"ds_read_b128 a[84:87], v163 offset:0\n"
"MM v[88:91], a[48:51], a[132:135], v[88:91]\n"
"ds_read_b64_tr_b16 a[172:173], v192 offset:6208\n"
"ds_read_b128 a[68:71], v162 offset:2048\n"
"MM v[92:95], a[32:35], a[116:119], v[92:95]\n"
"ds_read_b64_tr_b16 a[174:175], v192 offset:9280\n"
"ds_read_b128 a[88:91], v163 offset:2048\n"
"MM v[92:95], a[48:51], a[136:139], v[92:95]\n"
"ds_read_b64_tr_b16 a[176:177], v192 offset:6240\n"
"ds_read_b128 a[72:75], v162 offset:4096\n"
"MM v[96:99], a[32:35], a[120:123], v[96:99]\n"
"ds_read_b64_tr_b16 a[178:179], v192 offset:9312\n"
"ds_read_b128 a[92:95], v163 offset:4096\n"
"MM v[96:99], a[48:51], a[140:143], v[96:99]\n"
"ds_read_b64_tr_b16 a[180:181], v192 offset:6272\n"
"ds_read_b128 a[76:79], v162 offset:6144\n"
"MM v[100:103], a[36:39], a[104:107], v[100:103]\n"
"ds_read_b64_tr_b16 a[182:183], v192 offset:9344\n"
"ds_read_b128 a[96:99], v163 offset:6144\n"
"MM v[100:103], a[52:55], a[124:127], v[100:103]\n"
"ds_read_b128 a[80:83], v162 offset:8192\n"
"ds_read_b128 a[100:103], v163 offset:8192\n"
"MM v[104:107], a[36:39], a[108:111], v[104:107]\n"
"ds_read_b64_tr_b16 a[4:5], v161 offset:0\n"
"MM v[104:107], a[52:55], a[128:131], v[104:107]\n"
"ds_read_b64_tr_b16 a[6:7], v161 offset:1024\n"
"MM v[108:111], a[36:39], a[112:115], v[108:111]\n"
"s_add_u32 s68, s99, 40960\n"
"ds_read_b64_tr_b16 a[20:21], v161 offset:4096\n"
"MM v[108:111], a[52:55], a[132:135], v[108:111]\n"
"s_waitcnt lgkmcnt(15)\n"
"buffer_store_dwordx4 a[164:167], v193, s[56:59], s68 offen sc1\n"
"MM v[112:115], a[36:39], a[116:119], v[112:115]\n"
"buffer_store_dwordx4 a[168:171], v193, s[56:59], s68 offen offset:1024 sc1\n"
"MM v[112:115], a[52:55], a[136:139], v[112:115]\n"
"ds_read_b64_tr_b16 a[22:23], v161 offset:5120\n"
"ds_read_b64_tr_b16 a[8:9], v160 offset:512\n"
"MM v[116:119], a[36:39], a[120:123], v[116:119]\n"
"ds_read_b64_tr_b16 a[10:11], v160 offset:1536\n"
"MM v[116:119], a[52:55], a[140:143], v[116:119]\n"
"ds_read_b64_tr_b16 a[24:25], v160 offset:4608\n"
"MM v[120:123], a[40:43], a[104:107], v[120:123]\n"
"s_waitcnt lgkmcnt(15)\n"
"buffer_store_dwordx4 a[172:175], v193, s[56:59], s68 offen offset:2048 sc1\n"
"MM v[120:123], a[56:59], a[124:127], v[120:123]\n"
"ds_read_b64_tr_b16 a[26:27], v160 offset:5632\n"
"ds_read_b64_tr_b16 a[12:13], v161 offset:512\n"
"MM v[124:127], a[40:43], a[108:111], v[124:127]\n"
"MM v[124:127], a[56:59], a[128:131], v[124:127]\n"
"ds_read_b64_tr_b16 a[14:15], v161 offset:1536\n"
"MM v[128:131], a[40:43], a[112:115], v[128:131]\n"
"ds_read_b64_tr_b16 a[28:29], v161 offset:4608\n"
"MM v[128:131], a[56:59], a[132:135], v[128:131]\n"
"ds_read_b64_tr_b16 a[30:31], v161 offset:5632\n"
"MM v[132:135], a[40:43], a[116:119], v[132:135]\n"
"s_waitcnt lgkmcnt(15)\n"
"buffer_store_dwordx4 a[176:179], v193, s[56:59], s68 offen offset:3072 sc1\n"
"MM v[132:135], a[56:59], a[136:139], v[132:135]\n"
"MM v[136:139], a[40:43], a[120:123], v[136:139]\n"
"MM v[136:139], a[56:59], a[140:143], v[136:139]\n"
"MM v[140:143], a[44:47], a[104:107], v[140:143]\n"
"s_add_u32 s68, s99, 45056\n"
"MM v[140:143], a[60:63], a[124:127], v[140:143]\n"
"buffer_store_dwordx4 a[180:183], v193, s[56:59], s68 offen sc1\n"
"MM v[144:147], a[44:47], a[108:111], v[144:147]\n"
"MM v[144:147], a[60:63], a[128:131], v[144:147]\n"
"MM v[148:151], a[44:47], a[112:115], v[148:151]\n"
"MM v[148:151], a[60:63], a[132:135], v[148:151]\n"
"MM v[152:155], a[44:47], a[116:119], v[152:155]\n"
"MM v[152:155], a[60:63], a[136:139], v[152:155]\n"
"MM v[156:159], a[44:47], a[120:123], v[156:159]\n"
"MM v[156:159], a[60:63], a[140:143], v[156:159]\n"
"s_waitcnt lgkmcnt(0)\n"
"s_barrier\n"
"MM v[0:3], a[0:3], a[64:67], v[0:3]\n"
"ds_read_b64_tr_b16 a[32:33], v160 offset:16384\n"
"ds_read_b64_tr_b16 a[34:35], v160 offset:17408\n"
"MM v[0:3], a[16:19], a[84:87], v[0:3]\n"
"ds_read_b64_tr_b16 a[48:49], v160 offset:20480\n"
"ds_read_b64_tr_b16 a[50:51], v160 offset:21504\n"
"MM v[4:7], a[0:3], a[68:71], v[4:7]\n"
"s_add_u32 m0, s66, 10240\n"
"ds_read_b64_tr_b16 a[36:37], v161 offset:16384\n"
"MM v[4:7], a[16:19], a[88:91], v[4:7]\n"
"buffer_load_dwordx4 v164, s[36:39], s67 offen lds\n"
"MM v[8:11], a[0:3], a[72:75], v[8:11]\n"
"s_add_u32 m0, s66, 11264\n"
"ds_read_b64_tr_b16 a[38:39], v161 offset:17408\n"
"MM v[8:11], a[16:19], a[92:95], v[8:11]\n"
"buffer_load_dwordx4 v165, s[36:39], s67 offen lds\n"
"MM v[12:15], a[0:3], a[76:79], v[12:15]\n"
"ds_read_b64_tr_b16 a[52:53], v161 offset:20480\n"
"ds_read_b64_tr_b16 a[54:55], v161 offset:21504\n"
"MM v[12:15], a[16:19], a[96:99], v[12:15]\n"
"s_add_u32 m0, s66, 12288\n"
"ds_read_b64_tr_b16 a[40:41], v160 offset:16896\n"
"MM v[16:19], a[0:3], a[80:83], v[16:19]\n"
"buffer_load_dwordx4 v166, s[36:39], s67 offen lds\n"
"MM v[16:19], a[16:19], a[100:103], v[16:19]\n"
"ds_read_b64_tr_b16 a[42:43], v160 offset:17920\n"
"MM v[20:23], a[4:7], a[64:67], v[20:23]\n"
"ds_read_b64_tr_b16 a[56:57], v160 offset:20992\n"
"s_add_u32 m0, s66, 13312\n"
"MM v[20:23], a[20:23], a[84:87], v[20:23]\n"
"buffer_load_dwordx4 v167, s[36:39], s67 offen lds\n"
"MM v[24:27], a[4:7], a[68:71], v[24:27]\n"
"ds_read_b64_tr_b16 a[58:59], v160 offset:22016\n"
"MM v[24:27], a[20:23], a[88:91], v[24:27]\n"
"ds_read_b64_tr_b16 a[44:45], v161 offset:16896\n"
"MM v[28:31], a[4:7], a[72:75], v[28:31]\n"
"s_add_u32 m0, s66, 14336\n"
"ds_read_b64_tr_b16 a[46:47], v161 offset:17920\n"
"MM v[28:31], a[20:23], a[92:95], v[28:31]\n"
"buffer_load_dwordx4 v168, s[36:39], s67 offen lds\n"
"MM v[32:35], a[4:7], a[76:79], v[32:35]\n"
"ds_read_b64_tr_b16 a[60:61], v161 offset:20992\n"
"MM v[32:35], a[20:23], a[96:99], v[32:35]\n"
"ds_read_b64_tr_b16 a[62:63], v161 offset:22016\n"
"MM v[36:39], a[4:7], a[80:83], v[36:39]\n"
"s_add_u32 m0, s66, 15360\n"
"MM v[36:39], a[20:23], a[100:103], v[36:39]\n"
"buffer_load_dwordx4 v169, s[36:39], s67 offen lds\n"
"MM v[40:43], a[8:11], a[64:67], v[40:43]\n"
"MM v[40:43], a[24:27], a[84:87], v[40:43]\n"
"s_add_u32 m0, s66, 16384\n"
"MM v[44:47], a[8:11], a[68:71], v[44:47]\n"
"buffer_load_dwordx4 v170, s[36:39], s67 offen lds\n"
"MM v[44:47], a[24:27], a[88:91], v[44:47]\n"
"MM v[48:51], a[8:11], a[72:75], v[48:51]\n"
"MM v[48:51], a[24:27], a[92:95], v[48:51]\n"
"s_add_u32 m0, s66, 17408\n"
"MM v[52:55], a[8:11], a[76:79], v[52:55]\n"
"buffer_load_dwordx4 v171, s[36:39], s67 offen lds\n"
"MM v[52:55], a[24:27], a[96:99], v[52:55]\n"
"MM v[56:59], a[8:11], a[80:83], v[56:59]\n"
"MM v[56:59], a[24:27], a[100:103], v[56:59]\n"
"s_add_u32 m0, s66, 18432\n"
"MM v[60:63], a[12:15], a[64:67], v[60:63]\n"
"buffer_load_dwordx4 v172, s[36:39], s67 offen lds\n"
"MM v[60:63], a[28:31], a[84:87], v[60:63]\n"
"MM v[64:67], a[12:15], a[68:71], v[64:67]\n"
"s_add_u32 m0, s66, 19456\n"
"MM v[64:67], a[28:31], a[88:91], v[64:67]\n"
"buffer_load_dwordx4 v173, s[36:39], s67 offen lds\n"
"MM v[68:71], a[12:15], a[72:75], v[68:71]\n"
"MM v[68:71], a[28:31], a[92:95], v[68:71]\n"
"MM v[72:75], a[12:15], a[76:79], v[72:75]\n"
"s_add_u32 s67, s67, 128\n"
"MM v[72:75], a[28:31], a[96:99], v[72:75]\n"
"MM v[76:79], a[12:15], a[80:83], v[76:79]\n"
"MM v[76:79], a[28:31], a[100:103], v[76:79]\n"
"s_waitcnt vmcnt(15)\n"
"s_waitcnt lgkmcnt(0)\n"
"s_barrier\n"
"MM v[80:83], a[32:35], a[64:67], v[80:83]\n"
"ds_read_b64_tr_b16 a[0:1], v160 offset:32768\n"
"ds_read_b64_tr_b16 a[2:3], v160 offset:33792\n"
"MM v[80:83], a[48:51], a[84:87], v[80:83]\n"
"ds_read_b64_tr_b16 a[16:17], v160 offset:36864\n"
"ds_read_b64_tr_b16 a[18:19], v160 offset:37888\n"
"MM v[84:87], a[32:35], a[68:71], v[84:87]\n"
"ds_read_b64_tr_b16 a[164:165], v192 offset:18432\n"
"ds_read_b64_tr_b16 a[166:167], v192 offset:21504\n"
"MM v[84:87], a[48:51], a[88:91], v[84:87]\n"
"ds_read_b64_tr_b16 a[168:169], v192 offset:18464\n"
"ds_read_b128 a[104:107], v162 offset:20480\n"
"MM v[88:91], a[32:35], a[72:75], v[88:91]\n"
"ds_read_b64_tr_b16 a[170:171], v192 offset:21536\n"
"ds_read_b128 a[124:127], v163 offset:20480\n"
"MM v[88:91], a[48:51], a[92:95], v[88:91]\n"
"ds_read_b64_tr_b16 a[172:173], v192 offset:18496\n"
"ds_read_b128 a[108:111], v162 offset:22528\n"
"MM v[92:95], a[32:35], a[76:79], v[92:95]\n"
"ds_read_b64_tr_b16 a[174:175], v192 offset:21568\n"
"ds_read_b128 a[128:131], v163 offset:22528\n"
"MM v[92:95], a[48:51], a[96:99], v[92:95]\n"
"ds_read_b64_tr_b16 a[176:177], v192 offset:18528\n"
"ds_read_b128 a[112:115], v162 offset:24576\n"
"MM v[96:99], a[32:35], a[80:83], v[96:99]\n"
"ds_read_b64_tr_b16 a[178:179], v192 offset:21600\n"
"ds_read_b128 a[132:135], v163 offset:24576\n"
"MM v[96:99], a[48:51], a[100:103], v[96:99]\n"
"ds_read_b64_tr_b16 a[180:181], v192 offset:18560\n"
"ds_read_b128 a[116:119], v162 offset:26624\n"
"MM v[100:103], a[36:39], a[64:67], v[100:103]\n"
"ds_read_b64_tr_b16 a[182:183], v192 offset:21632\n"
"ds_read_b128 a[136:139], v163 offset:26624\n"
"MM v[100:103], a[52:55], a[84:87], v[100:103]\n"
"ds_read_b128 a[120:123], v162 offset:28672\n"
"ds_read_b128 a[140:143], v163 offset:28672\n"
"MM v[104:107], a[36:39], a[68:71], v[104:107]\n"
"ds_read_b64_tr_b16 a[4:5], v161 offset:32768\n"
"MM v[104:107], a[52:55], a[88:91], v[104:107]\n"
"ds_read_b64_tr_b16 a[6:7], v161 offset:33792\n"
"MM v[108:111], a[36:39], a[72:75], v[108:111]\n"
"s_add_u32 s68, s99, 122880\n"
"ds_read_b64_tr_b16 a[20:21], v161 offset:36864\n"
"MM v[108:111], a[52:55], a[92:95], v[108:111]\n"
"s_waitcnt lgkmcnt(15)\n"
"buffer_store_dwordx4 a[164:167], v193, s[56:59], s68 offen sc1\n"
"MM v[112:115], a[36:39], a[76:79], v[112:115]\n"
"buffer_store_dwordx4 a[168:171], v193, s[56:59], s68 offen offset:1024 sc1\n"
"MM v[112:115], a[52:55], a[96:99], v[112:115]\n"
"ds_read_b64_tr_b16 a[22:23], v161 offset:37888\n"
"ds_read_b64_tr_b16 a[8:9], v160 offset:33280\n"
"MM v[116:119], a[36:39], a[80:83], v[116:119]\n"
"ds_read_b64_tr_b16 a[10:11], v160 offset:34304\n"
"MM v[116:119], a[52:55], a[100:103], v[116:119]\n"
"ds_read_b64_tr_b16 a[24:25], v160 offset:37376\n"
"MM v[120:123], a[40:43], a[64:67], v[120:123]\n"
"s_waitcnt lgkmcnt(15)\n"
"buffer_store_dwordx4 a[172:175], v193, s[56:59], s68 offen offset:2048 sc1\n"
"MM v[120:123], a[56:59], a[84:87], v[120:123]\n"
"ds_read_b64_tr_b16 a[26:27], v160 offset:38400\n"
"ds_read_b64_tr_b16 a[12:13], v161 offset:33280\n"
"MM v[124:127], a[40:43], a[68:71], v[124:127]\n"
"MM v[124:127], a[56:59], a[88:91], v[124:127]\n"
"ds_read_b64_tr_b16 a[14:15], v161 offset:34304\n"
"MM v[128:131], a[40:43], a[72:75], v[128:131]\n"
"ds_read_b64_tr_b16 a[28:29], v161 offset:37376\n"
"MM v[128:131], a[56:59], a[92:95], v[128:131]\n"
"ds_read_b64_tr_b16 a[30:31], v161 offset:38400\n"
"MM v[132:135], a[40:43], a[76:79], v[132:135]\n"
"s_waitcnt lgkmcnt(15)\n"
"buffer_store_dwordx4 a[176:179], v193, s[56:59], s68 offen offset:3072 sc1\n"
"MM v[132:135], a[56:59], a[96:99], v[132:135]\n"
"MM v[136:139], a[40:43], a[80:83], v[136:139]\n"
"MM v[136:139], a[56:59], a[100:103], v[136:139]\n"
"MM v[140:143], a[44:47], a[64:67], v[140:143]\n"
"s_add_u32 s68, s99, 126976\n"
"MM v[140:143], a[60:63], a[84:87], v[140:143]\n"
"buffer_store_dwordx4 a[180:183], v193, s[56:59], s68 offen sc1\n"
"MM v[144:147], a[44:47], a[68:71], v[144:147]\n"
"MM v[144:147], a[60:63], a[88:91], v[144:147]\n"
"MM v[148:151], a[44:47], a[72:75], v[148:151]\n"
"MM v[148:151], a[60:63], a[92:95], v[148:151]\n"
"MM v[152:155], a[44:47], a[76:79], v[152:155]\n"
"MM v[152:155], a[60:63], a[96:99], v[152:155]\n"
"MM v[156:159], a[44:47], a[80:83], v[156:159]\n"
"MM v[156:159], a[60:63], a[100:103], v[156:159]\n"
"s_branch K1_EX3\n"
"K1_EW:\n"
"s_nop 3\n"
"s_waitcnt vmcnt(16)\n"
"s_waitcnt lgkmcnt(0)\n"
"s_barrier\n"
"MM v[0:3], a[0:3], a[104:107], v[0:3]\n"
"s_add_u32 m0, s66, 32768\n"
"ds_read_b64_tr_b16 a[32:33], v160 offset:49152\n"
"MM v[0:3], a[16:19], a[124:127], v[0:3]\n"
"buffer_load_dwordx4 v164, s[40:43], s67 offen lds\n"
"MM v[4:7], a[0:3], a[108:111], v[4:7]\n"
"ds_read_b64_tr_b16 a[34:35], v160 offset:50176\n"
"ds_read_b64_tr_b16 a[48:49], v160 offset:53248\n"
"MM v[4:7], a[16:19], a[128:131], v[4:7]\n"
"ds_read_b64_tr_b16 a[50:51], v160 offset:54272\n"
"s_add_u32 m0, s66, 33792\n"
"MM v[8:11], a[0:3], a[112:115], v[8:11]\n"
"ds_read_b64_tr_b16 a[36:37], v161 offset:49152\n"
"ds_read_b64_tr_b16 a[38:39], v161 offset:50176\n"
"MM v[8:11], a[16:19], a[132:135], v[8:11]\n"
"buffer_load_dwordx4 v165, s[40:43], s67 offen lds\n"
"MM v[12:15], a[0:3], a[116:119], v[12:15]\n"
"ds_read_b64_tr_b16 a[52:53], v161 offset:53248\n"
"ds_read_b64_tr_b16 a[54:55], v161 offset:54272\n"
"MM v[12:15], a[16:19], a[136:139], v[12:15]\n"
"s_add_u32 m0, s66, 34816\n"
"ds_read_b64_tr_b16 a[40:41], v160 offset:49664\n"
"MM v[16:19], a[0:3], a[120:123], v[16:19]\n"
"buffer_load_dwordx4 v166, s[40:43], s67 offen lds\n"
"MM v[16:19], a[16:19], a[140:143], v[16:19]\n"
"ds_read_b64_tr_b16 a[42:43], v160 offset:50688\n"
"MM v[20:23], a[4:7], a[104:107], v[20:23]\n"
"ds_read_b64_tr_b16 a[56:57], v160 offset:53760\n"
"MM v[20:23], a[20:23], a[124:127], v[20:23]\n"
"s_add_u32 m0, s66, 35840\n"
"ds_read_b64_tr_b16 a[58:59], v160 offset:54784\n"
"MM v[24:27], a[4:7], a[108:111], v[24:27]\n"
"buffer_load_dwordx4 v167, s[40:43], s67 offen lds\n"
"MM v[24:27], a[20:23], a[128:131], v[24:27]\n"
"ds_read_b64_tr_b16 a[44:45], v161 offset:49664\n"
"MM v[28:31], a[4:7], a[112:115], v[28:31]\n"
"ds_read_b64_tr_b16 a[46:47], v161 offset:50688\n"
"MM v[28:31], a[20:23], a[132:135], v[28:31]\n"
"MM v[32:35], a[4:7], a[116:119], v[32:35]\n"
"s_add_u32 m0, s66, 36864\n"
"ds_read_b64_tr_b16 a[60:61], v161 offset:53760\n"
"MM v[32:35], a[20:23], a[136:139], v[32:35]\n"
"buffer_load_dwordx4 v168, s[40:43], s67 offen lds\n"
"MM v[36:39], a[4:7], a[120:123], v[36:39]\n"
"ds_read_b64_tr_b16 a[62:63], v161 offset:54784\n"
"MM v[36:39], a[20:23], a[140:143], v[36:39]\n"
"MM v[40:43], a[8:11], a[104:107], v[40:43]\n"
"MM v[40:43], a[24:27], a[124:127], v[40:43]\n"
"s_add_u32 m0, s66, 37888\n"
"MM v[44:47], a[8:11], a[108:111], v[44:47]\n"
"buffer_load_dwordx4 v169, s[40:43], s67 offen lds\n"
"MM v[44:47], a[24:27], a[128:131], v[44:47]\n"
"MM v[48:51], a[8:11], a[112:115], v[48:51]\n"
"MM v[48:51], a[24:27], a[132:135], v[48:51]\n"
"MM v[52:55], a[8:11], a[116:119], v[52:55]\n"
"s_add_u32 m0, s66, 38912\n"
"MM v[52:55], a[24:27], a[136:139], v[52:55]\n"
"buffer_load_dwordx4 v170, s[40:43], s67 offen lds\n"
"MM v[56:59], a[8:11], a[120:123], v[56:59]\n"
"MM v[56:59], a[24:27], a[140:143], v[56:59]\n"
"MM v[60:63], a[12:15], a[104:107], v[60:63]\n"
"s_add_u32 m0, s66, 39936\n"
"MM v[60:63], a[28:31], a[124:127], v[60:63]\n"
"buffer_load_dwordx4 v171, s[40:43], s67 offen lds\n"
"MM v[64:67], a[12:15], a[108:111], v[64:67]\n"
"MM v[64:67], a[28:31], a[128:131], v[64:67]\n"
"MM v[68:71], a[12:15], a[112:115], v[68:71]\n"
"MM v[68:71], a[28:31], a[132:135], v[68:71]\n"
"s_add_u32 s67, s67, 128\n"
"MM v[72:75], a[12:15], a[116:119], v[72:75]\n"
"MM v[72:75], a[28:31], a[136:139], v[72:75]\n"
"MM v[76:79], a[12:15], a[120:123], v[76:79]\n"
"MM v[76:79], a[28:31], a[140:143], v[76:79]\n"
"s_waitcnt vmcnt(16)\n"
"s_waitcnt lgkmcnt(0)\n"
"s_barrier\n"
"MM v[80:83], a[32:35], a[104:107], v[80:83]\n"
"s_add_u32 m0, s66, 49152\n"
"ds_read_b64_tr_b16 a[0:1], v160 offset:0\n"
"MM v[80:83], a[48:51], a[124:127], v[80:83]\n"
"buffer_load_dwordx4 v164, s[40:43], s67 offen lds\n"
"MM v[84:87], a[32:35], a[108:111], v[84:87]\n"
"ds_read_b64_tr_b16 a[2:3], v160 offset:1024\n"
"ds_read_b64_tr_b16 a[16:17], v160 offset:4096\n"
"MM v[84:87], a[48:51], a[128:131], v[84:87]\n"
"ds_read_b64_tr_b16 a[18:19], v160 offset:5120\n"
"ds_read_b128 a[64:67], v162 offset:0\n"
"MM v[88:91], a[32:35], a[112:115], v[88:91]\n"
"s_add_u32 m0, s66, 50176\n"
"ds_read_b128 a[84:87], v163 offset:0\n"
"MM v[88:91], a[48:51], a[132:135], v[88:91]\n"
"buffer_load_dwordx4 v165, s[40:43], s67 offen lds\n"
"MM v[92:95], a[32:35], a[116:119], v[92:95]\n"
"ds_read_b128 a[68:71], v162 offset:2048\n"
"ds_read_b128 a[88:91], v163 offset:2048\n"
"MM v[92:95], a[48:51], a[136:139], v[92:95]\n"
"ds_read_b128 a[72:75], v162 offset:4096\n"
"s_add_u32 m0, s66, 51200\n"
"MM v[96:99], a[32:35], a[120:123], v[96:99]\n"
"ds_read_b128 a[92:95], v163 offset:4096\n"
"ds_read_b128 a[76:79], v162 offset:6144\n"
"MM v[96:99], a[48:51], a[140:143], v[96:99]\n"
"buffer_load_dwordx4 v166, s[40:43], s67 offen lds\n"
"MM v[100:103], a[36:39], a[104:107], v[100:103]\n"
"ds_read_b128 a[96:99], v163 offset:6144\n"
"ds_read_b128 a[80:83], v162 offset:8192\n"
"MM v[100:103], a[52:55], a[124:127], v[100:103]\n"
"s_add_u32 m0, s66, 52224\n"
"ds_read_b128 a[100:103], v163 offset:8192\n"
"MM v[104:107], a[36:39], a[108:111], v[104:107]\n"
"buffer_load_dwordx4 v167, s[40:43], s67 offen lds\n"
"MM v[104:107], a[52:55], a[128:131], v[104:107]\n"
"ds_read_b64_tr_b16 a[4:5], v161 offset:0\n"
"ds_read_b64_tr_b16 a[6:7], v161 offset:1024\n"
"MM v[108:111], a[36:39], a[112:115], v[108:111]\n"
"ds_read_b64_tr_b16 a[20:21], v161 offset:4096\n"
"MM v[108:111], a[52:55], a[132:135], v[108:111]\n"
"MM v[112:115], a[36:39], a[116:119], v[112:115]\n"
"ds_read_b64_tr_b16 a[22:23], v161 offset:5120\n"
"s_add_u32 m0, s66, 53248\n"
"MM v[112:115], a[52:55], a[136:139], v[112:115]\n"
"buffer_load_dwordx4 v168, s[40:43], s67 offen lds\n"
"MM v[116:119], a[36:39], a[120:123], v[116:119]\n"
"ds_read_b64_tr_b16 a[8:9], v160 offset:512\n"
"ds_read_b64_tr_b16 a[10:11], v160 offset:1536\n"
"MM v[116:119], a[52:55], a[140:143], v[116:119]\n"
"ds_read_b64_tr_b16 a[24:25], v160 offset:4608\n"
"MM v[120:123], a[40:43], a[104:107], v[120:123]\n"
"ds_read_b64_tr_b16 a[26:27], v160 offset:5632\n"
"MM v[120:123], a[56:59], a[124:127], v[120:123]\n"
"s_add_u32 m0, s66, 54272\n"
"ds_read_b64_tr_b16 a[12:13], v161 offset:512\n"
"MM v[124:127], a[40:43], a[108:111], v[124:127]\n"
"buffer_load_dwordx4 v169, s[40:43], s67 offen lds\n"
"MM v[124:127], a[56:59], a[128:131], v[124:127]\n"
"ds_read_b64_tr_b16 a[14:15], v161 offset:1536\n"
"MM v[128:131], a[40:43], a[112:115], v[128:131]\n"
"ds_read_b64_tr_b16 a[28:29], v161 offset:4608\n"
"MM v[128:131], a[56:59], a[132:135], v[128:131]\n"
"ds_read_b64_tr_b16 a[30:31], v161 offset:5632\n"
"MM v[132:135], a[40:43], a[116:119], v[132:135]\n"
"s_add_u32 m0, s66, 55296\n"
"MM v[132:135], a[56:59], a[136:139], v[132:135]\n"
"buffer_load_dwordx4 v170, s[40:43], s67 offen lds\n"
"MM v[136:139], a[40:43], a[120:123], v[136:139]\n"
"MM v[136:139], a[56:59], a[140:143], v[136:139]\n"
"MM v[140:143], a[44:47], a[104:107], v[140:143]\n"
"s_add_u32 m0, s66, 56320\n"
"MM v[140:143], a[60:63], a[124:127], v[140:143]\n"
"buffer_load_dwordx4 v171, s[40:43], s67 offen lds\n"
"MM v[144:147], a[44:47], a[108:111], v[144:147]\n"
"MM v[144:147], a[60:63], a[128:131], v[144:147]\n"
"MM v[148:151], a[44:47], a[112:115], v[148:151]\n"
"MM v[148:151], a[60:63], a[132:135], v[148:151]\n"
"s_add_u32 s67, s67, 524160\n"
"MM v[152:155], a[44:47], a[116:119], v[152:155]\n"
"MM v[152:155], a[60:63], a[136:139], v[152:155]\n"
"MM v[156:159], a[44:47], a[120:123], v[156:159]\n"
"MM v[156:159], a[60:63], a[140:143], v[156:159]\n"
"s_waitcnt vmcnt(16)\n"
"s_waitcnt lgkmcnt(0)\n"
"s_barrier\n"
"MM v[0:3], a[0:3], a[64:67], v[0:3]\n"
"s_add_u32 m0, s66, 0\n"
"ds_read_b64_tr_b16 a[32:33], v160 offset:16384\n"
"MM v[0:3], a[16:19], a[84:87], v[0:3]\n"
"buffer_load_dwordx4 v164, s[40:43], s67 offen lds\n"
"MM v[4:7], a[0:3], a[68:71], v[4:7]\n"
"ds_read_b64_tr_b16 a[34:35], v160 offset:17408\n"
"ds_read_b64_tr_b16 a[48:49], v160 offset:20480\n"
"MM v[4:7], a[16:19], a[88:91], v[4:7]\n"
"s_add_u32 m0, s66, 1024\n"
"ds_read_b64_tr_b16 a[50:51], v160 offset:21504\n"
"MM v[8:11], a[0:3], a[72:75], v[8:11]\n"
"buffer_load_dwordx4 v165, s[40:43], s67 offen lds\n"
"MM v[8:11], a[16:19], a[92:95], v[8:11]\n"
"ds_read_b64_tr_b16 a[36:37], v161 offset:16384\n"
"s_add_u32 m0, s66, 2048\n"
"MM v[12:15], a[0:3], a[76:79], v[12:15]\n"
"ds_read_b64_tr_b16 a[38:39], v161 offset:17408\n"
"ds_read_b64_tr_b16 a[52:53], v161 offset:20480\n"
"MM v[12:15], a[16:19], a[96:99], v[12:15]\n"
"buffer_load_dwordx4 v166, s[40:43], s67 offen lds\n"
"MM v[16:19], a[0:3], a[80:83], v[16:19]\n"
"ds_read_b64_tr_b16 a[54:55], v161 offset:21504\n"
"s_add_u32 m0, s66, 3072\n"
"MM v[16:19], a[16:19], a[100:103], v[16:19]\n"
"buffer_load_dwordx4 v167, s[40:43], s67 offen lds\n"
"MM v[20:23], a[4:7], a[64:67], v[20:23]\n"
"ds_read_b64_tr_b16 a[40:41], v160 offset:16896\n"
"ds_read_b64_tr_b16 a[42:43], v160 offset:17920\n"
"MM v[20:23], a[20:23], a[84:87], v[20:23]\n"
"s_add_u32 m0, s66, 4096\n"
"ds_read_b64_tr_b16 a[56:57], v160 offset:20992\n"
"MM v[24:27], a[4:7], a[68:71], v[24:27]\n"
"buffer_load_dwordx4 v168, s[40:43], s67 offen lds\n"
"MM v[24:27], a[20:23], a[88:91], v[24:27]\n"
"ds_read_b64_tr_b16 a[58:59], v160 offset:22016\n"
"ds_read_b64_tr_b16 a[44:45], v161 offset:16896\n"
"MM v[28:31], a[4:7], a[72:75], v[28:31]\n"
"s_add_u32 m0, s66, 5120\n"
"ds_read_b64_tr_b16 a[46:47], v161 offset:17920\n"
"MM v[28:31], a[20:23], a[92:95], v[28:31]\n"
"buffer_load_dwordx4 v169, s[40:43], s67 offen lds\n"
"MM v[32:35], a[4:7], a[76:79], v[32:35]\n"
"ds_read_b64_tr_b16 a[60:61], v161 offset:20992\n"
"MM v[32:35], a[20:23], a[96:99], v[32:35]\n"
"s_add_u32 m0, s66, 6144\n"
"ds_read_b64_tr_b16 a[62:63], v161 offset:22016\n"
"MM v[36:39], a[4:7], a[80:83], v[36:39]\n"
"buffer_load_dwordx4 v170, s[40:43], s67 offen lds\n"
"MM v[36:39], a[20:23], a[100:103], v[36:39]\n"
"MM v[40:43], a[8:11], a[64:67], v[40:43]\n"
"MM v[40:43], a[24:27], a[84:87], v[40:43]\n"
"s_add_u32 m0, s66, 7168\n"
"MM v[44:47], a[8:11], a[68:71], v[44:47]\n"
"buffer_load_dwordx4 v171, s[40:43], s67 offen lds\n"
"MM v[44:47], a[24:27], a[88:91], v[44:47]\n"
"MM v[48:51], a[8:11], a[72:75], v[48:51]\n"
"s_add_u32 s67, s67, 128\n"
"MM v[48:51], a[24:27], a[92:95], v[48:51]\n"
"s_add_u32 s71, s94, 0\n"
"s_and_b32 s71, s71, 0x1ffff\n"
"MM v[52:55], a[8:11], a[76:79], v[52:55]\n"
"s_add_u32 m0, s70, 0\n"
"MM v[52:55], a[24:27], a[96:99], v[52:55]\n"
"buffer_load_dwordx4 v249, s[44:47], s71 offen nt lds\n"
"MM v[56:59], a[8:11], a[80:83], v[56:59]\n"
"MM v[56:59], a[24:27], a[100:103], v[56:59]\n"
"buffer_load_dwordx4 v252, s[44:47], s71 offen offset:1024 nt lds\n"
"MM v[60:63], a[12:15], a[64:67], v[60:63]\n"
"MM v[60:63], a[28:31], a[84:87], v[60:63]\n"
"MM v[64:67], a[12:15], a[68:71], v[64:67]\n"
"buffer_load_dwordx4 v251, s[44:47], s71 offen offset:2048 nt lds\n"
"MM v[64:67], a[28:31], a[88:91], v[64:67]\n"
"MM v[68:71], a[12:15], a[72:75], v[68:71]\n"
"buffer_load_dwordx4 v250, s[44:47], s71 offen offset:3072 nt lds\n"
"MM v[68:71], a[28:31], a[92:95], v[68:71]\n"
"MM v[72:75], a[12:15], a[76:79], v[72:75]\n"
"MM v[72:75], a[28:31], a[96:99], v[72:75]\n"
"MM v[76:79], a[12:15], a[80:83], v[76:79]\n"
"MM v[76:79], a[28:31], a[100:103], v[76:79]\n"
"s_waitcnt vmcnt(20)\n"
"s_waitcnt lgkmcnt(0)\n"
"s_barrier\n"
"MM v[80:83], a[32:35], a[64:67], v[80:83]\n"
"s_add_u32 m0, s66, 16384\n"
"ds_read_b64_tr_b16 a[0:1], v160 offset:32768\n"
"MM v[80:83], a[48:51], a[84:87], v[80:83]\n"
"buffer_load_dwordx4 v164, s[40:43], s67 offen lds\n"
"MM v[84:87], a[32:35], a[68:71], v[84:87]\n"
"ds_read_b64_tr_b16 a[2:3], v160 offset:33792\n"
"ds_read_b64_tr_b16 a[16:17], v160 offset:36864\n"
"MM v[84:87], a[48:51], a[88:91], v[84:87]\n"
"s_add_u32 m0, s66, 17408\n"
"ds_read_b64_tr_b16 a[18:19], v160 offset:37888\n"
"MM v[88:91], a[32:35], a[72:75], v[88:91]\n"
"buffer_load_dwordx4 v165, s[40:43], s67 offen lds\n"
"MM v[88:91], a[48:51], a[92:95], v[88:91]\n"
"ds_read_b128 a[104:107], v162 offset:20480\n"
"ds_read_b128 a[124:127], v163 offset:20480\n"
"MM v[92:95], a[32:35], a[76:79], v[92:95]\n"
"s_add_u32 m0, s66, 18432\n"
"ds_read_b128 a[108:111], v162 offset:22528\n"
"MM v[92:95], a[48:51], a[96:99], v[92:95]\n"
"buffer_load_dwordx4 v166, s[40:43], s67 offen lds\n"
"MM v[96:99], a[32:35], a[80:83], v[96:99]\n"
"ds_read_b128 a[128:131], v163 offset:22528\n"
"ds_read_b128 a[112:115], v162 offset:24576\n"
"MM v[96:99], a[48:51], a[100:103], v[96:99]\n"
"s_add_u32 m0, s66, 19456\n"
"ds_read_b128 a[132:135], v163 offset:24576\n"
"MM v[100:103], a[36:39], a[64:67], v[100:103]\n"
"buffer_load_dwordx4 v167, s[40:43], s67 offen lds\n"
"MM v[100:103], a[52:55], a[84:87], v[100:103]\n"
"ds_read_b128 a[116:119], v162 offset:26624\n"
"ds_read_b128 a[136:139], v163 offset:26624\n"
"MM v[104:107], a[36:39], a[68:71], v[104:107]\n"
"s_add_u32 m0, s66, 20480\n"
"ds_read_b128 a[120:123], v162 offset:28672\n"
"MM v[104:107], a[52:55], a[88:91], v[104:107]\n"
"buffer_load_dwordx4 v168, s[40:43], s67 offen lds\n"
"MM v[108:111], a[36:39], a[72:75], v[108:111]\n"
"ds_read_b128 a[140:143], v163 offset:28672\n"
"ds_read_b64_tr_b16 a[4:5], v161 offset:32768\n"
"MM v[108:111], a[52:55], a[92:95], v[108:111]\n"
"s_add_u32 m0, s66, 21504\n"
"ds_read_b64_tr_b16 a[6:7], v161 offset:33792\n"
"MM v[112:115], a[36:39], a[76:79], v[112:115]\n"
"buffer_load_dwordx4 v169, s[40:43], s67 offen lds\n"
"MM v[112:115], a[52:55], a[96:99], v[112:115]\n"
"ds_read_b64_tr_b16 a[20:21], v161 offset:36864\n"
"ds_read_b64_tr_b16 a[22:23], v161 offset:37888\n"
"MM v[116:119], a[36:39], a[80:83], v[116:119]\n"
"s_add_u32 m0, s66, 22528\n"
"ds_read_b64_tr_b16 a[8:9], v160 offset:33280\n"
"MM v[116:119], a[52:55], a[100:103], v[116:119]\n"
"buffer_load_dwordx4 v170, s[40:43], s67 offen lds\n"
"MM v[120:123], a[40:43], a[64:67], v[120:123]\n"
"ds_read_b64_tr_b16 a[10:11], v160 offset:34304\n"
"ds_read_b64_tr_b16 a[24:25], v160 offset:37376\n"
"MM v[120:123], a[56:59], a[84:87], v[120:123]\n"
"s_add_u32 m0, s66, 23552\n"
"ds_read_b64_tr_b16 a[26:27], v160 offset:38400\n"
"MM v[124:127], a[40:43], a[68:71], v[124:127]\n"
"buffer_load_dwordx4 v171, s[40:43], s67 offen lds\n"
"MM v[124:127], a[56:59], a[88:91], v[124:127]\n"
"ds_read_b64_tr_b16 a[12:13], v161 offset:33280\n"
"ds_read_b64_tr_b16 a[14:15], v161 offset:34304\n"
"MM v[128:131], a[40:43], a[72:75], v[128:131]\n"
"s_add_u32 s67, s67, 524160\n"
"ds_read_b64_tr_b16 a[28:29], v161 offset:37376\n"
"MM v[128:131], a[56:59], a[92:95], v[128:131]\n"
"s_add_u32 s71, s94, 8192\n"
"ds_read_b64_tr_b16 a[30:31], v161 offset:38400\n"
"MM v[132:135], a[40:43], a[76:79], v[132:135]\n"
"s_and_b32 s71, s71, 0x1ffff\n"
"s_add_u32 m0, s70, 16384\n"
"MM v[132:135], a[56:59], a[96:99], v[132:135]\n"
"buffer_load_dwordx4 v249, s[44:47], s71 offen nt lds\n"
"MM v[136:139], a[40:43], a[80:83], v[136:139]\n"
"MM v[136:139], a[56:59], a[100:103], v[136:139]\n"
"buffer_load_dwordx4 v252, s[44:47], s71 offen offset:1024 nt lds\n"
"MM v[140:143], a[44:47], a[64:67], v[140:143]\n"
"MM v[140:143], a[60:63], a[84:87], v[140:143]\n"
"MM v[144:147], a[44:47], a[68:71], v[144:147]\n"
"buffer_load_dwordx4 v251, s[44:47], s71 offen offset:2048 nt lds\n"
"MM v[144:147], a[60:63], a[88:91], v[144:147]\n"
"MM v[148:151], a[44:47], a[72:75], v[148:151]\n"
"buffer_load_dwordx4 v250, s[44:47], s71 offen offset:3072 nt lds\n"
"MM v[148:151], a[60:63], a[92:95], v[148:151]\n"
"MM v[152:155], a[44:47], a[76:79], v[152:155]\n"
"MM v[152:155], a[60:63], a[96:99], v[152:155]\n"
"MM v[156:159], a[44:47], a[80:83], v[156:159]\n"
"MM v[156:159], a[60:63], a[100:103], v[156:159]\n"
"s_waitcnt vmcnt(24)\n"
"s_waitcnt lgkmcnt(0)\n"
"s_barrier\n"
"MM v[0:3], a[0:3], a[104:107], v[0:3]\n"
"s_add_u32 m0, s66, 32768\n"
"ds_read_b64_tr_b16 a[32:33], v160 offset:49152\n"
"MM v[0:3], a[16:19], a[124:127], v[0:3]\n"
"buffer_load_dwordx4 v164, s[40:43], s67 offen lds\n"
"MM v[4:7], a[0:3], a[108:111], v[4:7]\n"
"ds_read_b64_tr_b16 a[34:35], v160 offset:50176\n"
"ds_read_b64_tr_b16 a[48:49], v160 offset:53248\n"
"MM v[4:7], a[16:19], a[128:131], v[4:7]\n"
"ds_read_b64_tr_b16 a[50:51], v160 offset:54272\n"
"s_add_u32 m0, s66, 33792\n"
"MM v[8:11], a[0:3], a[112:115], v[8:11]\n"
"ds_read_b64_tr_b16 a[36:37], v161 offset:49152\n"
"ds_read_b64_tr_b16 a[38:39], v161 offset:50176\n"
"MM v[8:11], a[16:19], a[132:135], v[8:11]\n"
"buffer_load_dwordx4 v165, s[40:43], s67 offen lds\n"
"MM v[12:15], a[0:3], a[116:119], v[12:15]\n"
"ds_read_b64_tr_b16 a[52:53], v161 offset:53248\n"
"ds_read_b64_tr_b16 a[54:55], v161 offset:54272\n"
"MM v[12:15], a[16:19], a[136:139], v[12:15]\n"
"s_add_u32 m0, s66, 34816\n"
"ds_read_b64_tr_b16 a[40:41], v160 offset:49664\n"
"MM v[16:19], a[0:3], a[120:123], v[16:19]\n"
"buffer_load_dwordx4 v166, s[40:43], s67 offen lds\n"
"MM v[16:19], a[16:19], a[140:143], v[16:19]\n"
"ds_read_b64_tr_b16 a[42:43], v160 offset:50688\n"
"MM v[20:23], a[4:7], a[104:107], v[20:23]\n"
"ds_read_b64_tr_b16 a[56:57], v160 offset:53760\n"
"MM v[20:23], a[20:23], a[124:127], v[20:23]\n"
"s_add_u32 m0, s66, 35840\n"
"ds_read_b64_tr_b16 a[58:59], v160 offset:54784\n"
"MM v[24:27], a[4:7], a[108:111], v[24:27]\n"
"buffer_load_dwordx4 v167, s[40:43], s67 offen lds\n"
"MM v[24:27], a[20:23], a[128:131], v[24:27]\n"
"ds_read_b64_tr_b16 a[44:45], v161 offset:49664\n"
"MM v[28:31], a[4:7], a[112:115], v[28:31]\n"
"ds_read_b64_tr_b16 a[46:47], v161 offset:50688\n"
"MM v[28:31], a[20:23], a[132:135], v[28:31]\n"
"MM v[32:35], a[4:7], a[116:119], v[32:35]\n"
"s_add_u32 m0, s66, 36864\n"
"ds_read_b64_tr_b16 a[60:61], v161 offset:53760\n"
"MM v[32:35], a[20:23], a[136:139], v[32:35]\n"
"buffer_load_dwordx4 v168, s[40:43], s67 offen lds\n"
"MM v[36:39], a[4:7], a[120:123], v[36:39]\n"
"ds_read_b64_tr_b16 a[62:63], v161 offset:54784\n"
"MM v[36:39], a[20:23], a[140:143], v[36:39]\n"
"MM v[40:43], a[8:11], a[104:107], v[40:43]\n"
"MM v[40:43], a[24:27], a[124:127], v[40:43]\n"
"s_add_u32 m0, s66, 37888\n"
"MM v[44:47], a[8:11], a[108:111], v[44:47]\n"
"buffer_load_dwordx4 v169, s[40:43], s67 offen lds\n"
"MM v[44:47], a[24:27], a[128:131], v[44:47]\n"
"MM v[48:51], a[8:11], a[112:115], v[48:51]\n"
"MM v[48:51], a[24:27], a[132:135], v[48:51]\n"
"MM v[52:55], a[8:11], a[116:119], v[52:55]\n"
"s_add_u32 m0, s66, 38912\n"
"MM v[52:55], a[24:27], a[136:139], v[52:55]\n"
"buffer_load_dwordx4 v170, s[40:43], s67 offen lds\n"
"MM v[56:59], a[8:11], a[120:123], v[56:59]\n"
"MM v[56:59], a[24:27], a[140:143], v[56:59]\n"
"MM v[60:63], a[12:15], a[104:107], v[60:63]\n"
"s_add_u32 m0, s66, 39936\n"
"MM v[60:63], a[28:31], a[124:127], v[60:63]\n"
"buffer_load_dwordx4 v171, s[40:43], s67 offen lds\n"
"MM v[64:67], a[12:15], a[108:111], v[64:67]\n"
"MM v[64:67], a[28:31], a[128:131], v[64:67]\n"
"MM v[68:71], a[12:15], a[112:115], v[68:71]\n"
"MM v[68:71], a[28:31], a[132:135], v[68:71]\n"
"s_add_u32 s67, s67, 128\n"
"MM v[72:75], a[12:15], a[116:119], v[72:75]\n"
"MM v[72:75], a[28:31], a[136:139], v[72:75]\n"
"MM v[76:79], a[12:15], a[120:123], v[76:79]\n"
"MM v[76:79], a[28:31], a[140:143], v[76:79]\n"
"s_waitcnt vmcnt(24)\n"
"s_waitcnt lgkmcnt(0)\n"
"s_barrier\n"
"MM v[80:83], a[32:35], a[104:107], v[80:83]\n"
"s_add_u32 m0, s66, 49152\n"
"ds_read_b64_tr_b16 a[0:1], v160 offset:0\n"
"MM v[80:83], a[48:51], a[124:127], v[80:83]\n"
"buffer_load_dwordx4 v164, s[40:43], s67 offen lds\n"
"MM v[84:87], a[32:35], a[108:111], v[84:87]\n"
"ds_read_b64_tr_b16 a[2:3], v160 offset:1024\n"
"ds_read_b64_tr_b16 a[16:17], v160 offset:4096\n"
"MM v[84:87], a[48:51], a[128:131], v[84:87]\n"
"s_add_u32 m0, s66, 50176\n"
"ds_read_b64_tr_b16 a[18:19], v160 offset:5120\n"
"MM v[88:91], a[32:35], a[112:115], v[88:91]\n"
"buffer_load_dwordx4 v165, s[40:43], s67 offen lds\n"
"MM v[88:91], a[48:51], a[132:135], v[88:91]\n"
"ds_read_b128 a[64:67], v162 offset:0\n"
"ds_read_b128 a[84:87], v163 offset:0\n"
"MM v[92:95], a[32:35], a[116:119], v[92:95]\n"
"s_add_u32 m0, s66, 51200\n"
"ds_read_b128 a[68:71], v162 offset:2048\n"
"MM v[92:95], a[48:51], a[136:139], v[92:95]\n"
"buffer_load_dwordx4 v166, s[40:43], s67 offen lds\n"
"MM v[96:99], a[32:35], a[120:123], v[96:99]\n"
"ds_read_b128 a[88:91], v163 offset:2048\n"
"ds_read_b128 a[72:75], v162 offset:4096\n"
"MM v[96:99], a[48:51], a[140:143], v[96:99]\n"
"s_add_u32 m0, s66, 52224\n"
"ds_read_b128 a[92:95], v163 offset:4096\n"
"MM v[100:103], a[36:39], a[104:107], v[100:103]\n"
"buffer_load_dwordx4 v167, s[40:43], s67 offen lds\n"
"MM v[100:103], a[52:55], a[124:127], v[100:103]\n"
"ds_read_b128 a[76:79], v162 offset:6144\n"
"ds_read_b128 a[96:99], v163 offset:6144\n"
"MM v[104:107], a[36:39], a[108:111], v[104:107]\n"
"s_add_u32 m0, s66, 53248\n"
"ds_read_b128 a[80:83], v162 offset:8192\n"
"MM v[104:107], a[52:55], a[128:131], v[104:107]\n"
"buffer_load_dwordx4 v168, s[40:43], s67 offen lds\n"
"MM v[108:111], a[36:39], a[112:115], v[108:111]\n"
"ds_read_b128 a[100:103], v163 offset:8192\n"
"ds_read_b64_tr_b16 a[4:5], v161 offset:0\n"
"MM v[108:111], a[52:55], a[132:135], v[108:111]\n"
"s_add_u32 m0, s66, 54272\n"
"ds_read_b64_tr_b16 a[6:7], v161 offset:1024\n"
"MM v[112:115], a[36:39], a[116:119], v[112:115]\n"
"buffer_load_dwordx4 v169, s[40:43], s67 offen lds\n"
"MM v[112:115], a[52:55], a[136:139], v[112:115]\n"
"ds_read_b64_tr_b16 a[20:21], v161 offset:4096\n"
"ds_read_b64_tr_b16 a[22:23], v161 offset:5120\n"
"MM v[116:119], a[36:39], a[120:123], v[116:119]\n"
"s_add_u32 m0, s66, 55296\n"
"ds_read_b64_tr_b16 a[8:9], v160 offset:512\n"
"MM v[116:119], a[52:55], a[140:143], v[116:119]\n"
"buffer_load_dwordx4 v170, s[40:43], s67 offen lds\n"
"MM v[120:123], a[40:43], a[104:107], v[120:123]\n"
"ds_read_b64_tr_b16 a[10:11], v160 offset:1536\n"
"ds_read_b64_tr_b16 a[24:25], v160 offset:4608\n"
"MM v[120:123], a[56:59], a[124:127], v[120:123]\n"
"s_add_u32 m0, s66, 56320\n"
"ds_read_b64_tr_b16 a[26:27], v160 offset:5632\n"
"MM v[124:127], a[40:43], a[108:111], v[124:127]\n"
"buffer_load_dwordx4 v171, s[40:43], s67 offen lds\n"
"MM v[124:127], a[56:59], a[128:131], v[124:127]\n"
"ds_read_b64_tr_b16 a[12:13], v161 offset:512\n"
"ds_read_b64_tr_b16 a[14:15], v161 offset:1536\n"
"MM v[128:131], a[40:43], a[112:115], v[128:131]\n"
"s_add_u32 s67, s67, 524160\n"
"ds_read_b64_tr_b16 a[28:29], v161 offset:4608\n"
"MM v[128:131], a[56:59], a[132:135], v[128:131]\n"
"s_add_u32 s71, s94, 0\n"
"ds_read_b64_tr_b16 a[30:31], v161 offset:5632\n"
"MM v[132:135], a[40:43], a[116:119], v[132:135]\n"
"s_and_b32 s71, s71, 0x1ffff\n"
"s_add_u32 m0, s70, 32768\n"
"MM v[132:135], a[56:59], a[136:139], v[132:135]\n"
"buffer_load_dwordx4 v249, s[48:51], s71 offen nt lds\n"
"MM v[136:139], a[40:43], a[120:123], v[136:139]\n"
"MM v[136:139], a[56:59], a[140:143], v[136:139]\n"
"buffer_load_dwordx4 v251, s[48:51], s71 offen offset:1024 nt lds\n"
"MM v[140:143], a[44:47], a[104:107], v[140:143]\n"
"MM v[140:143], a[60:63], a[124:127], v[140:143]\n"
"MM v[144:147], a[44:47], a[108:111], v[144:147]\n"
"buffer_load_dwordx4 v249, s[48:51], s71 offen offset:2048 nt lds\n"
"MM v[144:147], a[60:63], a[128:131], v[144:147]\n"
"MM v[148:151], a[44:47], a[112:115], v[148:151]\n"
"buffer_load_dwordx4 v251, s[48:51], s71 offen offset:3072 nt lds\n"
"MM v[148:151], a[60:63], a[132:135], v[148:151]\n"
"MM v[152:155], a[44:47], a[116:119], v[152:155]\n"
"MM v[152:155], a[60:63], a[136:139], v[152:155]\n"
"MM v[156:159], a[44:47], a[120:123], v[156:159]\n"
"MM v[156:159], a[60:63], a[140:143], v[156:159]\n"
"s_waitcnt vmcnt(24)\n"
"s_waitcnt lgkmcnt(0)\n"
"s_barrier\n"
"MM v[0:3], a[0:3], a[64:67], v[0:3]\n"
"ds_read_b64_tr_b16 a[32:33], v160 offset:16384\n"
"s_add_u32 s71, s94, 16384\n"
"MM v[0:3], a[16:19], a[84:87], v[0:3]\n"
"ds_read_b64_tr_b16 a[34:35], v160 offset:17408\n"
"s_and_b32 s71, s71, 0x1ffff\n"
"MM v[4:7], a[0:3], a[68:71], v[4:7]\n"
"ds_read_b64_tr_b16 a[48:49], v160 offset:20480\n"
"s_add_u32 m0, s70, 49152\n"
"MM v[4:7], a[16:19], a[88:91], v[4:7]\n"
"ds_read_b64_tr_b16 a[50:51], v160 offset:21504\n"
"ds_read_b64_tr_b16 a[36:37], v161 offset:16384\n"
"MM v[8:11], a[0:3], a[72:75], v[8:11]\n"
"buffer_load_dwordx4 v249, s[44:47], s71 offen nt lds\n"
"MM v[8:11], a[16:19], a[92:95], v[8:11]\n"
"ds_read_b64_tr_b16 a[38:39], v161 offset:17408\n"
"ds_read_b64_tr_b16 a[52:53], v161 offset:20480\n"
"MM v[12:15], a[0:3], a[76:79], v[12:15]\n"
"ds_read_b64_tr_b16 a[54:55], v161 offset:21504\n"
"MM v[12:15], a[16:19], a[96:99], v[12:15]\n"
"ds_read_b64_tr_b16 a[40:41], v160 offset:16896\n"
"MM v[16:19], a[0:3], a[80:83], v[16:19]\n"
"buffer_load_dwordx4 v252, s[44:47], s71 offen offset:1024 nt lds\n"
"MM v[16:19], a[16:19], a[100:103], v[16:19]\n"
"ds_read_b64_tr_b16 a[42:43], v160 offset:17920\n"
"MM v[20:23], a[4:7], a[64:67], v[20:23]\n"
"ds_read_b64_tr_b16 a[56:57], v160 offset:20992\n"
"MM v[20:23], a[20:23], a[84:87], v[20:23]\n"
"ds_read_b64_tr_b16 a[58:59], v160 offset:22016\n"
"MM v[24:27], a[4:7], a[68:71], v[24:27]\n"
"MM v[24:27], a[20:23], a[88:91], v[24:27]\n"
"ds_read_b64_tr_b16 a[44:45], v161 offset:16896\n"
"MM v[28:31], a[4:7], a[72:75], v[28:31]\n"
"ds_read_b64_tr_b16 a[46:47], v161 offset:17920\n"
"MM v[28:31], a[20:23], a[92:95], v[28:31]\n"
"MM v[32:35], a[4:7], a[76:79], v[32:35]\n"
"ds_read_b64_tr_b16 a[60:61], v161 offset:20992\n"
"MM v[32:35], a[20:23], a[96:99], v[32:35]\n"
"buffer_load_dwordx4 v251, s[44:47], s71 offen offset:2048 nt lds\n"
"MM v[36:39], a[4:7], a[80:83], v[36:39]\n"
"ds_read_b64_tr_b16 a[62:63], v161 offset:22016\n"
"MM v[36:39], a[20:23], a[100:103], v[36:39]\n"
"MM v[40:43], a[8:11], a[64:67], v[40:43]\n"
"MM v[40:43], a[24:27], a[84:87], v[40:43]\n"
"MM v[44:47], a[8:11], a[68:71], v[44:47]\n"
"MM v[44:47], a[24:27], a[88:91], v[44:47]\n"
"MM v[48:51], a[8:11], a[72:75], v[48:51]\n"
"buffer_load_dwordx4 v250, s[44:47], s71 offen offset:3072 nt lds\n"
"MM v[48:51], a[24:27], a[92:95], v[48:51]\n"
"MM v[52:55], a[8:11], a[76:79], v[52:55]\n"
"MM v[52:55], a[24:27], a[96:99], v[52:55]\n"
"MM v[56:59], a[8:11], a[80:83], v[56:59]\n"
"MM v[56:59], a[24:27], a[100:103], v[56:59]\n"
"MM v[60:63], a[12:15], a[64:67], v[60:63]\n"
"MM v[60:63], a[28:31], a[84:87], v[60:63]\n"
"MM v[64:67], a[12:15], a[68:71], v[64:67]\n"
"MM v[64:67], a[28:31], a[88:91], v[64:67]\n"
"MM v[68:71], a[12:15], a[72:75], v[68:71]\n"
"MM v[68:71], a[28:31], a[92:95], v[68:71]\n"
"MM v[72:75], a[12:15], a[76:79], v[72:75]\n"
"MM v[72:75], a[28:31], a[96:99], v[72:75]\n"
"MM v[76:79], a[12:15], a[80:83], v[76:79]\n"
"MM v[76:79], a[28:31], a[100:103], v[76:79]\n"
"s_waitcnt vmcnt(16)\n"
"s_waitcnt lgkmcnt(0)\n"
"s_barrier\n"
"MM v[80:83], a[32:35], a[64:67], v[80:83]\n"
"ds_read_b64_tr_b16 a[0:1], v160 offset:32768\n"
"s_add_u32 s71, s94, 8192\n"
"MM v[80:83], a[48:51], a[84:87], v[80:83]\n"
"ds_read_b64_tr_b16 a[2:3], v160 offset:33792\n"
"s_and_b32 s71, s71, 0x1ffff\n"
"MM v[84:87], a[32:35], a[68:71], v[84:87]\n"
"ds_read_b64_tr_b16 a[16:17], v160 offset:36864\n"
"ds_read_b64_tr_b16 a[18:19], v160 offset:37888\n"
"MM v[84:87], a[48:51], a[88:91], v[84:87]\n"
"s_add_u32 m0, s70, 114688\n"
"ds_read_b128 a[104:107], v162 offset:20480\n"
"MM v[88:91], a[32:35], a[72:75], v[88:91]\n"
"buffer_load_dwordx4 v249, s[48:51], s71 offen nt lds\n"
"MM v[88:91], a[48:51], a[92:95], v[88:91]\n"
"ds_read_b128 a[124:127], v163 offset:20480\n"
"ds_read_b128 a[108:111], v162 offset:22528\n"
"MM v[92:95], a[32:35], a[76:79], v[92:95]\n"
"ds_read_b128 a[128:131], v163 offset:22528\n"
"ds_read_b128 a[112:115], v162 offset:24576\n"
"MM v[92:95], a[48:51], a[96:99], v[92:95]\n"
"ds_read_b128 a[132:135], v163 offset:24576\n"
"MM v[96:99], a[32:35], a[80:83], v[96:99]\n"
"ds_read_b128 a[116:119], v162 offset:26624\n"
"MM v[96:99], a[48:51], a[100:103], v[96:99]\n"
"buffer_load_dwordx4 v251, s[48:51], s71 offen offset:1024 nt lds\n"
"MM v[100:103], a[36:39], a[64:67], v[100:103]\n"
"ds_read_b128 a[136:139], v163 offset:26624\n"
"ds_read_b128 a[120:123], v162 offset:28672\n"
"MM v[100:103], a[52:55], a[84:87], v[100:103]\n"
"ds_read_b128 a[140:143], v163 offset:28672\n"
"MM v[104:107], a[36:39], a[68:71], v[104:107]\n"
"ds_read_b64_tr_b16 a[4:5], v161 offset:32768\n"
"MM v[104:107], a[52:55], a[88:91], v[104:107]\n"
"ds_read_b64_tr_b16 a[6:7], v161 offset:33792\n"
"MM v[108:111], a[36:39], a[72:75], v[108:111]\n"
"ds_read_b64_tr_b16 a[20:21], v161 offset:36864\n"
"MM v[108:111], a[52:55], a[92:95], v[108:111]\n"
"MM v[112:115], a[36:39], a[76:79], v[112:115]\n"
"ds_read_b64_tr_b16 a[22:23], v161 offset:37888\n"
"MM v[112:115], a[52:55], a[96:99], v[112:115]\n"
"buffer_load_dwordx4 v249, s[48:51], s71 offen offset:2048 nt lds\n"
"MM v[116:119], a[36:39], a[80:83], v[116:119]\n"
"ds_read_b64_tr_b16 a[8:9], v160 offset:33280\n"
"ds_read_b64_tr_b16 a[10:11], v160 offset:34304\n"
"MM v[116:119], a[52:55], a[100:103], v[116:119]\n"
"ds_read_b64_tr_b16 a[24:25], v160 offset:37376\n"
"MM v[120:123], a[40:43], a[64:67], v[120:123]\n"
"ds_read_b64_tr_b16 a[26:27], v160 offset:38400\n"
"MM v[120:123], a[56:59], a[84:87], v[120:123]\n"
"ds_read_b64_tr_b16 a[12:13], v161 offset:33280\n"
"MM v[124:127], a[40:43], a[68:71], v[124:127]\n"
"MM v[124:127], a[56:59], a[88:91], v[124:127]\n"
"ds_read_b64_tr_b16 a[14:15], v161 offset:34304\n"
"MM v[128:131], a[40:43], a[72:75], v[128:131]\n"
"ds_read_b64_tr_b16 a[28:29], v161 offset:37376\n"
"MM v[128:131], a[56:59], a[92:95], v[128:131]\n"
"ds_read_b64_tr_b16 a[30:31], v161 offset:38400\n"
"MM v[132:135], a[40:43], a[76:79], v[132:135]\n"
"buffer_load_dwordx4 v251, s[48:51], s71 offen offset:3072 nt lds\n"
"MM v[132:135], a[56:59], a[96:99], v[132:135]\n"
"MM v[136:139], a[40:43], a[80:83], v[136:139]\n"
"MM v[136:139], a[56:59], a[100:103], v[136:139]\n"
"MM v[140:143], a[44:47], a[64:67], v[140:143]\n"
"MM v[140:143], a[60:63], a[84:87], v[140:143]\n"
"MM v[144:147], a[44:47], a[68:71], v[144:147]\n"
"MM v[144:147], a[60:63], a[88:91], v[144:147]\n"
"MM v[148:151], a[44:47], a[72:75], v[148:151]\n"
"MM v[148:151], a[60:63], a[92:95], v[148:151]\n"
"MM v[152:155], a[44:47], a[76:79], v[152:155]\n"
"MM v[152:155], a[60:63], a[96:99], v[152:155]\n"
"MM v[156:159], a[44:47], a[80:83], v[156:159]\n"
"MM v[156:159], a[60:63], a[100:103], v[156:159]\n"
"s_waitcnt vmcnt(12)\n"
"s_waitcnt lgkmcnt(0)\n"
"s_barrier\n"
"MM v[0:3], a[0:3], a[104:107], v[0:3]\n"
"ds_read_b64_tr_b16 a[32:33], v160 offset:49152\n"
"s_add_u32 s71, s94, 24576\n"
"MM v[0:3], a[16:19], a[124:127], v[0:3]\n"
"ds_read_b64_tr_b16 a[34:35], v160 offset:50176\n"
"s_and_b32 s71, s71, 0x1ffff\n"
"MM v[4:7], a[0:3], a[108:111], v[4:7]\n"
"ds_read_b64_tr_b16 a[48:49], v160 offset:53248\n"
"s_add_u32 m0, s70, 65536\n"
"MM v[4:7], a[16:19], a[128:131], v[4:7]\n"
"ds_read_b64_tr_b16 a[50:51], v160 offset:54272\n"
"ds_read_b64_tr_b16 a[36:37], v161 offset:49152\n"
"MM v[8:11], a[0:3], a[112:115], v[8:11]\n"
"buffer_load_dwordx4 v249, s[44:47], s71 offen nt lds\n"
"MM v[8:11], a[16:19], a[132:135], v[8:11]\n"
"ds_read_b64_tr_b16 a[38:39], v161 offset:50176\n"
"ds_read_b64_tr_b16 a[52:53], v161 offset:53248\n"
"MM v[12:15], a[0:3], a[116:119], v[12:15]\n"
"ds_read_b64_tr_b16 a[54:55], v161 offset:54272\n"
"MM v[12:15], a[16:19], a[136:139], v[12:15]\n"
"ds_read_b64_tr_b16 a[40:41], v160 offset:49664\n"
"MM v[16:19], a[0:3], a[120:123], v[16:19]\n"
"buffer_load_dwordx4 v252, s[44:47], s71 offen offset:1024 nt lds\n"
"MM v[16:19], a[16:19], a[140:143], v[16:19]\n"
"ds_read_b64_tr_b16 a[42:43], v160 offset:50688\n"
"MM v[20:23], a[4:7], a[104:107], v[20:23]\n"
"ds_read_b64_tr_b16 a[56:57], v160 offset:53760\n"
"MM v[20:23], a[20:23], a[124:127], v[20:23]\n"
"ds_read_b64_tr_b16 a[58:59], v160 offset:54784\n"
"MM v[24:27], a[4:7], a[108:111], v[24:27]\n"
"MM v[24:27], a[20:23], a[128:131], v[24:27]\n"
"ds_read_b64_tr_b16 a[44:45], v161 offset:49664\n"
"MM v[28:31], a[4:7], a[112:115], v[28:31]\n"
"ds_read_b64_tr_b16 a[46:47], v161 offset:50688\n"
"MM v[28:31], a[20:23], a[132:135], v[28:31]\n"
"MM v[32:35], a[4:7], a[116:119], v[32:35]\n"
"ds_read_b64_tr_b16 a[60:61], v161 offset:53760\n"
"MM v[32:35], a[20:23], a[136:139], v[32:35]\n"
"buffer_load_dwordx4 v251, s[44:47], s71 offen offset:2048 nt lds\n"
"MM v[36:39], a[4:7], a[120:123], v[36:39]\n"
"ds_read_b64_tr_b16 a[62:63], v161 offset:54784\n"
"MM v[36:39], a[20:23], a[140:143], v[36:39]\n"
"MM v[40:43], a[8:11], a[104:107], v[40:43]\n"
"MM v[40:43], a[24:27], a[124:127], v[40:43]\n"
"MM v[44:47], a[8:11], a[108:111], v[44:47]\n"
"MM v[44:47], a[24:27], a[128:131], v[44:47]\n"
"MM v[48:51], a[8:11], a[112:115], v[48:51]\n"
"buffer_load_dwordx4 v250, s[44:47], s71 offen offset:3072 nt lds\n"
"MM v[48:51], a[24:27], a[132:135], v[48:51]\n"
"MM v[52:55], a[8:11], a[116:119], v[52:55]\n"
"MM v[52:55], a[24:27], a[136:139], v[52:55]\n"
"MM v[56:59], a[8:11], a[120:123], v[56:59]\n"
"MM v[56:59], a[24:27], a[140:143], v[56:59]\n"
"MM v[60:63], a[12:15], a[104:107], v[60:63]\n"
"MM v[60:63], a[28:31], a[124:127], v[60:63]\n"
"MM v[64:67], a[12:15], a[108:111], v[64:67]\n"
"MM v[64:67], a[28:31], a[128:131], v[64:67]\n"
"MM v[68:71], a[12:15], a[112:115], v[68:71]\n"
"MM v[68:71], a[28:31], a[132:135], v[68:71]\n"
"MM v[72:75], a[12:15], a[116:119], v[72:75]\n"
"MM v[72:75], a[28:31], a[136:139], v[72:75]\n"
"MM v[76:79], a[12:15], a[120:123], v[76:79]\n"
"MM v[76:79], a[28:31], a[140:143], v[76:79]\n"
"s_waitcnt lgkmcnt(0)\n"
"s_barrier\n"
"MM v[80:83], a[32:35], a[104:107], v[80:83]\n"
"v_mul_f32 v0, s78, v0\n"
"v_mul_f32 v1, s78, v1\n"
"MM v[80:83], a[48:51], a[124:127], v[80:83]\n"
"v_mul_f32 v2, s78, v2\n"
"s_add_u32 s71, s94, 16384\n"
"MM v[84:87], a[32:35], a[108:111], v[84:87]\n"
"v_mul_f32 v3, s78, v3\n"
"v_mul_f32 v20, s78, v20\n"
"MM v[84:87], a[48:51], a[128:131], v[84:87]\n"
"v_mul_f32 v21, s78, v21\n"
"v_mul_f32 v22, s78, v22\n"
"MM v[88:91], a[32:35], a[112:115], v[88:91]\n"
"v_mul_f32 v23, s78, v23\n"
"s_and_b32 s71, s71, 0x1ffff\n"
"MM v[88:91], a[48:51], a[132:135], v[88:91]\n"
"v_cvt_pk_bf16_f32 v160, v0, v1\n"
"v_cvt_pk_bf16_f32 v161, v2, v3\n"
"MM v[92:95], a[32:35], a[116:119], v[92:95]\n"
"v_cvt_pk_bf16_f32 v162, v20, v21\n"
"v_cvt_pk_bf16_f32 v163, v22, v23\n"
"MM v[92:95], a[48:51], a[136:139], v[92:95]\n"
"v_mul_f32 v4, s78, v4\n"
"s_add_u32 m0, s70, 81920\n"
"MM v[96:99], a[32:35], a[120:123], v[96:99]\n"
"v_mul_f32 v5, s78, v5\n"
"v_mul_f32 v6, s78, v6\n"
"MM v[96:99], a[48:51], a[140:143], v[96:99]\n"
"v_mul_f32 v7, s78, v7\n"
"v_mul_f32 v24, s78, v24\n"
"MM v[100:103], a[36:39], a[104:107], v[100:103]\n"
"v_mul_f32 v25, s78, v25\n"
"v_mul_f32 v26, s78, v26\n"
"MM v[100:103], a[52:55], a[124:127], v[100:103]\n"
"buffer_load_dwordx4 v249, s[48:51], s71 offen nt lds\n"
"MM v[104:107], a[36:39], a[108:111], v[104:107]\n"
"v_mul_f32 v27, s78, v27\n"
"v_cvt_pk_bf16_f32 v164, v4, v5\n"
"MM v[104:107], a[52:55], a[128:131], v[104:107]\n"
"v_cvt_pk_bf16_f32 v165, v6, v7\n"
"v_cvt_pk_bf16_f32 v166, v24, v25\n"
"MM v[108:111], a[36:39], a[112:115], v[108:111]\n"
"v_cvt_pk_bf16_f32 v167, v26, v27\n"
"v_mul_f32 v8, s78, v8\n"
"MM v[108:111], a[52:55], a[132:135], v[108:111]\n"
"v_mul_f32 v9, s78, v9\n"
"v_mul_f32 v10, s78, v10\n"
"MM v[112:115], a[36:39], a[116:119], v[112:115]\n"
"v_mul_f32 v11, s78, v11\n"
"v_mul_f32 v28, s78, v28\n"
"MM v[112:115], a[52:55], a[136:139], v[112:115]\n"
"v_mul_f32 v29, s78, v29\n"
"v_mul_f32 v30, s78, v30\n"
"MM v[116:119], a[36:39], a[120:123], v[116:119]\n"
"v_mul_f32 v31, s78, v31\n"
"v_cvt_pk_bf16_f32 v168, v8, v9\n"
"MM v[116:119], a[52:55], a[140:143], v[116:119]\n"
"v_cvt_pk_bf16_f32 v169, v10, v11\n"
"v_cvt_pk_bf16_f32 v170, v28, v29\n"
"MM v[120:123], a[40:43], a[104:107], v[120:123]\n"
"buffer_load_dwordx4 v251, s[48:51], s71 offen offset:1024 nt lds\n"
"MM v[120:123], a[56:59], a[124:127], v[120:123]\n"
"v_cvt_pk_bf16_f32 v171, v30, v31\n"
"v_mul_f32 v12, s78, v12\n"
"MM v[124:127], a[40:43], a[108:111], v[124:127]\n"
"v_mul_f32 v13, s78, v13\n"
"v_mul_f32 v14, s78, v14\n"
"MM v[124:127], a[56:59], a[128:131], v[124:127]\n"
"v_mul_f32 v15, s78, v15\n"
"v_mul_f32 v32, s78, v32\n"
"MM v[128:131], a[40:43], a[112:115], v[128:131]\n"
"v_mul_f32 v33, s78, v33\n"
"v_mul_f32 v34, s78, v34\n"
"MM v[128:131], a[56:59], a[132:135], v[128:131]\n"
"v_mul_f32 v35, s78, v35\n"
"v_cvt_pk_bf16_f32 v172, v12, v13\n"
"MM v[132:135], a[40:43], a[116:119], v[132:135]\n"
"v_cvt_pk_bf16_f32 v173, v14, v15\n"
"v_cvt_pk_bf16_f32 v174, v32, v33\n"
"MM v[132:135], a[56:59], a[136:139], v[132:135]\n"
"v_cvt_pk_bf16_f32 v175, v34, v35\n"
"v_mul_f32 v16, s78, v16\n"
"MM v[136:139], a[40:43], a[120:123], v[136:139]\n"
"v_mul_f32 v17, s78, v17\n"
"v_mul_f32 v18, s78, v18\n"
"MM v[136:139], a[56:59], a[140:143], v[136:139]\n"
"buffer_load_dwordx4 v249, s[48:51], s71 offen offset:2048 nt lds\n"
"MM v[140:143], a[44:47], a[104:107], v[140:143]\n"
"v_mul_f32 v19, s78, v19\n"
"v_mul_f32 v36, s78, v36\n"
"MM v[140:143], a[60:63], a[124:127], v[140:143]\n"
"v_mul_f32 v37, s78, v37\n"
"v_mul_f32 v38, s78, v38\n"
"MM v[144:147], a[44:47], a[108:111], v[144:147]\n"
"v_mul_f32 v39, s78, v39\n"
"v_cvt_pk_bf16_f32 v176, v16, v17\n"
"MM v[144:147], a[60:63], a[128:131], v[144:147]\n"
"v_cvt_pk_bf16_f32 v177, v18, v19\n"
"v_cvt_pk_bf16_f32 v178, v36, v37\n"
"MM v[148:151], a[44:47], a[112:115], v[148:151]\n"
"v_cvt_pk_bf16_f32 v179, v38, v39\n"
"v_mul_f32 v40, s78, v40\n"
"MM v[148:151], a[60:63], a[132:135], v[148:151]\n"
"v_mul_f32 v41, s78, v41\n"
"v_mul_f32 v42, s78, v42\n"
"MM v[152:155], a[44:47], a[116:119], v[152:155]\n"
"v_mul_f32 v43, s78, v43\n"
"v_mul_f32 v60, s78, v60\n"
"MM v[152:155], a[60:63], a[136:139], v[152:155]\n"
"v_mul_f32 v61, s78, v61\n"
"v_mul_f32 v62, s78, v62\n"
"MM v[156:159], a[44:47], a[120:123], v[156:159]\n"
"buffer_load_dwordx4 v251, s[48:51], s71 offen offset:3072 nt lds\n"
"v_mul_f32 v63, s78, v63\n"
"MM v[156:159], a[60:63], a[140:143], v[156:159]\n"
"v_cvt_pk_bf16_f32 v180, v40, v41\n"
"v_cvt_pk_bf16_f32 v181, v42, v43\n"
"v_cvt_pk_bf16_f32 v182, v60, v61\n"
"v_cvt_pk_bf16_f32 v183, v62, v63\n"
"s_branch K1_EATT\n"
"K1_EX2:\n"
"s_nop 3\n"
"s_waitcnt lgkmcnt(0)\n"
"s_barrier\n"
"MM v[0:3], a[0:3], a[104:107], v[0:3]\n"
"ds_read_b64_tr_b16 a[32:33], v160 offset:49152\n"
"ds_read_b64_tr_b16 a[34:35], v160 offset:50176\n"
"MM v[0:3], a[16:19], a[124:127], v[0:3]\n"
"s_add_u32 m0, s66, 20480\n"
"ds_read_b64_tr_b16 a[48:49], v160 offset:53248\n"
"MM v[4:7], a[0:3], a[108:111], v[4:7]\n"
"buffer_load_dwordx4 v164, s[36:39], s67 offen lds\n"
"MM v[4:7], a[16:19], a[128:131], v[4:7]\n"
"ds_read_b64_tr_b16 a[50:51], v160 offset:54272\n"
"s_add_u32 m0, s66, 21504\n"
"MM v[8:11], a[0:3], a[112:115], v[8:11]\n"
"ds_read_b64_tr_b16 a[36:37], v161 offset:49152\n"
"ds_read_b64_tr_b16 a[38:39], v161 offset:50176\n"
"MM v[8:11], a[16:19], a[132:135], v[8:11]\n"
"buffer_load_dwordx4 v165, s[36:39], s67 offen lds\n"
"MM v[12:15], a[0:3], a[116:119], v[12:15]\n"
"ds_read_b64_tr_b16 a[52:53], v161 offset:53248\n"
"s_add_u32 m0, s66, 22528\n"
"MM v[12:15], a[16:19], a[136:139], v[12:15]\n"
"ds_read_b64_tr_b16 a[54:55], v161 offset:54272\n"
"ds_read_b64_tr_b16 a[40:41], v160 offset:49664\n"
"MM v[16:19], a[0:3], a[120:123], v[16:19]\n"
"buffer_load_dwordx4 v166, s[36:39], s67 offen lds\n"
"MM v[16:19], a[16:19], a[140:143], v[16:19]\n"
"ds_read_b64_tr_b16 a[42:43], v160 offset:50688\n"
"MM v[20:23], a[4:7], a[104:107], v[20:23]\n"
"s_add_u32 m0, s66, 23552\n"
"ds_read_b64_tr_b16 a[56:57], v160 offset:53760\n"
"MM v[20:23], a[20:23], a[124:127], v[20:23]\n"
"buffer_load_dwordx4 v167, s[36:39], s67 offen lds\n"
"MM v[24:27], a[4:7], a[108:111], v[24:27]\n"
"ds_read_b64_tr_b16 a[58:59], v160 offset:54784\n"
"MM v[24:27], a[20:23], a[128:131], v[24:27]\n"
"ds_read_b64_tr_b16 a[44:45], v161 offset:49664\n"
"s_add_u32 m0, s66, 24576\n"
"MM v[28:31], a[4:7], a[112:115], v[28:31]\n"
"buffer_load_dwordx4 v168, s[36:39], s67 offen lds\n"
"MM v[28:31], a[20:23], a[132:135], v[28:31]\n"
"ds_read_b64_tr_b16 a[46:47], v161 offset:50688\n"
"MM v[32:35], a[4:7], a[116:119], v[32:35]\n"
"ds_read_b64_tr_b16 a[60:61], v161 offset:53760\n"
"MM v[32:35], a[20:23], a[136:139], v[32:35]\n"
"s_add_u32 m0, s66, 25600\n"
"ds_read_b64_tr_b16 a[62:63], v161 offset:54784\n"
"MM v[36:39], a[4:7], a[120:123], v[36:39]\n"
"buffer_load_dwordx4 v169, s[36:39], s67 offen lds\n"
"MM v[36:39], a[20:23], a[140:143], v[36:39]\n"
"MM v[40:43], a[8:11], a[104:107], v[40:43]\n"
"MM v[40:43], a[24:27], a[124:127], v[40:43]\n"
"s_add_u32 m0, s66, 26624\n"
"MM v[44:47], a[8:11], a[108:111], v[44:47]\n"
"buffer_load_dwordx4 v170, s[36:39], s67 offen lds\n"
"MM v[44:47], a[24:27], a[128:131], v[44:47]\n"
"MM v[48:51], a[8:11], a[112:115], v[48:51]\n"
"MM v[48:51], a[24:27], a[132:135], v[48:51]\n"
"s_add_u32 m0, s66, 27648\n"
"MM v[52:55], a[8:11], a[116:119], v[52:55]\n"
"buffer_load_dwordx4 v171, s[36:39], s67 offen lds\n"
"MM v[52:55], a[24:27], a[136:139], v[52:55]\n"
"MM v[56:59], a[8:11], a[120:123], v[56:59]\n"
"s_add_u32 m0, s66, 28672\n"
"MM v[56:59], a[24:27], a[140:143], v[56:59]\n"
"buffer_load_dwordx4 v172, s[36:39], s67 offen lds\n"
"MM v[60:63], a[12:15], a[104:107], v[60:63]\n"
"MM v[60:63], a[28:31], a[124:127], v[60:63]\n"
"MM v[64:67], a[12:15], a[108:111], v[64:67]\n"
"s_add_u32 m0, s66, 29696\n"
"MM v[64:67], a[28:31], a[128:131], v[64:67]\n"
"buffer_load_dwordx4 v173, s[36:39], s67 offen lds\n"
"MM v[68:71], a[12:15], a[112:115], v[68:71]\n"
"MM v[68:71], a[28:31], a[132:135], v[68:71]\n"
"MM v[72:75], a[12:15], a[116:119], v[72:75]\n"
"s_add_u32 s67, s67, 128\n"
"MM v[72:75], a[28:31], a[136:139], v[72:75]\n"
"MM v[76:79], a[12:15], a[120:123], v[76:79]\n"
"MM v[76:79], a[28:31], a[140:143], v[76:79]\n"
"s_waitcnt vmcnt(15)\n"
"s_waitcnt lgkmcnt(0)\n"
"s_barrier\n"
"MM v[80:83], a[32:35], a[104:107], v[80:83]\n"
"ds_read_b64_tr_b16 a[0:1], v160 offset:0\n"
"ds_read_b64_tr_b16 a[2:3], v160 offset:1024\n"
"MM v[80:83], a[48:51], a[124:127], v[80:83]\n"
"ds_read_b64_tr_b16 a[16:17], v160 offset:4096\n"
"ds_read_b64_tr_b16 a[18:19], v160 offset:5120\n"
"MM v[84:87], a[32:35], a[108:111], v[84:87]\n"
"ds_read_b128 a[64:67], v162 offset:0\n"
"ds_read_b128 a[84:87], v163 offset:0\n"
"MM v[84:87], a[48:51], a[128:131], v[84:87]\n"
"ds_read_b128 a[68:71], v162 offset:2048\n"
"MM v[88:91], a[32:35], a[112:115], v[88:91]\n"
"ds_read_b128 a[88:91], v163 offset:2048\n"
"MM v[88:91], a[48:51], a[132:135], v[88:91]\n"
"ds_read_b128 a[72:75], v162 offset:4096\n"
"MM v[92:95], a[32:35], a[116:119], v[92:95]\n"
"ds_read_b128 a[92:95], v163 offset:4096\n"
"MM v[92:95], a[48:51], a[136:139], v[92:95]\n"
"MM v[96:99], a[32:35], a[120:123], v[96:99]\n"
"ds_read_b128 a[76:79], v162 offset:6144\n"
"MM v[96:99], a[48:51], a[140:143], v[96:99]\n"
"ds_read_b128 a[96:99], v163 offset:6144\n"
"MM v[100:103], a[36:39], a[104:107], v[100:103]\n"
"ds_read_b128 a[80:83], v162 offset:8192\n"
"MM v[100:103], a[52:55], a[124:127], v[100:103]\n"
"ds_read_b128 a[100:103], v163 offset:8192\n"
"MM v[104:107], a[36:39], a[108:111], v[104:107]\n"
"ds_read_b64_tr_b16 a[4:5], v161 offset:0\n"
"MM v[104:107], a[52:55], a[128:131], v[104:107]\n"
"ds_read_b64_tr_b16 a[6:7], v161 offset:1024\n"
"MM v[108:111], a[36:39], a[112:115], v[108:111]\n"
"ds_read_b64_tr_b16 a[20:21], v161 offset:4096\n"
"MM v[108:111], a[52:55], a[132:135], v[108:111]\n"
"MM v[112:115], a[36:39], a[116:119], v[112:115]\n"
"ds_read_b64_tr_b16 a[22:23], v161 offset:5120\n"
"MM v[112:115], a[52:55], a[136:139], v[112:115]\n"
"ds_read_b64_tr_b16 a[8:9], v160 offset:512\n"
"MM v[116:119], a[36:39], a[120:123], v[116:119]\n"
"ds_read_b64_tr_b16 a[10:11], v160 offset:1536\n"
"MM v[116:119], a[52:55], a[140:143], v[116:119]\n"
"ds_read_b64_tr_b16 a[24:25], v160 offset:4608\n"
"MM v[120:123], a[40:43], a[104:107], v[120:123]\n"
"ds_read_b64_tr_b16 a[26:27], v160 offset:5632\n"
"MM v[120:123], a[56:59], a[124:127], v[120:123]\n"
"ds_read_b64_tr_b16 a[12:13], v161 offset:512\n"
"MM v[124:127], a[40:43], a[108:111], v[124:127]\n"
"MM v[124:127], a[56:59], a[128:131], v[124:127]\n"
"ds_read_b64_tr_b16 a[14:15], v161 offset:1536\n"
"MM v[128:131], a[40:43], a[112:115], v[128:131]\n"
"ds_read_b64_tr_b16 a[28:29], v161 offset:4608\n"
"MM v[128:131], a[56:59], a[132:135], v[128:131]\n"
"ds_read_b64_tr_b16 a[30:31], v161 offset:5632\n"
"MM v[132:135], a[40:43], a[116:119], v[132:135]\n"
"MM v[132:135], a[56:59], a[136:139], v[132:135]\n"
"MM v[136:139], a[40:43], a[120:123], v[136:139]\n"
"MM v[136:139], a[56:59], a[140:143], v[136:139]\n"
"MM v[140:143], a[44:47], a[104:107], v[140:143]\n"
"MM v[140:143], a[60:63], a[124:127], v[140:143]\n"
"MM v[144:147], a[44:47], a[108:111], v[144:147]\n"
"MM v[144:147], a[60:63], a[128:131], v[144:147]\n"
"MM v[148:151], a[44:47], a[112:115], v[148:151]\n"
"MM v[148:151], a[60:63], a[132:135], v[148:151]\n"
"MM v[152:155], a[44:47], a[116:119], v[152:155]\n"
"MM v[152:155], a[60:63], a[136:139], v[152:155]\n"
"MM v[156:159], a[44:47], a[120:123], v[156:159]\n"
"MM v[156:159], a[60:63], a[140:143], v[156:159]\n"
"s_waitcnt vmcnt(0)\n"
"s_waitcnt lgkmcnt(0)\n"
"s_barrier\n"
"MM v[0:3], a[0:3], a[64:67], v[0:3]\n"
"ds_read_b64_tr_b16 a[32:33], v160 offset:16384\n"
"ds_read_b64_tr_b16 a[34:35], v160 offset:17408\n"
"MM v[0:3], a[16:19], a[84:87], v[0:3]\n"
"s_add_u32 m0, s66, 0\n"
"ds_read_b64_tr_b16 a[48:49], v160 offset:20480\n"
"MM v[4:7], a[0:3], a[68:71], v[4:7]\n"
"buffer_load_dwordx4 v164, s[36:39], s67 offen lds\n"
"MM v[4:7], a[16:19], a[88:91], v[4:7]\n"
"s_add_u32 m0, s66, 1024\n"
"ds_read_b64_tr_b16 a[50:51], v160 offset:21504\n"
"MM v[8:11], a[0:3], a[72:75], v[8:11]\n"
"buffer_load_dwordx4 v165, s[36:39], s67 offen lds\n"
"MM v[8:11], a[16:19], a[92:95], v[8:11]\n"
"ds_read_b64_tr_b16 a[36:37], v161 offset:16384\n"
"s_add_u32 m0, s66, 2048\n"
"MM v[12:15], a[0:3], a[76:79], v[12:15]\n"
"buffer_load_dwordx4 v166, s[36:39], s67 offen lds\n"
"MM v[12:15], a[16:19], a[96:99], v[12:15]\n"
"ds_read_b64_tr_b16 a[38:39], v161 offset:17408\n"
"ds_read_b64_tr_b16 a[52:53], v161 offset:20480\n"
"MM v[16:19], a[0:3], a[80:83], v[16:19]\n"
"s_add_u32 m0, s66, 3072\n"
"ds_read_b64_tr_b16 a[54:55], v161 offset:21504\n"
"MM v[16:19], a[16:19], a[100:103], v[16:19]\n"
"buffer_load_dwordx4 v167, s[36:39], s67 offen lds\n"
"MM v[20:23], a[4:7], a[64:67], v[20:23]\n"
"ds_read_b64_tr_b16 a[40:41], v160 offset:16896\n"
"ds_read_b64_tr_b16 a[42:43], v160 offset:17920\n"
"MM v[20:23], a[20:23], a[84:87], v[20:23]\n"
"s_add_u32 m0, s66, 4096\n"
"ds_read_b64_tr_b16 a[56:57], v160 offset:20992\n"
"MM v[24:27], a[4:7], a[68:71], v[24:27]\n"
"buffer_load_dwordx4 v168, s[36:39], s67 offen lds\n"
"MM v[24:27], a[20:23], a[88:91], v[24:27]\n"
"s_add_u32 m0, s66, 5120\n"
"ds_read_b64_tr_b16 a[58:59], v160 offset:22016\n"
"MM v[28:31], a[4:7], a[72:75], v[28:31]\n"
"buffer_load_dwordx4 v169, s[36:39], s67 offen lds\n"
"MM v[28:31], a[20:23], a[92:95], v[28:31]\n"
"ds_read_b64_tr_b16 a[44:45], v161 offset:16896\n"
"ds_read_b64_tr_b16 a[46:47], v161 offset:17920\n"
"MM v[32:35], a[4:7], a[76:79], v[32:35]\n"
"s_add_u32 m0, s66, 6144\n"
"ds_read_b64_tr_b16 a[60:61], v161 offset:20992\n"
"MM v[32:35], a[20:23], a[96:99], v[32:35]\n"
"buffer_load_dwordx4 v170, s[36:39], s67 offen lds\n"
"MM v[36:39], a[4:7], a[80:83], v[36:39]\n"
"s_add_u32 m0, s66, 7168\n"
"ds_read_b64_tr_b16 a[62:63], v161 offset:22016\n"
"MM v[36:39], a[20:23], a[100:103], v[36:39]\n"
"buffer_load_dwordx4 v171, s[36:39], s67 offen lds\n"
"MM v[40:43], a[8:11], a[64:67], v[40:43]\n"
"MM v[40:43], a[24:27], a[84:87], v[40:43]\n"
"s_add_u32 m0, s66, 8192\n"
"MM v[44:47], a[8:11], a[68:71], v[44:47]\n"
"buffer_load_dwordx4 v172, s[36:39], s67 offen lds\n"
"MM v[44:47], a[24:27], a[88:91], v[44:47]\n"
"MM v[48:51], a[8:11], a[72:75], v[48:51]\n"
"s_add_u32 m0, s66, 9216\n"
"MM v[48:51], a[24:27], a[92:95], v[48:51]\n"
"buffer_load_dwordx4 v173, s[36:39], s67 offen lds\n"
"MM v[52:55], a[8:11], a[76:79], v[52:55]\n"
"s_add_u32 s67, s67, 128\n"
"MM v[52:55], a[24:27], a[96:99], v[52:55]\n"
"s_add_u32 s71, s94, 0\n"
"s_and_b32 s71, s71, 0x1ffff\n"
"MM v[56:59], a[8:11], a[80:83], v[56:59]\n"
"s_add_u32 m0, s70, 0\n"
"MM v[56:59], a[24:27], a[100:103], v[56:59]\n"
"buffer_load_dwordx4 v249, s[44:47], s71 offen nt lds\n"
"MM v[60:63], a[12:15], a[64:67], v[60:63]\n"
"MM v[60:63], a[28:31], a[84:87], v[60:63]\n"
"buffer_load_dwordx4 v252, s[44:47], s71 offen offset:1024 nt lds\n"
"MM v[64:67], a[12:15], a[68:71], v[64:67]\n"
"MM v[64:67], a[28:31], a[88:91], v[64:67]\n"
"buffer_load_dwordx4 v251, s[44:47], s71 offen offset:2048 nt lds\n"
"MM v[68:71], a[12:15], a[72:75], v[68:71]\n"
"MM v[68:71], a[28:31], a[92:95], v[68:71]\n"
"buffer_load_dwordx4 v250, s[44:47], s71 offen offset:3072 nt lds\n"
"MM v[72:75], a[12:15], a[76:79], v[72:75]\n"
"MM v[72:75], a[28:31], a[96:99], v[72:75]\n"
"MM v[76:79], a[12:15], a[80:83], v[76:79]\n"
"MM v[76:79], a[28:31], a[100:103], v[76:79]\n"
"s_waitcnt lgkmcnt(0)\n"
"s_barrier\n"
"MM v[80:83], a[32:35], a[64:67], v[80:83]\n"
"ds_read_b64_tr_b16 a[0:1], v160 offset:32768\n"
"ds_read_b64_tr_b16 a[2:3], v160 offset:33792\n"
"MM v[80:83], a[48:51], a[84:87], v[80:83]\n"
"s_add_u32 s71, s94, 8192\n"
"ds_read_b64_tr_b16 a[16:17], v160 offset:36864\n"
"MM v[84:87], a[32:35], a[68:71], v[84:87]\n"
"s_and_b32 s71, s71, 0x1ffff\n"
"ds_read_b64_tr_b16 a[18:19], v160 offset:37888\n"
"MM v[84:87], a[48:51], a[88:91], v[84:87]\n"
"s_add_u32 m0, s70, 16384\n"
"ds_read_b128 a[104:107], v162 offset:20480\n"
"MM v[88:91], a[32:35], a[72:75], v[88:91]\n"
"ds_read_b128 a[124:127], v163 offset:20480\n"
"ds_read_b128 a[108:111], v162 offset:22528\n"
"MM v[88:91], a[48:51], a[92:95], v[88:91]\n"
"buffer_load_dwordx4 v249, s[44:47], s71 offen nt lds\n"
"MM v[92:95], a[32:35], a[76:79], v[92:95]\n"
"ds_read_b128 a[128:131], v163 offset:22528\n"
"ds_read_b128 a[112:115], v162 offset:24576\n"
"MM v[92:95], a[48:51], a[96:99], v[92:95]\n"
"ds_read_b128 a[132:135], v163 offset:24576\n"
"MM v[96:99], a[32:35], a[80:83], v[96:99]\n"
"ds_read_b128 a[116:119], v162 offset:26624\n"
"MM v[96:99], a[48:51], a[100:103], v[96:99]\n"
"ds_read_b128 a[136:139], v163 offset:26624\n"
"MM v[100:103], a[36:39], a[64:67], v[100:103]\n"
"buffer_load_dwordx4 v252, s[44:47], s71 offen offset:1024 nt lds\n"
"MM v[100:103], a[52:55], a[84:87], v[100:103]\n"
"ds_read_b128 a[120:123], v162 offset:28672\n"
"ds_read_b128 a[140:143], v163 offset:28672\n"
"MM v[104:107], a[36:39], a[68:71], v[104:107]\n"
"ds_read_b64_tr_b16 a[4:5], v161 offset:32768\n"
"MM v[104:107], a[52:55], a[88:91], v[104:107]\n"
"ds_read_b64_tr_b16 a[6:7], v161 offset:33792\n"
"MM v[108:111], a[36:39], a[72:75], v[108:111]\n"
"ds_read_b64_tr_b16 a[20:21], v161 offset:36864\n"
"MM v[108:111], a[52:55], a[92:95], v[108:111]\n"
"MM v[112:115], a[36:39], a[76:79], v[112:115]\n"
"ds_read_b64_tr_b16 a[22:23], v161 offset:37888\n"
"MM v[112:115], a[52:55], a[96:99], v[112:115]\n"
"ds_read_b64_tr_b16 a[8:9], v160 offset:33280\n"
"MM v[116:119], a[36:39], a[80:83], v[116:119]\n"
"buffer_load_dwordx4 v251, s[44:47], s71 offen offset:2048 nt lds\n"
"MM v[116:119], a[52:55], a[100:103], v[116:119]\n"
"ds_read_b64_tr_b16 a[10:11], v160 offset:34304\n"
"ds_read_b64_tr_b16 a[24:25], v160 offset:37376\n"
"MM v[120:123], a[40:43], a[64:67], v[120:123]\n"
"ds_read_b64_tr_b16 a[26:27], v160 offset:38400\n"
"MM v[120:123], a[56:59], a[84:87], v[120:123]\n"
"ds_read_b64_tr_b16 a[12:13], v161 offset:33280\n"
"MM v[124:127], a[40:43], a[68:71], v[124:127]\n"
"MM v[124:127], a[56:59], a[88:91], v[124:127]\n"
"ds_read_b64_tr_b16 a[14:15], v161 offset:34304\n"
"MM v[128:131], a[40:43], a[72:75], v[128:131]\n"
"ds_read_b64_tr_b16 a[28:29], v161 offset:37376\n"
"MM v[128:131], a[56:59], a[92:95], v[128:131]\n"
"ds_read_b64_tr_b16 a[30:31], v161 offset:38400\n"
"MM v[132:135], a[40:43], a[76:79], v[132:135]\n"
"buffer_load_dwordx4 v250, s[44:47], s71 offen offset:3072 nt lds\n"
"MM v[132:135], a[56:59], a[96:99], v[132:135]\n"
"MM v[136:139], a[40:43], a[80:83], v[136:139]\n"
"MM v[136:139], a[56:59], a[100:103], v[136:139]\n"
"MM v[140:143], a[44:47], a[64:67], v[140:143]\n"
"MM v[140:143], a[60:63], a[84:87], v[140:143]\n"
"MM v[144:147], a[44:47], a[68:71], v[144:147]\n"
"MM v[144:147], a[60:63], a[88:91], v[144:147]\n"
"MM v[148:151], a[44:47], a[72:75], v[148:151]\n"
"MM v[148:151], a[60:63], a[92:95], v[148:151]\n"
"MM v[152:155], a[44:47], a[76:79], v[152:155]\n"
"MM v[152:155], a[60:63], a[96:99], v[152:155]\n"
"MM v[156:159], a[44:47], a[80:83], v[156:159]\n"
"MM v[156:159], a[60:63], a[100:103], v[156:159]\n"
"s_waitcnt lgkmcnt(0)\n"
"s_barrier\n"
"MM v[0:3], a[0:3], a[104:107], v[0:3]\n"
"ds_read_b64_tr_b16 a[32:33], v160 offset:49152\n"
"ds_read_b64_tr_b16 a[34:35], v160 offset:50176\n"
"MM v[0:3], a[16:19], a[124:127], v[0:3]\n"
"s_add_u32 m0, s66, 20480\n"
"ds_read_b64_tr_b16 a[48:49], v160 offset:53248\n"
"MM v[4:7], a[0:3], a[108:111], v[4:7]\n"
"buffer_load_dwordx4 v164, s[36:39], s67 offen lds\n"
"MM v[4:7], a[16:19], a[128:131], v[4:7]\n"
"ds_read_b64_tr_b16 a[50:51], v160 offset:54272\n"
"s_add_u32 m0, s66, 21504\n"
"MM v[8:11], a[0:3], a[112:115], v[8:11]\n"
"ds_read_b64_tr_b16 a[36:37], v161 offset:49152\n"
"ds_read_b64_tr_b16 a[38:39], v161 offset:50176\n"
"MM v[8:11], a[16:19], a[132:135], v[8:11]\n"
"buffer_load_dwordx4 v165, s[36:39], s67 offen lds\n"
"MM v[12:15], a[0:3], a[116:119], v[12:15]\n"
"ds_read_b64_tr_b16 a[52:53], v161 offset:53248\n"
"s_add_u32 m0, s66, 22528\n"
"MM v[12:15], a[16:19], a[136:139], v[12:15]\n"
"ds_read_b64_tr_b16 a[54:55], v161 offset:54272\n"
"ds_read_b64_tr_b16 a[40:41], v160 offset:49664\n"
"MM v[16:19], a[0:3], a[120:123], v[16:19]\n"
"buffer_load_dwordx4 v166, s[36:39], s67 offen lds\n"
"MM v[16:19], a[16:19], a[140:143], v[16:19]\n"
"ds_read_b64_tr_b16 a[42:43], v160 offset:50688\n"
"MM v[20:23], a[4:7], a[104:107], v[20:23]\n"
"s_add_u32 m0, s66, 23552\n"
"ds_read_b64_tr_b16 a[56:57], v160 offset:53760\n"
"MM v[20:23], a[20:23], a[124:127], v[20:23]\n"
"buffer_load_dwordx4 v167, s[36:39], s67 offen lds\n"
"MM v[24:27], a[4:7], a[108:111], v[24:27]\n"
"ds_read_b64_tr_b16 a[58:59], v160 offset:54784\n"
"MM v[24:27], a[20:23], a[128:131], v[24:27]\n"
"ds_read_b64_tr_b16 a[44:45], v161 offset:49664\n"
"s_add_u32 m0, s66, 24576\n"
"MM v[28:31], a[4:7], a[112:115], v[28:31]\n"
"buffer_load_dwordx4 v168, s[36:39], s67 offen lds\n"
"MM v[28:31], a[20:23], a[132:135], v[28:31]\n"
"ds_read_b64_tr_b16 a[46:47], v161 offset:50688\n"
"MM v[32:35], a[4:7], a[116:119], v[32:35]\n"
"ds_read_b64_tr_b16 a[60:61], v161 offset:53760\n"
"MM v[32:35], a[20:23], a[136:139], v[32:35]\n"
"s_add_u32 m0, s66, 25600\n"
"ds_read_b64_tr_b16 a[62:63], v161 offset:54784\n"
"MM v[36:39], a[4:7], a[120:123], v[36:39]\n"
"buffer_load_dwordx4 v169, s[36:39], s67 offen lds\n"
"MM v[36:39], a[20:23], a[140:143], v[36:39]\n"
"MM v[40:43], a[8:11], a[104:107], v[40:43]\n"
"MM v[40:43], a[24:27], a[124:127], v[40:43]\n"
"s_add_u32 m0, s66, 26624\n"
"MM v[44:47], a[8:11], a[108:111], v[44:47]\n"
"buffer_load_dwordx4 v170, s[36:39], s67 offen lds\n"
"MM v[44:47], a[24:27], a[128:131], v[44:47]\n"
"MM v[48:51], a[8:11], a[112:115], v[48:51]\n"
"MM v[48:51], a[24:27], a[132:135], v[48:51]\n"
"s_add_u32 m0, s66, 27648\n"
"MM v[52:55], a[8:11], a[116:119], v[52:55]\n"
"buffer_load_dwordx4 v171, s[36:39], s67 offen lds\n"
"MM v[52:55], a[24:27], a[136:139], v[52:55]\n"
"MM v[56:59], a[8:11], a[120:123], v[56:59]\n"
"s_add_u32 m0, s66, 28672\n"
"MM v[56:59], a[24:27], a[140:143], v[56:59]\n"
"buffer_load_dwordx4 v172, s[36:39], s67 offen lds\n"
"MM v[60:63], a[12:15], a[104:107], v[60:63]\n"
"MM v[60:63], a[28:31], a[124:127], v[60:63]\n"
"MM v[64:67], a[12:15], a[108:111], v[64:67]\n"
"s_add_u32 m0, s66, 29696\n"
"MM v[64:67], a[28:31], a[128:131], v[64:67]\n"
"buffer_load_dwordx4 v173, s[36:39], s67 offen lds\n"
"MM v[68:71], a[12:15], a[112:115], v[68:71]\n"
"MM v[68:71], a[28:31], a[132:135], v[68:71]\n"
"MM v[72:75], a[12:15], a[116:119], v[72:75]\n"
"s_add_u32 s67, s67, 128\n"
"MM v[72:75], a[28:31], a[136:139], v[72:75]\n"
"MM v[76:79], a[12:15], a[120:123], v[76:79]\n"
"MM v[76:79], a[28:31], a[140:143], v[76:79]\n"
"s_waitcnt vmcnt(18)\n"
"s_waitcnt lgkmcnt(0)\n"
"s_barrier\n"
"MM v[80:83], a[32:35], a[104:107], v[80:83]\n"
"ds_read_b64_tr_b16 a[0:1], v160 offset:0\n"
"ds_read_b64_tr_b16 a[2:3], v160 offset:1024\n"
"MM v[80:83], a[48:51], a[124:127], v[80:83]\n"
"s_add_u32 s71, s94, 0\n"
"ds_read_b64_tr_b16 a[16:17], v160 offset:4096\n"
"MM v[84:87], a[32:35], a[108:111], v[84:87]\n"
"s_and_b32 s71, s71, 0x1ffff\n"
"ds_read_b64_tr_b16 a[18:19], v160 offset:5120\n"
"MM v[84:87], a[48:51], a[128:131], v[84:87]\n"
"s_add_u32 m0, s70, 32768\n"
"ds_read_b128 a[64:67], v162 offset:0\n"
"MM v[88:91], a[32:35], a[112:115], v[88:91]\n"
"ds_read_b128 a[84:87], v163 offset:0\n"
"ds_read_b128 a[68:71], v162 offset:2048\n"
"MM v[88:91], a[48:51], a[132:135], v[88:91]\n"
"buffer_load_dwordx4 v249, s[48:51], s71 offen nt lds\n"
"MM v[92:95], a[32:35], a[116:119], v[92:95]\n"
"ds_read_b128 a[88:91], v163 offset:2048\n"
"ds_read_b128 a[72:75], v162 offset:4096\n"
"MM v[92:95], a[48:51], a[136:139], v[92:95]\n"
"ds_read_b128 a[92:95], v163 offset:4096\n"
"MM v[96:99], a[32:35], a[120:123], v[96:99]\n"
"ds_read_b128 a[76:79], v162 offset:6144\n"
"MM v[96:99], a[48:51], a[140:143], v[96:99]\n"
"ds_read_b128 a[96:99], v163 offset:6144\n"
"MM v[100:103], a[36:39], a[104:107], v[100:103]\n"
"buffer_load_dwordx4 v251, s[48:51], s71 offen offset:1024 nt lds\n"
"MM v[100:103], a[52:55], a[124:127], v[100:103]\n"
"ds_read_b128 a[80:83], v162 offset:8192\n"
"ds_read_b128 a[100:103], v163 offset:8192\n"
"MM v[104:107], a[36:39], a[108:111], v[104:107]\n"
"ds_read_b64_tr_b16 a[4:5], v161 offset:0\n"
"MM v[104:107], a[52:55], a[128:131], v[104:107]\n"
"ds_read_b64_tr_b16 a[6:7], v161 offset:1024\n"
"MM v[108:111], a[36:39], a[112:115], v[108:111]\n"
"ds_read_b64_tr_b16 a[20:21], v161 offset:4096\n"
"MM v[108:111], a[52:55], a[132:135], v[108:111]\n"
"MM v[112:115], a[36:39], a[116:119], v[112:115]\n"
"ds_read_b64_tr_b16 a[22:23], v161 offset:5120\n"
"MM v[112:115], a[52:55], a[136:139], v[112:115]\n"
"ds_read_b64_tr_b16 a[8:9], v160 offset:512\n"
"MM v[116:119], a[36:39], a[120:123], v[116:119]\n"
"buffer_load_dwordx4 v249, s[48:51], s71 offen offset:2048 nt lds\n"
"MM v[116:119], a[52:55], a[140:143], v[116:119]\n"
"ds_read_b64_tr_b16 a[10:11], v160 offset:1536\n"
"ds_read_b64_tr_b16 a[24:25], v160 offset:4608\n"
"MM v[120:123], a[40:43], a[104:107], v[120:123]\n"
"ds_read_b64_tr_b16 a[26:27], v160 offset:5632\n"
"MM v[120:123], a[56:59], a[124:127], v[120:123]\n"
"ds_read_b64_tr_b16 a[12:13], v161 offset:512\n"
"MM v[124:127], a[40:43], a[108:111], v[124:127]\n"
"MM v[124:127], a[56:59], a[128:131], v[124:127]\n"
"ds_read_b64_tr_b16 a[14:15], v161 offset:1536\n"
"MM v[128:131], a[40:43], a[112:115], v[128:131]\n"
"ds_read_b64_tr_b16 a[28:29], v161 offset:4608\n"
"MM v[128:131], a[56:59], a[132:135], v[128:131]\n"
"ds_read_b64_tr_b16 a[30:31], v161 offset:5632\n"
"MM v[132:135], a[40:43], a[116:119], v[132:135]\n"
"buffer_load_dwordx4 v251, s[48:51], s71 offen offset:3072 nt lds\n"
"MM v[132:135], a[56:59], a[136:139], v[132:135]\n"
"MM v[136:139], a[40:43], a[120:123], v[136:139]\n"
"MM v[136:139], a[56:59], a[140:143], v[136:139]\n"
"MM v[140:143], a[44:47], a[104:107], v[140:143]\n"
"MM v[140:143], a[60:63], a[124:127], v[140:143]\n"
"MM v[144:147], a[44:47], a[108:111], v[144:147]\n"
"MM v[144:147], a[60:63], a[128:131], v[144:147]\n"
"MM v[148:151], a[44:47], a[112:115], v[148:151]\n"
"MM v[148:151], a[60:63], a[132:135], v[148:151]\n"
"MM v[152:155], a[44:47], a[116:119], v[152:155]\n"
"MM v[152:155], a[60:63], a[136:139], v[152:155]\n"
"MM v[156:159], a[44:47], a[120:123], v[156:159]\n"
"MM v[156:159], a[60:63], a[140:143], v[156:159]\n"
"s_waitcnt lgkmcnt(0)\n"
"s_barrier\n"
"MM v[0:3], a[0:3], a[64:67], v[0:3]\n"
"ds_read_b64_tr_b16 a[32:33], v160 offset:16384\n"
"ds_read_b64_tr_b16 a[34:35], v160 offset:17408\n"
"MM v[0:3], a[16:19], a[84:87], v[0:3]\n"
"s_add_u32 s71, s94, 16384\n"
"s_and_b32 s71, s71, 0x1ffff\n"
"MM v[4:7], a[0:3], a[68:71], v[4:7]\n"
"ds_read_b64_tr_b16 a[48:49], v160 offset:20480\n"
"ds_read_b64_tr_b16 a[50:51], v160 offset:21504\n"
"MM v[4:7], a[16:19], a[88:91], v[4:7]\n"
"s_add_u32 m0, s70, 49152\n"
"ds_read_b64_tr_b16 a[36:37], v161 offset:16384\n"
"MM v[8:11], a[0:3], a[72:75], v[8:11]\n"
"buffer_load_dwordx4 v249, s[44:47], s71 offen nt lds\n"
"MM v[8:11], a[16:19], a[92:95], v[8:11]\n"
"ds_read_b64_tr_b16 a[38:39], v161 offset:17408\n"
"ds_read_b64_tr_b16 a[52:53], v161 offset:20480\n"
"MM v[12:15], a[0:3], a[76:79], v[12:15]\n"
"ds_read_b64_tr_b16 a[54:55], v161 offset:21504\n"
"MM v[12:15], a[16:19], a[96:99], v[12:15]\n"
"ds_read_b64_tr_b16 a[40:41], v160 offset:16896\n"
"MM v[16:19], a[0:3], a[80:83], v[16:19]\n"
"ds_read_b64_tr_b16 a[42:43], v160 offset:17920\n"
"MM v[16:19], a[16:19], a[100:103], v[16:19]\n"
"buffer_load_dwordx4 v252, s[44:47], s71 offen offset:1024 nt lds\n"
"MM v[20:23], a[4:7], a[64:67], v[20:23]\n"
"ds_read_b64_tr_b16 a[56:57], v160 offset:20992\n"
"MM v[20:23], a[20:23], a[84:87], v[20:23]\n"
"ds_read_b64_tr_b16 a[58:59], v160 offset:22016\n"
"MM v[24:27], a[4:7], a[68:71], v[24:27]\n"
"MM v[24:27], a[20:23], a[88:91], v[24:27]\n"
"ds_read_b64_tr_b16 a[44:45], v161 offset:16896\n"
"MM v[28:31], a[4:7], a[72:75], v[28:31]\n"
"ds_read_b64_tr_b16 a[46:47], v161 offset:17920\n"
"MM v[28:31], a[20:23], a[92:95], v[28:31]\n"
"MM v[32:35], a[4:7], a[76:79], v[32:35]\n"
"ds_read_b64_tr_b16 a[60:61], v161 offset:20992\n"
"MM v[32:35], a[20:23], a[96:99], v[32:35]\n"
"buffer_load_dwordx4 v251, s[44:47], s71 offen offset:2048 nt lds\n"
"MM v[36:39], a[4:7], a[80:83], v[36:39]\n"
"ds_read_b64_tr_b16 a[62:63], v161 offset:22016\n"
"MM v[36:39], a[20:23], a[100:103], v[36:39]\n"
"MM v[40:43], a[8:11], a[64:67], v[40:43]\n"
"MM v[40:43], a[24:27], a[84:87], v[40:43]\n"
"MM v[44:47], a[8:11], a[68:71], v[44:47]\n"
"MM v[44:47], a[24:27], a[88:91], v[44:47]\n"
"MM v[48:51], a[8:11], a[72:75], v[48:51]\n"
"MM v[48:51], a[24:27], a[92:95], v[48:51]\n"
"buffer_load_dwordx4 v250, s[44:47], s71 offen offset:3072 nt lds\n"
"MM v[52:55], a[8:11], a[76:79], v[52:55]\n"
"MM v[52:55], a[24:27], a[96:99], v[52:55]\n"
"MM v[56:59], a[8:11], a[80:83], v[56:59]\n"
"MM v[56:59], a[24:27], a[100:103], v[56:59]\n"
"MM v[60:63], a[12:15], a[64:67], v[60:63]\n"
"MM v[60:63], a[28:31], a[84:87], v[60:63]\n"
"MM v[64:67], a[12:15], a[68:71], v[64:67]\n"
"MM v[64:67], a[28:31], a[88:91], v[64:67]\n"
"MM v[68:71], a[12:15], a[72:75], v[68:71]\n"
"MM v[68:71], a[28:31], a[92:95], v[68:71]\n"
"MM v[72:75], a[12:15], a[76:79], v[72:75]\n"
"MM v[72:75], a[28:31], a[96:99], v[72:75]\n"
"MM v[76:79], a[12:15], a[80:83], v[76:79]\n"
"MM v[76:79], a[28:31], a[100:103], v[76:79]\n"
"s_waitcnt vmcnt(8)\n"
"s_waitcnt lgkmcnt(0)\n"
"s_barrier\n"
"MM v[80:83], a[32:35], a[64:67], v[80:83]\n"
"ds_read_b64_tr_b16 a[0:1], v160 offset:32768\n"
"ds_read_b64_tr_b16 a[2:3], v160 offset:33792\n"
"MM v[80:83], a[48:51], a[84:87], v[80:83]\n"
"s_add_u32 s71, s94, 8192\n"
"ds_read_b64_tr_b16 a[16:17], v160 offset:36864\n"
"MM v[84:87], a[32:35], a[68:71], v[84:87]\n"
"s_and_b32 s71, s71, 0x1ffff\n"
"ds_read_b64_tr_b16 a[18:19], v160 offset:37888\n"
"MM v[84:87], a[48:51], a[88:91], v[84:87]\n"
"s_add_u32 m0, s70, 114688\n"
"ds_read_b128 a[104:107], v162 offset:20480\n"
"MM v[88:91], a[32:35], a[72:75], v[88:91]\n"
"ds_read_b128 a[124:127], v163 offset:20480\n"
"ds_read_b128 a[108:111], v162 offset:22528\n"
"MM v[88:91], a[48:51], a[92:95], v[88:91]\n"
"buffer_load_dwordx4 v249, s[48:51], s71 offen nt lds\n"
"MM v[92:95], a[32:35], a[76:79], v[92:95]\n"
"ds_read_b128 a[128:131], v163 offset:22528\n"
"ds_read_b128 a[112:115], v162 offset:24576\n"
"MM v[92:95], a[48:51], a[96:99], v[92:95]\n"
"ds_read_b128 a[132:135], v163 offset:24576\n"
"MM v[96:99], a[32:35], a[80:83], v[96:99]\n"
"ds_read_b128 a[116:119], v162 offset:26624\n"
"MM v[96:99], a[48:51], a[100:103], v[96:99]\n"
"ds_read_b128 a[136:139], v163 offset:26624\n"
"MM v[100:103], a[36:39], a[64:67], v[100:103]\n"
"buffer_load_dwordx4 v251, s[48:51], s71 offen offset:1024 nt lds\n"
"MM v[100:103], a[52:55], a[84:87], v[100:103]\n"
"ds_read_b128 a[120:123], v162 offset:28672\n"
"ds_read_b128 a[140:143], v163 offset:28672\n"
"MM v[104:107], a[36:39], a[68:71], v[104:107]\n"
"ds_read_b64_tr_b16 a[4:5], v161 offset:32768\n"
"MM v[104:107], a[52:55], a[88:91], v[104:107]\n"
"ds_read_b64_tr_b16 a[6:7], v161 offset:33792\n"
"MM v[108:111], a[36:39], a[72:75], v[108:111]\n"
"ds_read_b64_tr_b16 a[20:21], v161 offset:36864\n"
"MM v[108:111], a[52:55], a[92:95], v[108:111]\n"
"MM v[112:115], a[36:39], a[76:79], v[112:115]\n"
"ds_read_b64_tr_b16 a[22:23], v161 offset:37888\n"
"MM v[112:115], a[52:55], a[96:99], v[112:115]\n"
"ds_read_b64_tr_b16 a[8:9], v160 offset:33280\n"
"MM v[116:119], a[36:39], a[80:83], v[116:119]\n"
"buffer_load_dwordx4 v249, s[48:51], s71 offen offset:2048 nt lds\n"
"MM v[116:119], a[52:55], a[100:103], v[116:119]\n"
"ds_read_b64_tr_b16 a[10:11], v160 offset:34304\n"
"ds_read_b64_tr_b16 a[24:25], v160 offset:37376\n"
"MM v[120:123], a[40:43], a[64:67], v[120:123]\n"
"ds_read_b64_tr_b16 a[26:27], v160 offset:38400\n"
"MM v[120:123], a[56:59], a[84:87], v[120:123]\n"
"ds_read_b64_tr_b16 a[12:13], v161 offset:33280\n"
"MM v[124:127], a[40:43], a[68:71], v[124:127]\n"
"MM v[124:127], a[56:59], a[88:91], v[124:127]\n"
"ds_read_b64_tr_b16 a[14:15], v161 offset:34304\n"
"MM v[128:131], a[40:43], a[72:75], v[128:131]\n"
"ds_read_b64_tr_b16 a[28:29], v161 offset:37376\n"
"MM v[128:131], a[56:59], a[92:95], v[128:131]\n"
"ds_read_b64_tr_b16 a[30:31], v161 offset:38400\n"
"MM v[132:135], a[40:43], a[76:79], v[132:135]\n"
"buffer_load_dwordx4 v251, s[48:51], s71 offen offset:3072 nt lds\n"
"MM v[132:135], a[56:59], a[96:99], v[132:135]\n"
"MM v[136:139], a[40:43], a[80:83], v[136:139]\n"
"MM v[136:139], a[56:59], a[100:103], v[136:139]\n"
"MM v[140:143], a[44:47], a[64:67], v[140:143]\n"
"MM v[140:143], a[60:63], a[84:87], v[140:143]\n"
"MM v[144:147], a[44:47], a[68:71], v[144:147]\n"
"MM v[144:147], a[60:63], a[88:91], v[144:147]\n"
"MM v[148:151], a[44:47], a[72:75], v[148:151]\n"
"MM v[148:151], a[60:63], a[92:95], v[148:151]\n"
"MM v[152:155], a[44:47], a[76:79], v[152:155]\n"
"MM v[152:155], a[60:63], a[96:99], v[152:155]\n"
"MM v[156:159], a[44:47], a[80:83], v[156:159]\n"
"MM v[156:159], a[60:63], a[100:103], v[156:159]\n"
"s_waitcnt lgkmcnt(0)\n"
"s_barrier\n"
"MM v[0:3], a[0:3], a[104:107], v[0:3]\n"
"ds_read_b64_tr_b16 a[32:33], v160 offset:49152\n"
"ds_read_b64_tr_b16 a[34:35], v160 offset:50176\n"
"MM v[0:3], a[16:19], a[124:127], v[0:3]\n"
"s_add_u32 s71, s94, 24576\n"
"s_and_b32 s71, s71, 0x1ffff\n"
"MM v[4:7], a[0:3], a[108:111], v[4:7]\n"
"ds_read_b64_tr_b16 a[48:49], v160 offset:53248\n"
"ds_read_b64_tr_b16 a[50:51], v160 offset:54272\n"
"MM v[4:7], a[16:19], a[128:131], v[4:7]\n"
"s_add_u32 m0, s70, 65536\n"
"ds_read_b64_tr_b16 a[36:37], v161 offset:49152\n"
"MM v[8:11], a[0:3], a[112:115], v[8:11]\n"
"buffer_load_dwordx4 v249, s[44:47], s71 offen nt lds\n"
"MM v[8:11], a[16:19], a[132:135], v[8:11]\n"
"ds_read_b64_tr_b16 a[38:39], v161 offset:50176\n"
"ds_read_b64_tr_b16 a[52:53], v161 offset:53248\n"
"MM v[12:15], a[0:3], a[116:119], v[12:15]\n"
"ds_read_b64_tr_b16 a[54:55], v161 offset:54272\n"
"MM v[12:15], a[16:19], a[136:139], v[12:15]\n"
"ds_read_b64_tr_b16 a[40:41], v160 offset:49664\n"
"MM v[16:19], a[0:3], a[120:123], v[16:19]\n"
"ds_read_b64_tr_b16 a[42:43], v160 offset:50688\n"
"MM v[16:19], a[16:19], a[140:143], v[16:19]\n"
"buffer_load_dwordx4 v252, s[44:47], s71 offen offset:1024 nt lds\n"
"MM v[20:23], a[4:7], a[104:107], v[20:23]\n"
"ds_read_b64_tr_b16 a[56:57], v160 offset:53760\n"
"MM v[20:23], a[20:23], a[124:127], v[20:23]\n"
"ds_read_b64_tr_b16 a[58:59], v160 offset:54784\n"
"MM v[24:27], a[4:7], a[108:111], v[24:27]\n"
"MM v[24:27], a[20:23], a[128:131], v[24:27]\n"
"ds_read_b64_tr_b16 a[44:45], v161 offset:49664\n"
"MM v[28:31], a[4:7], a[112:115], v[28:31]\n"
"ds_read_b64_tr_b16 a[46:47], v161 offset:50688\n"
"MM v[28:31], a[20:23], a[132:135], v[28:31]\n"
"MM v[32:35], a[4:7], a[116:119], v[32:35]\n"
"ds_read_b64_tr_b16 a[60:61], v161 offset:53760\n"
"MM v[32:35], a[20:23], a[136:139], v[32:35]\n"
"buffer_load_dwordx4 v251, s[44:47], s71 offen offset:2048 nt lds\n"
"MM v[36:39], a[4:7], a[120:123], v[36:39]\n"
"ds_read_b64_tr_b16 a[62:63], v161 offset:54784\n"
"MM v[36:39], a[20:23], a[140:143], v[36:39]\n"
"MM v[40:43], a[8:11], a[104:107], v[40:43]\n"
"MM v[40:43], a[24:27], a[124:127], v[40:43]\n"
"MM v[44:47], a[8:11], a[108:111], v[44:47]\n"
"MM v[44:47], a[24:27], a[128:131], v[44:47]\n"
"MM v[48:51], a[8:11], a[112:115], v[48:51]\n"
"MM v[48:51], a[24:27], a[132:135], v[48:51]\n"
"buffer_load_dwordx4 v250, s[44:47], s71 offen offset:3072 nt lds\n"
"MM v[52:55], a[8:11], a[116:119], v[52:55]\n"
"MM v[52:55], a[24:27], a[136:139], v[52:55]\n"
"MM v[56:59], a[8:11], a[120:123], v[56:59]\n"
"MM v[56:59], a[24:27], a[140:143], v[56:59]\n"
"MM v[60:63], a[12:15], a[104:107], v[60:63]\n"
"MM v[60:63], a[28:31], a[124:127], v[60:63]\n"
"MM v[64:67], a[12:15], a[108:111], v[64:67]\n"
"MM v[64:67], a[28:31], a[128:131], v[64:67]\n"
"MM v[68:71], a[12:15], a[112:115], v[68:71]\n"
"MM v[68:71], a[28:31], a[132:135], v[68:71]\n"
"MM v[72:75], a[12:15], a[116:119], v[72:75]\n"
"MM v[72:75], a[28:31], a[136:139], v[72:75]\n"
"MM v[76:79], a[12:15], a[120:123], v[76:79]\n"
"MM v[76:79], a[28:31], a[140:143], v[76:79]\n"
"s_waitcnt lgkmcnt(0)\n"
"s_barrier\n"
"MM v[80:83], a[32:35], a[104:107], v[80:83]\n"
"v_mul_f32 v0, s78, v0\n"
"v_mul_f32 v1, s78, v1\n"
"MM v[80:83], a[48:51], a[124:127], v[80:83]\n"
"v_mul_f32 v2, s78, v2\n"
"v_mul_f32 v3, s78, v3\n"
"MM v[84:87], a[32:35], a[108:111], v[84:87]\n"
"v_mul_f32 v20, s78, v20\n"
"s_add_u32 s71, s94, 16384\n"
"MM v[84:87], a[48:51], a[128:131], v[84:87]\n"
"v_mul_f32 v21, s78, v21\n"
"v_mul_f32 v22, s78, v22\n"
"MM v[88:91], a[32:35], a[112:115], v[88:91]\n"
"v_mul_f32 v23, s78, v23\n"
"v_cvt_pk_bf16_f32 v160, v0, v1\n"
"MM v[88:91], a[48:51], a[132:135], v[88:91]\n"
"v_cvt_pk_bf16_f32 v161, v2, v3\n"
"s_and_b32 s71, s71, 0x1ffff\n"
"MM v[92:95], a[32:35], a[116:119], v[92:95]\n"
"v_cvt_pk_bf16_f32 v162, v20, v21\n"
"v_cvt_pk_bf16_f32 v163, v22, v23\n"
"MM v[92:95], a[48:51], a[136:139], v[92:95]\n"
"v_mul_f32 v4, s78, v4\n"
"s_add_u32 m0, s70, 81920\n"
"MM v[96:99], a[32:35], a[120:123], v[96:99]\n"
"v_mul_f32 v5, s78, v5\n"
"v_mul_f32 v6, s78, v6\n"
"MM v[96:99], a[48:51], a[140:143], v[96:99]\n"
"v_mul_f32 v7, s78, v7\n"
"v_mul_f32 v24, s78, v24\n"
"MM v[100:103], a[36:39], a[104:107], v[100:103]\n"
"v_mul_f32 v25, s78, v25\n"
"v_mul_f32 v26, s78, v26\n"
"MM v[100:103], a[52:55], a[124:127], v[100:103]\n"
"v_mul_f32 v27, s78, v27\n"
"v_cvt_pk_bf16_f32 v164, v4, v5\n"
"MM v[104:107], a[36:39], a[108:111], v[104:107]\n"
"buffer_load_dwordx4 v249, s[48:51], s71 offen nt lds\n"
"MM v[104:107], a[52:55], a[128:131], v[104:107]\n"
"v_cvt_pk_bf16_f32 v165, v6, v7\n"
"v_cvt_pk_bf16_f32 v166, v24, v25\n"
"MM v[108:111], a[36:39], a[112:115], v[108:111]\n"
"v_cvt_pk_bf16_f32 v167, v26, v27\n"
"v_mul_f32 v8, s78, v8\n"
"MM v[108:111], a[52:55], a[132:135], v[108:111]\n"
"v_mul_f32 v9, s78, v9\n"
"v_mul_f32 v10, s78, v10\n"
"MM v[112:115], a[36:39], a[116:119], v[112:115]\n"
"v_mul_f32 v11, s78, v11\n"
"v_mul_f32 v28, s78, v28\n"
"MM v[112:115], a[52:55], a[136:139], v[112:115]\n"
"v_mul_f32 v29, s78, v29\n"
"v_mul_f32 v30, s78, v30\n"
"MM v[116:119], a[36:39], a[120:123], v[116:119]\n"
"v_mul_f32 v31, s78, v31\n"
"v_cvt_pk_bf16_f32 v168, v8, v9\n"
"MM v[116:119], a[52:55], a[140:143], v[116:119]\n"
"v_cvt_pk_bf16_f32 v169, v10, v11\n"
"v_cvt_pk_bf16_f32 v170, v28, v29\n"
"MM v[120:123], a[40:43], a[104:107], v[120:123]\n"
"buffer_load_dwordx4 v251, s[48:51], s71 offen offset:1024 nt lds\n"
"MM v[120:123], a[56:59], a[124:127], v[120:123]\n"
"v_cvt_pk_bf16_f32 v171, v30, v31\n"
"v_mul_f32 v12, s78, v12\n"
"MM v[124:127], a[40:43], a[108:111], v[124:127]\n"
"v_mul_f32 v13, s78, v13\n"
"v_mul_f32 v14, s78, v14\n"
"MM v[124:127], a[56:59], a[128:131], v[124:127]\n"
"v_mul_f32 v15, s78, v15\n"
"v_mul_f32 v32, s78, v32\n"
"MM v[128:131], a[40:43], a[112:115], v[128:131]\n"
"v_mul_f32 v33, s78, v33\n"
"v_mul_f32 v34, s78, v34\n"
"MM v[128:131], a[56:59], a[132:135], v[128:131]\n"
"v_mul_f32 v35, s78, v35\n"
"v_cvt_pk_bf16_f32 v172, v12, v13\n"
"MM v[132:135], a[40:43], a[116:119], v[132:135]\n"
"v_cvt_pk_bf16_f32 v173, v14, v15\n"
"v_cvt_pk_bf16_f32 v174, v32, v33\n"
"MM v[132:135], a[56:59], a[136:139], v[132:135]\n"
"v_cvt_pk_bf16_f32 v175, v34, v35\n"
"v_mul_f32 v16, s78, v16\n"
"MM v[136:139], a[40:43], a[120:123], v[136:139]\n"
"v_mul_f32 v17, s78, v17\n"
"v_mul_f32 v18, s78, v18\n"
"MM v[136:139], a[56:59], a[140:143], v[136:139]\n"
"buffer_load_dwordx4 v249, s[48:51], s71 offen offset:2048 nt lds\n"
"MM v[140:143], a[44:47], a[104:107], v[140:143]\n"
"v_mul_f32 v19, s78, v19\n"
"v_mul_f32 v36, s78, v36\n"
"MM v[140:143], a[60:63], a[124:127], v[140:143]\n"
"v_mul_f32 v37, s78, v37\n"
"v_mul_f32 v38, s78, v38\n"
"MM v[144:147], a[44:47], a[108:111], v[144:147]\n"
"v_mul_f32 v39, s78, v39\n"
"v_cvt_pk_bf16_f32 v176, v16, v17\n"
"MM v[144:147], a[60:63], a[128:131], v[144:147]\n"
"v_cvt_pk_bf16_f32 v177, v18, v19\n"
"v_cvt_pk_bf16_f32 v178, v36, v37\n"
"MM v[148:151], a[44:47], a[112:115], v[148:151]\n"
"v_cvt_pk_bf16_f32 v179, v38, v39\n"
"v_mul_f32 v40, s78, v40\n"
"MM v[148:151], a[60:63], a[132:135], v[148:151]\n"
"v_mul_f32 v41, s78, v41\n"
"v_mul_f32 v42, s78, v42\n"
"MM v[152:155], a[44:47], a[116:119], v[152:155]\n"
"v_mul_f32 v43, s78, v43\n"
"v_mul_f32 v60, s78, v60\n"
"MM v[152:155], a[60:63], a[136:139], v[152:155]\n"
"v_mul_f32 v61, s78, v61\n"
"v_mul_f32 v62, s78, v62\n"
"MM v[156:159], a[44:47], a[120:123], v[156:159]\n"
"buffer_load_dwordx4 v251, s[48:51], s71 offen offset:3072 nt lds\n"
"v_mul_f32 v63, s78, v63\n"
"MM v[156:159], a[60:63], a[140:143], v[156:159]\n"
"v_cvt_pk_bf16_f32 v180, v40, v41\n"
"v_cvt_pk_bf16_f32 v181, v42, v43\n"
"v_cvt_pk_bf16_f32 v182, v60, v61\n"
"v_cvt_pk_bf16_f32 v183, v62, v63\n"
"s_branch K1_EATT\n"
"K1_EX3:\n"
"s_nop 3\n"
"s_waitcnt lgkmcnt(0)\n"
"s_barrier\n"
"MM v[0:3], a[0:3], a[104:107], v[0:3]\n"
"ds_read_b64_tr_b16 a[32:33], v160 offset:49152\n"
"ds_read_b64_tr_b16 a[34:35], v160 offset:50176\n"
"MM v[0:3], a[16:19], a[124:127], v[0:3]\n"
"ds_read_b64_tr_b16 a[48:49], v160 offset:53248\n"
"ds_read_b64_tr_b16 a[50:51], v160 offset:54272\n"
"MM v[4:7], a[0:3], a[108:111], v[4:7]\n"
"s_add_u32 m0, s66, 30720\n"
"ds_read_b64_tr_b16 a[36:37], v161 offset:49152\n"
"MM v[4:7], a[16:19], a[128:131], v[4:7]\n"
"buffer_load_dwordx4 v164, s[36:39], s67 offen lds\n"
"MM v[8:11], a[0:3], a[112:115], v[8:11]\n"
"s_add_u32 m0, s66, 31744\n"
"ds_read_b64_tr_b16 a[38:39], v161 offset:50176\n"
"MM v[8:11], a[16:19], a[132:135], v[8:11]\n"
"buffer_load_dwordx4 v165, s[36:39], s67 offen lds\n"
"MM v[12:15], a[0:3], a[116:119], v[12:15]\n"
"s_add_u32 m0, s66, 32768\n"
"ds_read_b64_tr_b16 a[52:53], v161 offset:53248\n"
"MM v[12:15], a[16:19], a[136:139], v[12:15]\n"
"buffer_load_dwordx4 v166, s[36:39], s67 offen lds\n"
"MM v[16:19], a[0:3], a[120:123], v[16:19]\n"
"ds_read_b64_tr_b16 a[54:55], v161 offset:54272\n"
"s_add_u32 m0, s66, 33792\n"
"MM v[16:19], a[16:19], a[140:143], v[16:19]\n"
"buffer_load_dwordx4 v167, s[36:39], s67 offen lds\n"
"MM v[20:23], a[4:7], a[104:107], v[20:23]\n"
"ds_read_b64_tr_b16 a[40:41], v160 offset:49664\n"
"ds_read_b64_tr_b16 a[42:43], v160 offset:50688\n"
"MM v[20:23], a[20:23], a[124:127], v[20:23]\n"
"s_add_u32 m0, s66, 34816\n"
"ds_read_b64_tr_b16 a[56:57], v160 offset:53760\n"
"MM v[24:27], a[4:7], a[108:111], v[24:27]\n"
"buffer_load_dwordx4 v168, s[36:39], s67 offen lds\n"
"MM v[24:27], a[20:23], a[128:131], v[24:27]\n"
"ds_read_b64_tr_b16 a[58:59], v160 offset:54784\n"
"s_add_u32 m0, s66, 35840\n"
"MM v[28:31], a[4:7], a[112:115], v[28:31]\n"
"buffer_load_dwordx4 v169, s[36:39], s67 offen lds\n"
"MM v[28:31], a[20:23], a[132:135], v[28:31]\n"
"ds_read_b64_tr_b16 a[44:45], v161 offset:49664\n"
"ds_read_b64_tr_b16 a[46:47], v161 offset:50688\n"
"MM v[32:35], a[4:7], a[116:119], v[32:35]\n"
"s_add_u32 m0, s66, 36864\n"
"ds_read_b64_tr_b16 a[60:61], v161 offset:53760\n"
"MM v[32:35], a[20:23], a[136:139], v[32:35]\n"
"buffer_load_dwordx4 v170, s[36:39], s67 offen lds\n"
"MM v[36:39], a[4:7], a[120:123], v[36:39]\n"
"ds_read_b64_tr_b16 a[62:63], v161 offset:54784\n"
"MM v[36:39], a[20:23], a[140:143], v[36:39]\n"
"s_add_u32 m0, s66, 37888\n"
"MM v[40:43], a[8:11], a[104:107], v[40:43]\n"
"buffer_load_dwordx4 v171, s[36:39], s67 offen lds\n"
"MM v[40:43], a[24:27], a[124:127], v[40:43]\n"
"s_add_u32 m0, s66, 38912\n"
"MM v[44:47], a[8:11], a[108:111], v[44:47]\n"
"buffer_load_dwordx4 v172, s[36:39], s67 offen lds\n"
"MM v[44:47], a[24:27], a[128:131], v[44:47]\n"
"MM v[48:51], a[8:11], a[112:115], v[48:51]\n"
"s_add_u32 m0, s66, 39936\n"
"MM v[48:51], a[24:27], a[132:135], v[48:51]\n"
"buffer_load_dwordx4 v173, s[36:39], s67 offen lds\n"
"MM v[52:55], a[8:11], a[116:119], v[52:55]\n"
"MM v[52:55], a[24:27], a[136:139], v[52:55]\n"
"s_add_u32 s67, s67, 128\n"
"s_lshl_b32 s68, s65, 8\n"
"MM v[56:59], a[8:11], a[120:123], v[56:59]\n"
"s_add_u32 s68, s68, 43024384\n"
"v_lshlrev_b32 v182, 2, v253\n"
"MM v[56:59], a[24:27], a[140:143], v[56:59]\n"
"v_add_u32 v182, s68, v182\n"
"v_mov_b32 v183, s89\n"
"MM v[60:63], a[12:15], a[104:107], v[60:63]\n"
"v_mov_b32 v184, s74\n"
"v_cmp_eq_u32 vcc, 8, v253\n"
"MM v[60:63], a[28:31], a[124:127], v[60:63]\n"
"s_nop 0\n"
"v_cndmask_b32 v183, v183, v184, vcc\n"
"v_mov_b32 v184, s88\n"
"MM v[64:67], a[12:15], a[108:111], v[64:67]\n"
"v_cmp_gt_u32 vcc, 8, v253\n"
"MM v[64:67], a[28:31], a[128:131], v[64:67]\n"
"s_nop 0\n"
"v_cndmask_b32 v183, v183, v184, vcc\n"
"v_mov_b32 v184, s73\n"
"MM v[68:71], a[12:15], a[112:115], v[68:71]\n"
"v_cmp_le_u32 vcc, 56, v253\n"
"s_nop 1\n"
"v_cndmask_b32 v183, v183, v184, vcc\n"
"MM v[68:71], a[28:31], a[132:135], v[68:71]\n"
"v_xor_b32 v183, 0x5a3c96e1, v183\n"
"MM v[72:75], a[12:15], a[116:119], v[72:75]\n"
"buffer_load_dword v184, v182, s[56:59], 0 offen sc1\n"
"MM v[72:75], a[28:31], a[136:139], v[72:75]\n"
"MM v[76:79], a[12:15], a[120:123], v[76:79]\n"
"MM v[76:79], a[28:31], a[140:143], v[76:79]\n"
"s_waitcnt vmcnt(16)\n"
"s_waitcnt lgkmcnt(0)\n"
"s_barrier\n"
"MM v[80:83], a[32:35], a[104:107], v[80:83]\n"
"ds_read_b64_tr_b16 a[0:1], v160 offset:0\n"
"ds_read_b64_tr_b16 a[2:3], v160 offset:1024\n"
"MM v[80:83], a[48:51], a[124:127], v[80:83]\n"
"ds_read_b64_tr_b16 a[16:17], v160 offset:4096\n"
"ds_read_b64_tr_b16 a[18:19], v160 offset:5120\n"
"MM v[84:87], a[32:35], a[108:111], v[84:87]\n"
"ds_read_b128 a[64:67], v162 offset:0\n"
"ds_read_b128 a[84:87], v163 offset:0\n"
"MM v[84:87], a[48:51], a[128:131], v[84:87]\n"
"ds_read_b128 a[68:71], v162 offset:2048\n"
"MM v[88:91], a[32:35], a[112:115], v[88:91]\n"
"ds_read_b128 a[88:91], v163 offset:2048\n"
"MM v[88:91], a[48:51], a[132:135], v[88:91]\n"
"ds_read_b128 a[72:75], v162 offset:4096\n"
"MM v[92:95], a[32:35], a[116:119], v[92:95]\n"
"ds_read_b128 a[92:95], v163 offset:4096\n"
"MM v[92:95], a[48:51], a[136:139], v[92:95]\n"
"MM v[96:99], a[32:35], a[120:123], v[96:99]\n"
"ds_read_b128 a[76:79], v162 offset:6144\n"
"MM v[96:99], a[48:51], a[140:143], v[96:99]\n"
"ds_read_b128 a[96:99], v163 offset:6144\n"
"MM v[100:103], a[36:39], a[104:107], v[100:103]\n"
"ds_read_b128 a[80:83], v162 offset:8192\n"
"MM v[100:103], a[52:55], a[124:127], v[100:103]\n"
"ds_read_b128 a[100:103], v163 offset:8192\n"
"MM v[104:107], a[36:39], a[108:111], v[104:107]\n"
"ds_read_b64_tr_b16 a[4:5], v161 offset:0\n"
"MM v[104:107], a[52:55], a[128:131], v[104:107]\n"
"ds_read_b64_tr_b16 a[6:7], v161 offset:1024\n"
"MM v[108:111], a[36:39], a[112:115], v[108:111]\n"
"ds_read_b64_tr_b16 a[20:21], v161 offset:4096\n"
"MM v[108:111], a[52:55], a[132:135], v[108:111]\n"
"MM v[112:115], a[36:39], a[116:119], v[112:115]\n"
"ds_read_b64_tr_b16 a[22:23], v161 offset:5120\n"
"MM v[112:115], a[52:55], a[136:139], v[112:115]\n"
"ds_read_b64_tr_b16 a[8:9], v160 offset:512\n"
"MM v[116:119], a[36:39], a[120:123], v[116:119]\n"
"ds_read_b64_tr_b16 a[10:11], v160 offset:1536\n"
"MM v[116:119], a[52:55], a[140:143], v[116:119]\n"
"ds_read_b64_tr_b16 a[24:25], v160 offset:4608\n"
"MM v[120:123], a[40:43], a[104:107], v[120:123]\n"
"ds_read_b64_tr_b16 a[26:27], v160 offset:5632\n"
"MM v[120:123], a[56:59], a[124:127], v[120:123]\n"
"ds_read_b64_tr_b16 a[12:13], v161 offset:512\n"
"MM v[124:127], a[40:43], a[108:111], v[124:127]\n"
"MM v[124:127], a[56:59], a[128:131], v[124:127]\n"
"ds_read_b64_tr_b16 a[14:15], v161 offset:1536\n"
"MM v[128:131], a[40:43], a[112:115], v[128:131]\n"
"ds_read_b64_tr_b16 a[28:29], v161 offset:4608\n"
"MM v[128:131], a[56:59], a[132:135], v[128:131]\n"
"ds_read_b64_tr_b16 a[30:31], v161 offset:5632\n"
"MM v[132:135], a[40:43], a[116:119], v[132:135]\n"
"MM v[132:135], a[56:59], a[136:139], v[132:135]\n"
"MM v[136:139], a[40:43], a[120:123], v[136:139]\n"
"MM v[136:139], a[56:59], a[140:143], v[136:139]\n"
"MM v[140:143], a[44:47], a[104:107], v[140:143]\n"
"MM v[140:143], a[60:63], a[124:127], v[140:143]\n"
"MM v[144:147], a[44:47], a[108:111], v[144:147]\n"
"MM v[144:147], a[60:63], a[128:131], v[144:147]\n"
"MM v[148:151], a[44:47], a[112:115], v[148:151]\n"
"MM v[148:151], a[60:63], a[132:135], v[148:151]\n"
"MM v[152:155], a[44:47], a[116:119], v[152:155]\n"
"MM v[152:155], a[60:63], a[136:139], v[152:155]\n"
"MM v[156:159], a[44:47], a[120:123], v[156:159]\n"
"MM v[156:159], a[60:63], a[140:143], v[156:159]\n"
"s_waitcnt vmcnt(0)\n"
"s_mov_b32 s90, 262144\n"
"s_mov_b32 s96, 0xffff01ff\n"
"s_mov_b32 s97, 0xff00ffff\n"
"v_cmp_eq_u32 vcc, v184, v183\n"
"s_nop 1\n"
"s_andn2_b64 vcc, s[96:97], vcc\n"
"s_cbranch_scc0 K1_ACKE_OK\n"
"K1_ACKE_L:\n"
"s_nop 4\n"
"buffer_load_dword v184, v182, s[56:59], 0 offen sc1\n"
"s_waitcnt vmcnt(0)\n"
"v_cmp_eq_u32 vcc, v184, v183\n"
"s_nop 1\n"
"s_andn2_b64 vcc, s[96:97], vcc\n"
"s_cbranch_scc0 K1_ACKE_OK\n"
"s_sleep 2\n"
"s_sub_u32 s90, s90, 1\n"
"s_cmp_lg_u32 s90, 0\n"
"s_cbranch_scc1 K1_ACKE_L\n"
"K1_ACKE_OK:\n"
"s_nop 1\n"
"s_waitcnt lgkmcnt(0)\n"
"s_barrier\n"
"s_add_u32 s68, s89, 1\n"
"v_mov_b32 v184, s68\n"
"s_lshl_b32 s68, s65, 7\n"
"s_add_u32 s68, s68, 42991616\n"
"v_mov_b32 v183, 0\n"
"s_mov_b64 exec, 1\n"
"buffer_store_dword v184, v183, s[56:59], s68 offen sc1\n"
"s_mov_b64 exec, -1\n"
"s_nop 3\n"
"MM v[0:3], a[0:3], a[64:67], v[0:3]\n"
"ds_read_b64_tr_b16 a[32:33], v160 offset:16384\n"
"ds_read_b64_tr_b16 a[34:35], v160 offset:17408\n"
"MM v[0:3], a[16:19], a[84:87], v[0:3]\n"
"ds_read_b64_tr_b16 a[48:49], v160 offset:20480\n"
"ds_read_b64_tr_b16 a[50:51], v160 offset:21504\n"
"MM v[4:7], a[0:3], a[68:71], v[4:7]\n"
"s_add_u32 m0, s66, 10240\n"
"ds_read_b64_tr_b16 a[36:37], v161 offset:16384\n"
"MM v[4:7], a[16:19], a[88:91], v[4:7]\n"
"buffer_load_dwordx4 v164, s[36:39], s67 offen lds\n"
"MM v[8:11], a[0:3], a[72:75], v[8:11]\n"
"s_add_u32 m0, s66, 11264\n"
"ds_read_b64_tr_b16 a[38:39], v161 offset:17408\n"
"MM v[8:11], a[16:19], a[92:95], v[8:11]\n"
"buffer_load_dwordx4 v165, s[36:39], s67 offen lds\n"
"MM v[12:15], a[0:3], a[76:79], v[12:15]\n"
"s_add_u32 m0, s66, 12288\n"
"ds_read_b64_tr_b16 a[52:53], v161 offset:20480\n"
"MM v[12:15], a[16:19], a[96:99], v[12:15]\n"
"buffer_load_dwordx4 v166, s[36:39], s67 offen lds\n"
"MM v[16:19], a[0:3], a[80:83], v[16:19]\n"
"ds_read_b64_tr_b16 a[54:55], v161 offset:21504\n"
"s_add_u32 m0, s66, 13312\n"
"MM v[16:19], a[16:19], a[100:103], v[16:19]\n"
"buffer_load_dwordx4 v167, s[36:39], s67 offen lds\n"
"MM v[20:23], a[4:7], a[64:67], v[20:23]\n"
"ds_read_b64_tr_b16 a[40:41], v160 offset:16896\n"
"ds_read_b64_tr_b16 a[42:43], v160 offset:17920\n"
"MM v[20:23], a[20:23], a[84:87], v[20:23]\n"
"s_add_u32 m0, s66, 14336\n"
"ds_read_b64_tr_b16 a[56:57], v160 offset:20992\n"
"MM v[24:27], a[4:7], a[68:71], v[24:27]\n"
"buffer_load_dwordx4 v168, s[36:39], s67 offen lds\n"
"MM v[24:27], a[20:23], a[88:91], v[24:27]\n"
"ds_read_b64_tr_b16 a[58:59], v160 offset:22016\n"
"s_add_u32 m0, s66, 15360\n"
"MM v[28:31], a[4:7], a[72:75], v[28:31]\n"
"buffer_load_dwordx4 v169, s[36:39], s67 offen lds\n"
"MM v[28:31], a[20:23], a[92:95], v[28:31]\n"
"ds_read_b64_tr_b16 a[44:45], v161 offset:16896\n"
"ds_read_b64_tr_b16 a[46:47], v161 offset:17920\n"
"MM v[32:35], a[4:7], a[76:79], v[32:35]\n"
"s_add_u32 m0, s66, 16384\n"
"ds_read_b64_tr_b16 a[60:61], v161 offset:20992\n"
"MM v[32:35], a[20:23], a[96:99], v[32:35]\n"
"buffer_load_dwordx4 v170, s[36:39], s67 offen lds\n"
"MM v[36:39], a[4:7], a[80:83], v[36:39]\n"
"ds_read_b64_tr_b16 a[62:63], v161 offset:22016\n"
"MM v[36:39], a[20:23], a[100:103], v[36:39]\n"
"s_add_u32 m0, s66, 17408\n"
"MM v[40:43], a[8:11], a[64:67], v[40:43]\n"
"buffer_load_dwordx4 v171, s[36:39], s67 offen lds\n"
"MM v[40:43], a[24:27], a[84:87], v[40:43]\n"
"s_add_u32 m0, s66, 18432\n"
"MM v[44:47], a[8:11], a[68:71], v[44:47]\n"
"buffer_load_dwordx4 v172, s[36:39], s67 offen lds\n"
"MM v[44:47], a[24:27], a[88:91], v[44:47]\n"
"MM v[48:51], a[8:11], a[72:75], v[48:51]\n"
"s_add_u32 m0, s66, 19456\n"
"MM v[48:51], a[24:27], a[92:95], v[48:51]\n"
"buffer_load_dwordx4 v173, s[36:39], s67 offen lds\n"
"MM v[52:55], a[8:11], a[76:79], v[52:55]\n"
"MM v[52:55], a[24:27], a[96:99], v[52:55]\n"
"s_add_u32 s67, s67, 128\n"
"s_add_u32 s71, s94, 0\n"
"MM v[56:59], a[8:11], a[80:83], v[56:59]\n"
"s_and_b32 s71, s71, 0x1ffff\n"
"s_add_u32 m0, s70, 0\n"
"MM v[56:59], a[24:27], a[100:103], v[56:59]\n"
"buffer_load_dwordx4 v249, s[44:47], s71 offen nt lds\n"
"MM v[60:63], a[12:15], a[64:67], v[60:63]\n"
"MM v[60:63], a[28:31], a[84:87], v[60:63]\n"
"buffer_load_dwordx4 v252, s[44:47], s71 offen offset:1024 nt lds\n"
"MM v[64:67], a[12:15], a[68:71], v[64:67]\n"
"MM v[64:67], a[28:31], a[88:91], v[64:67]\n"
"buffer_load_dwordx4 v251, s[44:47], s71 offen offset:2048 nt lds\n"
"MM v[68:71], a[12:15], a[72:75], v[68:71]\n"
"MM v[68:71], a[28:31], a[92:95], v[68:71]\n"
"buffer_load_dwordx4 v250, s[44:47], s71 offen offset:3072 nt lds\n"
"MM v[72:75], a[12:15], a[76:79], v[72:75]\n"
"MM v[72:75], a[28:31], a[96:99], v[72:75]\n"
"MM v[76:79], a[12:15], a[80:83], v[76:79]\n"
"MM v[76:79], a[28:31], a[100:103], v[76:79]\n"
"s_waitcnt lgkmcnt(0)\n"
"s_barrier\n"
"MM v[80:83], a[32:35], a[64:67], v[80:83]\n"
"ds_read_b64_tr_b16 a[0:1], v160 offset:32768\n"
"ds_read_b64_tr_b16 a[2:3], v160 offset:33792\n"
"MM v[80:83], a[48:51], a[84:87], v[80:83]\n"
"ds_read_b64_tr_b16 a[16:17], v160 offset:36864\n"
"ds_read_b64_tr_b16 a[18:19], v160 offset:37888\n"
"MM v[84:87], a[32:35], a[68:71], v[84:87]\n"
"s_add_u32 s71, s94, 8192\n"
"s_and_b32 s71, s71, 0x1ffff\n"
"MM v[84:87], a[48:51], a[88:91], v[84:87]\n"
"ds_read_b128 a[104:107], v162 offset:20480\n"
"s_add_u32 m0, s70, 16384\n"
"MM v[88:91], a[32:35], a[72:75], v[88:91]\n"
"ds_read_b128 a[124:127], v163 offset:20480\n"
"ds_read_b128 a[108:111], v162 offset:22528\n"
"MM v[88:91], a[48:51], a[92:95], v[88:91]\n"
"buffer_load_dwordx4 v249, s[44:47], s71 offen nt lds\n"
"MM v[92:95], a[32:35], a[76:79], v[92:95]\n"
"ds_read_b128 a[128:131], v163 offset:22528\n"
"ds_read_b128 a[112:115], v162 offset:24576\n"
"MM v[92:95], a[48:51], a[96:99], v[92:95]\n"
"ds_read_b128 a[132:135], v163 offset:24576\n"
"MM v[96:99], a[32:35], a[80:83], v[96:99]\n"
"ds_read_b128 a[116:119], v162 offset:26624\n"
"MM v[96:99], a[48:51], a[100:103], v[96:99]\n"
"ds_read_b128 a[136:139], v163 offset:26624\n"
"MM v[100:103], a[36:39], a[64:67], v[100:103]\n"
"buffer_load_dwordx4 v252, s[44:47], s71 offen offset:1024 nt lds\n"
"MM v[100:103], a[52:55], a[84:87], v[100:103]\n"
"ds_read_b128 a[120:123], v162 offset:28672\n"
"ds_read_b128 a[140:143], v163 offset:28672\n"
"MM v[104:107], a[36:39], a[68:71], v[104:107]\n"
"ds_read_b64_tr_b16 a[4:5], v161 offset:32768\n"
"MM v[104:107], a[52:55], a[88:91], v[104:107]\n"
"ds_read_b64_tr_b16 a[6:7], v161 offset:33792\n"
"MM v[108:111], a[36:39], a[72:75], v[108:111]\n"
"ds_read_b64_tr_b16 a[20:21], v161 offset:36864\n"
"MM v[108:111], a[52:55], a[92:95], v[108:111]\n"
"MM v[112:115], a[36:39], a[76:79], v[112:115]\n"
"ds_read_b64_tr_b16 a[22:23], v161 offset:37888\n"
"MM v[112:115], a[52:55], a[96:99], v[112:115]\n"
"ds_read_b64_tr_b16 a[8:9], v160 offset:33280\n"
"MM v[116:119], a[36:39], a[80:83], v[116:119]\n"
"buffer_load_dwordx4 v251, s[44:47], s71 offen offset:2048 nt lds\n"
"MM v[116:119], a[52:55], a[100:103], v[116:119]\n"
"ds_read_b64_tr_b16 a[10:11], v160 offset:34304\n"
"ds_read_b64_tr_b16 a[24:25], v160 offset:37376\n"
"MM v[120:123], a[40:43], a[64:67], v[120:123]\n"
"ds_read_b64_tr_b16 a[26:27], v160 offset:38400\n"
"MM v[120:123], a[56:59], a[84:87], v[120:123]\n"
"ds_read_b64_tr_b16 a[12:13], v161 offset:33280\n"
"MM v[124:127], a[40:43], a[68:71], v[124:127]\n"
"MM v[124:127], a[56:59], a[88:91], v[124:127]\n"
"ds_read_b64_tr_b16 a[14:15], v161 offset:34304\n"
"MM v[128:131], a[40:43], a[72:75], v[128:131]\n"
"ds_read_b64_tr_b16 a[28:29], v161 offset:37376\n"
"MM v[128:131], a[56:59], a[92:95], v[128:131]\n"
"ds_read_b64_tr_b16 a[30:31], v161 offset:38400\n"
"MM v[132:135], a[40:43], a[76:79], v[132:135]\n"
"buffer_load_dwordx4 v250, s[44:47], s71 offen offset:3072 nt lds\n"
"MM v[132:135], a[56:59], a[96:99], v[132:135]\n"
"MM v[136:139], a[40:43], a[80:83], v[136:139]\n"
"MM v[136:139], a[56:59], a[100:103], v[136:139]\n"
"MM v[140:143], a[44:47], a[64:67], v[140:143]\n"
"MM v[140:143], a[60:63], a[84:87], v[140:143]\n"
"MM v[144:147], a[44:47], a[68:71], v[144:147]\n"
"MM v[144:147], a[60:63], a[88:91], v[144:147]\n"
"MM v[148:151], a[44:47], a[72:75], v[148:151]\n"
"MM v[148:151], a[60:63], a[92:95], v[148:151]\n"
"MM v[152:155], a[44:47], a[76:79], v[152:155]\n"
"MM v[152:155], a[60:63], a[96:99], v[152:155]\n"
"MM v[156:159], a[44:47], a[80:83], v[156:159]\n"
"MM v[156:159], a[60:63], a[100:103], v[156:159]\n"
"s_waitcnt lgkmcnt(0)\n"
"s_barrier\n"
"MM v[0:3], a[0:3], a[104:107], v[0:3]\n"
"ds_read_b64_tr_b16 a[32:33], v160 offset:49152\n"
"ds_read_b64_tr_b16 a[34:35], v160 offset:50176\n"
"MM v[0:3], a[16:19], a[124:127], v[0:3]\n"
"ds_read_b64_tr_b16 a[48:49], v160 offset:53248\n"
"ds_read_b64_tr_b16 a[50:51], v160 offset:54272\n"
"MM v[4:7], a[0:3], a[108:111], v[4:7]\n"
"s_add_u32 m0, s66, 30720\n"
"ds_read_b64_tr_b16 a[36:37], v161 offset:49152\n"
"MM v[4:7], a[16:19], a[128:131], v[4:7]\n"
"buffer_load_dwordx4 v164, s[36:39], s67 offen lds\n"
"MM v[8:11], a[0:3], a[112:115], v[8:11]\n"
"s_add_u32 m0, s66, 31744\n"
"ds_read_b64_tr_b16 a[38:39], v161 offset:50176\n"
"MM v[8:11], a[16:19], a[132:135], v[8:11]\n"
"buffer_load_dwordx4 v165, s[36:39], s67 offen lds\n"
"MM v[12:15], a[0:3], a[116:119], v[12:15]\n"
"ds_read_b64_tr_b16 a[52:53], v161 offset:53248\n"
"ds_read_b64_tr_b16 a[54:55], v161 offset:54272\n"
"MM v[12:15], a[16:19], a[136:139], v[12:15]\n"
"s_add_u32 m0, s66, 32768\n"
"ds_read_b64_tr_b16 a[40:41], v160 offset:49664\n"
"MM v[16:19], a[0:3], a[120:123], v[16:19]\n"
"buffer_load_dwordx4 v166, s[36:39], s67 offen lds\n"
"MM v[16:19], a[16:19], a[140:143], v[16:19]\n"
"ds_read_b64_tr_b16 a[42:43], v160 offset:50688\n"
"MM v[20:23], a[4:7], a[104:107], v[20:23]\n"
"ds_read_b64_tr_b16 a[56:57], v160 offset:53760\n"
"s_add_u32 m0, s66, 33792\n"
"MM v[20:23], a[20:23], a[124:127], v[20:23]\n"
"buffer_load_dwordx4 v167, s[36:39], s67 offen lds\n"
"MM v[24:27], a[4:7], a[108:111], v[24:27]\n"
"ds_read_b64_tr_b16 a[58:59], v160 offset:54784\n"
"MM v[24:27], a[20:23], a[128:131], v[24:27]\n"
"ds_read_b64_tr_b16 a[44:45], v161 offset:49664\n"
"MM v[28:31], a[4:7], a[112:115], v[28:31]\n"
"s_add_u32 m0, s66, 34816\n"
"ds_read_b64_tr_b16 a[46:47], v161 offset:50688\n"
"MM v[28:31], a[20:23], a[132:135], v[28:31]\n"
"buffer_load_dwordx4 v168, s[36:39], s67 offen lds\n"
"MM v[32:35], a[4:7], a[116:119], v[32:35]\n"
"ds_read_b64_tr_b16 a[60:61], v161 offset:53760\n"
"MM v[32:35], a[20:23], a[136:139], v[32:35]\n"
"ds_read_b64_tr_b16 a[62:63], v161 offset:54784\n"
"MM v[36:39], a[4:7], a[120:123], v[36:39]\n"
"s_add_u32 m0, s66, 35840\n"
"MM v[36:39], a[20:23], a[140:143], v[36:39]\n"
"buffer_load_dwordx4 v169, s[36:39], s67 offen lds\n"
"MM v[40:43], a[8:11], a[104:107], v[40:43]\n"
"MM v[40:43], a[24:27], a[124:127], v[40:43]\n"
"s_add_u32 m0, s66, 36864\n"
"MM v[44:47], a[8:11], a[108:111], v[44:47]\n"
"buffer_load_dwordx4 v170, s[36:39], s67 offen lds\n"
"MM v[44:47], a[24:27], a[128:131], v[44:47]\n"
"MM v[48:51], a[8:11], a[112:115], v[48:51]\n"
"MM v[48:51], a[24:27], a[132:135], v[48:51]\n"
"s_add_u32 m0, s66, 37888\n"
"MM v[52:55], a[8:11], a[116:119], v[52:55]\n"
"buffer_load_dwordx4 v171, s[36:39], s67 offen lds\n"
"MM v[52:55], a[24:27], a[136:139], v[52:55]\n"
"MM v[56:59], a[8:11], a[120:123], v[56:59]\n"
"MM v[56:59], a[24:27], a[140:143], v[56:59]\n"
"s_add_u32 m0, s66, 38912\n"
"MM v[60:63], a[12:15], a[104:107], v[60:63]\n"
"buffer_load_dwordx4 v172, s[36:39], s67 offen lds\n"
"MM v[60:63], a[28:31], a[124:127], v[60:63]\n"
"MM v[64:67], a[12:15], a[108:111], v[64:67]\n"
"s_add_u32 m0, s66, 39936\n"
"MM v[64:67], a[28:31], a[128:131], v[64:67]\n"
"buffer_load_dwordx4 v173, s[36:39], s67 offen lds\n"
"MM v[68:71], a[12:15], a[112:115], v[68:71]\n"
"MM v[68:71], a[28:31], a[132:135], v[68:71]\n"
"MM v[72:75], a[12:15], a[116:119], v[72:75]\n"
"s_add_u32 s67, s67, 128\n"
"MM v[72:75], a[28:31], a[136:139], v[72:75]\n"
"MM v[76:79], a[12:15], a[120:123], v[76:79]\n"
"MM v[76:79], a[28:31], a[140:143], v[76:79]\n"
"s_waitcnt vmcnt(18)\n"
"s_waitcnt lgkmcnt(0)\n"
"s_barrier\n"
"MM v[80:83], a[32:35], a[104:107], v[80:83]\n"
"ds_read_b64_tr_b16 a[0:1], v160 offset:0\n"
"ds_read_b64_tr_b16 a[2:3], v160 offset:1024\n"
"MM v[80:83], a[48:51], a[124:127], v[80:83]\n"
"ds_read_b64_tr_b16 a[16:17], v160 offset:4096\n"
"ds_read_b64_tr_b16 a[18:19], v160 offset:5120\n"
"MM v[84:87], a[32:35], a[108:111], v[84:87]\n"
"s_add_u32 s71, s94, 0\n"
"s_and_b32 s71, s71, 0x1ffff\n"
"MM v[84:87], a[48:51], a[128:131], v[84:87]\n"
"ds_read_b128 a[64:67], v162 offset:0\n"
"s_add_u32 m0, s70, 32768\n"
"MM v[88:91], a[32:35], a[112:115], v[88:91]\n"
"ds_read_b128 a[84:87], v163 offset:0\n"
"ds_read_b128 a[68:71], v162 offset:2048\n"
"MM v[88:91], a[48:51], a[132:135], v[88:91]\n"
"buffer_load_dwordx4 v249, s[48:51], s71 offen nt lds\n"
"MM v[92:95], a[32:35], a[116:119], v[92:95]\n"
"ds_read_b128 a[88:91], v163 offset:2048\n"
"ds_read_b128 a[72:75], v162 offset:4096\n"
"MM v[92:95], a[48:51], a[136:139], v[92:95]\n"
"ds_read_b128 a[92:95], v163 offset:4096\n"
"MM v[96:99], a[32:35], a[120:123], v[96:99]\n"
"ds_read_b128 a[76:79], v162 offset:6144\n"
"MM v[96:99], a[48:51], a[140:143], v[96:99]\n"
"ds_read_b128 a[96:99], v163 offset:6144\n"
"MM v[100:103], a[36:39], a[104:107], v[100:103]\n"
"buffer_load_dwordx4 v251, s[48:51], s71 offen offset:1024 nt lds\n"
"MM v[100:103], a[52:55], a[124:127], v[100:103]\n"
"ds_read_b128 a[80:83], v162 offset:8192\n"
"ds_read_b128 a[100:103], v163 offset:8192\n"
"MM v[104:107], a[36:39], a[108:111], v[104:107]\n"
"ds_read_b64_tr_b16 a[4:5], v161 offset:0\n"
"MM v[104:107], a[52:55], a[128:131], v[104:107]\n"
"ds_read_b64_tr_b16 a[6:7], v161 offset:1024\n"
"MM v[108:111], a[36:39], a[112:115], v[108:111]\n"
"ds_read_b64_tr_b16 a[20:21], v161 offset:4096\n"
"MM v[108:111], a[52:55], a[132:135], v[108:111]\n"
"MM v[112:115], a[36:39], a[116:119], v[112:115]\n"
"ds_read_b64_tr_b16 a[22:23], v161 offset:5120\n"
"MM v[112:115], a[52:55], a[136:139], v[112:115]\n"
"ds_read_b64_tr_b16 a[8:9], v160 offset:512\n"
"MM v[116:119], a[36:39], a[120:123], v[116:119]\n"
"buffer_load_dwordx4 v249, s[48:51], s71 offen offset:2048 nt lds\n"
"MM v[116:119], a[52:55], a[140:143], v[116:119]\n"
"ds_read_b64_tr_b16 a[10:11], v160 offset:1536\n"
"ds_read_b64_tr_b16 a[24:25], v160 offset:4608\n"
"MM v[120:123], a[40:43], a[104:107], v[120:123]\n"
"ds_read_b64_tr_b16 a[26:27], v160 offset:5632\n"
"MM v[120:123], a[56:59], a[124:127], v[120:123]\n"
"ds_read_b64_tr_b16 a[12:13], v161 offset:512\n"
"MM v[124:127], a[40:43], a[108:111], v[124:127]\n"
"MM v[124:127], a[56:59], a[128:131], v[124:127]\n"
"ds_read_b64_tr_b16 a[14:15], v161 offset:1536\n"
"MM v[128:131], a[40:43], a[112:115], v[128:131]\n"
"ds_read_b64_tr_b16 a[28:29], v161 offset:4608\n"
"MM v[128:131], a[56:59], a[132:135], v[128:131]\n"
"ds_read_b64_tr_b16 a[30:31], v161 offset:5632\n"
"MM v[132:135], a[40:43], a[116:119], v[132:135]\n"
"buffer_load_dwordx4 v251, s[48:51], s71 offen offset:3072 nt lds\n"
"MM v[132:135], a[56:59], a[136:139], v[132:135]\n"
"MM v[136:139], a[40:43], a[120:123], v[136:139]\n"
"MM v[136:139], a[56:59], a[140:143], v[136:139]\n"
"MM v[140:143], a[44:47], a[104:107], v[140:143]\n"
"MM v[140:143], a[60:63], a[124:127], v[140:143]\n"
"MM v[144:147], a[44:47], a[108:111], v[144:147]\n"
"MM v[144:147], a[60:63], a[128:131], v[144:147]\n"
"MM v[148:151], a[44:47], a[112:115], v[148:151]\n"
"MM v[148:151], a[60:63], a[132:135], v[148:151]\n"
"MM v[152:155], a[44:47], a[116:119], v[152:155]\n"
"MM v[152:155], a[60:63], a[136:139], v[152:155]\n"
"MM v[156:159], a[44:47], a[120:123], v[156:159]\n"
"MM v[156:159], a[60:63], a[140:143], v[156:159]\n"
"s_waitcnt lgkmcnt(0)\n"
"s_barrier\n"
"MM v[0:3], a[0:3], a[64:67], v[0:3]\n"
"ds_read_b64_tr_b16 a[32:33], v160 offset:16384\n"
"ds_read_b64_tr_b16 a[34:35], v160 offset:17408\n"
"MM v[0:3], a[16:19], a[84:87], v[0:3]\n"
"ds_read_b64_tr_b16 a[48:49], v160 offset:20480\n"
"ds_read_b64_tr_b16 a[50:51], v160 offset:21504\n"
"MM v[4:7], a[0:3], a[68:71], v[4:7]\n"
"s_add_u32 s71, s94, 16384\n"
"s_and_b32 s71, s71, 0x1ffff\n"
"MM v[4:7], a[16:19], a[88:91], v[4:7]\n"
"s_add_u32 m0, s70, 49152\n"
"ds_read_b64_tr_b16 a[36:37], v161 offset:16384\n"
"MM v[8:11], a[0:3], a[72:75], v[8:11]\n"
"buffer_load_dwordx4 v249, s[44:47], s71 offen nt lds\n"
"MM v[8:11], a[16:19], a[92:95], v[8:11]\n"
"ds_read_b64_tr_b16 a[38:39], v161 offset:17408\n"
"ds_read_b64_tr_b16 a[52:53], v161 offset:20480\n"
"MM v[12:15], a[0:3], a[76:79], v[12:15]\n"
"ds_read_b64_tr_b16 a[54:55], v161 offset:21504\n"
"MM v[12:15], a[16:19], a[96:99], v[12:15]\n"
"ds_read_b64_tr_b16 a[40:41], v160 offset:16896\n"
"MM v[16:19], a[0:3], a[80:83], v[16:19]\n"
"ds_read_b64_tr_b16 a[42:43], v160 offset:17920\n"
"MM v[16:19], a[16:19], a[100:103], v[16:19]\n"
"buffer_load_dwordx4 v252, s[44:47], s71 offen offset:1024 nt lds\n"
"MM v[20:23], a[4:7], a[64:67], v[20:23]\n"
"ds_read_b64_tr_b16 a[56:57], v160 offset:20992\n"
"MM v[20:23], a[20:23], a[84:87], v[20:23]\n"
"ds_read_b64_tr_b16 a[58:59], v160 offset:22016\n"
"MM v[24:27], a[4:7], a[68:71], v[24:27]\n"
"MM v[24:27], a[20:23], a[88:91], v[24:27]\n"
"ds_read_b64_tr_b16 a[44:45], v161 offset:16896\n"
"MM v[28:31], a[4:7], a[72:75], v[28:31]\n"
"ds_read_b64_tr_b16 a[46:47], v161 offset:17920\n"
"MM v[28:31], a[20:23], a[92:95], v[28:31]\n"
"MM v[32:35], a[4:7], a[76:79], v[32:35]\n"
"ds_read_b64_tr_b16 a[60:61], v161 offset:20992\n"
"MM v[32:35], a[20:23], a[96:99], v[32:35]\n"
"buffer_load_dwordx4 v251, s[44:47], s71 offen offset:2048 nt lds\n"
"MM v[36:39], a[4:7], a[80:83], v[36:39]\n"
"ds_read_b64_tr_b16 a[62:63], v161 offset:22016\n"
"MM v[36:39], a[20:23], a[100:103], v[36:39]\n"
"MM v[40:43], a[8:11], a[64:67], v[40:43]\n"
"MM v[40:43], a[24:27], a[84:87], v[40:43]\n"
"MM v[44:47], a[8:11], a[68:71], v[44:47]\n"
"MM v[44:47], a[24:27], a[88:91], v[44:47]\n"
"MM v[48:51], a[8:11], a[72:75], v[48:51]\n"
"MM v[48:51], a[24:27], a[92:95], v[48:51]\n"
"buffer_load_dwordx4 v250, s[44:47], s71 offen offset:3072 nt lds\n"
"MM v[52:55], a[8:11], a[76:79], v[52:55]\n"
"MM v[52:55], a[24:27], a[96:99], v[52:55]\n"
"MM v[56:59], a[8:11], a[80:83], v[56:59]\n"
"MM v[56:59], a[24:27], a[100:103], v[56:59]\n"
"MM v[60:63], a[12:15], a[64:67], v[60:63]\n"
"MM v[60:63], a[28:31], a[84:87], v[60:63]\n"
"MM v[64:67], a[12:15], a[68:71], v[64:67]\n"
"MM v[64:67], a[28:31], a[88:91], v[64:67]\n"
"MM v[68:71], a[12:15], a[72:75], v[68:71]\n"
"MM v[68:71], a[28:31], a[92:95], v[68:71]\n"
"MM v[72:75], a[12:15], a[76:79], v[72:75]\n"
"MM v[72:75], a[28:31], a[96:99], v[72:75]\n"
"MM v[76:79], a[12:15], a[80:83], v[76:79]\n"
"MM v[76:79], a[28:31], a[100:103], v[76:79]\n"
"s_waitcnt vmcnt(8)\n"
"s_waitcnt lgkmcnt(0)\n"
"s_barrier\n"
"MM v[80:83], a[32:35], a[64:67], v[80:83]\n"
"ds_read_b64_tr_b16 a[0:1], v160 offset:32768\n"
"ds_read_b64_tr_b16 a[2:3], v160 offset:33792\n"
"MM v[80:83], a[48:51], a[84:87], v[80:83]\n"
"ds_read_b64_tr_b16 a[16:17], v160 offset:36864\n"
"ds_read_b64_tr_b16 a[18:19], v160 offset:37888\n"
"MM v[84:87], a[32:35], a[68:71], v[84:87]\n"
"s_add_u32 s71, s94, 8192\n"
"s_and_b32 s71, s71, 0x1ffff\n"
"MM v[84:87], a[48:51], a[88:91], v[84:87]\n"
"ds_read_b128 a[104:107], v162 offset:20480\n"
"s_add_u32 m0, s70, 114688\n"
"MM v[88:91], a[32:35], a[72:75], v[88:91]\n"
"ds_read_b128 a[124:127], v163 offset:20480\n"
"ds_read_b128 a[108:111], v162 offset:22528\n"
"MM v[88:91], a[48:51], a[92:95], v[88:91]\n"
"buffer_load_dwordx4 v249, s[48:51], s71 offen nt lds\n"
"MM v[92:95], a[32:35], a[76:79], v[92:95]\n"
"ds_read_b128 a[128:131], v163 offset:22528\n"
"ds_read_b128 a[112:115], v162 offset:24576\n"
"MM v[92:95], a[48:51], a[96:99], v[92:95]\n"
"ds_read_b128 a[132:135], v163 offset:24576\n"
"MM v[96:99], a[32:35], a[80:83], v[96:99]\n"
"ds_read_b128 a[116:119], v162 offset:26624\n"
"MM v[96:99], a[48:51], a[100:103], v[96:99]\n"
"ds_read_b128 a[136:139], v163 offset:26624\n"
"MM v[100:103], a[36:39], a[64:67], v[100:103]\n"
"buffer_load_dwordx4 v251, s[48:51], s71 offen offset:1024 nt lds\n"
"MM v[100:103], a[52:55], a[84:87], v[100:103]\n"
"ds_read_b128 a[120:123], v162 offset:28672\n"
"ds_read_b128 a[140:143], v163 offset:28672\n"
"MM v[104:107], a[36:39], a[68:71], v[104:107]\n"
"ds_read_b64_tr_b16 a[4:5], v161 offset:32768\n"
"MM v[104:107], a[52:55], a[88:91], v[104:107]\n"
"ds_read_b64_tr_b16 a[6:7], v161 offset:33792\n"
"MM v[108:111], a[36:39], a[72:75], v[108:111]\n"
"ds_read_b64_tr_b16 a[20:21], v161 offset:36864\n"
"MM v[108:111], a[52:55], a[92:95], v[108:111]\n"
"MM v[112:115], a[36:39], a[76:79], v[112:115]\n"
"ds_read_b64_tr_b16 a[22:23], v161 offset:37888\n"
"MM v[112:115], a[52:55], a[96:99], v[112:115]\n"
"ds_read_b64_tr_b16 a[8:9], v160 offset:33280\n"
"MM v[116:119], a[36:39], a[80:83], v[116:119]\n"
"buffer_load_dwordx4 v249, s[48:51], s71 offen offset:2048 nt lds\n"
"MM v[116:119], a[52:55], a[100:103], v[116:119]\n"
"ds_read_b64_tr_b16 a[10:11], v160 offset:34304\n"
"ds_read_b64_tr_b16 a[24:25], v160 offset:37376\n"
"MM v[120:123], a[40:43], a[64:67], v[120:123]\n"
"ds_read_b64_tr_b16 a[26:27], v160 offset:38400\n"
"MM v[120:123], a[56:59], a[84:87], v[120:123]\n"
"ds_read_b64_tr_b16 a[12:13], v161 offset:33280\n"
"MM v[124:127], a[40:43], a[68:71], v[124:127]\n"
"MM v[124:127], a[56:59], a[88:91], v[124:127]\n"
"ds_read_b64_tr_b16 a[14:15], v161 offset:34304\n"
"MM v[128:131], a[40:43], a[72:75], v[128:131]\n"
"ds_read_b64_tr_b16 a[28:29], v161 offset:37376\n"
"MM v[128:131], a[56:59], a[92:95], v[128:131]\n"
"ds_read_b64_tr_b16 a[30:31], v161 offset:38400\n"
"MM v[132:135], a[40:43], a[76:79], v[132:135]\n"
"buffer_load_dwordx4 v251, s[48:51], s71 offen offset:3072 nt lds\n"
"MM v[132:135], a[56:59], a[96:99], v[132:135]\n"
"MM v[136:139], a[40:43], a[80:83], v[136:139]\n"
"MM v[136:139], a[56:59], a[100:103], v[136:139]\n"
"MM v[140:143], a[44:47], a[64:67], v[140:143]\n"
"MM v[140:143], a[60:63], a[84:87], v[140:143]\n"
"MM v[144:147], a[44:47], a[68:71], v[144:147]\n"
"MM v[144:147], a[60:63], a[88:91], v[144:147]\n"
"MM v[148:151], a[44:47], a[72:75], v[148:151]\n"
"MM v[148:151], a[60:63], a[92:95], v[148:151]\n"
"MM v[152:155], a[44:47], a[76:79], v[152:155]\n"
"MM v[152:155], a[60:63], a[96:99], v[152:155]\n"
"MM v[156:159], a[44:47], a[80:83], v[156:159]\n"
"MM v[156:159], a[60:63], a[100:103], v[156:159]\n"
"s_waitcnt lgkmcnt(0)\n"
"s_barrier\n"
"MM v[0:3], a[0:3], a[104:107], v[0:3]\n"
"ds_read_b64_tr_b16 a[32:33], v160 offset:49152\n"
"ds_read_b64_tr_b16 a[34:35], v160 offset:50176\n"
"MM v[0:3], a[16:19], a[124:127], v[0:3]\n"
"ds_read_b64_tr_b16 a[48:49], v160 offset:53248\n"
"ds_read_b64_tr_b16 a[50:51], v160 offset:54272\n"
"MM v[4:7], a[0:3], a[108:111], v[4:7]\n"
"s_add_u32 s71, s94, 24576\n"
"s_and_b32 s71, s71, 0x1ffff\n"
"MM v[4:7], a[16:19], a[128:131], v[4:7]\n"
"s_add_u32 m0, s70, 65536\n"
"ds_read_b64_tr_b16 a[36:37], v161 offset:49152\n"
"MM v[8:11], a[0:3], a[112:115], v[8:11]\n"
"buffer_load_dwordx4 v249, s[44:47], s71 offen nt lds\n"
"MM v[8:11], a[16:19], a[132:135], v[8:11]\n"
"ds_read_b64_tr_b16 a[38:39], v161 offset:50176\n"
"ds_read_b64_tr_b16 a[52:53], v161 offset:53248\n"
"MM v[12:15], a[0:3], a[116:119], v[12:15]\n"
"ds_read_b64_tr_b16 a[54:55], v161 offset:54272\n"
"MM v[12:15], a[16:19], a[136:139], v[12:15]\n"
"ds_read_b64_tr_b16 a[40:41], v160 offset:49664\n"
"MM v[16:19], a[0:3], a[120:123], v[16:19]\n"
"ds_read_b64_tr_b16 a[42:43], v160 offset:50688\n"
"MM v[16:19], a[16:19], a[140:143], v[16:19]\n"
"buffer_load_dwordx4 v252, s[44:47], s71 offen offset:1024 nt lds\n"
"MM v[20:23], a[4:7], a[104:107], v[20:23]\n"
"ds_read_b64_tr_b16 a[56:57], v160 offset:53760\n"
"MM v[20:23], a[20:23], a[124:127], v[20:23]\n"
"ds_read_b64_tr_b16 a[58:59], v160 offset:54784\n"
"MM v[24:27], a[4:7], a[108:111], v[24:27]\n"
"MM v[24:27], a[20:23], a[128:131], v[24:27]\n"
"ds_read_b64_tr_b16 a[44:45], v161 offset:49664\n"
"MM v[28:31], a[4:7], a[112:115], v[28:31]\n"
"ds_read_b64_tr_b16 a[46:47], v161 offset:50688\n"
"MM v[28:31], a[20:23], a[132:135], v[28:31]\n"
"MM v[32:35], a[4:7], a[116:119], v[32:35]\n"
"ds_read_b64_tr_b16 a[60:61], v161 offset:53760\n"
"MM v[32:35], a[20:23], a[136:139], v[32:35]\n"
"buffer_load_dwordx4 v251, s[44:47], s71 offen offset:2048 nt lds\n"
"MM v[36:39], a[4:7], a[120:123], v[36:39]\n"
"ds_read_b64_tr_b16 a[62:63], v161 offset:54784\n"
"MM v[36:39], a[20:23], a[140:143], v[36:39]\n"
"MM v[40:43], a[8:11], a[104:107], v[40:43]\n"
"MM v[40:43], a[24:27], a[124:127], v[40:43]\n"
"MM v[44:47], a[8:11], a[108:111], v[44:47]\n"
"MM v[44:47], a[24:27], a[128:131], v[44:47]\n"
"MM v[48:51], a[8:11], a[112:115], v[48:51]\n"
"MM v[48:51], a[24:27], a[132:135], v[48:51]\n"
"buffer_load_dwordx4 v250, s[44:47], s71 offen offset:3072 nt lds\n"
"MM v[52:55], a[8:11], a[116:119], v[52:55]\n"
"MM v[52:55], a[24:27], a[136:139], v[52:55]\n"
"MM v[56:59], a[8:11], a[120:123], v[56:59]\n"
"MM v[56:59], a[24:27], a[140:143], v[56:59]\n"
"MM v[60:63], a[12:15], a[104:107], v[60:63]\n"
"MM v[60:63], a[28:31], a[124:127], v[60:63]\n"
"MM v[64:67], a[12:15], a[108:111], v[64:67]\n"
"MM v[64:67], a[28:31], a[128:131], v[64:67]\n"
"MM v[68:71], a[12:15], a[112:115], v[68:71]\n"
"MM v[68:71], a[28:31], a[132:135], v[68:71]\n"
"MM v[72:75], a[12:15], a[116:119], v[72:75]\n"
"MM v[72:75], a[28:31], a[136:139], v[72:75]\n"
"MM v[76:79], a[12:15], a[120:123], v[76:79]\n"
"MM v[76:79], a[28:31], a[140:143], v[76:79]\n"
"s_waitcnt lgkmcnt(0)\n"
"s_barrier\n"
"MM v[80:83], a[32:35], a[104:107], v[80:83]\n"
"v_mul_f32 v0, s78, v0\n"
"v_mul_f32 v1, s78, v1\n"
"MM v[80:83], a[48:51], a[124:127], v[80:83]\n"
"v_mul_f32 v2, s78, v2\n"
"v_mul_f32 v3, s78, v3\n"
"MM v[84:87], a[32:35], a[108:111], v[84:87]\n"
"v_mul_f32 v20, s78, v20\n"
"s_add_u32 s71, s94, 16384\n"
"MM v[84:87], a[48:51], a[128:131], v[84:87]\n"
"v_mul_f32 v21, s78, v21\n"
"v_mul_f32 v22, s78, v22\n"
"MM v[88:91], a[32:35], a[112:115], v[88:91]\n"
"v_mul_f32 v23, s78, v23\n"
"v_cvt_pk_bf16_f32 v160, v0, v1\n"
"MM v[88:91], a[48:51], a[132:135], v[88:91]\n"
"v_cvt_pk_bf16_f32 v161, v2, v3\n"
"s_and_b32 s71, s71, 0x1ffff\n"
"MM v[92:95], a[32:35], a[116:119], v[92:95]\n"
"v_cvt_pk_bf16_f32 v162, v20, v21\n"
"v_cvt_pk_bf16_f32 v163, v22, v23\n"
"MM v[92:95], a[48:51], a[136:139], v[92:95]\n"
"v_mul_f32 v4, s78, v4\n"
"v_mul_f32 v5, s78, v5\n"
"MM v[96:99], a[32:35], a[120:123], v[96:99]\n"
"v_mul_f32 v6, s78, v6\n"
"s_add_u32 m0, s70, 81920\n"
"MM v[96:99], a[48:51], a[140:143], v[96:99]\n"
"v_mul_f32 v7, s78, v7\n"
"v_mul_f32 v24, s78, v24\n"
"MM v[100:103], a[36:39], a[104:107], v[100:103]\n"
"v_mul_f32 v25, s78, v25\n"
"v_mul_f32 v26, s78, v26\n"
"MM v[100:103], a[52:55], a[124:127], v[100:103]\n"
"v_mul_f32 v27, s78, v27\n"
"v_cvt_pk_bf16_f32 v164, v4, v5\n"
"MM v[104:107], a[36:39], a[108:111], v[104:107]\n"
"buffer_load_dwordx4 v249, s[48:51], s71 offen nt lds\n"
"MM v[104:107], a[52:55], a[128:131], v[104:107]\n"
"v_cvt_pk_bf16_f32 v165, v6, v7\n"
"v_cvt_pk_bf16_f32 v166, v24, v25\n"
"MM v[108:111], a[36:39], a[112:115], v[108:111]\n"
"v_cvt_pk_bf16_f32 v167, v26, v27\n"
"v_mul_f32 v8, s78, v8\n"
"MM v[108:111], a[52:55], a[132:135], v[108:111]\n"
"v_mul_f32 v9, s78, v9\n"
"v_mul_f32 v10, s78, v10\n"
"MM v[112:115], a[36:39], a[116:119], v[112:115]\n"
"v_mul_f32 v11, s78, v11\n"
"v_mul_f32 v28, s78, v28\n"
"MM v[112:115], a[52:55], a[136:139], v[112:115]\n"
"v_mul_f32 v29, s78, v29\n"
"v_mul_f32 v30, s78, v30\n"
"MM v[116:119], a[36:39], a[120:123], v[116:119]\n"
"v_mul_f32 v31, s78, v31\n"
"v_cvt_pk_bf16_f32 v168, v8, v9\n"
"MM v[116:119], a[52:55], a[140:143], v[116:119]\n"
"v_cvt_pk_bf16_f32 v169, v10, v11\n"
"v_cvt_pk_bf16_f32 v170, v28, v29\n"
"MM v[120:123], a[40:43], a[104:107], v[120:123]\n"
"v_cvt_pk_bf16_f32 v171, v30, v31\n"
"v_mul_f32 v12, s78, v12\n"
"MM v[120:123], a[56:59], a[124:127], v[120:123]\n"
"buffer_load_dwordx4 v251, s[48:51], s71 offen offset:1024 nt lds\n"
"MM v[124:127], a[40:43], a[108:111], v[124:127]\n"
"v_mul_f32 v13, s78, v13\n"
"v_mul_f32 v14, s78, v14\n"
"MM v[124:127], a[56:59], a[128:131], v[124:127]\n"
"v_mul_f32 v15, s78, v15\n"
"v_mul_f32 v32, s78, v32\n"
"MM v[128:131], a[40:43], a[112:115], v[128:131]\n"
"v_mul_f32 v33, s78, v33\n"
"v_mul_f32 v34, s78, v34\n"
"MM v[128:131], a[56:59], a[132:135], v[128:131]\n"
"v_mul_f32 v35, s78, v35\n"
"v_cvt_pk_bf16_f32 v172, v12, v13\n"
"MM v[132:135], a[40:43], a[116:119], v[132:135]\n"
"v_cvt_pk_bf16_f32 v173, v14, v15\n"
"v_cvt_pk_bf16_f32 v174, v32, v33\n"
"MM v[132:135], a[56:59], a[136:139], v[132:135]\n"
"v_cvt_pk_bf16_f32 v175, v34, v35\n"
"v_mul_f32 v16, s78, v16\n"
"MM v[136:139], a[40:43], a[120:123], v[136:139]\n"
"v_mul_f32 v17, s78, v17\n"
"v_mul_f32 v18, s78, v18\n"
"MM v[136:139], a[56:59], a[140:143], v[136:139]\n"
"buffer_load_dwordx4 v249, s[48:51], s71 offen offset:2048 nt lds\n"
"MM v[140:143], a[44:47], a[104:107], v[140:143]\n"
"v_mul_f32 v19, s78, v19\n"
"v_mul_f32 v36, s78, v36\n"
"MM v[140:143], a[60:63], a[124:127], v[140:143]\n"
"v_mul_f32 v37, s78, v37\n"
"v_mul_f32 v38, s78, v38\n"
"MM v[144:147], a[44:47], a[108:111], v[144:147]\n"
"v_mul_f32 v39, s78, v39\n"
"v_cvt_pk_bf16_f32 v176, v16, v17\n"
"MM v[144:147], a[60:63], a[128:131], v[144:147]\n"
"v_cvt_pk_bf16_f32 v177, v18, v19\n"
"v_cvt_pk_bf16_f32 v178, v36, v37\n"
"MM v[148:151], a[44:47], a[112:115], v[148:151]\n"
"v_cvt_pk_bf16_f32 v179, v38, v39\n"
"v_mul_f32 v40, s78, v40\n"
"MM v[148:151], a[60:63], a[132:135], v[148:151]\n"
"v_mul_f32 v41, s78, v41\n"
"v_mul_f32 v42, s78, v42\n"
"MM v[152:155], a[44:47], a[116:119], v[152:155]\n"
"v_mul_f32 v43, s78, v43\n"
"v_mul_f32 v60, s78, v60\n"
"MM v[152:155], a[60:63], a[136:139], v[152:155]\n"
"v_mul_f32 v61, s78, v61\n"
"v_mul_f32 v62, s78, v62\n"
"MM v[156:159], a[44:47], a[120:123], v[156:159]\n"
"buffer_load_dwordx4 v251, s[48:51], s71 offen offset:3072 nt lds\n"
"v_mul_f32 v63, s78, v63\n"
"MM v[156:159], a[60:63], a[140:143], v[156:159]\n"
"v_cvt_pk_bf16_f32 v180, v40, v41\n"
"v_cvt_pk_bf16_f32 v181, v42, v43\n"
"v_cvt_pk_bf16_f32 v182, v60, v61\n"
"v_cvt_pk_bf16_f32 v183, v62, v63\n"
"K1_EATT:\n"
"s_nop 7\n"
"s_waitcnt lgkmcnt(0)\n"
"s_waitcnt vmcnt(24)\n"
"v_mov_b32 v248, 21\n"
"ds_write_b32 v242, v248 offset:0\n"
"v_pk_mul_f32 v[44:45], v[44:45], s[78:79]\n"
"v_pk_mul_f32 v[46:47], v[46:47], s[78:79]\n"
"v_pk_mul_f32 v[64:65], v[64:65], s[78:79]\n"
"v_pk_mul_f32 v[66:67], v[66:67], s[78:79]\n"
"v_cvt_pk_bf16_f32 v184, v44, v45\n"
"v_cvt_pk_bf16_f32 v185, v46, v47\n"
"v_cvt_pk_bf16_f32 v186, v64, v65\n"
"v_cvt_pk_bf16_f32 v187, v66, v67\n"
"v_pk_mul_f32 v[48:49], v[48:49], s[78:79]\n"
"v_pk_mul_f32 v[50:51], v[50:51], s[78:79]\n"
"v_pk_mul_f32 v[68:69], v[68:69], s[78:79]\n"
"v_pk_mul_f32 v[70:71], v[70:71], s[78:79]\n"
"v_cvt_pk_bf16_f32 v188, v48, v49\n"
"v_cvt_pk_bf16_f32 v189, v50, v51\n"
"v_cvt_pk_bf16_f32 v190, v68, v69\n"
"v_cvt_pk_bf16_f32 v191, v70, v71\n"
"v_pk_mul_f32 v[52:53], v[52:53], s[78:79]\n"
"v_pk_mul_f32 v[54:55], v[54:55], s[78:79]\n"
"v_pk_mul_f32 v[72:73], v[72:73], s[78:79]\n"
"v_pk_mul_f32 v[74:75], v[74:75], s[78:79]\n"
"v_cvt_pk_bf16_f32 v192, v52, v53\n"
"v_cvt_pk_bf16_f32 v193, v54, v55\n"
"v_cvt_pk_bf16_f32 v194, v72, v73\n"
"v_cvt_pk_bf16_f32 v195, v74, v75\n"
"ds_read_b32 v248, v245 offset:0\n"
"v_pk_mul_f32 v[56:57], v[56:57], s[78:79]\n"
"v_pk_mul_f32 v[58:59], v[58:59], s[78:79]\n"
"v_pk_mul_f32 v[76:77], v[76:77], s[78:79]\n"
"v_pk_mul_f32 v[78:79], v[78:79], s[78:79]\n"
"v_cvt_pk_bf16_f32 v196, v56, v57\n"
"v_cvt_pk_bf16_f32 v197, v58, v59\n"
"v_cvt_pk_bf16_f32 v198, v76, v77\n"
"v_cvt_pk_bf16_f32 v199, v78, v79\n"
"v_pk_mul_f32 v[80:81], v[80:81], s[78:79]\n"
"v_pk_mul_f32 v[82:83], v[82:83], s[78:79]\n"
"v_pk_mul_f32 v[100:101], v[100:101], s[78:79]\n"
"v_pk_mul_f32 v[102:103], v[102:103], s[78:79]\n"
"v_cvt_pk_bf16_f32 v200, v80, v81\n"
"v_cvt_pk_bf16_f32 v201, v82, v83\n"
"v_cvt_pk_bf16_f32 v202, v100, v101\n"
"v_cvt_pk_bf16_f32 v203, v102, v103\n"
"v_pk_mul_f32 v[84:85], v[84:85], s[78:79]\n"
"v_pk_mul_f32 v[86:87], v[86:87], s[78:79]\n"
"v_pk_mul_f32 v[104:105], v[104:105], s[78:79]\n"
"v_pk_mul_f32 v[106:107], v[106:107], s[78:79]\n"
"v_cvt_pk_bf16_f32 v204, v84, v85\n"
"v_cvt_pk_bf16_f32 v205, v86, v87\n"
"v_cvt_pk_bf16_f32 v206, v104, v105\n"
"v_cvt_pk_bf16_f32 v207, v106, v107\n"
"s_waitcnt lgkmcnt(0)\n"
"v_cmp_gt_u32 vcc, 21, v248\n"
"v_pk_mul_f32 v[88:89], v[88:89], s[78:79]\n"
"v_pk_mul_f32 v[90:91], v[90:91], s[78:79]\n"
"s_cbranch_vccnz K1_A21_S\n"
"K1_A21_R:\n"
"ds_read_b128 a[160:163], v240 offset:0\n"
"ds_read_b128 a[164:167], v240 offset:256\n"
"ds_read_b128 a[168:171], v240 offset:512\n"
"ds_read_b128 a[172:175], v240 offset:768\n"
"ds_read_b128 a[176:179], v240 offset:4096\n"
"ds_read_b128 a[180:183], v240 offset:4352\n"
"ds_read_b128 a[184:187], v240 offset:4608\n"
"ds_read_b128 a[188:191], v240 offset:4864\n"
"v_pk_mul_f32 v[108:109], v[108:109], s[78:79]\n"
"v_pk_mul_f32 v[110:111], v[110:111], s[78:79]\n"
"v_cvt_pk_bf16_f32 v208, v88, v89\n"
"v_cvt_pk_bf16_f32 v209, v90, v91\n"
"v_cvt_pk_bf16_f32 v210, v108, v109\n"
"v_cvt_pk_bf16_f32 v211, v110, v111\n"
"v_pk_mul_f32 v[92:93], v[92:93], s[78:79]\n"
"v_pk_mul_f32 v[94:95], v[94:95], s[78:79]\n"
"v_pk_mul_f32 v[112:113], v[112:113], s[78:79]\n"
"v_pk_mul_f32 v[114:115], v[114:115], s[78:79]\n"
"v_cvt_pk_bf16_f32 v212, v92, v93\n"
"v_cvt_pk_bf16_f32 v213, v94, v95\n"
"v_cvt_pk_bf16_f32 v214, v112, v113\n"
"v_cvt_pk_bf16_f32 v215, v114, v115\n"
"v_pk_mul_f32 v[96:97], v[96:97], s[78:79]\n"
"v_pk_mul_f32 v[98:99], v[98:99], s[78:79]\n"
"v_pk_mul_f32 v[116:117], v[116:117], s[78:79]\n"
"v_pk_mul_f32 v[118:119], v[118:119], s[78:79]\n"
"v_cvt_pk_bf16_f32 v216, v96, v97\n"
"v_cvt_pk_bf16_f32 v217, v98, v99\n"
"v_cvt_pk_bf16_f32 v218, v116, v117\n"
"v_cvt_pk_bf16_f32 v219, v118, v119\n"
"v_pk_mul_f32 v[120:121], v[120:121], s[78:79]\n"
"v_pk_mul_f32 v[122:123], v[122:123], s[78:79]\n"
"v_pk_mul_f32 v[140:141], v[140:141], s[78:79]\n"
"v_pk_mul_f32 v[142:143], v[142:143], s[78:79]\n"
"v_cvt_pk_bf16_f32 v220, v120, v121\n"
"v_cvt_pk_bf16_f32 v221, v122, v123\n"
"v_cvt_pk_bf16_f32 v222, v140, v141\n"
"v_cvt_pk_bf16_f32 v223, v142, v143\n"
"v_pk_mul_f32 v[124:125], v[124:125], s[78:79]\n"
"v_pk_mul_f32 v[126:127], v[126:127], s[78:79]\n"
"v_pk_mul_f32 v[144:145], v[144:145], s[78:79]\n"
"v_pk_mul_f32 v[146:147], v[146:147], s[78:79]\n"
"v_cvt_pk_bf16_f32 v224, v124, v125\n"
"v_cvt_pk_bf16_f32 v225, v126, v127\n"
"v_cvt_pk_bf16_f32 v226, v144, v145\n"
"v_cvt_pk_bf16_f32 v227, v146, v147\n"
"v_pk_mul_f32 v[128:129], v[128:129], s[78:79]\n"
"v_pk_mul_f32 v[130:131], v[130:131], s[78:79]\n"
"v_pk_mul_f32 v[148:149], v[148:149], s[78:79]\n"
"v_pk_mul_f32 v[150:151], v[150:151], s[78:79]\n"
"v_cvt_pk_bf16_f32 v228, v128, v129\n"
"v_cvt_pk_bf16_f32 v229, v130, v131\n"
"v_cvt_pk_bf16_f32 v230, v148, v149\n"
"v_cvt_pk_bf16_f32 v231, v150, v151\n"
"v_pk_mul_f32 v[132:133], v[132:133], s[78:79]\n"
"v_pk_mul_f32 v[134:135], v[134:135], s[78:79]\n"
"v_pk_mul_f32 v[152:153], v[152:153], s[78:79]\n"
"v_pk_mul_f32 v[154:155], v[154:155], s[78:79]\n"
"v_cvt_pk_bf16_f32 v232, v132, v133\n"
"v_cvt_pk_bf16_f32 v233, v134, v135\n"
"v_cvt_pk_bf16_f32 v234, v152, v153\n"
"v_cvt_pk_bf16_f32 v235, v154, v155\n"
"v_pk_mul_f32 v[136:137], v[136:137], s[78:79]\n"
"v_pk_mul_f32 v[138:139], v[138:139], s[78:79]\n"
"v_pk_mul_f32 v[156:157], v[156:157], s[78:79]\n"
"v_pk_mul_f32 v[158:159], v[158:159], s[78:79]\n"
"v_cvt_pk_bf16_f32 v236, v136, v137\n"
"v_cvt_pk_bf16_f32 v237, v138, v139\n"
"v_cvt_pk_bf16_f32 v238, v156, v157\n"
"v_cvt_pk_bf16_f32 v239, v158, v159\n"
"s_mov_b64 exec, s[82:83]\n"
"v_swap_b32 v160, v162\n"
"v_swap_b32 v161, v163\n"
"v_swap_b32 v164, v166\n"
"v_swap_b32 v165, v167\n"
"v_swap_b32 v168, v170\n"
"v_swap_b32 v169, v171\n"
"v_swap_b32 v172, v174\n"
"v_swap_b32 v173, v175\n"
"v_swap_b32 v176, v178\n"
"v_swap_b32 v177, v179\n"
"v_swap_b32 v180, v182\n"
"v_swap_b32 v181, v183\n"
"v_swap_b32 v184, v186\n"
"v_swap_b32 v185, v187\n"
"v_swap_b32 v188, v190\n"
"v_swap_b32 v189, v191\n"
"v_swap_b32 v192, v194\n"
"v_swap_b32 v193, v195\n"
"v_swap_b32 v196, v198\n"
"v_swap_b32 v197, v199\n"
"v_swap_b32 v200, v202\n"
"v_swap_b32 v201, v203\n"
"v_swap_b32 v204, v206\n"
"v_swap_b32 v205, v207\n"
"v_swap_b32 v208, v210\n"
"v_swap_b32 v209, v211\n"
"v_swap_b32 v212, v214\n"
"v_swap_b32 v213, v215\n"
"v_swap_b32 v216, v218\n"
"v_swap_b32 v217, v219\n"
"v_swap_b32 v220, v222\n"
"v_swap_b32 v221, v223\n"
"v_swap_b32 v224, v226\n"
"v_swap_b32 v225, v227\n"
"v_swap_b32 v228, v230\n"
"v_swap_b32 v229, v231\n"
"v_swap_b32 v232, v234\n"
"v_swap_b32 v233, v235\n"
"v_swap_b32 v236, v238\n"
"v_swap_b32 v237, v239\n"
"s_mov_b64 exec, -1\n"
"s_waitcnt lgkmcnt(7)\n"
"s_nop 2\n"
"MM v[0:3], a[160:163], v[160:163], 0\n"
"s_waitcnt lgkmcnt(6)\n"
"MM v[0:3], a[164:167], v[180:183], v[0:3]\n"
"s_waitcnt lgkmcnt(5)\n"
"MM v[0:3], a[168:171], v[200:203], v[0:3]\n"
"s_waitcnt lgkmcnt(4)\n"
"MM v[0:3], a[172:175], v[220:223], v[0:3]\n"
"s_waitcnt lgkmcnt(3)\n"
"MM v[4:7], a[176:179], v[160:163], 0\n"
"s_waitcnt lgkmcnt(2)\n"
"MM v[4:7], a[180:183], v[180:183], v[4:7]\n"
"s_waitcnt lgkmcnt(1)\n"
"MM v[4:7], a[184:187], v[200:203], v[4:7]\n"
"s_waitcnt lgkmcnt(0)\n"
"MM v[4:7], a[188:191], v[220:223], v[4:7]\n"
"v_max3_f32 v140, v0, v1, v2\n"
"MM v[8:11], a[160:163], v[164:167], 0\n"
"MM v[8:11], a[164:167], v[184:187], v[8:11]\n"
"MM v[8:11], a[168:171], v[204:207], v[8:11]\n"
"MM v[8:11], a[172:175], v[224:227], v[8:11]\n"
"s_nop 2\n"
"v_max3_f32 v140, v3, v4, v140\n"
"v_max3_f32 v140, v5, v6, v140\n"
"MM v[12:15], a[176:179], v[164:167], 0\n"
"v_max_f32 v140, v7, v140\n"
"MM v[12:15], a[180:183], v[184:187], v[12:15]\n"
"MM v[12:15], a[184:187], v[204:207], v[12:15]\n"
"MM v[12:15], a[188:191], v[224:227], v[12:15]\n"
"v_max3_f32 v141, v8, v9, v10\n"
"MM v[16:19], a[160:163], v[168:171], 0\n"
"MM v[16:19], a[164:167], v[188:191], v[16:19]\n"
"MM v[16:19], a[168:171], v[208:211], v[16:19]\n"
"MM v[16:19], a[172:175], v[228:231], v[16:19]\n"
"s_nop 2\n"
"v_max3_f32 v141, v11, v12, v141\n"
"v_max3_f32 v141, v13, v14, v141\n"
"MM v[20:23], a[176:179], v[168:171], 0\n"
"v_max_f32 v141, v15, v141\n"
"MM v[20:23], a[180:183], v[188:191], v[20:23]\n"
"MM v[20:23], a[184:187], v[208:211], v[20:23]\n"
"MM v[20:23], a[188:191], v[228:231], v[20:23]\n"
"v_max3_f32 v142, v16, v17, v18\n"
"MM v[24:27], a[160:163], v[172:175], 0\n"
"MM v[24:27], a[164:167], v[192:195], v[24:27]\n"
"MM v[24:27], a[168:171], v[212:215], v[24:27]\n"
"MM v[24:27], a[172:175], v[232:235], v[24:27]\n"
"s_nop 2\n"
"v_max3_f32 v142, v19, v20, v142\n"
"v_max3_f32 v142, v21, v22, v142\n"
"MM v[28:31], a[176:179], v[172:175], 0\n"
"v_max_f32 v142, v23, v142\n"
"MM v[28:31], a[180:183], v[192:195], v[28:31]\n"
"MM v[28:31], a[184:187], v[212:215], v[28:31]\n"
"MM v[28:31], a[188:191], v[232:235], v[28:31]\n"
"v_max3_f32 v143, v24, v25, v26\n"
"MM v[32:35], a[160:163], v[176:179], 0\n"
"MM v[32:35], a[164:167], v[196:199], v[32:35]\n"
"MM v[32:35], a[168:171], v[216:219], v[32:35]\n"
"MM v[32:35], a[172:175], v[236:239], v[32:35]\n"
"s_nop 2\n"
"v_max3_f32 v143, v27, v28, v143\n"
"v_max3_f32 v143, v29, v30, v143\n"
"MM v[36:39], a[176:179], v[176:179], 0\n"
"v_max_f32 v143, v31, v143\n"
"MM v[36:39], a[180:183], v[196:199], v[36:39]\n"
"MM v[36:39], a[184:187], v[216:219], v[36:39]\n"
"MM v[36:39], a[188:191], v[236:239], v[36:39]\n"
"s_waitcnt vmcnt(16)\n"
"v_mov_b32 v248, 22\n"
"ds_write_b32 v242, v248 offset:0\n"
"v_max3_f32 v144, v32, v33, v34\n"
"s_nop 3\n"
"v_max3_f32 v144, v35, v36, v144\n"
"v_max3_f32 v144, v37, v38, v144\n"
"v_max_f32 v144, v39, v144\n"
"v_mov_b32 v145, v140\n"
"s_nop 1\n"
"v_permlane32_swap_b32 v140, v145\n"
"v_max_f32 v140, v140, v145\n"
"v_mov_b32 v145, v140\n"
"s_nop 1\n"
"v_permlane16_swap_b32 v140, v145\n"
"v_max_f32 v140, v140, v145\n"
"v_mov_b32 v146, v141\n"
"s_nop 1\n"
"v_permlane32_swap_b32 v141, v146\n"
"v_max_f32 v141, v141, v146\n"
"v_mov_b32 v146, v141\n"
"s_nop 1\n"
"v_permlane16_swap_b32 v141, v146\n"
"v_max_f32 v141, v141, v146\n"
"v_mov_b32 v147, v142\n"
"s_nop 1\n"
"v_permlane32_swap_b32 v142, v147\n"
"v_max_f32 v142, v142, v147\n"
"v_mov_b32 v147, v142\n"
"s_nop 1\n"
"v_permlane16_swap_b32 v142, v147\n"
"v_max_f32 v142, v142, v147\n"
"v_mov_b32 v148, v143\n"
"s_nop 1\n"
"v_permlane32_swap_b32 v143, v148\n"
"v_max_f32 v143, v143, v148\n"
"v_mov_b32 v148, v143\n"
"s_nop 1\n"
"v_permlane16_swap_b32 v143, v148\n"
"v_max_f32 v143, v143, v148\n"
"v_mov_b32 v149, v144\n"
"s_nop 1\n"
"v_permlane32_swap_b32 v144, v149\n"
"v_max_f32 v144, v144, v149\n"
"v_mov_b32 v149, v144\n"
"s_nop 1\n"
"v_permlane16_swap_b32 v144, v149\n"
"v_max_f32 v144, v144, v149\n"
"ds_read_b32 v248, v245 offset:0\n"
"v_sub_f32 v80, 0, v140\n"
"v_sub_f32 v81, 0, v140\n"
"v_sub_f32 v82, 0, v140\n"
"v_sub_f32 v83, 0, v140\n"
"v_sub_f32 v84, 0, v141\n"
"v_sub_f32 v85, 0, v141\n"
"v_sub_f32 v86, 0, v141\n"
"v_sub_f32 v87, 0, v141\n"
"v_sub_f32 v88, 0, v142\n"
"v_sub_f32 v89, 0, v142\n"
"v_sub_f32 v90, 0, v142\n"
"v_sub_f32 v91, 0, v142\n"
"v_sub_f32 v92, 0, v143\n"
"v_sub_f32 v93, 0, v143\n"
"v_sub_f32 v94, 0, v143\n"
"v_sub_f32 v95, 0, v143\n"
"v_sub_f32 v96, 0, v144\n"
"v_sub_f32 v97, 0, v144\n"
"v_sub_f32 v98, 0, v144\n"
"v_sub_f32 v99, 0, v144\n"
"s_waitcnt lgkmcnt(0)\n"
"v_cmp_gt_u32 vcc, 22, v248\n"
"s_nop 1\n"
"s_cbranch_vccnz K1_A22_S\n"
"K1_A22_R:\n"
"ds_read_b128 a[160:163], v240 offset:16384\n"
"ds_read_b128 a[164:167], v240 offset:16640\n"
"ds_read_b128 a[168:171], v240 offset:16896\n"
"ds_read_b128 a[172:175], v240 offset:17152\n"
"ds_read_b128 a[176:179], v240 offset:20480\n"
"ds_read_b128 a[180:183], v240 offset:20736\n"
"ds_read_b128 a[184:187], v240 offset:20992\n"
"ds_read_b128 a[188:191], v240 offset:21248\n"
"s_add_u32 s71, s94, 32768\n"
"s_and_b32 s71, s71, 0x1ffff\n"
"s_add_u32 m0, s70, 98304\n"
"s_nop 0\n"
"buffer_load_dwordx4 v249, s[44:47], s71 offen nt lds\n"
"buffer_load_dwordx4 v252, s[44:47], s71 offen offset:1024 nt lds\n"
"buffer_load_dwordx4 v251, s[44:47], s71 offen offset:2048 nt lds\n"
"buffer_load_dwordx4 v250, s[44:47], s71 offen offset:3072 nt lds\n"
"v_pk_add_f32 v[0:1], v[0:1], v[80:81]\n"
"v_pk_add_f32 v[2:3], v[2:3], v[80:81]\n"
"v_pk_add_f32 v[4:5], v[4:5], v[80:81]\n"
"v_pk_add_f32 v[6:7], v[6:7], v[80:81]\n"
"v_pk_add_f32 v[8:9], v[8:9], v[84:85]\n"
"v_pk_add_f32 v[10:11], v[10:11], v[84:85]\n"
"v_pk_add_f32 v[12:13], v[12:13], v[84:85]\n"
"v_pk_add_f32 v[14:15], v[14:15], v[84:85]\n"
"v_pk_add_f32 v[16:17], v[16:17], v[88:89]\n"
"v_pk_add_f32 v[18:19], v[18:19], v[88:89]\n"
"v_pk_add_f32 v[20:21], v[20:21], v[88:89]\n"
"v_pk_add_f32 v[22:23], v[22:23], v[88:89]\n"
"v_pk_add_f32 v[24:25], v[24:25], v[92:93]\n"
"v_pk_add_f32 v[26:27], v[26:27], v[92:93]\n"
"v_pk_add_f32 v[28:29], v[28:29], v[92:93]\n"
"v_pk_add_f32 v[30:31], v[30:31], v[92:93]\n"
"v_pk_add_f32 v[32:33], v[32:33], v[96:97]\n"
"v_pk_add_f32 v[34:35], v[34:35], v[96:97]\n"
"v_pk_add_f32 v[36:37], v[36:37], v[96:97]\n"
"v_pk_add_f32 v[38:39], v[38:39], v[96:97]\n"
"v_exp_f32 v0, v0\n"
"v_exp_f32 v1, v1\n"
"v_exp_f32 v2, v2\n"
"v_exp_f32 v3, v3\n"
"v_exp_f32 v4, v4\n"
"v_exp_f32 v5, v5\n"
"v_exp_f32 v6, v6\n"
"v_exp_f32 v7, v7\n"
"v_exp_f32 v8, v8\n"
"v_exp_f32 v9, v9\n"
"v_exp_f32 v10, v10\n"
"v_exp_f32 v11, v11\n"
"v_exp_f32 v12, v12\n"
"v_exp_f32 v13, v13\n"
"v_exp_f32 v14, v14\n"
"v_exp_f32 v15, v15\n"
"s_waitcnt lgkmcnt(0)\n"
"MM v[40:43], a[160:163], v[160:163], v[80:83]\n"
"v_cvt_pk_bf16_f32 v100, v0, v1\n"
"v_cvt_pk_bf16_f32 v101, v2, v3\n"
"MM v[40:43], a[164:167], v[180:183], v[40:43]\n"
"v_cvt_pk_bf16_f32 v102, v4, v5\n"
"ds_read_b64_tr_b16 a[192:193], v243 offset:32768\n"
"MM v[40:43], a[168:171], v[200:203], v[40:43]\n"
"v_cvt_pk_bf16_f32 v103, v6, v7\n"
"ds_read_b64_tr_b16 a[194:195], v243 offset:36864\n"
"MM v[40:43], a[172:175], v[220:223], v[40:43]\n"
"v_exp_f32 v16, v16\n"
"MM v[44:47], a[176:179], v[160:163], v[80:83]\n"
"v_exp_f32 v17, v17\n"
"MM v[44:47], a[180:183], v[180:183], v[44:47]\n"
"v_exp_f32 v18, v18\n"
"MM v[44:47], a[184:187], v[200:203], v[44:47]\n"
"v_exp_f32 v19, v19\n"
"MM v[44:47], a[188:191], v[220:223], v[44:47]\n"
"ds_read_b64_tr_b16 a[196:197], v246 offset:32768\n"
"ds_read_b64_tr_b16 a[198:199], v246 offset:36864\n"
"MM v[48:51], a[160:163], v[164:167], v[84:87]\n"
"v_exp_f32 v20, v20\n"
"MM v[48:51], a[164:167], v[184:187], v[48:51]\n"
"v_exp_f32 v21, v21\n"
"MM v[48:51], a[168:171], v[204:207], v[48:51]\n"
"v_exp_f32 v22, v22\n"
"MM v[48:51], a[172:175], v[224:227], v[48:51]\n"
"v_max_f32 v140, v40, v41\n"
"ds_read_b64_tr_b16 a[200:201], v243 offset:33024\n"
"MM v[52:55], a[176:179], v[164:167], v[84:87]\n"
"v_exp_f32 v23, v23\n"
"MM v[52:55], a[180:183], v[184:187], v[52:55]\n"
"v_cvt_pk_bf16_f32 v104, v8, v9\n"
"v_cvt_pk_bf16_f32 v105, v10, v11\n"
"MM v[52:55], a[184:187], v[204:207], v[52:55]\n"
"v_cvt_pk_bf16_f32 v106, v12, v13\n"
"v_max3_f32 v140, v42, v43, v140\n"
"MM v[52:55], a[188:191], v[224:227], v[52:55]\n"
"v_cvt_pk_bf16_f32 v107, v14, v15\n"
"ds_read_b64_tr_b16 a[202:203], v243 offset:37120\n"
"MM v[56:59], a[160:163], v[168:171], v[88:91]\n"
"v_exp_f32 v24, v24\n"
"MM v[56:59], a[164:167], v[188:191], v[56:59]\n"
"v_exp_f32 v25, v25\n"
"v_max3_f32 v140, v44, v45, v140\n"
"MM v[56:59], a[168:171], v[208:211], v[56:59]\n"
"v_exp_f32 v26, v26\n"
"ds_read_b64_tr_b16 a[204:205], v246 offset:33024\n"
"MM v[56:59], a[172:175], v[228:231], v[56:59]\n"
"v_exp_f32 v27, v27\n"
"v_max3_f32 v140, v46, v47, v140\n"
"MM v[60:63], a[176:179], v[168:171], v[88:91]\n"
"v_exp_f32 v28, v28\n"
"ds_read_b64_tr_b16 a[206:207], v246 offset:37120\n"
"MM v[60:63], a[180:183], v[188:191], v[60:63]\n"
"v_exp_f32 v29, v29\n"
"v_max3_f32 v140, v48, v49, v140\n"
"MM v[60:63], a[184:187], v[208:211], v[60:63]\n"
"ds_read_b64_tr_b16 a[208:209], v243 offset:33280\n"
"v_max3_f32 v140, v50, v51, v140\n"
"MM v[60:63], a[188:191], v[228:231], v[60:63]\n"
"v_exp_f32 v30, v30\n"
"MM v[64:67], a[160:163], v[172:175], v[92:95]\n"
"v_exp_f32 v31, v31\n"
"ds_read_b64_tr_b16 a[210:211], v243 offset:37376\n"
"MM v[64:67], a[164:167], v[192:195], v[64:67]\n"
"v_cvt_pk_bf16_f32 v108, v16, v17\n"
"v_max3_f32 v140, v52, v53, v140\n"
"v_cvt_pk_bf16_f32 v109, v18, v19\n"
"MM v[64:67], a[168:171], v[212:215], v[64:67]\n"
"v_cvt_pk_bf16_f32 v110, v20, v21\n"
"ds_read_b64_tr_b16 a[212:213], v246 offset:33280\n"
"MM v[64:67], a[172:175], v[232:235], v[64:67]\n"
"v_cvt_pk_bf16_f32 v111, v22, v23\n"
"v_max3_f32 v140, v54, v55, v140\n"
"MM v[68:71], a[176:179], v[172:175], v[92:95]\n"
"v_exp_f32 v32, v32\n"
"ds_read_b64_tr_b16 a[214:215], v246 offset:37376\n"
"MM v[68:71], a[180:183], v[192:195], v[68:71]\n"
"v_exp_f32 v33, v33\n"
"v_max3_f32 v140, v56, v57, v140\n"
"MM v[68:71], a[184:187], v[212:215], v[68:71]\n"
"v_exp_f32 v34, v34\n"
"ds_read_b64_tr_b16 a[216:217], v243 offset:33536\n"
"MM v[68:71], a[188:191], v[232:235], v[68:71]\n"
"v_exp_f32 v35, v35\n"
"v_max3_f32 v140, v58, v59, v140\n"
"ds_read_b64_tr_b16 a[218:219], v243 offset:37632\n"
"ds_read_b64_tr_b16 a[220:221], v246 offset:33536\n"
"ds_read_b64_tr_b16 a[222:223], v246 offset:37632\n"
"MM v[72:75], a[160:163], v[176:179], v[96:99]\n"
"v_exp_f32 v36, v36\n"
"MM v[72:75], a[164:167], v[196:199], v[72:75]\n"
"v_exp_f32 v37, v37\n"
"v_max3_f32 v140, v60, v61, v140\n"
"MM v[72:75], a[168:171], v[216:219], v[72:75]\n"
"v_exp_f32 v38, v38\n"
"MM v[72:75], a[172:175], v[236:239], v[72:75]\n"
"v_exp_f32 v39, v39\n"
"v_max3_f32 v140, v62, v63, v140\n"
"MM v[76:79], a[176:179], v[176:179], v[96:99]\n"
"v_cvt_pk_bf16_f32 v112, v24, v25\n"
"v_max3_f32 v140, v64, v65, v140\n"
"MM v[76:79], a[180:183], v[196:199], v[76:79]\n"
"v_cvt_pk_bf16_f32 v113, v26, v27\n"
"v_cvt_pk_bf16_f32 v114, v28, v29\n"
"MM v[76:79], a[184:187], v[216:219], v[76:79]\n"
"v_cvt_pk_bf16_f32 v115, v30, v31\n"
"v_max3_f32 v140, v66, v67, v140\n"
"MM v[76:79], a[188:191], v[236:239], v[76:79]\n"
"v_cvt_pk_bf16_f32 v116, v32, v33\n"
"v_cvt_pk_bf16_f32 v117, v34, v35\n"
"v_max3_f32 v140, v68, v69, v140\n"
"v_max3_f32 v140, v70, v71, v140\n"
"v_cvt_pk_bf16_f32 v118, v36, v37\n"
"v_cvt_pk_bf16_f32 v119, v38, v39\n"
"s_waitcnt vmcnt(12)\n"
"s_waitcnt lgkmcnt(0)\n"
"v_mov_b32 v248, 23\n"
"ds_write_b32 v242, v248 offset:0\n"
"MM a[224:227], a[244:247], v[100:103], 0\n"
"v_max3_f32 v140, v72, v73, v140\n"
"v_max3_f32 v140, v74, v75, v140\n"
"MM a[0:3], a[192:195], v[100:103], 0\n"
"MM a[4:7], a[196:199], v[100:103], 0\n"
"v_max3_f32 v140, v76, v77, v140\n"
"v_max3_f32 v140, v78, v79, v140\n"
"MM a[8:11], a[200:203], v[100:103], 0\n"
"v_cmp_lt_f32 vcc, s76, v140\n"
"MM a[12:15], a[204:207], v[100:103], 0\n"
"s_nop 0\n"
"s_cbranch_vccnz K1_ESA1\n"
"K1_ENA1:\n"
"MM a[16:19], a[208:211], v[100:103], 0\n"
"v_exp_f32 v40, v40\n"
"MM a[20:23], a[212:215], v[100:103], 0\n"
"ds_read_b32 v248, v245 offset:0\n"
"MM a[24:27], a[216:219], v[100:103], 0\n"
"v_exp_f32 v41, v41\n"
"MM a[28:31], a[220:223], v[100:103], 0\n"
"v_exp_f32 v42, v42\n"
"MM a[228:231], a[244:247], v[104:107], 0\n"
"v_exp_f32 v43, v43\n"
"MM a[32:35], a[192:195], v[104:107], 0\n"
"s_waitcnt lgkmcnt(0)\n"
"v_cmp_gt_u32 vcc, 23, v248\n"
"MM a[36:39], a[196:199], v[104:107], 0\n"
"v_exp_f32 v44, v44\n"
"MM a[40:43], a[200:203], v[104:107], 0\n"
"s_cbranch_vccnz K1_A23_S\n"
"K1_A23_R:\n"
"s_add_u32 s71, s94, 24576\n"
"MM a[44:47], a[204:207], v[104:107], 0\n"
"v_exp_f32 v45, v45\n"
"MM a[48:51], a[208:211], v[104:107], 0\n"
"v_exp_f32 v46, v46\n"
"MM a[52:55], a[212:215], v[104:107], 0\n"
"ds_read_b128 a[160:163], v240 offset:49152\n"
"s_and_b32 s71, s71, 0x1ffff\n"
"MM a[56:59], a[216:219], v[104:107], 0\n"
"v_exp_f32 v47, v47\n"
"MM a[60:63], a[220:223], v[104:107], 0\n"
"v_exp_f32 v48, v48\n"
"MM a[232:235], a[244:247], v[108:111], 0\n"
"s_add_u32 m0, s70, 0\n"
"ds_read_b128 a[164:167], v240 offset:49408\n"
"MM a[64:67], a[192:195], v[108:111], 0\n"
"v_exp_f32 v49, v49\n"
"MM a[68:71], a[196:199], v[108:111], 0\n"
"buffer_load_dwordx4 v249, s[48:51], s71 offen nt lds\n"
"MM a[72:75], a[200:203], v[108:111], 0\n"
"v_exp_f32 v50, v50\n"
"MM a[76:79], a[204:207], v[108:111], 0\n"
"v_exp_f32 v51, v51\n"
"MM a[80:83], a[208:211], v[108:111], 0\n"
"ds_read_b128 a[168:171], v240 offset:49664\n"
"ds_read_b128 a[172:175], v240 offset:49920\n"
"MM a[84:87], a[212:215], v[108:111], 0\n"
"v_exp_f32 v52, v52\n"
"MM a[88:91], a[216:219], v[108:111], 0\n"
"v_exp_f32 v53, v53\n"
"MM a[92:95], a[220:223], v[108:111], 0\n"
"v_exp_f32 v54, v54\n"
"MM a[236:239], a[244:247], v[112:115], 0\n"
"buffer_load_dwordx4 v251, s[48:51], s71 offen offset:1024 nt lds\n"
"MM a[96:99], a[192:195], v[112:115], 0\n"
"v_exp_f32 v55, v55\n"
"MM a[100:103], a[196:199], v[112:115], 0\n"
"ds_read_b128 a[176:179], v240 offset:53248\n"
"v_cvt_pk_bf16_f32 v120, v40, v41\n"
"MM a[104:107], a[200:203], v[112:115], 0\n"
"v_cvt_pk_bf16_f32 v121, v42, v43\n"
"ds_read_b128 a[180:183], v240 offset:53504\n"
"MM a[108:111], a[204:207], v[112:115], 0\n"
"v_cvt_pk_bf16_f32 v122, v44, v45\n"
"v_cvt_pk_bf16_f32 v123, v46, v47\n"
"MM a[112:115], a[208:211], v[112:115], 0\n"
"v_exp_f32 v56, v56\n"
"MM a[116:119], a[212:215], v[112:115], 0\n"
"buffer_load_dwordx4 v249, s[48:51], s71 offen offset:2048 nt lds\n"
"MM a[120:123], a[216:219], v[112:115], 0\n"
"v_exp_f32 v57, v57\n"
"ds_read_b128 a[184:187], v240 offset:53760\n"
"ds_read_b128 a[188:191], v240 offset:54016\n"
"MM a[124:127], a[220:223], v[112:115], 0\n"
"v_exp_f32 v58, v58\n"
"MM a[240:243], a[244:247], v[116:119], 0\n"
"v_exp_f32 v59, v59\n"
"MM a[128:131], a[192:195], v[116:119], 0\n"
"v_exp_f32 v60, v60\n"
"MM a[132:135], a[196:199], v[116:119], 0\n"
"buffer_load_dwordx4 v251, s[48:51], s71 offen offset:3072 nt lds\n"
"MM a[136:139], a[200:203], v[116:119], 0\n"
"v_exp_f32 v61, v61\n"
"MM a[140:143], a[204:207], v[116:119], 0\n"
"v_exp_f32 v62, v62\n"
"MM a[144:147], a[208:211], v[116:119], 0\n"
"v_exp_f32 v63, v63\n"
"MM a[148:151], a[212:215], v[116:119], 0\n"
"v_cvt_pk_bf16_f32 v124, v48, v49\n"
"v_cvt_pk_bf16_f32 v125, v50, v51\n"
"MM a[152:155], a[216:219], v[116:119], 0\n"
"MM a[156:159], a[220:223], v[116:119], 0\n"
"s_waitcnt lgkmcnt(0)\n"
"s_cmp_lg_u32 s75, 0\n"
"s_cbranch_scc1 K1_ESB1\n"
"K1_ENB1:\n"
"s_cmp_lg_u32 s60, 3\n"
"s_cbranch_scc1 K1_EFWA\n"
"v_subrev_u32 v159, 16, v253\n"
"v_bfe_u32 v157, v159, 4, 1\n"
"s_lshl_b32 s68, s87, 1\n"
"v_add_u32 v157, s68, v157\n"
"v_and_b32 v158, 7, v159\n"
"v_bfe_u32 v156, v159, 3, 1\n"
"v_lshl_add_u32 v158, v158, 1, v156\n"
"v_cmp_gt_u32 vcc, 32, v159\n"
"v_mov_b32 v156, s63\n"
"s_nop 0\n"
"v_cndmask_b32 v157, v156, v157, vcc\n"
"v_mov_b32 v156, s64\n"
"v_cndmask_b32 v158, v156, v158, vcc\n"
"s_and_b32 s68, s64, 1\n"
"v_and_b32 v156, 7, v253\n"
"v_lshl_add_u32 v156, v156, 1, s68\n"
"s_mov_b32 vcc_lo, 0xff\n"
"s_mov_b32 vcc_hi, 0xff000000\n"
"v_cndmask_b32 v158, v158, v156, vcc\n"
"s_xor_b32 s68, s64, 1\n"
"v_mov_b32 v156, s68\n"
"v_cmp_eq_u32 vcc, 8, v253\n"
"s_nop 1\n"
"v_cndmask_b32 v158, v158, v156, vcc\n"
"v_and_b32 v156, 7, v157\n"
"v_lshlrev_b32 v156, 5, v156\n"
"v_and_b32 v159, 3, v158\n"
"v_lshl_add_u32 v156, v159, 3, v156\n"
"v_lshrrev_b32 v159, 3, v157\n"
"v_lshl_add_u32 v156, v159, 2, v156\n"
"v_lshrrev_b32 v159, 2, v158\n"
"v_add_u32 v156, v156, v159\n"
"v_and_b32 v157, 3, v253\n"
"v_lshlrev_b32 v157, 5, v157\n"
"v_mov_b32 v155, 0\n"
"v_cmp_gt_u32 vcc, 48, v253\n"
"s_nop 1\n"
"v_cndmask_b32 v157, v157, v155, vcc\n"
"v_mov_b32 v155, 32\n"
"v_cmp_eq_u32 vcc, 8, v253\n"
"s_nop 1\n"
"v_cndmask_b32 v157, v157, v155, vcc\n"
"v_mov_b32 v155, 64\n"
"v_cmp_gt_u32 vcc, 8, v253\n"
"s_nop 1\n"
"v_cndmask_b32 v157, v157, v155, vcc\n"
"v_mov_b32 v155, 96\n"
"v_cmp_le_u32 vcc, 56, v253\n"
"s_nop 1\n"
"v_cndmask_b32 v157, v157, v155, vcc\n"
"v_lshl_add_u32 v155, v156, 7, v157\n"
"v_add_u32 v155, 42991616, v155\n"
"s_nop 1\n"
"buffer_load_dword v156, v155, s[56:59], 0 offen sc1\n"
"K1_EFWA:\n"
"s_nop 1\n"
"MM v[0:3], a[160:163], v[160:163], v[80:83]\n"
"v_cvt_pk_bf16_f32 v126, v52, v53\n"
"ds_read_b64_tr_b16 a[192:193], v244 offset:49152\n"
"MM v[0:3], a[164:167], v[180:183], v[0:3]\n"
"v_cvt_pk_bf16_f32 v127, v54, v55\n"
"ds_read_b64_tr_b16 a[194:195], v244 offset:53248\n"
"MM v[0:3], a[168:171], v[200:203], v[0:3]\n"
"v_exp_f32 v64, v64\n"
"MM v[0:3], a[172:175], v[220:223], v[0:3]\n"
"v_exp_f32 v65, v65\n"
"MM v[4:7], a[176:179], v[160:163], v[80:83]\n"
"ds_read_b64_tr_b16 a[196:197], v247 offset:49152\n"
"ds_read_b64_tr_b16 a[198:199], v247 offset:53248\n"
"MM v[4:7], a[180:183], v[180:183], v[4:7]\n"
"v_exp_f32 v66, v66\n"
"MM v[4:7], a[184:187], v[200:203], v[4:7]\n"
"v_exp_f32 v67, v67\n"
"MM v[4:7], a[188:191], v[220:223], v[4:7]\n"
"v_max_f32 v140, v0, v1\n"
"ds_read_b64_tr_b16 a[200:201], v244 offset:49408\n"
"MM v[8:11], a[160:163], v[164:167], v[84:87]\n"
"v_exp_f32 v68, v68\n"
"MM v[8:11], a[164:167], v[184:187], v[8:11]\n"
"v_max3_f32 v140, v2, v3, v140\n"
"ds_read_b64_tr_b16 a[202:203], v244 offset:53504\n"
"MM v[8:11], a[168:171], v[204:207], v[8:11]\n"
"v_exp_f32 v69, v69\n"
"MM v[8:11], a[172:175], v[224:227], v[8:11]\n"
"v_max3_f32 v140, v4, v5, v140\n"
"ds_read_b64_tr_b16 a[204:205], v247 offset:49408\n"
"MM v[12:15], a[176:179], v[164:167], v[84:87]\n"
"v_exp_f32 v70, v70\n"
"MM v[12:15], a[180:183], v[184:187], v[12:15]\n"
"v_max3_f32 v140, v6, v7, v140\n"
"ds_read_b64_tr_b16 a[206:207], v247 offset:53504\n"
"MM v[12:15], a[184:187], v[204:207], v[12:15]\n"
"v_exp_f32 v71, v71\n"
"MM v[12:15], a[188:191], v[224:227], v[12:15]\n"
"v_cvt_pk_bf16_f32 v128, v56, v57\n"
"v_max3_f32 v140, v8, v9, v140\n"
"MM v[16:19], a[160:163], v[168:171], v[88:91]\n"
"v_cvt_pk_bf16_f32 v129, v58, v59\n"
"ds_read_b64_tr_b16 a[208:209], v244 offset:49664\n"
"MM v[16:19], a[164:167], v[188:191], v[16:19]\n"
"v_cvt_pk_bf16_f32 v130, v60, v61\n"
"v_max3_f32 v140, v10, v11, v140\n"
"MM v[16:19], a[168:171], v[208:211], v[16:19]\n"
"v_cvt_pk_bf16_f32 v131, v62, v63\n"
"ds_read_b64_tr_b16 a[210:211], v244 offset:53760\n"
"MM v[16:19], a[172:175], v[228:231], v[16:19]\n"
"v_exp_f32 v72, v72\n"
"MM v[20:23], a[176:179], v[168:171], v[88:91]\n"
"v_max3_f32 v140, v12, v13, v140\n"
"ds_read_b64_tr_b16 a[212:213], v247 offset:49664\n"
"MM v[20:23], a[180:183], v[188:191], v[20:23]\n"
"v_exp_f32 v73, v73\n"
"MM v[20:23], a[184:187], v[208:211], v[20:23]\n"
"v_max3_f32 v140, v14, v15, v140\n"
"ds_read_b64_tr_b16 a[214:215], v247 offset:53760\n"
"MM v[20:23], a[188:191], v[228:231], v[20:23]\n"
"v_exp_f32 v74, v74\n"
"MM v[24:27], a[160:163], v[172:175], v[92:95]\n"
"v_exp_f32 v75, v75\n"
"MM v[24:27], a[164:167], v[192:195], v[24:27]\n"
"v_max3_f32 v140, v16, v17, v140\n"
"ds_read_b64_tr_b16 a[216:217], v244 offset:49920\n"
"MM v[24:27], a[168:171], v[212:215], v[24:27]\n"
"v_exp_f32 v76, v76\n"
"MM v[24:27], a[172:175], v[232:235], v[24:27]\n"
"v_max3_f32 v140, v18, v19, v140\n"
"ds_read_b64_tr_b16 a[218:219], v244 offset:54016\n"
"MM v[28:31], a[176:179], v[172:175], v[92:95]\n"
"v_exp_f32 v77, v77\n"
"MM v[28:31], a[180:183], v[192:195], v[28:31]\n"
"v_max3_f32 v140, v20, v21, v140\n"
"ds_read_b64_tr_b16 a[220:221], v247 offset:49920\n"
"MM v[28:31], a[184:187], v[212:215], v[28:31]\n"
"v_exp_f32 v78, v78\n"
"MM v[28:31], a[188:191], v[232:235], v[28:31]\n"
"v_max3_f32 v140, v22, v23, v140\n"
"ds_read_b64_tr_b16 a[222:223], v247 offset:54016\n"
"MM v[32:35], a[160:163], v[176:179], v[96:99]\n"
"v_exp_f32 v79, v79\n"
"MM v[32:35], a[164:167], v[196:199], v[32:35]\n"
"v_cvt_pk_bf16_f32 v132, v64, v65\n"
"v_max3_f32 v140, v24, v25, v140\n"
"MM v[32:35], a[168:171], v[216:219], v[32:35]\n"
"v_cvt_pk_bf16_f32 v133, v66, v67\n"
"v_max3_f32 v140, v26, v27, v140\n"
"MM v[32:35], a[172:175], v[236:239], v[32:35]\n"
"v_cvt_pk_bf16_f32 v134, v68, v69\n"
"v_cvt_pk_bf16_f32 v135, v70, v71\n"
"MM v[36:39], a[176:179], v[176:179], v[96:99]\n"
"v_cvt_pk_bf16_f32 v136, v72, v73\n"
"v_max3_f32 v140, v28, v29, v140\n"
"MM v[36:39], a[180:183], v[196:199], v[36:39]\n"
"v_cvt_pk_bf16_f32 v137, v74, v75\n"
"v_max3_f32 v140, v30, v31, v140\n"
"MM v[36:39], a[184:187], v[216:219], v[36:39]\n"
"v_cvt_pk_bf16_f32 v138, v76, v77\n"
"v_cvt_pk_bf16_f32 v139, v78, v79\n"
"MM v[36:39], a[188:191], v[236:239], v[36:39]\n"
"s_waitcnt vmcnt(8)\n"
"s_waitcnt lgkmcnt(0)\n"
"v_mov_b32 v248, 24\n"
"ds_write_b32 v242, v248 offset:0\n"
"MM a[224:227], a[244:247], v[120:123], a[224:227]\n"
"v_max3_f32 v140, v32, v33, v140\n"
"v_max3_f32 v140, v34, v35, v140\n"
"MM a[0:3], a[192:195], v[120:123], a[0:3]\n"
"MM a[4:7], a[196:199], v[120:123], a[4:7]\n"
"v_max3_f32 v140, v36, v37, v140\n"
"v_max3_f32 v140, v38, v39, v140\n"
"MM a[8:11], a[200:203], v[120:123], a[8:11]\n"
"v_cmp_lt_f32 vcc, s76, v140\n"
"MM a[12:15], a[204:207], v[120:123], a[12:15]\n"
"s_nop 0\n"
"s_cbranch_vccnz K1_ESA2\n"
"K1_ENA2:\n"
"MM a[16:19], a[208:211], v[120:123], a[16:19]\n"
"v_exp_f32 v0, v0\n"
"MM a[20:23], a[212:215], v[120:123], a[20:23]\n"
"ds_read_b32 v248, v245 offset:0\n"
"MM a[24:27], a[216:219], v[120:123], a[24:27]\n"
"v_exp_f32 v1, v1\n"
"MM a[28:31], a[220:223], v[120:123], a[28:31]\n"
"v_exp_f32 v2, v2\n"
"MM a[228:231], a[244:247], v[124:127], a[228:231]\n"
"v_exp_f32 v3, v3\n"
"MM a[32:35], a[192:195], v[124:127], a[32:35]\n"
"s_waitcnt lgkmcnt(0)\n"
"v_cmp_gt_u32 vcc, 24, v248\n"
"MM a[36:39], a[196:199], v[124:127], a[36:39]\n"
"v_exp_f32 v4, v4\n"
"MM a[40:43], a[200:203], v[124:127], a[40:43]\n"
"s_cbranch_vccnz K1_A24_S\n"
"K1_A24_R:\n"
"s_add_u32 s71, s94, 40960\n"
"MM a[44:47], a[204:207], v[124:127], a[44:47]\n"
"v_exp_f32 v5, v5\n"
"MM a[48:51], a[208:211], v[124:127], a[48:51]\n"
"s_and_b32 s71, s71, 0x1ffff\n"
"s_add_u32 m0, s70, 16384\n"
"MM a[52:55], a[212:215], v[124:127], a[52:55]\n"
"v_exp_f32 v6, v6\n"
"MM a[56:59], a[216:219], v[124:127], a[56:59]\n"
"v_exp_f32 v7, v7\n"
"MM a[60:63], a[220:223], v[124:127], a[60:63]\n"
"buffer_load_dwordx4 v249, s[44:47], s71 offen nt lds\n"
"MM a[232:235], a[244:247], v[128:131], a[232:235]\n"
"v_exp_f32 v8, v8\n"
"MM a[64:67], a[192:195], v[128:131], a[64:67]\n"
"ds_read_b128 a[160:163], v241 offset:0\n"
"ds_read_b128 a[164:167], v241 offset:256\n"
"MM a[68:71], a[196:199], v[128:131], a[68:71]\n"
"v_exp_f32 v9, v9\n"
"MM a[72:75], a[200:203], v[128:131], a[72:75]\n"
"buffer_load_dwordx4 v252, s[44:47], s71 offen offset:1024 nt lds\n"
"MM a[76:79], a[204:207], v[128:131], a[76:79]\n"
"v_exp_f32 v10, v10\n"
"MM a[80:83], a[208:211], v[128:131], a[80:83]\n"
"v_exp_f32 v11, v11\n"
"MM a[84:87], a[212:215], v[128:131], a[84:87]\n"
"v_exp_f32 v12, v12\n"
"MM a[88:91], a[216:219], v[128:131], a[88:91]\n"
"buffer_load_dwordx4 v251, s[44:47], s71 offen offset:2048 nt lds\n"
"MM a[92:95], a[220:223], v[128:131], a[92:95]\n"
"v_exp_f32 v13, v13\n"
"ds_read_b128 a[168:171], v241 offset:512\n"
"MM a[236:239], a[244:247], v[132:135], a[236:239]\n"
"v_exp_f32 v14, v14\n"
"MM a[96:99], a[192:195], v[132:135], a[96:99]\n"
"ds_read_b128 a[172:175], v241 offset:768\n"
"ds_read_b128 a[176:179], v241 offset:4096\n"
"MM a[100:103], a[196:199], v[132:135], a[100:103]\n"
"v_exp_f32 v15, v15\n"
"MM a[104:107], a[200:203], v[132:135], a[104:107]\n"
"buffer_load_dwordx4 v250, s[44:47], s71 offen offset:3072 nt lds\n"
"v_cvt_pk_bf16_f32 v100, v0, v1\n"
"MM a[108:111], a[204:207], v[132:135], a[108:111]\n"
"v_cvt_pk_bf16_f32 v101, v2, v3\n"
"v_cvt_pk_bf16_f32 v102, v4, v5\n"
"MM a[112:115], a[208:211], v[132:135], a[112:115]\n"
"v_cvt_pk_bf16_f32 v103, v6, v7\n"
"ds_read_b128 a[180:183], v241 offset:4352\n"
"MM a[116:119], a[212:215], v[132:135], a[116:119]\n"
"s_getpc_b64 s[52:53]\n"
"K1_PC2:\n"
"s_sub_u32 s52, s52, K1_PC2-K1_START\n"
"s_subb_u32 s53, s53, 0\n"
"MM a[120:123], a[216:219], v[132:135], a[120:123]\n"
"v_exp_f32 v16, v16\n"
"ds_read_b128 a[184:187], v241 offset:4608\n"
"ds_read_b128 a[188:191], v241 offset:4864\n"
"MM a[124:127], a[220:223], v[132:135], a[124:127]\n"
"v_exp_f32 v17, v17\n"
"MM a[240:243], a[244:247], v[136:139], a[240:243]\n"
"s_and_b32 s53, s53, 0xffff\n"
"s_mov_b32 s54, K1_CODE_END-K1_START\n"
"MM a[128:131], a[192:195], v[136:139], a[128:131]\n"
"v_exp_f32 v18, v18\n"
"MM a[132:135], a[196:199], v[136:139], a[132:135]\n"
"v_exp_f32 v19, v19\n"
"s_mov_b32 s55, 0x20000\n"
"MM a[136:139], a[200:203], v[136:139], a[136:139]\n"
"s_lshr_b32 s98, s65, 3\n"
"s_lshl_b32 s98, s98, 2\n"
"MM a[140:143], a[204:207], v[136:139], a[140:143]\n"
"v_exp_f32 v20, v20\n"
"MM a[144:147], a[208:211], v[136:139], a[144:147]\n"
"v_exp_f32 v21, v21\n"
"s_add_u32 s98, s98, s60\n"
"MM a[148:151], a[212:215], v[136:139], a[148:151]\n"
"s_lshl_b32 s98, s98, 10\n"
"v_lshlrev_b32 v149, 4, v253\n"
"MM a[152:155], a[216:219], v[136:139], a[152:155]\n"
"v_exp_f32 v22, v22\n"
"MM a[156:159], a[220:223], v[136:139], a[156:159]\n"
"v_exp_f32 v23, v23\n"
"v_cvt_pk_bf16_f32 v104, v8, v9\n"
"v_cvt_pk_bf16_f32 v105, v10, v11\n"
"buffer_load_dwordx4 a[248:251], v149, s[52:55], s98 offen\n"
"s_waitcnt lgkmcnt(0)\n"
"s_cmp_lg_u32 s75, 0\n"
"s_cbranch_scc1 K1_ESB2\n"
"K1_ENB2:\n"
"MM v[40:43], a[160:163], v[160:163], v[80:83]\n"
"v_cvt_pk_bf16_f32 v106, v12, v13\n"
"ds_read_b64_tr_b16 a[192:193], v244 offset:16384\n"
"MM v[40:43], a[164:167], v[180:183], v[40:43]\n"
"v_cvt_pk_bf16_f32 v107, v14, v15\n"
"ds_read_b64_tr_b16 a[194:195], v244 offset:20480\n"
"MM v[40:43], a[168:171], v[200:203], v[40:43]\n"
"v_exp_f32 v24, v24\n"
"MM v[40:43], a[172:175], v[220:223], v[40:43]\n"
"v_exp_f32 v25, v25\n"
"MM v[44:47], a[176:179], v[160:163], v[80:83]\n"
"ds_read_b64_tr_b16 a[196:197], v247 offset:16384\n"
"ds_read_b64_tr_b16 a[198:199], v247 offset:20480\n"
"MM v[44:47], a[180:183], v[180:183], v[44:47]\n"
"v_exp_f32 v26, v26\n"
"MM v[44:47], a[184:187], v[200:203], v[44:47]\n"
"v_exp_f32 v27, v27\n"
"MM v[44:47], a[188:191], v[220:223], v[44:47]\n"
"v_max_f32 v140, v40, v41\n"
"ds_read_b64_tr_b16 a[200:201], v244 offset:16640\n"
"MM v[48:51], a[160:163], v[164:167], v[84:87]\n"
"v_exp_f32 v28, v28\n"
"MM v[48:51], a[164:167], v[184:187], v[48:51]\n"
"v_max3_f32 v140, v42, v43, v140\n"
"ds_read_b64_tr_b16 a[202:203], v244 offset:20736\n"
"MM v[48:51], a[168:171], v[204:207], v[48:51]\n"
"v_exp_f32 v29, v29\n"
"MM v[48:51], a[172:175], v[224:227], v[48:51]\n"
"v_max3_f32 v140, v44, v45, v140\n"
"ds_read_b64_tr_b16 a[204:205], v247 offset:16640\n"
"MM v[52:55], a[176:179], v[164:167], v[84:87]\n"
"v_exp_f32 v30, v30\n"
"MM v[52:55], a[180:183], v[184:187], v[52:55]\n"
"v_max3_f32 v140, v46, v47, v140\n"
"ds_read_b64_tr_b16 a[206:207], v247 offset:20736\n"
"MM v[52:55], a[184:187], v[204:207], v[52:55]\n"
"v_exp_f32 v31, v31\n"
"MM v[52:55], a[188:191], v[224:227], v[52:55]\n"
"v_cvt_pk_bf16_f32 v108, v16, v17\n"
"v_max3_f32 v140, v48, v49, v140\n"
"MM v[56:59], a[160:163], v[168:171], v[88:91]\n"
"v_cvt_pk_bf16_f32 v109, v18, v19\n"
"ds_read_b64_tr_b16 a[208:209], v244 offset:16896\n"
"MM v[56:59], a[164:167], v[188:191], v[56:59]\n"
"v_cvt_pk_bf16_f32 v110, v20, v21\n"
"v_max3_f32 v140, v50, v51, v140\n"
"MM v[56:59], a[168:171], v[208:211], v[56:59]\n"
"v_cvt_pk_bf16_f32 v111, v22, v23\n"
"ds_read_b64_tr_b16 a[210:211], v244 offset:20992\n"
"MM v[56:59], a[172:175], v[228:231], v[56:59]\n"
"v_exp_f32 v32, v32\n"
"MM v[60:63], a[176:179], v[168:171], v[88:91]\n"
"v_max3_f32 v140, v52, v53, v140\n"
"ds_read_b64_tr_b16 a[212:213], v247 offset:16896\n"
"MM v[60:63], a[180:183], v[188:191], v[60:63]\n"
"v_exp_f32 v33, v33\n"
"MM v[60:63], a[184:187], v[208:211], v[60:63]\n"
"v_max3_f32 v140, v54, v55, v140\n"
"ds_read_b64_tr_b16 a[214:215], v247 offset:20992\n"
"MM v[60:63], a[188:191], v[228:231], v[60:63]\n"
"v_exp_f32 v34, v34\n"
"MM v[64:67], a[160:163], v[172:175], v[92:95]\n"
"v_exp_f32 v35, v35\n"
"MM v[64:67], a[164:167], v[192:195], v[64:67]\n"
"v_max3_f32 v140, v56, v57, v140\n"
"ds_read_b64_tr_b16 a[216:217], v244 offset:17152\n"
"MM v[64:67], a[168:171], v[212:215], v[64:67]\n"
"v_exp_f32 v36, v36\n"
"MM v[64:67], a[172:175], v[232:235], v[64:67]\n"
"v_max3_f32 v140, v58, v59, v140\n"
"ds_read_b64_tr_b16 a[218:219], v244 offset:21248\n"
"MM v[68:71], a[176:179], v[172:175], v[92:95]\n"
"v_exp_f32 v37, v37\n"
"MM v[68:71], a[180:183], v[192:195], v[68:71]\n"
"v_max3_f32 v140, v60, v61, v140\n"
"ds_read_b64_tr_b16 a[220:221], v247 offset:17152\n"
"MM v[68:71], a[184:187], v[212:215], v[68:71]\n"
"v_exp_f32 v38, v38\n"
"MM v[68:71], a[188:191], v[232:235], v[68:71]\n"
"v_max3_f32 v140, v62, v63, v140\n"
"ds_read_b64_tr_b16 a[222:223], v247 offset:21248\n"
"MM v[72:75], a[160:163], v[176:179], v[96:99]\n"
"v_exp_f32 v39, v39\n"
"MM v[72:75], a[164:167], v[196:199], v[72:75]\n"
"v_cvt_pk_bf16_f32 v112, v24, v25\n"
"v_max3_f32 v140, v64, v65, v140\n"
"MM v[72:75], a[168:171], v[216:219], v[72:75]\n"
"v_cvt_pk_bf16_f32 v113, v26, v27\n"
"v_max3_f32 v140, v66, v67, v140\n"
"MM v[72:75], a[172:175], v[236:239], v[72:75]\n"
"v_cvt_pk_bf16_f32 v114, v28, v29\n"
"v_cvt_pk_bf16_f32 v115, v30, v31\n"
"MM v[76:79], a[176:179], v[176:179], v[96:99]\n"
"v_cvt_pk_bf16_f32 v116, v32, v33\n"
"v_max3_f32 v140, v68, v69, v140\n"
"MM v[76:79], a[180:183], v[196:199], v[76:79]\n"
"v_cvt_pk_bf16_f32 v117, v34, v35\n"
"v_max3_f32 v140, v70, v71, v140\n"
"MM v[76:79], a[184:187], v[216:219], v[76:79]\n"
"v_cvt_pk_bf16_f32 v118, v36, v37\n"
"v_cvt_pk_bf16_f32 v119, v38, v39\n"
"MM v[76:79], a[188:191], v[236:239], v[76:79]\n"
"s_waitcnt vmcnt(5)\n"
"s_waitcnt lgkmcnt(0)\n"
"v_mov_b32 v248, 25\n"
"ds_write_b32 v242, v248 offset:0\n"
"MM a[224:227], a[244:247], v[100:103], a[224:227]\n"
"v_max3_f32 v140, v72, v73, v140\n"
"v_max3_f32 v140, v74, v75, v140\n"
"MM a[0:3], a[192:195], v[100:103], a[0:3]\n"
"MM a[4:7], a[196:199], v[100:103], a[4:7]\n"
"v_max3_f32 v140, v76, v77, v140\n"
"v_max3_f32 v140, v78, v79, v140\n"
"MM a[8:11], a[200:203], v[100:103], a[8:11]\n"
"v_cmp_lt_f32 vcc, s76, v140\n"
"MM a[12:15], a[204:207], v[100:103], a[12:15]\n"
"s_nop 0\n"
"s_cbranch_vccnz K1_ESA3\n"
"K1_ENA3:\n"
"MM a[16:19], a[208:211], v[100:103], a[16:19]\n"
"v_exp_f32 v40, v40\n"
"MM a[20:23], a[212:215], v[100:103], a[20:23]\n"
"ds_read_b32 v248, v245 offset:0\n"
"MM a[24:27], a[216:219], v[100:103], a[24:27]\n"
"v_exp_f32 v41, v41\n"
"MM a[28:31], a[220:223], v[100:103], a[28:31]\n"
"v_exp_f32 v42, v42\n"
"MM a[228:231], a[244:247], v[104:107], a[228:231]\n"
"v_exp_f32 v43, v43\n"
"MM a[32:35], a[192:195], v[104:107], a[32:35]\n"
"s_waitcnt lgkmcnt(0)\n"
"v_cmp_gt_u32 vcc, 25, v248\n"
"MM a[36:39], a[196:199], v[104:107], a[36:39]\n"
"v_exp_f32 v44, v44\n"
"MM a[40:43], a[200:203], v[104:107], a[40:43]\n"
"s_cbranch_vccnz K1_A25_S\n"
"K1_A25_R:\n"
"s_add_u32 s71, s94, 32768\n"
"MM a[44:47], a[204:207], v[104:107], a[44:47]\n"
"v_exp_f32 v45, v45\n"
"MM a[48:51], a[208:211], v[104:107], a[48:51]\n"
"v_exp_f32 v46, v46\n"
"MM a[52:55], a[212:215], v[104:107], a[52:55]\n"
"v_exp_f32 v47, v47\n"
"MM a[56:59], a[216:219], v[104:107], a[56:59]\n"
"v_exp_f32 v48, v48\n"
"MM a[60:63], a[220:223], v[104:107], a[60:63]\n"
"s_and_b32 s71, s71, 0x1ffff\n"
"ds_read_b128 a[160:163], v241 offset:32768\n"
"MM a[232:235], a[244:247], v[108:111], a[232:235]\n"
"v_exp_f32 v49, v49\n"
"MM a[64:67], a[192:195], v[108:111], a[64:67]\n"
"s_add_u32 m0, s70, 32768\n"
"ds_read_b128 a[164:167], v241 offset:33024\n"
"MM a[68:71], a[196:199], v[108:111], a[68:71]\n"
"v_exp_f32 v50, v50\n"
"MM a[72:75], a[200:203], v[108:111], a[72:75]\n"
"v_exp_f32 v51, v51\n"
"MM a[76:79], a[204:207], v[108:111], a[76:79]\n"
"buffer_load_dwordx4 v249, s[48:51], s71 offen nt lds\n"
"MM a[80:83], a[208:211], v[108:111], a[80:83]\n"
"v_exp_f32 v52, v52\n"
"MM a[84:87], a[212:215], v[108:111], a[84:87]\n"
"v_exp_f32 v53, v53\n"
"MM a[88:91], a[216:219], v[108:111], a[88:91]\n"
"v_exp_f32 v54, v54\n"
"MM a[92:95], a[220:223], v[108:111], a[92:95]\n"
"ds_read_b128 a[168:171], v241 offset:33280\n"
"ds_read_b128 a[172:175], v241 offset:33536\n"
"MM a[236:239], a[244:247], v[112:115], a[236:239]\n"
"v_exp_f32 v55, v55\n"
"MM a[96:99], a[192:195], v[112:115], a[96:99]\n"
"v_cvt_pk_bf16_f32 v120, v40, v41\n"
"v_cvt_pk_bf16_f32 v121, v42, v43\n"
"MM a[100:103], a[196:199], v[112:115], a[100:103]\n"
"v_cvt_pk_bf16_f32 v122, v44, v45\n"
"v_cvt_pk_bf16_f32 v123, v46, v47\n"
"MM a[104:107], a[200:203], v[112:115], a[104:107]\n"
"buffer_load_dwordx4 v251, s[48:51], s71 offen offset:1024 nt lds\n"
"MM a[108:111], a[204:207], v[112:115], a[108:111]\n"
"v_exp_f32 v56, v56\n"
"MM a[112:115], a[208:211], v[112:115], a[112:115]\n"
"v_exp_f32 v57, v57\n"
"ds_read_b128 a[176:179], v241 offset:36864\n"
"MM a[116:119], a[212:215], v[112:115], a[116:119]\n"
"v_exp_f32 v58, v58\n"
"MM a[120:123], a[216:219], v[112:115], a[120:123]\n"
"v_exp_f32 v59, v59\n"
"ds_read_b128 a[180:183], v241 offset:37120\n"
"ds_read_b128 a[184:187], v241 offset:37376\n"
"ds_read_b128 a[188:191], v241 offset:37632\n"
"MM a[124:127], a[220:223], v[112:115], a[124:127]\n"
"buffer_load_dwordx4 v249, s[48:51], s71 offen offset:2048 nt lds\n"
"MM a[240:243], a[244:247], v[116:119], a[240:243]\n"
"v_exp_f32 v60, v60\n"
"MM a[128:131], a[192:195], v[116:119], a[128:131]\n"
"v_exp_f32 v61, v61\n"
"MM a[132:135], a[196:199], v[116:119], a[132:135]\n"
"v_exp_f32 v62, v62\n"
"MM a[136:139], a[200:203], v[116:119], a[136:139]\n"
"v_exp_f32 v63, v63\n"
"MM a[140:143], a[204:207], v[116:119], a[140:143]\n"
"v_cvt_pk_bf16_f32 v124, v48, v49\n"
"v_cvt_pk_bf16_f32 v125, v50, v51\n"
"MM a[144:147], a[208:211], v[116:119], a[144:147]\n"
"buffer_load_dwordx4 v251, s[48:51], s71 offen offset:3072 nt lds\n"
"MM a[148:151], a[212:215], v[116:119], a[148:151]\n"
"v_cvt_pk_bf16_f32 v126, v52, v53\n"
"v_cvt_pk_bf16_f32 v127, v54, v55\n"
"MM a[152:155], a[216:219], v[116:119], a[152:155]\n"
"v_exp_f32 v64, v64\n"
"MM a[156:159], a[220:223], v[116:119], a[156:159]\n"
"v_exp_f32 v65, v65\n"
"s_waitcnt lgkmcnt(0)\n"
"s_cmp_lg_u32 s75, 0\n"
"s_cbranch_scc1 K1_ESB3\n"
"K1_ENB3:\n"
"MM v[0:3], a[160:163], v[160:163], v[80:83]\n"
"v_exp_f32 v66, v66\n"
"MM v[0:3], a[164:167], v[180:183], v[0:3]\n"
"ds_read_b64_tr_b16 a[192:193], v243 offset:0\n"
"s_add_u32 s71, s94, 49152\n"
"MM v[0:3], a[168:171], v[200:203], v[0:3]\n"
"v_exp_f32 v67, v67\n"
"MM v[0:3], a[172:175], v[220:223], v[0:3]\n"
"ds_read_b64_tr_b16 a[194:195], v243 offset:4096\n"
"s_and_b32 s71, s71, 0x1ffff\n"
"MM v[4:7], a[176:179], v[160:163], v[80:83]\n"
"v_exp_f32 v68, v68\n"
"MM v[4:7], a[180:183], v[180:183], v[4:7]\n"
"ds_read_b64_tr_b16 a[196:197], v246 offset:0\n"
"s_add_u32 m0, s70, 49152\n"
"MM v[4:7], a[184:187], v[200:203], v[4:7]\n"
"v_exp_f32 v69, v69\n"
"MM v[4:7], a[188:191], v[220:223], v[4:7]\n"
"ds_read_b64_tr_b16 a[198:199], v246 offset:4096\n"
"v_max_f32 v140, v0, v1\n"
"MM v[8:11], a[160:163], v[164:167], v[84:87]\n"
"buffer_load_dwordx4 v249, s[44:47], s71 offen nt lds\n"
"MM v[8:11], a[164:167], v[184:187], v[8:11]\n"
"v_exp_f32 v70, v70\n"
"MM v[8:11], a[168:171], v[204:207], v[8:11]\n"
"ds_read_b64_tr_b16 a[200:201], v243 offset:256\n"
"v_max3_f32 v140, v2, v3, v140\n"
"MM v[8:11], a[172:175], v[224:227], v[8:11]\n"
"v_exp_f32 v71, v71\n"
"MM v[12:15], a[176:179], v[164:167], v[84:87]\n"
"ds_read_b64_tr_b16 a[202:203], v243 offset:4352\n"
"v_cvt_pk_bf16_f32 v128, v56, v57\n"
"MM v[12:15], a[180:183], v[184:187], v[12:15]\n"
"v_max3_f32 v140, v4, v5, v140\n"
"v_cvt_pk_bf16_f32 v129, v58, v59\n"
"MM v[12:15], a[184:187], v[204:207], v[12:15]\n"
"ds_read_b64_tr_b16 a[204:205], v246 offset:256\n"
"v_cvt_pk_bf16_f32 v130, v60, v61\n"
"MM v[12:15], a[188:191], v[224:227], v[12:15]\n"
"v_max3_f32 v140, v6, v7, v140\n"
"v_cvt_pk_bf16_f32 v131, v62, v63\n"
"MM v[16:19], a[160:163], v[168:171], v[88:91]\n"
"ds_read_b64_tr_b16 a[206:207], v246 offset:4352\n"
"v_max3_f32 v140, v8, v9, v140\n"
"MM v[16:19], a[164:167], v[188:191], v[16:19]\n"
"v_exp_f32 v72, v72\n"
"MM v[16:19], a[168:171], v[208:211], v[16:19]\n"
"buffer_load_dwordx4 v252, s[44:47], s71 offen offset:1024 nt lds\n"
"MM v[16:19], a[172:175], v[228:231], v[16:19]\n"
"v_exp_f32 v73, v73\n"
"MM v[20:23], a[176:179], v[168:171], v[88:91]\n"
"ds_read_b64_tr_b16 a[208:209], v243 offset:512\n"
"v_max3_f32 v140, v10, v11, v140\n"
"MM v[20:23], a[180:183], v[188:191], v[20:23]\n"
"v_exp_f32 v74, v74\n"
"MM v[20:23], a[184:187], v[208:211], v[20:23]\n"
"ds_read_b64_tr_b16 a[210:211], v243 offset:4608\n"
"v_max3_f32 v140, v12, v13, v140\n"
"MM v[20:23], a[188:191], v[228:231], v[20:23]\n"
"v_exp_f32 v75, v75\n"
"MM v[24:27], a[160:163], v[172:175], v[92:95]\n"
"ds_read_b64_tr_b16 a[212:213], v246 offset:512\n"
"v_max3_f32 v140, v14, v15, v140\n"
"MM v[24:27], a[164:167], v[192:195], v[24:27]\n"
"v_exp_f32 v76, v76\n"
"MM v[24:27], a[168:171], v[212:215], v[24:27]\n"
"ds_read_b64_tr_b16 a[214:215], v246 offset:4608\n"
"v_max3_f32 v140, v16, v17, v140\n"
"MM v[24:27], a[172:175], v[232:235], v[24:27]\n"
"buffer_load_dwordx4 v251, s[44:47], s71 offen offset:2048 nt lds\n"
"MM v[28:31], a[176:179], v[172:175], v[92:95]\n"
"v_exp_f32 v77, v77\n"
"MM v[28:31], a[180:183], v[192:195], v[28:31]\n"
"ds_read_b64_tr_b16 a[216:217], v243 offset:768\n"
"v_max3_f32 v140, v18, v19, v140\n"
"MM v[28:31], a[184:187], v[212:215], v[28:31]\n"
"v_exp_f32 v78, v78\n"
"MM v[28:31], a[188:191], v[232:235], v[28:31]\n"
"ds_read_b64_tr_b16 a[218:219], v243 offset:4864\n"
"v_max3_f32 v140, v20, v21, v140\n"
"ds_read_b64_tr_b16 a[220:221], v246 offset:768\n"
"ds_read_b64_tr_b16 a[222:223], v246 offset:4864\n"
"MM v[32:35], a[160:163], v[176:179], v[96:99]\n"
"v_exp_f32 v79, v79\n"
"MM v[32:35], a[164:167], v[196:199], v[32:35]\n"
"v_max3_f32 v140, v22, v23, v140\n"
"v_cvt_pk_bf16_f32 v132, v64, v65\n"
"MM v[32:35], a[168:171], v[216:219], v[32:35]\n"
"v_cvt_pk_bf16_f32 v133, v66, v67\n"
"v_max3_f32 v140, v24, v25, v140\n"
"MM v[32:35], a[172:175], v[236:239], v[32:35]\n"
"buffer_load_dwordx4 v250, s[44:47], s71 offen offset:3072 nt lds\n"
"MM v[36:39], a[176:179], v[176:179], v[96:99]\n"
"v_cvt_pk_bf16_f32 v134, v68, v69\n"
"v_cvt_pk_bf16_f32 v135, v70, v71\n"
"MM v[36:39], a[180:183], v[196:199], v[36:39]\n"
"v_max3_f32 v140, v26, v27, v140\n"
"v_cvt_pk_bf16_f32 v136, v72, v73\n"
"MM v[36:39], a[184:187], v[216:219], v[36:39]\n"
"v_cvt_pk_bf16_f32 v137, v74, v75\n"
"v_max3_f32 v140, v28, v29, v140\n"
"MM v[36:39], a[188:191], v[236:239], v[36:39]\n"
"v_cvt_pk_bf16_f32 v138, v76, v77\n"
"v_max3_f32 v140, v30, v31, v140\n"
"v_cvt_pk_bf16_f32 v139, v78, v79\n"
"s_waitcnt vmcnt(4)\n"
"s_waitcnt lgkmcnt(0)\n"
"v_mov_b32 v248, 26\n"
"ds_write_b32 v242, v248 offset:0\n"
"MM a[224:227], a[244:247], v[120:123], a[224:227]\n"
"v_max3_f32 v140, v32, v33, v140\n"
"v_max3_f32 v140, v34, v35, v140\n"
"MM a[0:3], a[192:195], v[120:123], a[0:3]\n"
"MM a[4:7], a[196:199], v[120:123], a[4:7]\n"
"v_max3_f32 v140, v36, v37, v140\n"
"v_max3_f32 v140, v38, v39, v140\n"
"MM a[8:11], a[200:203], v[120:123], a[8:11]\n"
"v_cmp_lt_f32 vcc, s76, v140\n"
"MM a[12:15], a[204:207], v[120:123], a[12:15]\n"
"s_nop 0\n"
"s_cbranch_vccnz K1_ESA4\n"
"K1_ENA4:\n"
"MM a[16:19], a[208:211], v[120:123], a[16:19]\n"
"v_exp_f32 v0, v0\n"
"MM a[20:23], a[212:215], v[120:123], a[20:23]\n"
"ds_read_b32 v248, v245 offset:0\n"
"MM a[24:27], a[216:219], v[120:123], a[24:27]\n"
"v_exp_f32 v1, v1\n"
"MM a[28:31], a[220:223], v[120:123], a[28:31]\n"
"v_exp_f32 v2, v2\n"
"MM a[228:231], a[244:247], v[124:127], a[228:231]\n"
"v_exp_f32 v3, v3\n"
"MM a[32:35], a[192:195], v[124:127], a[32:35]\n"
"s_waitcnt lgkmcnt(0)\n"
"v_cmp_gt_u32 vcc, 26, v248\n"
"MM a[36:39], a[196:199], v[124:127], a[36:39]\n"
"v_exp_f32 v4, v4\n"
"MM a[40:43], a[200:203], v[124:127], a[40:43]\n"
"s_cbranch_vccnz K1_A26_S\n"
"K1_A26_R:\n"
"s_add_u32 s71, s94, 40960\n"
"MM a[44:47], a[204:207], v[124:127], a[44:47]\n"
"v_exp_f32 v5, v5\n"
"MM a[48:51], a[208:211], v[124:127], a[48:51]\n"
"v_exp_f32 v6, v6\n"
"MM a[52:55], a[212:215], v[124:127], a[52:55]\n"
"v_exp_f32 v7, v7\n"
"MM a[56:59], a[216:219], v[124:127], a[56:59]\n"
"v_exp_f32 v8, v8\n"
"MM a[60:63], a[220:223], v[124:127], a[60:63]\n"
"s_and_b32 s71, s71, 0x1ffff\n"
"ds_read_b128 a[160:163], v240 offset:16384\n"
"MM a[232:235], a[244:247], v[128:131], a[232:235]\n"
"v_exp_f32 v9, v9\n"
"MM a[64:67], a[192:195], v[128:131], a[64:67]\n"
"s_add_u32 m0, s70, 114688\n"
"ds_read_b128 a[164:167], v240 offset:16640\n"
"MM a[68:71], a[196:199], v[128:131], a[68:71]\n"
"v_exp_f32 v10, v10\n"
"MM a[72:75], a[200:203], v[128:131], a[72:75]\n"
"v_exp_f32 v11, v11\n"
"MM a[76:79], a[204:207], v[128:131], a[76:79]\n"
"buffer_load_dwordx4 v249, s[48:51], s71 offen nt lds\n"
"MM a[80:83], a[208:211], v[128:131], a[80:83]\n"
"v_exp_f32 v12, v12\n"
"MM a[84:87], a[212:215], v[128:131], a[84:87]\n"
"v_exp_f32 v13, v13\n"
"MM a[88:91], a[216:219], v[128:131], a[88:91]\n"
"v_exp_f32 v14, v14\n"
"MM a[92:95], a[220:223], v[128:131], a[92:95]\n"
"ds_read_b128 a[168:171], v240 offset:16896\n"
"ds_read_b128 a[172:175], v240 offset:17152\n"
"MM a[236:239], a[244:247], v[132:135], a[236:239]\n"
"v_exp_f32 v15, v15\n"
"MM a[96:99], a[192:195], v[132:135], a[96:99]\n"
"v_cvt_pk_bf16_f32 v100, v0, v1\n"
"v_cvt_pk_bf16_f32 v101, v2, v3\n"
"MM a[100:103], a[196:199], v[132:135], a[100:103]\n"
"v_cvt_pk_bf16_f32 v102, v4, v5\n"
"v_cvt_pk_bf16_f32 v103, v6, v7\n"
"MM a[104:107], a[200:203], v[132:135], a[104:107]\n"
"buffer_load_dwordx4 v251, s[48:51], s71 offen offset:1024 nt lds\n"
"MM a[108:111], a[204:207], v[132:135], a[108:111]\n"
"v_exp_f32 v16, v16\n"
"MM a[112:115], a[208:211], v[132:135], a[112:115]\n"
"v_exp_f32 v17, v17\n"
"ds_read_b128 a[176:179], v240 offset:20480\n"
"MM a[116:119], a[212:215], v[132:135], a[116:119]\n"
"v_exp_f32 v18, v18\n"
"MM a[120:123], a[216:219], v[132:135], a[120:123]\n"
"v_exp_f32 v19, v19\n"
"ds_read_b128 a[180:183], v240 offset:20736\n"
"ds_read_b128 a[184:187], v240 offset:20992\n"
"ds_read_b128 a[188:191], v240 offset:21248\n"
"MM a[124:127], a[220:223], v[132:135], a[124:127]\n"
"buffer_load_dwordx4 v249, s[48:51], s71 offen offset:2048 nt lds\n"
"MM a[240:243], a[244:247], v[136:139], a[240:243]\n"
"v_exp_f32 v20, v20\n"
"MM a[128:131], a[192:195], v[136:139], a[128:131]\n"
"v_exp_f32 v21, v21\n"
"MM a[132:135], a[196:199], v[136:139], a[132:135]\n"
"v_exp_f32 v22, v22\n"
"MM a[136:139], a[200:203], v[136:139], a[136:139]\n"
"v_exp_f32 v23, v23\n"
"MM a[140:143], a[204:207], v[136:139], a[140:143]\n"
"v_cvt_pk_bf16_f32 v104, v8, v9\n"
"v_cvt_pk_bf16_f32 v105, v10, v11\n"
"MM a[144:147], a[208:211], v[136:139], a[144:147]\n"
"buffer_load_dwordx4 v251, s[48:51], s71 offen offset:3072 nt lds\n"
"MM a[148:151], a[212:215], v[136:139], a[148:151]\n"
"v_cvt_pk_bf16_f32 v106, v12, v13\n"
"v_cvt_pk_bf16_f32 v107, v14, v15\n"
"MM a[152:155], a[216:219], v[136:139], a[152:155]\n"
"v_exp_f32 v24, v24\n"
"MM a[156:159], a[220:223], v[136:139], a[156:159]\n"
"v_exp_f32 v25, v25\n"
"s_waitcnt lgkmcnt(0)\n"
"s_cmp_lg_u32 s75, 0\n"
"s_cbranch_scc1 K1_ESB4\n"
"K1_ENB4:\n"
"MM v[40:43], a[160:163], v[160:163], v[80:83]\n"
"v_exp_f32 v26, v26\n"
"MM v[40:43], a[164:167], v[180:183], v[40:43]\n"
"ds_read_b64_tr_b16 a[192:193], v243 offset:32768\n"
"s_add_u32 s71, s94, 57344\n"
"MM v[40:43], a[168:171], v[200:203], v[40:43]\n"
"v_exp_f32 v27, v27\n"
"MM v[40:43], a[172:175], v[220:223], v[40:43]\n"
"ds_read_b64_tr_b16 a[194:195], v243 offset:36864\n"
"s_and_b32 s71, s71, 0x1ffff\n"
"MM v[44:47], a[176:179], v[160:163], v[80:83]\n"
"v_exp_f32 v28, v28\n"
"MM v[44:47], a[180:183], v[180:183], v[44:47]\n"
"ds_read_b64_tr_b16 a[196:197], v246 offset:32768\n"
"s_add_u32 m0, s70, 65536\n"
"MM v[44:47], a[184:187], v[200:203], v[44:47]\n"
"v_exp_f32 v29, v29\n"
"MM v[44:47], a[188:191], v[220:223], v[44:47]\n"
"ds_read_b64_tr_b16 a[198:199], v246 offset:36864\n"
"v_max_f32 v140, v40, v41\n"
"MM v[48:51], a[160:163], v[164:167], v[84:87]\n"
"buffer_load_dwordx4 v249, s[44:47], s71 offen nt lds\n"
"MM v[48:51], a[164:167], v[184:187], v[48:51]\n"
"v_exp_f32 v30, v30\n"
"MM v[48:51], a[168:171], v[204:207], v[48:51]\n"
"ds_read_b64_tr_b16 a[200:201], v243 offset:33024\n"
"v_max3_f32 v140, v42, v43, v140\n"
"MM v[48:51], a[172:175], v[224:227], v[48:51]\n"
"v_exp_f32 v31, v31\n"
"MM v[52:55], a[176:179], v[164:167], v[84:87]\n"
"ds_read_b64_tr_b16 a[202:203], v243 offset:37120\n"
"v_cvt_pk_bf16_f32 v108, v16, v17\n"
"MM v[52:55], a[180:183], v[184:187], v[52:55]\n"
"v_max3_f32 v140, v44, v45, v140\n"
"v_cvt_pk_bf16_f32 v109, v18, v19\n"
"MM v[52:55], a[184:187], v[204:207], v[52:55]\n"
"ds_read_b64_tr_b16 a[204:205], v246 offset:33024\n"
"v_cvt_pk_bf16_f32 v110, v20, v21\n"
"MM v[52:55], a[188:191], v[224:227], v[52:55]\n"
"v_max3_f32 v140, v46, v47, v140\n"
"v_cvt_pk_bf16_f32 v111, v22, v23\n"
"MM v[56:59], a[160:163], v[168:171], v[88:91]\n"
"ds_read_b64_tr_b16 a[206:207], v246 offset:37120\n"
"v_max3_f32 v140, v48, v49, v140\n"
"MM v[56:59], a[164:167], v[188:191], v[56:59]\n"
"v_exp_f32 v32, v32\n"
"MM v[56:59], a[168:171], v[208:211], v[56:59]\n"
"buffer_load_dwordx4 v252, s[44:47], s71 offen offset:1024 nt lds\n"
"MM v[56:59], a[172:175], v[228:231], v[56:59]\n"
"v_exp_f32 v33, v33\n"
"MM v[60:63], a[176:179], v[168:171], v[88:91]\n"
"ds_read_b64_tr_b16 a[208:209], v243 offset:33280\n"
"v_max3_f32 v140, v50, v51, v140\n"
"MM v[60:63], a[180:183], v[188:191], v[60:63]\n"
"v_exp_f32 v34, v34\n"
"MM v[60:63], a[184:187], v[208:211], v[60:63]\n"
"ds_read_b64_tr_b16 a[210:211], v243 offset:37376\n"
"v_max3_f32 v140, v52, v53, v140\n"
"MM v[60:63], a[188:191], v[228:231], v[60:63]\n"
"v_exp_f32 v35, v35\n"
"MM v[64:67], a[160:163], v[172:175], v[92:95]\n"
"ds_read_b64_tr_b16 a[212:213], v246 offset:33280\n"
"v_max3_f32 v140, v54, v55, v140\n"
"MM v[64:67], a[164:167], v[192:195], v[64:67]\n"
"v_exp_f32 v36, v36\n"
"MM v[64:67], a[168:171], v[212:215], v[64:67]\n"
"ds_read_b64_tr_b16 a[214:215], v246 offset:37376\n"
"v_max3_f32 v140, v56, v57, v140\n"
"MM v[64:67], a[172:175], v[232:235], v[64:67]\n"
"buffer_load_dwordx4 v251, s[44:47], s71 offen offset:2048 nt lds\n"
"MM v[68:71], a[176:179], v[172:175], v[92:95]\n"
"v_exp_f32 v37, v37\n"
"MM v[68:71], a[180:183], v[192:195], v[68:71]\n"
"ds_read_b64_tr_b16 a[216:217], v243 offset:33536\n"
"v_max3_f32 v140, v58, v59, v140\n"
"MM v[68:71], a[184:187], v[212:215], v[68:71]\n"
"v_exp_f32 v38, v38\n"
"MM v[68:71], a[188:191], v[232:235], v[68:71]\n"
"ds_read_b64_tr_b16 a[218:219], v243 offset:37632\n"
"v_max3_f32 v140, v60, v61, v140\n"
"ds_read_b64_tr_b16 a[220:221], v246 offset:33536\n"
"ds_read_b64_tr_b16 a[222:223], v246 offset:37632\n"
"MM v[72:75], a[160:163], v[176:179], v[96:99]\n"
"v_exp_f32 v39, v39\n"
"MM v[72:75], a[164:167], v[196:199], v[72:75]\n"
"v_max3_f32 v140, v62, v63, v140\n"
"v_cvt_pk_bf16_f32 v112, v24, v25\n"
"MM v[72:75], a[168:171], v[216:219], v[72:75]\n"
"v_cvt_pk_bf16_f32 v113, v26, v27\n"
"v_max3_f32 v140, v64, v65, v140\n"
"MM v[72:75], a[172:175], v[236:239], v[72:75]\n"
"buffer_load_dwordx4 v250, s[44:47], s71 offen offset:3072 nt lds\n"
"MM v[76:79], a[176:179], v[176:179], v[96:99]\n"
"v_cvt_pk_bf16_f32 v114, v28, v29\n"
"v_cvt_pk_bf16_f32 v115, v30, v31\n"
"MM v[76:79], a[180:183], v[196:199], v[76:79]\n"
"v_max3_f32 v140, v66, v67, v140\n"
"v_cvt_pk_bf16_f32 v116, v32, v33\n"
"MM v[76:79], a[184:187], v[216:219], v[76:79]\n"
"v_cvt_pk_bf16_f32 v117, v34, v35\n"
"v_max3_f32 v140, v68, v69, v140\n"
"MM v[76:79], a[188:191], v[236:239], v[76:79]\n"
"v_cvt_pk_bf16_f32 v118, v36, v37\n"
"v_max3_f32 v140, v70, v71, v140\n"
"v_cvt_pk_bf16_f32 v119, v38, v39\n"
"s_waitcnt vmcnt(4)\n"
"s_waitcnt lgkmcnt(0)\n"
"v_mov_b32 v248, 27\n"
"ds_write_b32 v242, v248 offset:0\n"
"MM a[224:227], a[244:247], v[100:103], a[224:227]\n"
"v_max3_f32 v140, v72, v73, v140\n"
"v_max3_f32 v140, v74, v75, v140\n"
"MM a[0:3], a[192:195], v[100:103], a[0:3]\n"
"MM a[4:7], a[196:199], v[100:103], a[4:7]\n"
"v_max3_f32 v140, v76, v77, v140\n"
"v_max3_f32 v140, v78, v79, v140\n"
"MM a[8:11], a[200:203], v[100:103], a[8:11]\n"
"v_cmp_lt_f32 vcc, s76, v140\n"
"MM a[12:15], a[204:207], v[100:103], a[12:15]\n"
"s_nop 0\n"
"s_cbranch_vccnz K1_ESA5\n"
"K1_ENA5:\n"
"MM a[16:19], a[208:211], v[100:103], a[16:19]\n"
"v_exp_f32 v40, v40\n"
"MM a[20:23], a[212:215], v[100:103], a[20:23]\n"
"ds_read_b32 v248, v245 offset:0\n"
"MM a[24:27], a[216:219], v[100:103], a[24:27]\n"
"v_exp_f32 v41, v41\n"
"MM a[28:31], a[220:223], v[100:103], a[28:31]\n"
"v_exp_f32 v42, v42\n"
"MM a[228:231], a[244:247], v[104:107], a[228:231]\n"
"v_exp_f32 v43, v43\n"
"MM a[32:35], a[192:195], v[104:107], a[32:35]\n"
"s_waitcnt lgkmcnt(0)\n"
"v_cmp_gt_u32 vcc, 27, v248\n"
"MM a[36:39], a[196:199], v[104:107], a[36:39]\n"
"v_exp_f32 v44, v44\n"
"MM a[40:43], a[200:203], v[104:107], a[40:43]\n"
"s_cbranch_vccnz K1_A27_S\n"
"K1_A27_R:\n"
"s_add_u32 s71, s94, 49152\n"
"MM a[44:47], a[204:207], v[104:107], a[44:47]\n"
"v_exp_f32 v45, v45\n"
"MM a[48:51], a[208:211], v[104:107], a[48:51]\n"
"v_exp_f32 v46, v46\n"
"MM a[52:55], a[212:215], v[104:107], a[52:55]\n"
"v_exp_f32 v47, v47\n"
"MM a[56:59], a[216:219], v[104:107], a[56:59]\n"
"v_exp_f32 v48, v48\n"
"MM a[60:63], a[220:223], v[104:107], a[60:63]\n"
"s_and_b32 s71, s71, 0x1ffff\n"
"ds_read_b128 a[160:163], v240 offset:49152\n"
"MM a[232:235], a[244:247], v[108:111], a[232:235]\n"
"v_exp_f32 v49, v49\n"
"MM a[64:67], a[192:195], v[108:111], a[64:67]\n"
"s_add_u32 m0, s70, 81920\n"
"ds_read_b128 a[164:167], v240 offset:49408\n"
"MM a[68:71], a[196:199], v[108:111], a[68:71]\n"
"v_exp_f32 v50, v50\n"
"MM a[72:75], a[200:203], v[108:111], a[72:75]\n"
"v_exp_f32 v51, v51\n"
"MM a[76:79], a[204:207], v[108:111], a[76:79]\n"
"buffer_load_dwordx4 v249, s[48:51], s71 offen nt lds\n"
"MM a[80:83], a[208:211], v[108:111], a[80:83]\n"
"v_exp_f32 v52, v52\n"
"MM a[84:87], a[212:215], v[108:111], a[84:87]\n"
"v_exp_f32 v53, v53\n"
"MM a[88:91], a[216:219], v[108:111], a[88:91]\n"
"v_exp_f32 v54, v54\n"
"MM a[92:95], a[220:223], v[108:111], a[92:95]\n"
"ds_read_b128 a[168:171], v240 offset:49664\n"
"ds_read_b128 a[172:175], v240 offset:49920\n"
"MM a[236:239], a[244:247], v[112:115], a[236:239]\n"
"v_exp_f32 v55, v55\n"
"MM a[96:99], a[192:195], v[112:115], a[96:99]\n"
"v_cvt_pk_bf16_f32 v120, v40, v41\n"
"v_cvt_pk_bf16_f32 v121, v42, v43\n"
"MM a[100:103], a[196:199], v[112:115], a[100:103]\n"
"v_cvt_pk_bf16_f32 v122, v44, v45\n"
"v_cvt_pk_bf16_f32 v123, v46, v47\n"
"MM a[104:107], a[200:203], v[112:115], a[104:107]\n"
"buffer_load_dwordx4 v251, s[48:51], s71 offen offset:1024 nt lds\n"
"MM a[108:111], a[204:207], v[112:115], a[108:111]\n"
"v_exp_f32 v56, v56\n"
"MM a[112:115], a[208:211], v[112:115], a[112:115]\n"
"v_exp_f32 v57, v57\n"
"ds_read_b128 a[176:179], v240 offset:53248\n"
"MM a[116:119], a[212:215], v[112:115], a[116:119]\n"
"v_exp_f32 v58, v58\n"
"MM a[120:123], a[216:219], v[112:115], a[120:123]\n"
"v_exp_f32 v59, v59\n"
"ds_read_b128 a[180:183], v240 offset:53504\n"
"ds_read_b128 a[184:187], v240 offset:53760\n"
"ds_read_b128 a[188:191], v240 offset:54016\n"
"MM a[124:127], a[220:223], v[112:115], a[124:127]\n"
"buffer_load_dwordx4 v249, s[48:51], s71 offen offset:2048 nt lds\n"
"MM a[240:243], a[244:247], v[116:119], a[240:243]\n"
"v_exp_f32 v60, v60\n"
"MM a[128:131], a[192:195], v[116:119], a[128:131]\n"
"v_exp_f32 v61, v61\n"
"MM a[132:135], a[196:199], v[116:119], a[132:135]\n"
"v_exp_f32 v62, v62\n"
"MM a[136:139], a[200:203], v[116:119], a[136:139]\n"
"v_exp_f32 v63, v63\n"
"MM a[140:143], a[204:207], v[116:119], a[140:143]\n"
"v_cvt_pk_bf16_f32 v124, v48, v49\n"
"v_cvt_pk_bf16_f32 v125, v50, v51\n"
"MM a[144:147], a[208:211], v[116:119], a[144:147]\n"
"buffer_load_dwordx4 v251, s[48:51], s71 offen offset:3072 nt lds\n"
"MM a[148:151], a[212:215], v[116:119], a[148:151]\n"
"v_cvt_pk_bf16_f32 v126, v52, v53\n"
"v_cvt_pk_bf16_f32 v127, v54, v55\n"
"MM a[152:155], a[216:219], v[116:119], a[152:155]\n"
"v_exp_f32 v64, v64\n"
"MM a[156:159], a[220:223], v[116:119], a[156:159]\n"
"v_exp_f32 v65, v65\n"
"s_waitcnt lgkmcnt(0)\n"
"s_cmp_lg_u32 s75, 0\n"
"s_cbranch_scc1 K1_ESB5\n"
"K1_ENB5:\n"
"MM v[0:3], a[160:163], v[160:163], v[80:83]\n"
"v_exp_f32 v66, v66\n"
"MM v[0:3], a[164:167], v[180:183], v[0:3]\n"
"ds_read_b64_tr_b16 a[192:193], v244 offset:49152\n"
"s_add_u32 s71, s94, 65536\n"
"MM v[0:3], a[168:171], v[200:203], v[0:3]\n"
"v_exp_f32 v67, v67\n"
"MM v[0:3], a[172:175], v[220:223], v[0:3]\n"
"ds_read_b64_tr_b16 a[194:195], v244 offset:53248\n"
"s_and_b32 s71, s71, 0x1ffff\n"
"MM v[4:7], a[176:179], v[160:163], v[80:83]\n"
"v_exp_f32 v68, v68\n"
"MM v[4:7], a[180:183], v[180:183], v[4:7]\n"
"ds_read_b64_tr_b16 a[196:197], v247 offset:49152\n"
"s_add_u32 m0, s70, 98304\n"
"MM v[4:7], a[184:187], v[200:203], v[4:7]\n"
"v_exp_f32 v69, v69\n"
"MM v[4:7], a[188:191], v[220:223], v[4:7]\n"
"ds_read_b64_tr_b16 a[198:199], v247 offset:53248\n"
"v_max_f32 v140, v0, v1\n"
"MM v[8:11], a[160:163], v[164:167], v[84:87]\n"
"buffer_load_dwordx4 v249, s[44:47], s71 offen nt lds\n"
"MM v[8:11], a[164:167], v[184:187], v[8:11]\n"
"v_exp_f32 v70, v70\n"
"MM v[8:11], a[168:171], v[204:207], v[8:11]\n"
"ds_read_b64_tr_b16 a[200:201], v244 offset:49408\n"
"v_max3_f32 v140, v2, v3, v140\n"
"MM v[8:11], a[172:175], v[224:227], v[8:11]\n"
"v_exp_f32 v71, v71\n"
"MM v[12:15], a[176:179], v[164:167], v[84:87]\n"
"ds_read_b64_tr_b16 a[202:203], v244 offset:53504\n"
"v_cvt_pk_bf16_f32 v128, v56, v57\n"
"MM v[12:15], a[180:183], v[184:187], v[12:15]\n"
"v_max3_f32 v140, v4, v5, v140\n"
"v_cvt_pk_bf16_f32 v129, v58, v59\n"
"MM v[12:15], a[184:187], v[204:207], v[12:15]\n"
"ds_read_b64_tr_b16 a[204:205], v247 offset:49408\n"
"v_cvt_pk_bf16_f32 v130, v60, v61\n"
"MM v[12:15], a[188:191], v[224:227], v[12:15]\n"
"v_max3_f32 v140, v6, v7, v140\n"
"v_cvt_pk_bf16_f32 v131, v62, v63\n"
"MM v[16:19], a[160:163], v[168:171], v[88:91]\n"
"ds_read_b64_tr_b16 a[206:207], v247 offset:53504\n"
"v_max3_f32 v140, v8, v9, v140\n"
"MM v[16:19], a[164:167], v[188:191], v[16:19]\n"
"v_exp_f32 v72, v72\n"
"MM v[16:19], a[168:171], v[208:211], v[16:19]\n"
"buffer_load_dwordx4 v252, s[44:47], s71 offen offset:1024 nt lds\n"
"MM v[16:19], a[172:175], v[228:231], v[16:19]\n"
"v_exp_f32 v73, v73\n"
"MM v[20:23], a[176:179], v[168:171], v[88:91]\n"
"ds_read_b64_tr_b16 a[208:209], v244 offset:49664\n"
"v_max3_f32 v140, v10, v11, v140\n"
"MM v[20:23], a[180:183], v[188:191], v[20:23]\n"
"v_exp_f32 v74, v74\n"
"MM v[20:23], a[184:187], v[208:211], v[20:23]\n"
"ds_read_b64_tr_b16 a[210:211], v244 offset:53760\n"
"v_max3_f32 v140, v12, v13, v140\n"
"MM v[20:23], a[188:191], v[228:231], v[20:23]\n"
"v_exp_f32 v75, v75\n"
"MM v[24:27], a[160:163], v[172:175], v[92:95]\n"
"ds_read_b64_tr_b16 a[212:213], v247 offset:49664\n"
"v_max3_f32 v140, v14, v15, v140\n"
"MM v[24:27], a[164:167], v[192:195], v[24:27]\n"
"v_exp_f32 v76, v76\n"
"MM v[24:27], a[168:171], v[212:215], v[24:27]\n"
"ds_read_b64_tr_b16 a[214:215], v247 offset:53760\n"
"v_max3_f32 v140, v16, v17, v140\n"
"MM v[24:27], a[172:175], v[232:235], v[24:27]\n"
"buffer_load_dwordx4 v251, s[44:47], s71 offen offset:2048 nt lds\n"
"MM v[28:31], a[176:179], v[172:175], v[92:95]\n"
"v_exp_f32 v77, v77\n"
"MM v[28:31], a[180:183], v[192:195], v[28:31]\n"
"ds_read_b64_tr_b16 a[216:217], v244 offset:49920\n"
"v_max3_f32 v140, v18, v19, v140\n"
"MM v[28:31], a[184:187], v[212:215], v[28:31]\n"
"v_exp_f32 v78, v78\n"
"MM v[28:31], a[188:191], v[232:235], v[28:31]\n"
"ds_read_b64_tr_b16 a[218:219], v244 offset:54016\n"
"v_max3_f32 v140, v20, v21, v140\n"
"ds_read_b64_tr_b16 a[220:221], v247 offset:49920\n"
"ds_read_b64_tr_b16 a[222:223], v247 offset:54016\n"
"MM v[32:35], a[160:163], v[176:179], v[96:99]\n"
"v_exp_f32 v79, v79\n"
"MM v[32:35], a[164:167], v[196:199], v[32:35]\n"
"v_max3_f32 v140, v22, v23, v140\n"
"v_cvt_pk_bf16_f32 v132, v64, v65\n"
"MM v[32:35], a[168:171], v[216:219], v[32:35]\n"
"v_cvt_pk_bf16_f32 v133, v66, v67\n"
"v_max3_f32 v140, v24, v25, v140\n"
"MM v[32:35], a[172:175], v[236:239], v[32:35]\n"
"buffer_load_dwordx4 v250, s[44:47], s71 offen offset:3072 nt lds\n"
"MM v[36:39], a[176:179], v[176:179], v[96:99]\n"
"v_cvt_pk_bf16_f32 v134, v68, v69\n"
"v_cvt_pk_bf16_f32 v135, v70, v71\n"
"MM v[36:39], a[180:183], v[196:199], v[36:39]\n"
"v_max3_f32 v140, v26, v27, v140\n"
"v_cvt_pk_bf16_f32 v136, v72, v73\n"
"MM v[36:39], a[184:187], v[216:219], v[36:39]\n"
"v_cvt_pk_bf16_f32 v137, v74, v75\n"
"v_max3_f32 v140, v28, v29, v140\n"
"MM v[36:39], a[188:191], v[236:239], v[36:39]\n"
"v_cvt_pk_bf16_f32 v138, v76, v77\n"
"v_max3_f32 v140, v30, v31, v140\n"
"v_cvt_pk_bf16_f32 v139, v78, v79\n"
"s_waitcnt vmcnt(4)\n"
"s_waitcnt lgkmcnt(0)\n"
"v_mov_b32 v248, 28\n"
"ds_write_b32 v242, v248 offset:0\n"
"MM a[224:227], a[244:247], v[120:123], a[224:227]\n"
"v_max3_f32 v140, v32, v33, v140\n"
"v_max3_f32 v140, v34, v35, v140\n"
"MM a[0:3], a[192:195], v[120:123], a[0:3]\n"
"MM a[4:7], a[196:199], v[120:123], a[4:7]\n"
"v_max3_f32 v140, v36, v37, v140\n"
"v_max3_f32 v140, v38, v39, v140\n"
"MM a[8:11], a[200:203], v[120:123], a[8:11]\n"
"v_cmp_lt_f32 vcc, s76, v140\n"
"MM a[12:15], a[204:207], v[120:123], a[12:15]\n"
"s_nop 0\n"
"s_cbranch_vccnz K1_ESA6\n"
"K1_ENA6:\n"
"MM a[16:19], a[208:211], v[120:123], a[16:19]\n"
"v_exp_f32 v0, v0\n"
"MM a[20:23], a[212:215], v[120:123], a[20:23]\n"
"ds_read_b32 v248, v245 offset:0\n"
"MM a[24:27], a[216:219], v[120:123], a[24:27]\n"
"v_exp_f32 v1, v1\n"
"MM a[28:31], a[220:223], v[120:123], a[28:31]\n"
"v_exp_f32 v2, v2\n"
"MM a[228:231], a[244:247], v[124:127], a[228:231]\n"
"v_exp_f32 v3, v3\n"
"MM a[32:35], a[192:195], v[124:127], a[32:35]\n"
"s_waitcnt lgkmcnt(0)\n"
"v_cmp_gt_u32 vcc, 28, v248\n"
"MM a[36:39], a[196:199], v[124:127], a[36:39]\n"
"v_exp_f32 v4, v4\n"
"MM a[40:43], a[200:203], v[124:127], a[40:43]\n"
"s_cbranch_vccnz K1_A28_S\n"
"K1_A28_R:\n"
"s_add_u32 s71, s94, 57344\n"
"MM a[44:47], a[204:207], v[124:127], a[44:47]\n"
"v_exp_f32 v5, v5\n"
"MM a[48:51], a[208:211], v[124:127], a[48:51]\n"
"v_exp_f32 v6, v6\n"
"MM a[52:55], a[212:215], v[124:127], a[52:55]\n"
"v_exp_f32 v7, v7\n"
"MM a[56:59], a[216:219], v[124:127], a[56:59]\n"
"v_exp_f32 v8, v8\n"
"MM a[60:63], a[220:223], v[124:127], a[60:63]\n"
"s_and_b32 s71, s71, 0x1ffff\n"
"ds_read_b128 a[160:163], v241 offset:0\n"
"MM a[232:235], a[244:247], v[128:131], a[232:235]\n"
"v_exp_f32 v9, v9\n"
"MM a[64:67], a[192:195], v[128:131], a[64:67]\n"
"s_add_u32 m0, s70, 0\n"
"ds_read_b128 a[164:167], v241 offset:256\n"
"MM a[68:71], a[196:199], v[128:131], a[68:71]\n"
"v_exp_f32 v10, v10\n"
"MM a[72:75], a[200:203], v[128:131], a[72:75]\n"
"v_exp_f32 v11, v11\n"
"MM a[76:79], a[204:207], v[128:131], a[76:79]\n"
"buffer_load_dwordx4 v249, s[48:51], s71 offen nt lds\n"
"MM a[80:83], a[208:211], v[128:131], a[80:83]\n"
"v_exp_f32 v12, v12\n"
"MM a[84:87], a[212:215], v[128:131], a[84:87]\n"
"v_exp_f32 v13, v13\n"
"MM a[88:91], a[216:219], v[128:131], a[88:91]\n"
"v_exp_f32 v14, v14\n"
"MM a[92:95], a[220:223], v[128:131], a[92:95]\n"
"ds_read_b128 a[168:171], v241 offset:512\n"
"ds_read_b128 a[172:175], v241 offset:768\n"
"MM a[236:239], a[244:247], v[132:135], a[236:239]\n"
"v_exp_f32 v15, v15\n"
"MM a[96:99], a[192:195], v[132:135], a[96:99]\n"
"v_cvt_pk_bf16_f32 v100, v0, v1\n"
"v_cvt_pk_bf16_f32 v101, v2, v3\n"
"MM a[100:103], a[196:199], v[132:135], a[100:103]\n"
"v_cvt_pk_bf16_f32 v102, v4, v5\n"
"v_cvt_pk_bf16_f32 v103, v6, v7\n"
"MM a[104:107], a[200:203], v[132:135], a[104:107]\n"
"buffer_load_dwordx4 v251, s[48:51], s71 offen offset:1024 nt lds\n"
"MM a[108:111], a[204:207], v[132:135], a[108:111]\n"
"v_exp_f32 v16, v16\n"
"MM a[112:115], a[208:211], v[132:135], a[112:115]\n"
"v_exp_f32 v17, v17\n"
"ds_read_b128 a[176:179], v241 offset:4096\n"
"MM a[116:119], a[212:215], v[132:135], a[116:119]\n"
"v_exp_f32 v18, v18\n"
"MM a[120:123], a[216:219], v[132:135], a[120:123]\n"
"v_exp_f32 v19, v19\n"
"ds_read_b128 a[180:183], v241 offset:4352\n"
"ds_read_b128 a[184:187], v241 offset:4608\n"
"ds_read_b128 a[188:191], v241 offset:4864\n"
"MM a[124:127], a[220:223], v[132:135], a[124:127]\n"
"buffer_load_dwordx4 v249, s[48:51], s71 offen offset:2048 nt lds\n"
"MM a[240:243], a[244:247], v[136:139], a[240:243]\n"
"v_exp_f32 v20, v20\n"
"MM a[128:131], a[192:195], v[136:139], a[128:131]\n"
"v_exp_f32 v21, v21\n"
"MM a[132:135], a[196:199], v[136:139], a[132:135]\n"
"v_exp_f32 v22, v22\n"
"MM a[136:139], a[200:203], v[136:139], a[136:139]\n"
"v_exp_f32 v23, v23\n"
"MM a[140:143], a[204:207], v[136:139], a[140:143]\n"
"v_cvt_pk_bf16_f32 v104, v8, v9\n"
"v_cvt_pk_bf16_f32 v105, v10, v11\n"
"MM a[144:147], a[208:211], v[136:139], a[144:147]\n"
"buffer_load_dwordx4 v251, s[48:51], s71 offen offset:3072 nt lds\n"
"MM a[148:151], a[212:215], v[136:139], a[148:151]\n"
"v_cvt_pk_bf16_f32 v106, v12, v13\n"
"v_cvt_pk_bf16_f32 v107, v14, v15\n"
"MM a[152:155], a[216:219], v[136:139], a[152:155]\n"
"v_exp_f32 v24, v24\n"
"MM a[156:159], a[220:223], v[136:139], a[156:159]\n"
"v_exp_f32 v25, v25\n"
"s_waitcnt lgkmcnt(0)\n"
"s_cmp_lg_u32 s75, 0\n"
"s_cbranch_scc1 K1_ESB6\n"
"K1_ENB6:\n"
"MM v[40:43], a[160:163], v[160:163], v[80:83]\n"
"v_exp_f32 v26, v26\n"
"MM v[40:43], a[164:167], v[180:183], v[40:43]\n"
"ds_read_b64_tr_b16 a[192:193], v244 offset:16384\n"
"s_add_u32 s71, s94, 73728\n"
"MM v[40:43], a[168:171], v[200:203], v[40:43]\n"
"v_exp_f32 v27, v27\n"
"MM v[40:43], a[172:175], v[220:223], v[40:43]\n"
"ds_read_b64_tr_b16 a[194:195], v244 offset:20480\n"
"s_and_b32 s71, s71, 0x1ffff\n"
"MM v[44:47], a[176:179], v[160:163], v[80:83]\n"
"v_exp_f32 v28, v28\n"
"MM v[44:47], a[180:183], v[180:183], v[44:47]\n"
"ds_read_b64_tr_b16 a[196:197], v247 offset:16384\n"
"s_add_u32 m0, s70, 16384\n"
"MM v[44:47], a[184:187], v[200:203], v[44:47]\n"
"v_exp_f32 v29, v29\n"
"MM v[44:47], a[188:191], v[220:223], v[44:47]\n"
"ds_read_b64_tr_b16 a[198:199], v247 offset:20480\n"
"v_max_f32 v140, v40, v41\n"
"MM v[48:51], a[160:163], v[164:167], v[84:87]\n"
"buffer_load_dwordx4 v249, s[44:47], s71 offen nt lds\n"
"MM v[48:51], a[164:167], v[184:187], v[48:51]\n"
"v_exp_f32 v30, v30\n"
"MM v[48:51], a[168:171], v[204:207], v[48:51]\n"
"ds_read_b64_tr_b16 a[200:201], v244 offset:16640\n"
"v_max3_f32 v140, v42, v43, v140\n"
"MM v[48:51], a[172:175], v[224:227], v[48:51]\n"
"v_exp_f32 v31, v31\n"
"MM v[52:55], a[176:179], v[164:167], v[84:87]\n"
"ds_read_b64_tr_b16 a[202:203], v244 offset:20736\n"
"v_cvt_pk_bf16_f32 v108, v16, v17\n"
"MM v[52:55], a[180:183], v[184:187], v[52:55]\n"
"v_max3_f32 v140, v44, v45, v140\n"
"v_cvt_pk_bf16_f32 v109, v18, v19\n"
"MM v[52:55], a[184:187], v[204:207], v[52:55]\n"
"ds_read_b64_tr_b16 a[204:205], v247 offset:16640\n"
"v_cvt_pk_bf16_f32 v110, v20, v21\n"
"MM v[52:55], a[188:191], v[224:227], v[52:55]\n"
"v_max3_f32 v140, v46, v47, v140\n"
"v_cvt_pk_bf16_f32 v111, v22, v23\n"
"MM v[56:59], a[160:163], v[168:171], v[88:91]\n"
"ds_read_b64_tr_b16 a[206:207], v247 offset:20736\n"
"v_max3_f32 v140, v48, v49, v140\n"
"MM v[56:59], a[164:167], v[188:191], v[56:59]\n"
"v_exp_f32 v32, v32\n"
"MM v[56:59], a[168:171], v[208:211], v[56:59]\n"
"buffer_load_dwordx4 v252, s[44:47], s71 offen offset:1024 nt lds\n"
"MM v[56:59], a[172:175], v[228:231], v[56:59]\n"
"v_exp_f32 v33, v33\n"
"MM v[60:63], a[176:179], v[168:171], v[88:91]\n"
"ds_read_b64_tr_b16 a[208:209], v244 offset:16896\n"
"v_max3_f32 v140, v50, v51, v140\n"
"MM v[60:63], a[180:183], v[188:191], v[60:63]\n"
"v_exp_f32 v34, v34\n"
"MM v[60:63], a[184:187], v[208:211], v[60:63]\n"
"ds_read_b64_tr_b16 a[210:211], v244 offset:20992\n"
"v_max3_f32 v140, v52, v53, v140\n"
"MM v[60:63], a[188:191], v[228:231], v[60:63]\n"
"v_exp_f32 v35, v35\n"
"MM v[64:67], a[160:163], v[172:175], v[92:95]\n"
"ds_read_b64_tr_b16 a[212:213], v247 offset:16896\n"
"v_max3_f32 v140, v54, v55, v140\n"
"MM v[64:67], a[164:167], v[192:195], v[64:67]\n"
"v_exp_f32 v36, v36\n"
"MM v[64:67], a[168:171], v[212:215], v[64:67]\n"
"ds_read_b64_tr_b16 a[214:215], v247 offset:20992\n"
"v_max3_f32 v140, v56, v57, v140\n"
"MM v[64:67], a[172:175], v[232:235], v[64:67]\n"
"buffer_load_dwordx4 v251, s[44:47], s71 offen offset:2048 nt lds\n"
"MM v[68:71], a[176:179], v[172:175], v[92:95]\n"
"v_exp_f32 v37, v37\n"
"MM v[68:71], a[180:183], v[192:195], v[68:71]\n"
"ds_read_b64_tr_b16 a[216:217], v244 offset:17152\n"
"v_max3_f32 v140, v58, v59, v140\n"
"MM v[68:71], a[184:187], v[212:215], v[68:71]\n"
"v_exp_f32 v38, v38\n"
"MM v[68:71], a[188:191], v[232:235], v[68:71]\n"
"ds_read_b64_tr_b16 a[218:219], v244 offset:21248\n"
"v_max3_f32 v140, v60, v61, v140\n"
"ds_read_b64_tr_b16 a[220:221], v247 offset:17152\n"
"ds_read_b64_tr_b16 a[222:223], v247 offset:21248\n"
"MM v[72:75], a[160:163], v[176:179], v[96:99]\n"
"v_exp_f32 v39, v39\n"
"MM v[72:75], a[164:167], v[196:199], v[72:75]\n"
"v_max3_f32 v140, v62, v63, v140\n"
"v_cvt_pk_bf16_f32 v112, v24, v25\n"
"MM v[72:75], a[168:171], v[216:219], v[72:75]\n"
"v_cvt_pk_bf16_f32 v113, v26, v27\n"
"v_max3_f32 v140, v64, v65, v140\n"
"MM v[72:75], a[172:175], v[236:239], v[72:75]\n"
"buffer_load_dwordx4 v250, s[44:47], s71 offen offset:3072 nt lds\n"
"MM v[76:79], a[176:179], v[176:179], v[96:99]\n"
"v_cvt_pk_bf16_f32 v114, v28, v29\n"
"v_cvt_pk_bf16_f32 v115, v30, v31\n"
"MM v[76:79], a[180:183], v[196:199], v[76:79]\n"
"v_max3_f32 v140, v66, v67, v140\n"
"v_cvt_pk_bf16_f32 v116, v32, v33\n"
"MM v[76:79], a[184:187], v[216:219], v[76:79]\n"
"v_cvt_pk_bf16_f32 v117, v34, v35\n"
"v_max3_f32 v140, v68, v69, v140\n"
"MM v[76:79], a[188:191], v[236:239], v[76:79]\n"
"v_cvt_pk_bf16_f32 v118, v36, v37\n"
"v_max3_f32 v140, v70, v71, v140\n"
"v_cvt_pk_bf16_f32 v119, v38, v39\n"
"s_waitcnt vmcnt(4)\n"
"s_waitcnt lgkmcnt(0)\n"
"v_mov_b32 v248, 29\n"
"ds_write_b32 v242, v248 offset:0\n"
"MM a[224:227], a[244:247], v[100:103], a[224:227]\n"
"v_max3_f32 v140, v72, v73, v140\n"
"v_max3_f32 v140, v74, v75, v140\n"
"MM a[0:3], a[192:195], v[100:103], a[0:3]\n"
"MM a[4:7], a[196:199], v[100:103], a[4:7]\n"
"v_max3_f32 v140, v76, v77, v140\n"
"v_max3_f32 v140, v78, v79, v140\n"
"MM a[8:11], a[200:203], v[100:103], a[8:11]\n"
"v_cmp_lt_f32 vcc, s76, v140\n"
"MM a[12:15], a[204:207], v[100:103], a[12:15]\n"
"s_nop 0\n"
"s_cbranch_vccnz K1_ESA7\n"
"K1_ENA7:\n"
"MM a[16:19], a[208:211], v[100:103], a[16:19]\n"
"v_exp_f32 v40, v40\n"
"MM a[20:23], a[212:215], v[100:103], a[20:23]\n"
"ds_read_b32 v248, v245 offset:0\n"
"MM a[24:27], a[216:219], v[100:103], a[24:27]\n"
"v_exp_f32 v41, v41\n"
"MM a[28:31], a[220:223], v[100:103], a[28:31]\n"
"v_exp_f32 v42, v42\n"
"MM a[228:231], a[244:247], v[104:107], a[228:231]\n"
"v_exp_f32 v43, v43\n"
"MM a[32:35], a[192:195], v[104:107], a[32:35]\n"
"s_waitcnt lgkmcnt(0)\n"
"v_cmp_gt_u32 vcc, 29, v248\n"
"MM a[36:39], a[196:199], v[104:107], a[36:39]\n"
"v_exp_f32 v44, v44\n"
"MM a[40:43], a[200:203], v[104:107], a[40:43]\n"
"s_cbranch_vccnz K1_A29_S\n"
"K1_A29_R:\n"
"s_add_u32 s71, s94, 65536\n"
"MM a[44:47], a[204:207], v[104:107], a[44:47]\n"
"v_exp_f32 v45, v45\n"
"MM a[48:51], a[208:211], v[104:107], a[48:51]\n"
"v_exp_f32 v46, v46\n"
"MM a[52:55], a[212:215], v[104:107], a[52:55]\n"
"v_exp_f32 v47, v47\n"
"MM a[56:59], a[216:219], v[104:107], a[56:59]\n"
"s_and_b32 s71, s71, 0x1ffff\n"
"ds_read_b128 a[160:163], v241 offset:32768\n"
"MM a[60:63], a[220:223], v[104:107], a[60:63]\n"
"v_exp_f32 v48, v48\n"
"MM a[232:235], a[244:247], v[108:111], a[232:235]\n"
"s_add_u32 m0, s70, 32768\n"
"ds_read_b128 a[164:167], v241 offset:33024\n"
"MM a[64:67], a[192:195], v[108:111], a[64:67]\n"
"v_exp_f32 v49, v49\n"
"MM a[68:71], a[196:199], v[108:111], a[68:71]\n"
"v_exp_f32 v50, v50\n"
"MM a[72:75], a[200:203], v[108:111], a[72:75]\n"
"buffer_load_dwordx4 v249, s[48:51], s71 offen nt lds\n"
"MM a[76:79], a[204:207], v[108:111], a[76:79]\n"
"v_exp_f32 v51, v51\n"
"MM a[80:83], a[208:211], v[108:111], a[80:83]\n"
"v_exp_f32 v52, v52\n"
"MM a[84:87], a[212:215], v[108:111], a[84:87]\n"
"v_exp_f32 v53, v53\n"
"MM a[88:91], a[216:219], v[108:111], a[88:91]\n"
"v_exp_f32 v54, v54\n"
"MM a[92:95], a[220:223], v[108:111], a[92:95]\n"
"buffer_load_dwordx4 v251, s[48:51], s71 offen offset:1024 nt lds\n"
"ds_read_b128 a[168:171], v241 offset:33280\n"
"MM a[236:239], a[244:247], v[112:115], a[236:239]\n"
"v_exp_f32 v55, v55\n"
"MM a[96:99], a[192:195], v[112:115], a[96:99]\n"
"v_cvt_pk_bf16_f32 v120, v40, v41\n"
"v_cvt_pk_bf16_f32 v121, v42, v43\n"
"MM a[100:103], a[196:199], v[112:115], a[100:103]\n"
"v_cvt_pk_bf16_f32 v122, v44, v45\n"
"ds_read_b128 a[172:175], v241 offset:33536\n"
"MM a[104:107], a[200:203], v[112:115], a[104:107]\n"
"v_cvt_pk_bf16_f32 v123, v46, v47\n"
"ds_read_b128 a[176:179], v241 offset:36864\n"
"MM a[108:111], a[204:207], v[112:115], a[108:111]\n"
"v_exp_f32 v56, v56\n"
"MM a[112:115], a[208:211], v[112:115], a[112:115]\n"
"buffer_load_dwordx4 v249, s[48:51], s71 offen offset:2048 nt lds\n"
"MM a[116:119], a[212:215], v[112:115], a[116:119]\n"
"v_exp_f32 v57, v57\n"
"MM a[120:123], a[216:219], v[112:115], a[120:123]\n"
"v_exp_f32 v58, v58\n"
"ds_read_b128 a[180:183], v241 offset:37120\n"
"ds_read_b128 a[184:187], v241 offset:37376\n"
"ds_read_b128 a[188:191], v241 offset:37632\n"
"MM a[124:127], a[220:223], v[112:115], a[124:127]\n"
"v_exp_f32 v59, v59\n"
"MM a[240:243], a[244:247], v[116:119], a[240:243]\n"
"v_exp_f32 v60, v60\n"
"MM a[128:131], a[192:195], v[116:119], a[128:131]\n"
"buffer_load_dwordx4 v251, s[48:51], s71 offen offset:3072 nt lds\n"
"MM a[132:135], a[196:199], v[116:119], a[132:135]\n"
"v_exp_f32 v61, v61\n"
"MM a[136:139], a[200:203], v[116:119], a[136:139]\n"
"v_exp_f32 v62, v62\n"
"MM a[140:143], a[204:207], v[116:119], a[140:143]\n"
"v_exp_f32 v63, v63\n"
"MM a[144:147], a[208:211], v[116:119], a[144:147]\n"
"v_cvt_pk_bf16_f32 v124, v48, v49\n"
"v_lshlrev_b32 v149, 4, v253\n"
"MM a[148:151], a[212:215], v[116:119], a[148:151]\n"
"v_cvt_pk_bf16_f32 v125, v50, v51\n"
"v_cvt_pk_bf16_f32 v126, v52, v53\n"
"MM a[152:155], a[216:219], v[116:119], a[152:155]\n"
"v_cvt_pk_bf16_f32 v127, v54, v55\n"
"MM a[156:159], a[220:223], v[116:119], a[156:159]\n"
"v_exp_f32 v64, v64\n"
"v_exp_f32 v65, v65\n"
"buffer_load_dwordx4 a[248:251], v149, s[52:55], s98 offen\n"
"s_waitcnt lgkmcnt(0)\n"
"s_cmp_lg_u32 s75, 0\n"
"s_cbranch_scc1 K1_ESB7\n"
"K1_ENB7:\n"
"s_cmp_lg_u32 s60, 3\n"
"s_cbranch_scc1 K1_EFW_OK\n"
"s_mov_b32 s90, 262144\n"
"s_mov_b32 s96, 0xffff0000\n"
"s_mov_b32 s97, 0xffff\n"
"v_cmp_ne_u32 vcc, v156, v254\n"
"s_nop 1\n"
"s_andn2_b64 vcc, s[96:97], vcc\n"
"s_cbranch_scc0 K1_EFW_OK\n"
"K1_EFW_L:\n"
"s_nop 4\n"
"buffer_load_dword v156, v155, s[56:59], 0 offen sc1\n"
"s_waitcnt vmcnt(0)\n"
"v_cmp_ne_u32 vcc, v156, v254\n"
"s_nop 1\n"
"s_andn2_b64 vcc, s[96:97], vcc\n"
"s_cbranch_scc0 K1_EFW_OK\n"
"s_sleep 2\n"
"s_sub_u32 s90, s90, 1\n"
"s_cmp_lg_u32 s90, 0\n"
"s_cbranch_scc1 K1_EFW_L\n"
"K1_EFW_OK:\n"
"s_nop 1\n"
"MM v[0:3], a[160:163], v[160:163], v[80:83]\n"
"v_exp_f32 v66, v66\n"
"MM v[0:3], a[164:167], v[180:183], v[0:3]\n"
"ds_read_b64_tr_b16 a[192:193], v243 offset:0\n"
"s_add_u32 s71, s94, 81920\n"
"MM v[0:3], a[168:171], v[200:203], v[0:3]\n"
"v_exp_f32 v67, v67\n"
"MM v[0:3], a[172:175], v[220:223], v[0:3]\n"
"ds_read_b64_tr_b16 a[194:195], v243 offset:4096\n"
"s_and_b32 s71, s71, 0x1ffff\n"
"MM v[4:7], a[176:179], v[160:163], v[80:83]\n"
"v_exp_f32 v68, v68\n"
"MM v[4:7], a[180:183], v[180:183], v[4:7]\n"
"ds_read_b64_tr_b16 a[196:197], v246 offset:0\n"
"s_add_u32 m0, s70, 49152\n"
"MM v[4:7], a[184:187], v[200:203], v[4:7]\n"
"v_exp_f32 v69, v69\n"
"MM v[4:7], a[188:191], v[220:223], v[4:7]\n"
"ds_read_b64_tr_b16 a[198:199], v246 offset:4096\n"
"v_max_f32 v140, v0, v1\n"
"MM v[8:11], a[160:163], v[164:167], v[84:87]\n"
"buffer_load_dwordx4 v249, s[44:47], s71 offen nt lds\n"
"MM v[8:11], a[164:167], v[184:187], v[8:11]\n"
"v_exp_f32 v70, v70\n"
"MM v[8:11], a[168:171], v[204:207], v[8:11]\n"
"ds_read_b64_tr_b16 a[200:201], v243 offset:256\n"
"v_max3_f32 v140, v2, v3, v140\n"
"MM v[8:11], a[172:175], v[224:227], v[8:11]\n"
"v_exp_f32 v71, v71\n"
"MM v[12:15], a[176:179], v[164:167], v[84:87]\n"
"ds_read_b64_tr_b16 a[202:203], v243 offset:4352\n"
"v_cvt_pk_bf16_f32 v128, v56, v57\n"
"MM v[12:15], a[180:183], v[184:187], v[12:15]\n"
"v_max3_f32 v140, v4, v5, v140\n"
"v_cvt_pk_bf16_f32 v129, v58, v59\n"
"MM v[12:15], a[184:187], v[204:207], v[12:15]\n"
"ds_read_b64_tr_b16 a[204:205], v246 offset:256\n"
"v_cvt_pk_bf16_f32 v130, v60, v61\n"
"MM v[12:15], a[188:191], v[224:227], v[12:15]\n"
"v_max3_f32 v140, v6, v7, v140\n"
"v_cvt_pk_bf16_f32 v131, v62, v63\n"
"MM v[16:19], a[160:163], v[168:171], v[88:91]\n"
"ds_read_b64_tr_b16 a[206:207], v246 offset:4352\n"
"v_max3_f32 v140, v8, v9, v140\n"
"MM v[16:19], a[164:167], v[188:191], v[16:19]\n"
"v_exp_f32 v72, v72\n"
"MM v[16:19], a[168:171], v[208:211], v[16:19]\n"
"buffer_load_dwordx4 v252, s[44:47], s71 offen offset:1024 nt lds\n"
"MM v[16:19], a[172:175], v[228:231], v[16:19]\n"
"v_exp_f32 v73, v73\n"
"MM v[20:23], a[176:179], v[168:171], v[88:91]\n"
"ds_read_b64_tr_b16 a[208:209], v243 offset:512\n"
"v_max3_f32 v140, v10, v11, v140\n"
"MM v[20:23], a[180:183], v[188:191], v[20:23]\n"
"v_exp_f32 v74, v74\n"
"MM v[20:23], a[184:187], v[208:211], v[20:23]\n"
"ds_read_b64_tr_b16 a[210:211], v243 offset:4608\n"
"v_max3_f32 v140, v12, v13, v140\n"
"MM v[20:23], a[188:191], v[228:231], v[20:23]\n"
"v_exp_f32 v75, v75\n"
"MM v[24:27], a[160:163], v[172:175], v[92:95]\n"
"ds_read_b64_tr_b16 a[212:213], v246 offset:512\n"
"v_max3_f32 v140, v14, v15, v140\n"
"MM v[24:27], a[164:167], v[192:195], v[24:27]\n"
"v_exp_f32 v76, v76\n"
"MM v[24:27], a[168:171], v[212:215], v[24:27]\n"
"ds_read_b64_tr_b16 a[214:215], v246 offset:4608\n"
"v_max3_f32 v140, v16, v17, v140\n"
"MM v[24:27], a[172:175], v[232:235], v[24:27]\n"
"buffer_load_dwordx4 v251, s[44:47], s71 offen offset:2048 nt lds\n"
"MM v[28:31], a[176:179], v[172:175], v[92:95]\n"
"v_exp_f32 v77, v77\n"
"MM v[28:31], a[180:183], v[192:195], v[28:31]\n"
"ds_read_b64_tr_b16 a[216:217], v243 offset:768\n"
"v_max3_f32 v140, v18, v19, v140\n"
"MM v[28:31], a[184:187], v[212:215], v[28:31]\n"
"v_exp_f32 v78, v78\n"
"MM v[28:31], a[188:191], v[232:235], v[28:31]\n"
"ds_read_b64_tr_b16 a[218:219], v243 offset:4864\n"
"v_max3_f32 v140, v20, v21, v140\n"
"ds_read_b64_tr_b16 a[220:221], v246 offset:768\n"
"ds_read_b64_tr_b16 a[222:223], v246 offset:4864\n"
"MM v[32:35], a[160:163], v[176:179], v[96:99]\n"
"v_exp_f32 v79, v79\n"
"MM v[32:35], a[164:167], v[196:199], v[32:35]\n"
"v_max3_f32 v140, v22, v23, v140\n"
"v_cvt_pk_bf16_f32 v132, v64, v65\n"
"MM v[32:35], a[168:171], v[216:219], v[32:35]\n"
"v_cvt_pk_bf16_f32 v133, v66, v67\n"
"v_max3_f32 v140, v24, v25, v140\n"
"MM v[32:35], a[172:175], v[236:239], v[32:35]\n"
"buffer_load_dwordx4 v250, s[44:47], s71 offen offset:3072 nt lds\n"
"MM v[36:39], a[176:179], v[176:179], v[96:99]\n"
"v_cvt_pk_bf16_f32 v134, v68, v69\n"
"v_cvt_pk_bf16_f32 v135, v70, v71\n"
"MM v[36:39], a[180:183], v[196:199], v[36:39]\n"
"v_max3_f32 v140, v26, v27, v140\n"
"v_cvt_pk_bf16_f32 v136, v72, v73\n"
"MM v[36:39], a[184:187], v[216:219], v[36:39]\n"
"v_cvt_pk_bf16_f32 v137, v74, v75\n"
"v_max3_f32 v140, v28, v29, v140\n"
"MM v[36:39], a[188:191], v[236:239], v[36:39]\n"
"v_cvt_pk_bf16_f32 v138, v76, v77\n"
"v_max3_f32 v140, v30, v31, v140\n"
"v_cvt_pk_bf16_f32 v139, v78, v79\n"
"s_waitcnt vmcnt(5)\n"
"s_waitcnt lgkmcnt(0)\n"
"v_mov_b32 v248, 30\n"
"ds_write_b32 v242, v248 offset:0\n"
"MM a[224:227], a[244:247], v[120:123], a[224:227]\n"
"v_max3_f32 v140, v32, v33, v140\n"
"v_max3_f32 v140, v34, v35, v140\n"
"MM a[0:3], a[192:195], v[120:123], a[0:3]\n"
"MM a[4:7], a[196:199], v[120:123], a[4:7]\n"
"v_max3_f32 v140, v36, v37, v140\n"
"v_max3_f32 v140, v38, v39, v140\n"
"MM a[8:11], a[200:203], v[120:123], a[8:11]\n"
"v_cmp_lt_f32 vcc, s76, v140\n"
"MM a[12:15], a[204:207], v[120:123], a[12:15]\n"
"s_nop 0\n"
"s_cbranch_vccnz K1_ESA8\n"
"K1_ENA8:\n"
"MM a[16:19], a[208:211], v[120:123], a[16:19]\n"
"v_exp_f32 v0, v0\n"
"MM a[20:23], a[212:215], v[120:123], a[20:23]\n"
"ds_read_b32 v248, v245 offset:0\n"
"MM a[24:27], a[216:219], v[120:123], a[24:27]\n"
"v_exp_f32 v1, v1\n"
"MM a[28:31], a[220:223], v[120:123], a[28:31]\n"
"v_exp_f32 v2, v2\n"
"MM a[228:231], a[244:247], v[124:127], a[228:231]\n"
"v_exp_f32 v3, v3\n"
"MM a[32:35], a[192:195], v[124:127], a[32:35]\n"
"s_waitcnt lgkmcnt(0)\n"
"v_cmp_gt_u32 vcc, 30, v248\n"
"MM a[36:39], a[196:199], v[124:127], a[36:39]\n"
"v_exp_f32 v4, v4\n"
"MM a[40:43], a[200:203], v[124:127], a[40:43]\n"
"s_cbranch_vccnz K1_A30_S\n"
"K1_A30_R:\n"
"s_add_u32 s71, s94, 73728\n"
"MM a[44:47], a[204:207], v[124:127], a[44:47]\n"
"v_exp_f32 v5, v5\n"
"MM a[48:51], a[208:211], v[124:127], a[48:51]\n"
"v_exp_f32 v6, v6\n"
"MM a[52:55], a[212:215], v[124:127], a[52:55]\n"
"v_exp_f32 v7, v7\n"
"MM a[56:59], a[216:219], v[124:127], a[56:59]\n"
"v_exp_f32 v8, v8\n"
"MM a[60:63], a[220:223], v[124:127], a[60:63]\n"
"s_and_b32 s71, s71, 0x1ffff\n"
"ds_read_b128 a[160:163], v240 offset:16384\n"
"MM a[232:235], a[244:247], v[128:131], a[232:235]\n"
"v_exp_f32 v9, v9\n"
"MM a[64:67], a[192:195], v[128:131], a[64:67]\n"
"s_add_u32 m0, s70, 114688\n"
"ds_read_b128 a[164:167], v240 offset:16640\n"
"MM a[68:71], a[196:199], v[128:131], a[68:71]\n"
"v_exp_f32 v10, v10\n"
"MM a[72:75], a[200:203], v[128:131], a[72:75]\n"
"v_exp_f32 v11, v11\n"
"MM a[76:79], a[204:207], v[128:131], a[76:79]\n"
"buffer_load_dwordx4 v249, s[48:51], s71 offen nt lds\n"
"MM a[80:83], a[208:211], v[128:131], a[80:83]\n"
"v_exp_f32 v12, v12\n"
"MM a[84:87], a[212:215], v[128:131], a[84:87]\n"
"v_exp_f32 v13, v13\n"
"MM a[88:91], a[216:219], v[128:131], a[88:91]\n"
"v_exp_f32 v14, v14\n"
"MM a[92:95], a[220:223], v[128:131], a[92:95]\n"
"ds_read_b128 a[168:171], v240 offset:16896\n"
"ds_read_b128 a[172:175], v240 offset:17152\n"
"MM a[236:239], a[244:247], v[132:135], a[236:239]\n"
"v_exp_f32 v15, v15\n"
"MM a[96:99], a[192:195], v[132:135], a[96:99]\n"
"v_cvt_pk_bf16_f32 v100, v0, v1\n"
"v_cvt_pk_bf16_f32 v101, v2, v3\n"
"MM a[100:103], a[196:199], v[132:135], a[100:103]\n"
"v_cvt_pk_bf16_f32 v102, v4, v5\n"
"v_cvt_pk_bf16_f32 v103, v6, v7\n"
"MM a[104:107], a[200:203], v[132:135], a[104:107]\n"
"buffer_load_dwordx4 v251, s[48:51], s71 offen offset:1024 nt lds\n"
"MM a[108:111], a[204:207], v[132:135], a[108:111]\n"
"v_exp_f32 v16, v16\n"
"MM a[112:115], a[208:211], v[132:135], a[112:115]\n"
"v_exp_f32 v17, v17\n"
"ds_read_b128 a[176:179], v240 offset:20480\n"
"MM a[116:119], a[212:215], v[132:135], a[116:119]\n"
"v_exp_f32 v18, v18\n"
"MM a[120:123], a[216:219], v[132:135], a[120:123]\n"
"v_exp_f32 v19, v19\n"
"ds_read_b128 a[180:183], v240 offset:20736\n"
"ds_read_b128 a[184:187], v240 offset:20992\n"
"ds_read_b128 a[188:191], v240 offset:21248\n"
"MM a[124:127], a[220:223], v[132:135], a[124:127]\n"
"buffer_load_dwordx4 v249, s[48:51], s71 offen offset:2048 nt lds\n"
"MM a[240:243], a[244:247], v[136:139], a[240:243]\n"
"v_exp_f32 v20, v20\n"
"MM a[128:131], a[192:195], v[136:139], a[128:131]\n"
"v_exp_f32 v21, v21\n"
"MM a[132:135], a[196:199], v[136:139], a[132:135]\n"
"v_exp_f32 v22, v22\n"
"MM a[136:139], a[200:203], v[136:139], a[136:139]\n"
"v_exp_f32 v23, v23\n"
"MM a[140:143], a[204:207], v[136:139], a[140:143]\n"
"v_cvt_pk_bf16_f32 v104, v8, v9\n"
"v_cvt_pk_bf16_f32 v105, v10, v11\n"
"MM a[144:147], a[208:211], v[136:139], a[144:147]\n"
"buffer_load_dwordx4 v251, s[48:51], s71 offen offset:3072 nt lds\n"
"MM a[148:151], a[212:215], v[136:139], a[148:151]\n"
"v_cvt_pk_bf16_f32 v106, v12, v13\n"
"v_cvt_pk_bf16_f32 v107, v14, v15\n"
"MM a[152:155], a[216:219], v[136:139], a[152:155]\n"
"v_exp_f32 v24, v24\n"
"MM a[156:159], a[220:223], v[136:139], a[156:159]\n"
"v_exp_f32 v25, v25\n"
"s_waitcnt lgkmcnt(0)\n"
"s_cmp_lg_u32 s75, 0\n"
"s_cbranch_scc1 K1_ESB8\n"
"K1_ENB8:\n"
"MM v[40:43], a[160:163], v[160:163], v[80:83]\n"
"v_exp_f32 v26, v26\n"
"MM v[40:43], a[164:167], v[180:183], v[40:43]\n"
"ds_read_b64_tr_b16 a[192:193], v243 offset:32768\n"
"s_add_u32 s71, s94, 90112\n"
"MM v[40:43], a[168:171], v[200:203], v[40:43]\n"
"v_exp_f32 v27, v27\n"
"MM v[40:43], a[172:175], v[220:223], v[40:43]\n"
"ds_read_b64_tr_b16 a[194:195], v243 offset:36864\n"
"s_and_b32 s71, s71, 0x1ffff\n"
"MM v[44:47], a[176:179], v[160:163], v[80:83]\n"
"v_exp_f32 v28, v28\n"
"MM v[44:47], a[180:183], v[180:183], v[44:47]\n"
"ds_read_b64_tr_b16 a[196:197], v246 offset:32768\n"
"s_add_u32 m0, s70, 65536\n"
"MM v[44:47], a[184:187], v[200:203], v[44:47]\n"
"v_exp_f32 v29, v29\n"
"MM v[44:47], a[188:191], v[220:223], v[44:47]\n"
"ds_read_b64_tr_b16 a[198:199], v246 offset:36864\n"
"v_max_f32 v140, v40, v41\n"
"MM v[48:51], a[160:163], v[164:167], v[84:87]\n"
"buffer_load_dwordx4 v249, s[44:47], s71 offen nt lds\n"
"MM v[48:51], a[164:167], v[184:187], v[48:51]\n"
"v_exp_f32 v30, v30\n"
"MM v[48:51], a[168:171], v[204:207], v[48:51]\n"
"ds_read_b64_tr_b16 a[200:201], v243 offset:33024\n"
"v_max3_f32 v140, v42, v43, v140\n"
"MM v[48:51], a[172:175], v[224:227], v[48:51]\n"
"v_exp_f32 v31, v31\n"
"MM v[52:55], a[176:179], v[164:167], v[84:87]\n"
"ds_read_b64_tr_b16 a[202:203], v243 offset:37120\n"
"v_cvt_pk_bf16_f32 v108, v16, v17\n"
"MM v[52:55], a[180:183], v[184:187], v[52:55]\n"
"v_max3_f32 v140, v44, v45, v140\n"
"v_cvt_pk_bf16_f32 v109, v18, v19\n"
"MM v[52:55], a[184:187], v[204:207], v[52:55]\n"
"ds_read_b64_tr_b16 a[204:205], v246 offset:33024\n"
"v_cvt_pk_bf16_f32 v110, v20, v21\n"
"MM v[52:55], a[188:191], v[224:227], v[52:55]\n"
"v_max3_f32 v140, v46, v47, v140\n"
"v_cvt_pk_bf16_f32 v111, v22, v23\n"
"MM v[56:59], a[160:163], v[168:171], v[88:91]\n"
"ds_read_b64_tr_b16 a[206:207], v246 offset:37120\n"
"v_max3_f32 v140, v48, v49, v140\n"
"MM v[56:59], a[164:167], v[188:191], v[56:59]\n"
"v_exp_f32 v32, v32\n"
"MM v[56:59], a[168:171], v[208:211], v[56:59]\n"
"buffer_load_dwordx4 v252, s[44:47], s71 offen offset:1024 nt lds\n"
"MM v[56:59], a[172:175], v[228:231], v[56:59]\n"
"v_exp_f32 v33, v33\n"
"MM v[60:63], a[176:179], v[168:171], v[88:91]\n"
"ds_read_b64_tr_b16 a[208:209], v243 offset:33280\n"
"v_max3_f32 v140, v50, v51, v140\n"
"MM v[60:63], a[180:183], v[188:191], v[60:63]\n"
"v_exp_f32 v34, v34\n"
"MM v[60:63], a[184:187], v[208:211], v[60:63]\n"
"ds_read_b64_tr_b16 a[210:211], v243 offset:37376\n"
"v_max3_f32 v140, v52, v53, v140\n"
"MM v[60:63], a[188:191], v[228:231], v[60:63]\n"
"v_exp_f32 v35, v35\n"
"MM v[64:67], a[160:163], v[172:175], v[92:95]\n"
"ds_read_b64_tr_b16 a[212:213], v246 offset:33280\n"
"v_max3_f32 v140, v54, v55, v140\n"
"MM v[64:67], a[164:167], v[192:195], v[64:67]\n"
"v_exp_f32 v36, v36\n"
"MM v[64:67], a[168:171], v[212:215], v[64:67]\n"
"ds_read_b64_tr_b16 a[214:215], v246 offset:37376\n"
"v_max3_f32 v140, v56, v57, v140\n"
"MM v[64:67], a[172:175], v[232:235], v[64:67]\n"
"buffer_load_dwordx4 v251, s[44:47], s71 offen offset:2048 nt lds\n"
"MM v[68:71], a[176:179], v[172:175], v[92:95]\n"
"v_exp_f32 v37, v37\n"
"MM v[68:71], a[180:183], v[192:195], v[68:71]\n"
"ds_read_b64_tr_b16 a[216:217], v243 offset:33536\n"
"v_max3_f32 v140, v58, v59, v140\n"
"MM v[68:71], a[184:187], v[212:215], v[68:71]\n"
"v_exp_f32 v38, v38\n"
"MM v[68:71], a[188:191], v[232:235], v[68:71]\n"
"ds_read_b64_tr_b16 a[218:219], v243 offset:37632\n"
"v_max3_f32 v140, v60, v61, v140\n"
"ds_read_b64_tr_b16 a[220:221], v246 offset:33536\n"
"ds_read_b64_tr_b16 a[222:223], v246 offset:37632\n"
"MM v[72:75], a[160:163], v[176:179], v[96:99]\n"
"v_exp_f32 v39, v39\n"
"MM v[72:75], a[164:167], v[196:199], v[72:75]\n"
"v_max3_f32 v140, v62, v63, v140\n"
"v_cvt_pk_bf16_f32 v112, v24, v25\n"
"MM v[72:75], a[168:171], v[216:219], v[72:75]\n"
"v_cvt_pk_bf16_f32 v113, v26, v27\n"
"v_max3_f32 v140, v64, v65, v140\n"
"MM v[72:75], a[172:175], v[236:239], v[72:75]\n"
"buffer_load_dwordx4 v250, s[44:47], s71 offen offset:3072 nt lds\n"
"MM v[76:79], a[176:179], v[176:179], v[96:99]\n"
"v_cvt_pk_bf16_f32 v114, v28, v29\n"
"v_cvt_pk_bf16_f32 v115, v30, v31\n"
"MM v[76:79], a[180:183], v[196:199], v[76:79]\n"
"v_max3_f32 v140, v66, v67, v140\n"
"v_cvt_pk_bf16_f32 v116, v32, v33\n"
"MM v[76:79], a[184:187], v[216:219], v[76:79]\n"
"v_cvt_pk_bf16_f32 v117, v34, v35\n"
"v_max3_f32 v140, v68, v69, v140\n"
"MM v[76:79], a[188:191], v[236:239], v[76:79]\n"
"v_cvt_pk_bf16_f32 v118, v36, v37\n"
"v_max3_f32 v140, v70, v71, v140\n"
"v_cvt_pk_bf16_f32 v119, v38, v39\n"
"s_waitcnt vmcnt(4)\n"
"s_waitcnt lgkmcnt(0)\n"
"v_mov_b32 v248, 31\n"
"ds_write_b32 v242, v248 offset:0\n"
"MM a[224:227], a[244:247], v[100:103], a[224:227]\n"
"v_max3_f32 v140, v72, v73, v140\n"
"v_max3_f32 v140, v74, v75, v140\n"
"MM a[0:3], a[192:195], v[100:103], a[0:3]\n"
"MM a[4:7], a[196:199], v[100:103], a[4:7]\n"
"v_max3_f32 v140, v76, v77, v140\n"
"v_max3_f32 v140, v78, v79, v140\n"
"MM a[8:11], a[200:203], v[100:103], a[8:11]\n"
"v_cmp_lt_f32 vcc, s76, v140\n"
"MM a[12:15], a[204:207], v[100:103], a[12:15]\n"
"s_nop 0\n"
"s_cbranch_vccnz K1_ESA9\n"
"K1_ENA9:\n"
"MM a[16:19], a[208:211], v[100:103], a[16:19]\n"
"v_exp_f32 v40, v40\n"
"MM a[20:23], a[212:215], v[100:103], a[20:23]\n"
"ds_read_b32 v248, v245 offset:0\n"
"MM a[24:27], a[216:219], v[100:103], a[24:27]\n"
"v_exp_f32 v41, v41\n"
"MM a[28:31], a[220:223], v[100:103], a[28:31]\n"
"v_exp_f32 v42, v42\n"
"MM a[228:231], a[244:247], v[104:107], a[228:231]\n"
"v_exp_f32 v43, v43\n"
"MM a[32:35], a[192:195], v[104:107], a[32:35]\n"
"s_waitcnt lgkmcnt(0)\n"
"v_cmp_gt_u32 vcc, 31, v248\n"
"MM a[36:39], a[196:199], v[104:107], a[36:39]\n"
"v_exp_f32 v44, v44\n"
"MM a[40:43], a[200:203], v[104:107], a[40:43]\n"
"s_cbranch_vccnz K1_A31_S\n"
"K1_A31_R:\n"
"s_add_u32 s71, s94, 81920\n"
"MM a[44:47], a[204:207], v[104:107], a[44:47]\n"
"v_exp_f32 v45, v45\n"
"MM a[48:51], a[208:211], v[104:107], a[48:51]\n"
"v_exp_f32 v46, v46\n"
"MM a[52:55], a[212:215], v[104:107], a[52:55]\n"
"v_exp_f32 v47, v47\n"
"MM a[56:59], a[216:219], v[104:107], a[56:59]\n"
"v_exp_f32 v48, v48\n"
"MM a[60:63], a[220:223], v[104:107], a[60:63]\n"
"s_and_b32 s71, s71, 0x1ffff\n"
"ds_read_b128 a[160:163], v240 offset:49152\n"
"MM a[232:235], a[244:247], v[108:111], a[232:235]\n"
"v_exp_f32 v49, v49\n"
"MM a[64:67], a[192:195], v[108:111], a[64:67]\n"
"s_add_u32 m0, s70, 81920\n"
"ds_read_b128 a[164:167], v240 offset:49408\n"
"MM a[68:71], a[196:199], v[108:111], a[68:71]\n"
"v_exp_f32 v50, v50\n"
"MM a[72:75], a[200:203], v[108:111], a[72:75]\n"
"v_exp_f32 v51, v51\n"
"MM a[76:79], a[204:207], v[108:111], a[76:79]\n"
"buffer_load_dwordx4 v249, s[48:51], s71 offen nt lds\n"
"MM a[80:83], a[208:211], v[108:111], a[80:83]\n"
"v_exp_f32 v52, v52\n"
"MM a[84:87], a[212:215], v[108:111], a[84:87]\n"
"v_exp_f32 v53, v53\n"
"MM a[88:91], a[216:219], v[108:111], a[88:91]\n"
"v_exp_f32 v54, v54\n"
"MM a[92:95], a[220:223], v[108:111], a[92:95]\n"
"ds_read_b128 a[168:171], v240 offset:49664\n"
"ds_read_b128 a[172:175], v240 offset:49920\n"
"MM a[236:239], a[244:247], v[112:115], a[236:239]\n"
"v_exp_f32 v55, v55\n"
"MM a[96:99], a[192:195], v[112:115], a[96:99]\n"
"v_cvt_pk_bf16_f32 v120, v40, v41\n"
"v_cvt_pk_bf16_f32 v121, v42, v43\n"
"MM a[100:103], a[196:199], v[112:115], a[100:103]\n"
"v_cvt_pk_bf16_f32 v122, v44, v45\n"
"v_cvt_pk_bf16_f32 v123, v46, v47\n"
"MM a[104:107], a[200:203], v[112:115], a[104:107]\n"
"buffer_load_dwordx4 v251, s[48:51], s71 offen offset:1024 nt lds\n"
"MM a[108:111], a[204:207], v[112:115], a[108:111]\n"
"v_exp_f32 v56, v56\n"
"MM a[112:115], a[208:211], v[112:115], a[112:115]\n"
"v_exp_f32 v57, v57\n"
"ds_read_b128 a[176:179], v240 offset:53248\n"
"MM a[116:119], a[212:215], v[112:115], a[116:119]\n"
"v_exp_f32 v58, v58\n"
"MM a[120:123], a[216:219], v[112:115], a[120:123]\n"
"v_exp_f32 v59, v59\n"
"ds_read_b128 a[180:183], v240 offset:53504\n"
"ds_read_b128 a[184:187], v240 offset:53760\n"
"ds_read_b128 a[188:191], v240 offset:54016\n"
"MM a[124:127], a[220:223], v[112:115], a[124:127]\n"
"buffer_load_dwordx4 v249, s[48:51], s71 offen offset:2048 nt lds\n"
"MM a[240:243], a[244:247], v[116:119], a[240:243]\n"
"v_exp_f32 v60, v60\n"
"MM a[128:131], a[192:195], v[116:119], a[128:131]\n"
"v_exp_f32 v61, v61\n"
"MM a[132:135], a[196:199], v[116:119], a[132:135]\n"
"v_exp_f32 v62, v62\n"
"MM a[136:139], a[200:203], v[116:119], a[136:139]\n"
"v_exp_f32 v63, v63\n"
"MM a[140:143], a[204:207], v[116:119], a[140:143]\n"
"v_cvt_pk_bf16_f32 v124, v48, v49\n"
"v_cvt_pk_bf16_f32 v125, v50, v51\n"
"MM a[144:147], a[208:211], v[116:119], a[144:147]\n"
"buffer_load_dwordx4 v251, s[48:51], s71 offen offset:3072 nt lds\n"
"MM a[148:151], a[212:215], v[116:119], a[148:151]\n"
"v_cvt_pk_bf16_f32 v126, v52, v53\n"
"v_cvt_pk_bf16_f32 v127, v54, v55\n"
"MM a[152:155], a[216:219], v[116:119], a[152:155]\n"
"v_exp_f32 v64, v64\n"
"MM a[156:159], a[220:223], v[116:119], a[156:159]\n"
"v_exp_f32 v65, v65\n"
"s_waitcnt lgkmcnt(0)\n"
"s_cmp_lg_u32 s75, 0\n"
"s_cbranch_scc1 K1_ESB9\n"
"K1_ENB9:\n"
"MM v[0:3], a[160:163], v[160:163], v[80:83]\n"
"v_exp_f32 v66, v66\n"
"MM v[0:3], a[164:167], v[180:183], v[0:3]\n"
"ds_read_b64_tr_b16 a[192:193], v244 offset:49152\n"
"s_add_u32 s71, s94, 98304\n"
"MM v[0:3], a[168:171], v[200:203], v[0:3]\n"
"v_exp_f32 v67, v67\n"
"MM v[0:3], a[172:175], v[220:223], v[0:3]\n"
"ds_read_b64_tr_b16 a[194:195], v244 offset:53248\n"
"s_and_b32 s71, s71, 0x1ffff\n"
"MM v[4:7], a[176:179], v[160:163], v[80:83]\n"
"v_exp_f32 v68, v68\n"
"MM v[4:7], a[180:183], v[180:183], v[4:7]\n"
"ds_read_b64_tr_b16 a[196:197], v247 offset:49152\n"
"s_add_u32 m0, s70, 98304\n"
"MM v[4:7], a[184:187], v[200:203], v[4:7]\n"
"v_exp_f32 v69, v69\n"
"MM v[4:7], a[188:191], v[220:223], v[4:7]\n"
"ds_read_b64_tr_b16 a[198:199], v247 offset:53248\n"
"v_max_f32 v140, v0, v1\n"
"MM v[8:11], a[160:163], v[164:167], v[84:87]\n"
"buffer_load_dwordx4 v249, s[44:47], s71 offen nt lds\n"
"MM v[8:11], a[164:167], v[184:187], v[8:11]\n"
"v_exp_f32 v70, v70\n"
"MM v[8:11], a[168:171], v[204:207], v[8:11]\n"
"ds_read_b64_tr_b16 a[200:201], v244 offset:49408\n"
"v_max3_f32 v140, v2, v3, v140\n"
"MM v[8:11], a[172:175], v[224:227], v[8:11]\n"
"v_exp_f32 v71, v71\n"
"MM v[12:15], a[176:179], v[164:167], v[84:87]\n"
"ds_read_b64_tr_b16 a[202:203], v244 offset:53504\n"
"v_cvt_pk_bf16_f32 v128, v56, v57\n"
"MM v[12:15], a[180:183], v[184:187], v[12:15]\n"
"v_max3_f32 v140, v4, v5, v140\n"
"v_cvt_pk_bf16_f32 v129, v58, v59\n"
"MM v[12:15], a[184:187], v[204:207], v[12:15]\n"
"ds_read_b64_tr_b16 a[204:205], v247 offset:49408\n"
"v_cvt_pk_bf16_f32 v130, v60, v61\n"
"MM v[12:15], a[188:191], v[224:227], v[12:15]\n"
"v_max3_f32 v140, v6, v7, v140\n"
"v_cvt_pk_bf16_f32 v131, v62, v63\n"
"MM v[16:19], a[160:163], v[168:171], v[88:91]\n"
"ds_read_b64_tr_b16 a[206:207], v247 offset:53504\n"
"v_max3_f32 v140, v8, v9, v140\n"
"MM v[16:19], a[164:167], v[188:191], v[16:19]\n"
"v_exp_f32 v72, v72\n"
"MM v[16:19], a[168:171], v[208:211], v[16:19]\n"
"buffer_load_dwordx4 v252, s[44:47], s71 offen offset:1024 nt lds\n"
"MM v[16:19], a[172:175], v[228:231], v[16:19]\n"
"v_exp_f32 v73, v73\n"
"MM v[20:23], a[176:179], v[168:171], v[88:91]\n"
"ds_read_b64_tr_b16 a[208:209], v244 offset:49664\n"
"v_max3_f32 v140, v10, v11, v140\n"
"MM v[20:23], a[180:183], v[188:191], v[20:23]\n"
"v_exp_f32 v74, v74\n"
"MM v[20:23], a[184:187], v[208:211], v[20:23]\n"
"ds_read_b64_tr_b16 a[210:211], v244 offset:53760\n"
"v_max3_f32 v140, v12, v13, v140\n"
"MM v[20:23], a[188:191], v[228:231], v[20:23]\n"
"v_exp_f32 v75, v75\n"
"MM v[24:27], a[160:163], v[172:175], v[92:95]\n"
"ds_read_b64_tr_b16 a[212:213], v247 offset:49664\n"
"v_max3_f32 v140, v14, v15, v140\n"
"MM v[24:27], a[164:167], v[192:195], v[24:27]\n"
"v_exp_f32 v76, v76\n"
"MM v[24:27], a[168:171], v[212:215], v[24:27]\n"
"ds_read_b64_tr_b16 a[214:215], v247 offset:53760\n"
"v_max3_f32 v140, v16, v17, v140\n"
"MM v[24:27], a[172:175], v[232:235], v[24:27]\n"
"buffer_load_dwordx4 v251, s[44:47], s71 offen offset:2048 nt lds\n"
"MM v[28:31], a[176:179], v[172:175], v[92:95]\n"
"v_exp_f32 v77, v77\n"
"MM v[28:31], a[180:183], v[192:195], v[28:31]\n"
"ds_read_b64_tr_b16 a[216:217], v244 offset:49920\n"
"v_max3_f32 v140, v18, v19, v140\n"
"MM v[28:31], a[184:187], v[212:215], v[28:31]\n"
"v_exp_f32 v78, v78\n"
"MM v[28:31], a[188:191], v[232:235], v[28:31]\n"
"ds_read_b64_tr_b16 a[218:219], v244 offset:54016\n"
"v_max3_f32 v140, v20, v21, v140\n"
"ds_read_b64_tr_b16 a[220:221], v247 offset:49920\n"
"ds_read_b64_tr_b16 a[222:223], v247 offset:54016\n"
"MM v[32:35], a[160:163], v[176:179], v[96:99]\n"
"v_exp_f32 v79, v79\n"
"MM v[32:35], a[164:167], v[196:199], v[32:35]\n"
"v_max3_f32 v140, v22, v23, v140\n"
"v_cvt_pk_bf16_f32 v132, v64, v65\n"
"MM v[32:35], a[168:171], v[216:219], v[32:35]\n"
"v_cvt_pk_bf16_f32 v133, v66, v67\n"
"v_max3_f32 v140, v24, v25, v140\n"
"MM v[32:35], a[172:175], v[236:239], v[32:35]\n"
"buffer_load_dwordx4 v250, s[44:47], s71 offen offset:3072 nt lds\n"
"MM v[36:39], a[176:179], v[176:179], v[96:99]\n"
"v_cvt_pk_bf16_f32 v134, v68, v69\n"
"v_cvt_pk_bf16_f32 v135, v70, v71\n"
"MM v[36:39], a[180:183], v[196:199], v[36:39]\n"
"v_max3_f32 v140, v26, v27, v140\n"
"v_cvt_pk_bf16_f32 v136, v72, v73\n"
"MM v[36:39], a[184:187], v[216:219], v[36:39]\n"
"v_cvt_pk_bf16_f32 v137, v74, v75\n"
"v_max3_f32 v140, v28, v29, v140\n"
"MM v[36:39], a[188:191], v[236:239], v[36:39]\n"
"v_cvt_pk_bf16_f32 v138, v76, v77\n"
"v_max3_f32 v140, v30, v31, v140\n"
"v_cvt_pk_bf16_f32 v139, v78, v79\n"
"s_waitcnt vmcnt(4)\n"
"s_waitcnt lgkmcnt(0)\n"
"v_mov_b32 v248, 32\n"
"ds_write_b32 v242, v248 offset:0\n"
"MM a[224:227], a[244:247], v[120:123], a[224:227]\n"
"v_max3_f32 v140, v32, v33, v140\n"
"v_max3_f32 v140, v34, v35, v140\n"
"MM a[0:3], a[192:195], v[120:123], a[0:3]\n"
"MM a[4:7], a[196:199], v[120:123], a[4:7]\n"
"v_max3_f32 v140, v36, v37, v140\n"
"v_max3_f32 v140, v38, v39, v140\n"
"MM a[8:11], a[200:203], v[120:123], a[8:11]\n"
"v_cmp_lt_f32 vcc, s76, v140\n"
"MM a[12:15], a[204:207], v[120:123], a[12:15]\n"
"s_nop 0\n"
"s_cbranch_vccnz K1_ESA10\n"
"K1_ENA10:\n"
"MM a[16:19], a[208:211], v[120:123], a[16:19]\n"
"v_exp_f32 v0, v0\n"
"MM a[20:23], a[212:215], v[120:123], a[20:23]\n"
"ds_read_b32 v248, v245 offset:0\n"
"MM a[24:27], a[216:219], v[120:123], a[24:27]\n"
"v_exp_f32 v1, v1\n"
"MM a[28:31], a[220:223], v[120:123], a[28:31]\n"
"v_exp_f32 v2, v2\n"
"MM a[228:231], a[244:247], v[124:127], a[228:231]\n"
"v_exp_f32 v3, v3\n"
"MM a[32:35], a[192:195], v[124:127], a[32:35]\n"
"s_waitcnt lgkmcnt(0)\n"
"v_cmp_gt_u32 vcc, 32, v248\n"
"MM a[36:39], a[196:199], v[124:127], a[36:39]\n"
"v_exp_f32 v4, v4\n"
"MM a[40:43], a[200:203], v[124:127], a[40:43]\n"
"s_cbranch_vccnz K1_A32_S\n"
"K1_A32_R:\n"
"s_add_u32 s71, s94, 90112\n"
"MM a[44:47], a[204:207], v[124:127], a[44:47]\n"
"v_exp_f32 v5, v5\n"
"MM a[48:51], a[208:211], v[124:127], a[48:51]\n"
"v_exp_f32 v6, v6\n"
"MM a[52:55], a[212:215], v[124:127], a[52:55]\n"
"v_exp_f32 v7, v7\n"
"MM a[56:59], a[216:219], v[124:127], a[56:59]\n"
"v_exp_f32 v8, v8\n"
"MM a[60:63], a[220:223], v[124:127], a[60:63]\n"
"s_and_b32 s71, s71, 0x1ffff\n"
"ds_read_b128 a[160:163], v241 offset:0\n"
"MM a[232:235], a[244:247], v[128:131], a[232:235]\n"
"v_exp_f32 v9, v9\n"
"MM a[64:67], a[192:195], v[128:131], a[64:67]\n"
"s_add_u32 m0, s70, 0\n"
"ds_read_b128 a[164:167], v241 offset:256\n"
"MM a[68:71], a[196:199], v[128:131], a[68:71]\n"
"v_exp_f32 v10, v10\n"
"MM a[72:75], a[200:203], v[128:131], a[72:75]\n"
"v_exp_f32 v11, v11\n"
"MM a[76:79], a[204:207], v[128:131], a[76:79]\n"
"buffer_load_dwordx4 v249, s[48:51], s71 offen nt lds\n"
"MM a[80:83], a[208:211], v[128:131], a[80:83]\n"
"v_exp_f32 v12, v12\n"
"MM a[84:87], a[212:215], v[128:131], a[84:87]\n"
"v_exp_f32 v13, v13\n"
"MM a[88:91], a[216:219], v[128:131], a[88:91]\n"
"v_exp_f32 v14, v14\n"
"MM a[92:95], a[220:223], v[128:131], a[92:95]\n"
"ds_read_b128 a[168:171], v241 offset:512\n"
"ds_read_b128 a[172:175], v241 offset:768\n"
"MM a[236:239], a[244:247], v[132:135], a[236:239]\n"
"v_exp_f32 v15, v15\n"
"MM a[96:99], a[192:195], v[132:135], a[96:99]\n"
"v_cvt_pk_bf16_f32 v100, v0, v1\n"
"v_cvt_pk_bf16_f32 v101, v2, v3\n"
"MM a[100:103], a[196:199], v[132:135], a[100:103]\n"
"v_cvt_pk_bf16_f32 v102, v4, v5\n"
"v_cvt_pk_bf16_f32 v103, v6, v7\n"
"MM a[104:107], a[200:203], v[132:135], a[104:107]\n"
"buffer_load_dwordx4 v251, s[48:51], s71 offen offset:1024 nt lds\n"
"MM a[108:111], a[204:207], v[132:135], a[108:111]\n"
"v_exp_f32 v16, v16\n"
"MM a[112:115], a[208:211], v[132:135], a[112:115]\n"
"v_exp_f32 v17, v17\n"
"ds_read_b128 a[176:179], v241 offset:4096\n"
"MM a[116:119], a[212:215], v[132:135], a[116:119]\n"
"v_exp_f32 v18, v18\n"
"MM a[120:123], a[216:219], v[132:135], a[120:123]\n"
"v_exp_f32 v19, v19\n"
"ds_read_b128 a[180:183], v241 offset:4352\n"
"ds_read_b128 a[184:187], v241 offset:4608\n"
"ds_read_b128 a[188:191], v241 offset:4864\n"
"MM a[124:127], a[220:223], v[132:135], a[124:127]\n"
"buffer_load_dwordx4 v249, s[48:51], s71 offen offset:2048 nt lds\n"
"MM a[240:243], a[244:247], v[136:139], a[240:243]\n"
"v_exp_f32 v20, v20\n"
"MM a[128:131], a[192:195], v[136:139], a[128:131]\n"
"v_exp_f32 v21, v21\n"
"MM a[132:135], a[196:199], v[136:139], a[132:135]\n"
"v_exp_f32 v22, v22\n"
"MM a[136:139], a[200:203], v[136:139], a[136:139]\n"
"v_exp_f32 v23, v23\n"
"MM a[140:143], a[204:207], v[136:139], a[140:143]\n"
"v_cvt_pk_bf16_f32 v104, v8, v9\n"
"v_cvt_pk_bf16_f32 v105, v10, v11\n"
"MM a[144:147], a[208:211], v[136:139], a[144:147]\n"
"buffer_load_dwordx4 v251, s[48:51], s71 offen offset:3072 nt lds\n"
"MM a[148:151], a[212:215], v[136:139], a[148:151]\n"
"v_cvt_pk_bf16_f32 v106, v12, v13\n"
"v_cvt_pk_bf16_f32 v107, v14, v15\n"
"MM a[152:155], a[216:219], v[136:139], a[152:155]\n"
"v_exp_f32 v24, v24\n"
"MM a[156:159], a[220:223], v[136:139], a[156:159]\n"
"v_exp_f32 v25, v25\n"
"s_waitcnt lgkmcnt(0)\n"
"s_cmp_lg_u32 s75, 0\n"
"s_cbranch_scc1 K1_ESB10\n"
"K1_ENB10:\n"
"MM v[40:43], a[160:163], v[160:163], v[80:83]\n"
"v_exp_f32 v26, v26\n"
"MM v[40:43], a[164:167], v[180:183], v[40:43]\n"
"ds_read_b64_tr_b16 a[192:193], v244 offset:16384\n"
"s_add_u32 s71, s94, 106496\n"
"MM v[40:43], a[168:171], v[200:203], v[40:43]\n"
"v_exp_f32 v27, v27\n"
"MM v[40:43], a[172:175], v[220:223], v[40:43]\n"
"ds_read_b64_tr_b16 a[194:195], v244 offset:20480\n"
"s_and_b32 s71, s71, 0x1ffff\n"
"MM v[44:47], a[176:179], v[160:163], v[80:83]\n"
"v_exp_f32 v28, v28\n"
"MM v[44:47], a[180:183], v[180:183], v[44:47]\n"
"ds_read_b64_tr_b16 a[196:197], v247 offset:16384\n"
"s_add_u32 m0, s70, 16384\n"
"MM v[44:47], a[184:187], v[200:203], v[44:47]\n"
"v_exp_f32 v29, v29\n"
"MM v[44:47], a[188:191], v[220:223], v[44:47]\n"
"ds_read_b64_tr_b16 a[198:199], v247 offset:20480\n"
"v_max_f32 v140, v40, v41\n"
"MM v[48:51], a[160:163], v[164:167], v[84:87]\n"
"buffer_load_dwordx4 v249, s[44:47], s71 offen nt lds\n"
"MM v[48:51], a[164:167], v[184:187], v[48:51]\n"
"v_exp_f32 v30, v30\n"
"MM v[48:51], a[168:171], v[204:207], v[48:51]\n"
"ds_read_b64_tr_b16 a[200:201], v244 offset:16640\n"
"v_max3_f32 v140, v42, v43, v140\n"
"MM v[48:51], a[172:175], v[224:227], v[48:51]\n"
"v_exp_f32 v31, v31\n"
"MM v[52:55], a[176:179], v[164:167], v[84:87]\n"
"ds_read_b64_tr_b16 a[202:203], v244 offset:20736\n"
"v_cvt_pk_bf16_f32 v108, v16, v17\n"
"MM v[52:55], a[180:183], v[184:187], v[52:55]\n"
"v_max3_f32 v140, v44, v45, v140\n"
"v_cvt_pk_bf16_f32 v109, v18, v19\n"
"MM v[52:55], a[184:187], v[204:207], v[52:55]\n"
"ds_read_b64_tr_b16 a[204:205], v247 offset:16640\n"
"v_cvt_pk_bf16_f32 v110, v20, v21\n"
"MM v[52:55], a[188:191], v[224:227], v[52:55]\n"
"v_max3_f32 v140, v46, v47, v140\n"
"v_cvt_pk_bf16_f32 v111, v22, v23\n"
"MM v[56:59], a[160:163], v[168:171], v[88:91]\n"
"ds_read_b64_tr_b16 a[206:207], v247 offset:20736\n"
"v_max3_f32 v140, v48, v49, v140\n"
"MM v[56:59], a[164:167], v[188:191], v[56:59]\n"
"v_exp_f32 v32, v32\n"
"MM v[56:59], a[168:171], v[208:211], v[56:59]\n"
"buffer_load_dwordx4 v252, s[44:47], s71 offen offset:1024 nt lds\n"
"MM v[56:59], a[172:175], v[228:231], v[56:59]\n"
"v_exp_f32 v33, v33\n"
"MM v[60:63], a[176:179], v[168:171], v[88:91]\n"
"ds_read_b64_tr_b16 a[208:209], v244 offset:16896\n"
"v_max3_f32 v140, v50, v51, v140\n"
"MM v[60:63], a[180:183], v[188:191], v[60:63]\n"
"v_exp_f32 v34, v34\n"
"MM v[60:63], a[184:187], v[208:211], v[60:63]\n"
"ds_read_b64_tr_b16 a[210:211], v244 offset:20992\n"
"v_max3_f32 v140, v52, v53, v140\n"
"MM v[60:63], a[188:191], v[228:231], v[60:63]\n"
"v_exp_f32 v35, v35\n"
"MM v[64:67], a[160:163], v[172:175], v[92:95]\n"
"ds_read_b64_tr_b16 a[212:213], v247 offset:16896\n"
"v_max3_f32 v140, v54, v55, v140\n"
"MM v[64:67], a[164:167], v[192:195], v[64:67]\n"
"v_exp_f32 v36, v36\n"
"MM v[64:67], a[168:171], v[212:215], v[64:67]\n"
"ds_read_b64_tr_b16 a[214:215], v247 offset:20992\n"
"v_max3_f32 v140, v56, v57, v140\n"
"MM v[64:67], a[172:175], v[232:235], v[64:67]\n"
"buffer_load_dwordx4 v251, s[44:47], s71 offen offset:2048 nt lds\n"
"MM v[68:71], a[176:179], v[172:175], v[92:95]\n"
"v_exp_f32 v37, v37\n"
"MM v[68:71], a[180:183], v[192:195], v[68:71]\n"
"ds_read_b64_tr_b16 a[216:217], v244 offset:17152\n"
"v_max3_f32 v140, v58, v59, v140\n"
"MM v[68:71], a[184:187], v[212:215], v[68:71]\n"
"v_exp_f32 v38, v38\n"
"MM v[68:71], a[188:191], v[232:235], v[68:71]\n"
"ds_read_b64_tr_b16 a[218:219], v244 offset:21248\n"
"v_max3_f32 v140, v60, v61, v140\n"
"ds_read_b64_tr_b16 a[220:221], v247 offset:17152\n"
"ds_read_b64_tr_b16 a[222:223], v247 offset:21248\n"
"MM v[72:75], a[160:163], v[176:179], v[96:99]\n"
"v_exp_f32 v39, v39\n"
"MM v[72:75], a[164:167], v[196:199], v[72:75]\n"
"v_max3_f32 v140, v62, v63, v140\n"
"v_cvt_pk_bf16_f32 v112, v24, v25\n"
"MM v[72:75], a[168:171], v[216:219], v[72:75]\n"
"v_cvt_pk_bf16_f32 v113, v26, v27\n"
"v_max3_f32 v140, v64, v65, v140\n"
"MM v[72:75], a[172:175], v[236:239], v[72:75]\n"
"buffer_load_dwordx4 v250, s[44:47], s71 offen offset:3072 nt lds\n"
"MM v[76:79], a[176:179], v[176:179], v[96:99]\n"
"v_cvt_pk_bf16_f32 v114, v28, v29\n"
"v_cvt_pk_bf16_f32 v115, v30, v31\n"
"MM v[76:79], a[180:183], v[196:199], v[76:79]\n"
"v_max3_f32 v140, v66, v67, v140\n"
"v_cvt_pk_bf16_f32 v116, v32, v33\n"
"MM v[76:79], a[184:187], v[216:219], v[76:79]\n"
"v_cvt_pk_bf16_f32 v117, v34, v35\n"
"v_max3_f32 v140, v68, v69, v140\n"
"MM v[76:79], a[188:191], v[236:239], v[76:79]\n"
"v_cvt_pk_bf16_f32 v118, v36, v37\n"
"v_max3_f32 v140, v70, v71, v140\n"
"v_cvt_pk_bf16_f32 v119, v38, v39\n"
"s_waitcnt vmcnt(4)\n"
"s_waitcnt lgkmcnt(0)\n"
"v_mov_b32 v248, 33\n"
"ds_write_b32 v242, v248 offset:0\n"
"MM a[224:227], a[244:247], v[100:103], a[224:227]\n"
"v_max3_f32 v140, v72, v73, v140\n"
"v_max3_f32 v140, v74, v75, v140\n"
"MM a[0:3], a[192:195], v[100:103], a[0:3]\n"
"MM a[4:7], a[196:199], v[100:103], a[4:7]\n"
"v_max3_f32 v140, v76, v77, v140\n"
"v_max3_f32 v140, v78, v79, v140\n"
"MM a[8:11], a[200:203], v[100:103], a[8:11]\n"
"v_cmp_lt_f32 vcc, s76, v140\n"
"MM a[12:15], a[204:207], v[100:103], a[12:15]\n"
"s_nop 0\n"
"s_cbranch_vccnz K1_ESA11\n"
"K1_ENA11:\n"
"MM a[16:19], a[208:211], v[100:103], a[16:19]\n"
"v_exp_f32 v40, v40\n"
"MM a[20:23], a[212:215], v[100:103], a[20:23]\n"
"ds_read_b32 v248, v245 offset:0\n"
"MM a[24:27], a[216:219], v[100:103], a[24:27]\n"
"v_exp_f32 v41, v41\n"
"MM a[28:31], a[220:223], v[100:103], a[28:31]\n"
"v_exp_f32 v42, v42\n"
"MM a[228:231], a[244:247], v[104:107], a[228:231]\n"
"v_exp_f32 v43, v43\n"
"MM a[32:35], a[192:195], v[104:107], a[32:35]\n"
"s_waitcnt lgkmcnt(0)\n"
"v_cmp_gt_u32 vcc, 33, v248\n"
"MM a[36:39], a[196:199], v[104:107], a[36:39]\n"
"v_exp_f32 v44, v44\n"
"MM a[40:43], a[200:203], v[104:107], a[40:43]\n"
"s_cbranch_vccnz K1_A33_S\n"
"K1_A33_R:\n"
"s_add_u32 s71, s94, 98304\n"
"MM a[44:47], a[204:207], v[104:107], a[44:47]\n"
"v_exp_f32 v45, v45\n"
"MM a[48:51], a[208:211], v[104:107], a[48:51]\n"
"v_exp_f32 v46, v46\n"
"MM a[52:55], a[212:215], v[104:107], a[52:55]\n"
"v_exp_f32 v47, v47\n"
"MM a[56:59], a[216:219], v[104:107], a[56:59]\n"
"v_exp_f32 v48, v48\n"
"MM a[60:63], a[220:223], v[104:107], a[60:63]\n"
"s_and_b32 s71, s71, 0x1ffff\n"
"ds_read_b128 a[160:163], v241 offset:32768\n"
"MM a[232:235], a[244:247], v[108:111], a[232:235]\n"
"v_exp_f32 v49, v49\n"
"MM a[64:67], a[192:195], v[108:111], a[64:67]\n"
"s_add_u32 m0, s70, 32768\n"
"ds_read_b128 a[164:167], v241 offset:33024\n"
"MM a[68:71], a[196:199], v[108:111], a[68:71]\n"
"v_exp_f32 v50, v50\n"
"MM a[72:75], a[200:203], v[108:111], a[72:75]\n"
"v_exp_f32 v51, v51\n"
"MM a[76:79], a[204:207], v[108:111], a[76:79]\n"
"buffer_load_dwordx4 v249, s[48:51], s71 offen nt lds\n"
"MM a[80:83], a[208:211], v[108:111], a[80:83]\n"
"v_exp_f32 v52, v52\n"
"MM a[84:87], a[212:215], v[108:111], a[84:87]\n"
"v_exp_f32 v53, v53\n"
"MM a[88:91], a[216:219], v[108:111], a[88:91]\n"
"v_exp_f32 v54, v54\n"
"MM a[92:95], a[220:223], v[108:111], a[92:95]\n"
"ds_read_b128 a[168:171], v241 offset:33280\n"
"ds_read_b128 a[172:175], v241 offset:33536\n"
"MM a[236:239], a[244:247], v[112:115], a[236:239]\n"
"v_exp_f32 v55, v55\n"
"MM a[96:99], a[192:195], v[112:115], a[96:99]\n"
"v_cvt_pk_bf16_f32 v120, v40, v41\n"
"v_cvt_pk_bf16_f32 v121, v42, v43\n"
"MM a[100:103], a[196:199], v[112:115], a[100:103]\n"
"v_cvt_pk_bf16_f32 v122, v44, v45\n"
"v_cvt_pk_bf16_f32 v123, v46, v47\n"
"MM a[104:107], a[200:203], v[112:115], a[104:107]\n"
"buffer_load_dwordx4 v251, s[48:51], s71 offen offset:1024 nt lds\n"
"MM a[108:111], a[204:207], v[112:115], a[108:111]\n"
"v_exp_f32 v56, v56\n"
"MM a[112:115], a[208:211], v[112:115], a[112:115]\n"
"v_exp_f32 v57, v57\n"
"ds_read_b128 a[176:179], v241 offset:36864\n"
"MM a[116:119], a[212:215], v[112:115], a[116:119]\n"
"v_exp_f32 v58, v58\n"
"MM a[120:123], a[216:219], v[112:115], a[120:123]\n"
"v_exp_f32 v59, v59\n"
"ds_read_b128 a[180:183], v241 offset:37120\n"
"ds_read_b128 a[184:187], v241 offset:37376\n"
"ds_read_b128 a[188:191], v241 offset:37632\n"
"MM a[124:127], a[220:223], v[112:115], a[124:127]\n"
"buffer_load_dwordx4 v249, s[48:51], s71 offen offset:2048 nt lds\n"
"MM a[240:243], a[244:247], v[116:119], a[240:243]\n"
"v_exp_f32 v60, v60\n"
"MM a[128:131], a[192:195], v[116:119], a[128:131]\n"
"v_exp_f32 v61, v61\n"
"MM a[132:135], a[196:199], v[116:119], a[132:135]\n"
"v_exp_f32 v62, v62\n"
"MM a[136:139], a[200:203], v[116:119], a[136:139]\n"
"v_exp_f32 v63, v63\n"
"MM a[140:143], a[204:207], v[116:119], a[140:143]\n"
"v_cvt_pk_bf16_f32 v124, v48, v49\n"
"v_cvt_pk_bf16_f32 v125, v50, v51\n"
"MM a[144:147], a[208:211], v[116:119], a[144:147]\n"
"buffer_load_dwordx4 v251, s[48:51], s71 offen offset:3072 nt lds\n"
"MM a[148:151], a[212:215], v[116:119], a[148:151]\n"
"v_cvt_pk_bf16_f32 v126, v52, v53\n"
"v_cvt_pk_bf16_f32 v127, v54, v55\n"
"MM a[152:155], a[216:219], v[116:119], a[152:155]\n"
"v_exp_f32 v64, v64\n"
"MM a[156:159], a[220:223], v[116:119], a[156:159]\n"
"v_exp_f32 v65, v65\n"
"s_waitcnt lgkmcnt(0)\n"
"s_cmp_lg_u32 s75, 0\n"
"s_cbranch_scc1 K1_ESB11\n"
"K1_ENB11:\n"
"MM v[0:3], a[160:163], v[160:163], v[80:83]\n"
"v_exp_f32 v66, v66\n"
"MM v[0:3], a[164:167], v[180:183], v[0:3]\n"
"ds_read_b64_tr_b16 a[192:193], v243 offset:0\n"
"s_add_u32 s71, s94, 114688\n"
"MM v[0:3], a[168:171], v[200:203], v[0:3]\n"
"v_exp_f32 v67, v67\n"
"MM v[0:3], a[172:175], v[220:223], v[0:3]\n"
"ds_read_b64_tr_b16 a[194:195], v243 offset:4096\n"
"s_and_b32 s71, s71, 0x1ffff\n"
"MM v[4:7], a[176:179], v[160:163], v[80:83]\n"
"v_exp_f32 v68, v68\n"
"MM v[4:7], a[180:183], v[180:183], v[4:7]\n"
"ds_read_b64_tr_b16 a[196:197], v246 offset:0\n"
"s_add_u32 m0, s70, 49152\n"
"MM v[4:7], a[184:187], v[200:203], v[4:7]\n"
"v_exp_f32 v69, v69\n"
"MM v[4:7], a[188:191], v[220:223], v[4:7]\n"
"ds_read_b64_tr_b16 a[198:199], v246 offset:4096\n"
"v_max_f32 v140, v0, v1\n"
"MM v[8:11], a[160:163], v[164:167], v[84:87]\n"
"buffer_load_dwordx4 v249, s[44:47], s71 offen nt lds\n"
"MM v[8:11], a[164:167], v[184:187], v[8:11]\n"
"v_exp_f32 v70, v70\n"
"MM v[8:11], a[168:171], v[204:207], v[8:11]\n"
"ds_read_b64_tr_b16 a[200:201], v243 offset:256\n"
"v_max3_f32 v140, v2, v3, v140\n"
"MM v[8:11], a[172:175], v[224:227], v[8:11]\n"
"v_exp_f32 v71, v71\n"
"MM v[12:15], a[176:179], v[164:167], v[84:87]\n"
"ds_read_b64_tr_b16 a[202:203], v243 offset:4352\n"
"v_cvt_pk_bf16_f32 v128, v56, v57\n"
"MM v[12:15], a[180:183], v[184:187], v[12:15]\n"
"v_max3_f32 v140, v4, v5, v140\n"
"v_cvt_pk_bf16_f32 v129, v58, v59\n"
"MM v[12:15], a[184:187], v[204:207], v[12:15]\n"
"ds_read_b64_tr_b16 a[204:205], v246 offset:256\n"
"v_cvt_pk_bf16_f32 v130, v60, v61\n"
"MM v[12:15], a[188:191], v[224:227], v[12:15]\n"
"v_max3_f32 v140, v6, v7, v140\n"
"v_cvt_pk_bf16_f32 v131, v62, v63\n"
"MM v[16:19], a[160:163], v[168:171], v[88:91]\n"
"ds_read_b64_tr_b16 a[206:207], v246 offset:4352\n"
"v_max3_f32 v140, v8, v9, v140\n"
"MM v[16:19], a[164:167], v[188:191], v[16:19]\n"
"v_exp_f32 v72, v72\n"
"MM v[16:19], a[168:171], v[208:211], v[16:19]\n"
"buffer_load_dwordx4 v252, s[44:47], s71 offen offset:1024 nt lds\n"
"MM v[16:19], a[172:175], v[228:231], v[16:19]\n"
"v_exp_f32 v73, v73\n"
"MM v[20:23], a[176:179], v[168:171], v[88:91]\n"
"ds_read_b64_tr_b16 a[208:209], v243 offset:512\n"
"v_max3_f32 v140, v10, v11, v140\n"
"MM v[20:23], a[180:183], v[188:191], v[20:23]\n"
"v_exp_f32 v74, v74\n"
"MM v[20:23], a[184:187], v[208:211], v[20:23]\n"
"ds_read_b64_tr_b16 a[210:211], v243 offset:4608\n"
"v_max3_f32 v140, v12, v13, v140\n"
"MM v[20:23], a[188:191], v[228:231], v[20:23]\n"
"v_exp_f32 v75, v75\n"
"MM v[24:27], a[160:163], v[172:175], v[92:95]\n"
"ds_read_b64_tr_b16 a[212:213], v246 offset:512\n"
"v_max3_f32 v140, v14, v15, v140\n"
"MM v[24:27], a[164:167], v[192:195], v[24:27]\n"
"v_exp_f32 v76, v76\n"
"MM v[24:27], a[168:171], v[212:215], v[24:27]\n"
"ds_read_b64_tr_b16 a[214:215], v246 offset:4608\n"
"v_max3_f32 v140, v16, v17, v140\n"
"MM v[24:27], a[172:175], v[232:235], v[24:27]\n"
"buffer_load_dwordx4 v251, s[44:47], s71 offen offset:2048 nt lds\n"
"MM v[28:31], a[176:179], v[172:175], v[92:95]\n"
"v_exp_f32 v77, v77\n"
"MM v[28:31], a[180:183], v[192:195], v[28:31]\n"
"ds_read_b64_tr_b16 a[216:217], v243 offset:768\n"
"v_max3_f32 v140, v18, v19, v140\n"
"MM v[28:31], a[184:187], v[212:215], v[28:31]\n"
"v_exp_f32 v78, v78\n"
"MM v[28:31], a[188:191], v[232:235], v[28:31]\n"
"ds_read_b64_tr_b16 a[218:219], v243 offset:4864\n"
"v_max3_f32 v140, v20, v21, v140\n"
"ds_read_b64_tr_b16 a[220:221], v246 offset:768\n"
"ds_read_b64_tr_b16 a[222:223], v246 offset:4864\n"
"MM v[32:35], a[160:163], v[176:179], v[96:99]\n"
"v_exp_f32 v79, v79\n"
"MM v[32:35], a[164:167], v[196:199], v[32:35]\n"
"v_max3_f32 v140, v22, v23, v140\n"
"v_cvt_pk_bf16_f32 v132, v64, v65\n"
"MM v[32:35], a[168:171], v[216:219], v[32:35]\n"
"v_cvt_pk_bf16_f32 v133, v66, v67\n"
"v_max3_f32 v140, v24, v25, v140\n"
"MM v[32:35], a[172:175], v[236:239], v[32:35]\n"
"buffer_load_dwordx4 v250, s[44:47], s71 offen offset:3072 nt lds\n"
"MM v[36:39], a[176:179], v[176:179], v[96:99]\n"
"v_cvt_pk_bf16_f32 v134, v68, v69\n"
"v_cvt_pk_bf16_f32 v135, v70, v71\n"
"MM v[36:39], a[180:183], v[196:199], v[36:39]\n"
"v_max3_f32 v140, v26, v27, v140\n"
"v_cvt_pk_bf16_f32 v136, v72, v73\n"
"MM v[36:39], a[184:187], v[216:219], v[36:39]\n"
"v_cvt_pk_bf16_f32 v137, v74, v75\n"
"v_max3_f32 v140, v28, v29, v140\n"
"MM v[36:39], a[188:191], v[236:239], v[36:39]\n"
"v_cvt_pk_bf16_f32 v138, v76, v77\n"
"v_max3_f32 v140, v30, v31, v140\n"
"v_cvt_pk_bf16_f32 v139, v78, v79\n"
"s_waitcnt vmcnt(4)\n"
"s_waitcnt lgkmcnt(0)\n"
"v_mov_b32 v248, 34\n"
"ds_write_b32 v242, v248 offset:0\n"
"MM a[224:227], a[244:247], v[120:123], a[224:227]\n"
"v_max3_f32 v140, v32, v33, v140\n"
"v_max3_f32 v140, v34, v35, v140\n"
"MM a[0:3], a[192:195], v[120:123], a[0:3]\n"
"MM a[4:7], a[196:199], v[120:123], a[4:7]\n"
"v_max3_f32 v140, v36, v37, v140\n"
"v_max3_f32 v140, v38, v39, v140\n"
"MM a[8:11], a[200:203], v[120:123], a[8:11]\n"
"v_cmp_lt_f32 vcc, s76, v140\n"
"MM a[12:15], a[204:207], v[120:123], a[12:15]\n"
"s_nop 0\n"
"s_cbranch_vccnz K1_ESA12\n"
"K1_ENA12:\n"
"MM a[16:19], a[208:211], v[120:123], a[16:19]\n"
"v_exp_f32 v0, v0\n"
"MM a[20:23], a[212:215], v[120:123], a[20:23]\n"
"ds_read_b32 v248, v245 offset:0\n"
"MM a[24:27], a[216:219], v[120:123], a[24:27]\n"
"v_exp_f32 v1, v1\n"
"MM a[28:31], a[220:223], v[120:123], a[28:31]\n"
"v_exp_f32 v2, v2\n"
"MM a[228:231], a[244:247], v[124:127], a[228:231]\n"
"v_exp_f32 v3, v3\n"
"MM a[32:35], a[192:195], v[124:127], a[32:35]\n"
"s_waitcnt lgkmcnt(0)\n"
"v_cmp_gt_u32 vcc, 34, v248\n"
"MM a[36:39], a[196:199], v[124:127], a[36:39]\n"
"v_exp_f32 v4, v4\n"
"MM a[40:43], a[200:203], v[124:127], a[40:43]\n"
"s_cbranch_vccnz K1_A34_S\n"
"K1_A34_R:\n"
"s_add_u32 s71, s94, 106496\n"
"MM a[44:47], a[204:207], v[124:127], a[44:47]\n"
"v_exp_f32 v5, v5\n"
"MM a[48:51], a[208:211], v[124:127], a[48:51]\n"
"v_exp_f32 v6, v6\n"
"MM a[52:55], a[212:215], v[124:127], a[52:55]\n"
"v_exp_f32 v7, v7\n"
"MM a[56:59], a[216:219], v[124:127], a[56:59]\n"
"s_and_b32 s71, s71, 0x1ffff\n"
"ds_read_b128 a[160:163], v240 offset:16384\n"
"MM a[60:63], a[220:223], v[124:127], a[60:63]\n"
"v_exp_f32 v8, v8\n"
"MM a[232:235], a[244:247], v[128:131], a[232:235]\n"
"s_add_u32 m0, s70, 114688\n"
"ds_read_b128 a[164:167], v240 offset:16640\n"
"MM a[64:67], a[192:195], v[128:131], a[64:67]\n"
"v_exp_f32 v9, v9\n"
"MM a[68:71], a[196:199], v[128:131], a[68:71]\n"
"v_exp_f32 v10, v10\n"
"MM a[72:75], a[200:203], v[128:131], a[72:75]\n"
"buffer_load_dwordx4 v249, s[48:51], s71 offen nt lds\n"
"MM a[76:79], a[204:207], v[128:131], a[76:79]\n"
"v_exp_f32 v11, v11\n"
"MM a[80:83], a[208:211], v[128:131], a[80:83]\n"
"v_exp_f32 v12, v12\n"
"MM a[84:87], a[212:215], v[128:131], a[84:87]\n"
"v_exp_f32 v13, v13\n"
"MM a[88:91], a[216:219], v[128:131], a[88:91]\n"
"v_exp_f32 v14, v14\n"
"MM a[92:95], a[220:223], v[128:131], a[92:95]\n"
"buffer_load_dwordx4 v251, s[48:51], s71 offen offset:1024 nt lds\n"
"ds_read_b128 a[168:171], v240 offset:16896\n"
"MM a[236:239], a[244:247], v[132:135], a[236:239]\n"
"v_exp_f32 v15, v15\n"
"MM a[96:99], a[192:195], v[132:135], a[96:99]\n"
"v_cvt_pk_bf16_f32 v100, v0, v1\n"
"v_cvt_pk_bf16_f32 v101, v2, v3\n"
"MM a[100:103], a[196:199], v[132:135], a[100:103]\n"
"v_cvt_pk_bf16_f32 v102, v4, v5\n"
"ds_read_b128 a[172:175], v240 offset:17152\n"
"MM a[104:107], a[200:203], v[132:135], a[104:107]\n"
"v_cvt_pk_bf16_f32 v103, v6, v7\n"
"ds_read_b128 a[176:179], v240 offset:20480\n"
"MM a[108:111], a[204:207], v[132:135], a[108:111]\n"
"v_exp_f32 v16, v16\n"
"MM a[112:115], a[208:211], v[132:135], a[112:115]\n"
"buffer_load_dwordx4 v249, s[48:51], s71 offen offset:2048 nt lds\n"
"MM a[116:119], a[212:215], v[132:135], a[116:119]\n"
"v_exp_f32 v17, v17\n"
"MM a[120:123], a[216:219], v[132:135], a[120:123]\n"
"v_exp_f32 v18, v18\n"
"ds_read_b128 a[180:183], v240 offset:20736\n"
"ds_read_b128 a[184:187], v240 offset:20992\n"
"ds_read_b128 a[188:191], v240 offset:21248\n"
"MM a[124:127], a[220:223], v[132:135], a[124:127]\n"
"v_exp_f32 v19, v19\n"
"MM a[240:243], a[244:247], v[136:139], a[240:243]\n"
"v_exp_f32 v20, v20\n"
"MM a[128:131], a[192:195], v[136:139], a[128:131]\n"
"buffer_load_dwordx4 v251, s[48:51], s71 offen offset:3072 nt lds\n"
"MM a[132:135], a[196:199], v[136:139], a[132:135]\n"
"v_exp_f32 v21, v21\n"
"MM a[136:139], a[200:203], v[136:139], a[136:139]\n"
"v_exp_f32 v22, v22\n"
"MM a[140:143], a[204:207], v[136:139], a[140:143]\n"
"v_exp_f32 v23, v23\n"
"MM a[144:147], a[208:211], v[136:139], a[144:147]\n"
"v_cvt_pk_bf16_f32 v104, v8, v9\n"
"v_lshlrev_b32 v149, 4, v253\n"
"MM a[148:151], a[212:215], v[136:139], a[148:151]\n"
"v_cvt_pk_bf16_f32 v105, v10, v11\n"
"v_cvt_pk_bf16_f32 v106, v12, v13\n"
"MM a[152:155], a[216:219], v[136:139], a[152:155]\n"
"v_cvt_pk_bf16_f32 v107, v14, v15\n"
"MM a[156:159], a[220:223], v[136:139], a[156:159]\n"
"v_exp_f32 v24, v24\n"
"v_exp_f32 v25, v25\n"
"buffer_load_dwordx4 a[248:251], v149, s[52:55], s98 offen\n"
"s_waitcnt lgkmcnt(0)\n"
"s_cmp_lg_u32 s75, 0\n"
"s_cbranch_scc1 K1_ESB12\n"
"K1_ENB12:\n"
"MM v[40:43], a[160:163], v[160:163], v[80:83]\n"
"v_exp_f32 v26, v26\n"
"MM v[40:43], a[164:167], v[180:183], v[40:43]\n"
"ds_read_b64_tr_b16 a[192:193], v243 offset:32768\n"
"s_add_u32 s71, s94, 122880\n"
"MM v[40:43], a[168:171], v[200:203], v[40:43]\n"
"v_exp_f32 v27, v27\n"
"MM v[40:43], a[172:175], v[220:223], v[40:43]\n"
"ds_read_b64_tr_b16 a[194:195], v243 offset:36864\n"
"s_and_b32 s71, s71, 0x1ffff\n"
"MM v[44:47], a[176:179], v[160:163], v[80:83]\n"
"v_exp_f32 v28, v28\n"
"MM v[44:47], a[180:183], v[180:183], v[44:47]\n"
"ds_read_b64_tr_b16 a[196:197], v246 offset:32768\n"
"s_add_u32 m0, s70, 65536\n"
"MM v[44:47], a[184:187], v[200:203], v[44:47]\n"
"v_exp_f32 v29, v29\n"
"MM v[44:47], a[188:191], v[220:223], v[44:47]\n"
"ds_read_b64_tr_b16 a[198:199], v246 offset:36864\n"
"v_max_f32 v140, v40, v41\n"
"MM v[48:51], a[160:163], v[164:167], v[84:87]\n"
"buffer_load_dwordx4 v249, s[44:47], s71 offen nt lds\n"
"MM v[48:51], a[164:167], v[184:187], v[48:51]\n"
"v_exp_f32 v30, v30\n"
"MM v[48:51], a[168:171], v[204:207], v[48:51]\n"
"ds_read_b64_tr_b16 a[200:201], v243 offset:33024\n"
"v_max3_f32 v140, v42, v43, v140\n"
"MM v[48:51], a[172:175], v[224:227], v[48:51]\n"
"v_exp_f32 v31, v31\n"
"MM v[52:55], a[176:179], v[164:167], v[84:87]\n"
"ds_read_b64_tr_b16 a[202:203], v243 offset:37120\n"
"v_cvt_pk_bf16_f32 v108, v16, v17\n"
"MM v[52:55], a[180:183], v[184:187], v[52:55]\n"
"v_max3_f32 v140, v44, v45, v140\n"
"v_cvt_pk_bf16_f32 v109, v18, v19\n"
"MM v[52:55], a[184:187], v[204:207], v[52:55]\n"
"ds_read_b64_tr_b16 a[204:205], v246 offset:33024\n"
"v_cvt_pk_bf16_f32 v110, v20, v21\n"
"MM v[52:55], a[188:191], v[224:227], v[52:55]\n"
"v_max3_f32 v140, v46, v47, v140\n"
"v_cvt_pk_bf16_f32 v111, v22, v23\n"
"MM v[56:59], a[160:163], v[168:171], v[88:91]\n"
"ds_read_b64_tr_b16 a[206:207], v246 offset:37120\n"
"v_max3_f32 v140, v48, v49, v140\n"
"MM v[56:59], a[164:167], v[188:191], v[56:59]\n"
"v_exp_f32 v32, v32\n"
"MM v[56:59], a[168:171], v[208:211], v[56:59]\n"
"buffer_load_dwordx4 v252, s[44:47], s71 offen offset:1024 nt lds\n"
"MM v[56:59], a[172:175], v[228:231], v[56:59]\n"
"v_exp_f32 v33, v33\n"
"MM v[60:63], a[176:179], v[168:171], v[88:91]\n"
"ds_read_b64_tr_b16 a[208:209], v243 offset:33280\n"
"v_max3_f32 v140, v50, v51, v140\n"
"MM v[60:63], a[180:183], v[188:191], v[60:63]\n"
"v_exp_f32 v34, v34\n"
"MM v[60:63], a[184:187], v[208:211], v[60:63]\n"
"ds_read_b64_tr_b16 a[210:211], v243 offset:37376\n"
"v_max3_f32 v140, v52, v53, v140\n"
"MM v[60:63], a[188:191], v[228:231], v[60:63]\n"
"v_exp_f32 v35, v35\n"
"MM v[64:67], a[160:163], v[172:175], v[92:95]\n"
"ds_read_b64_tr_b16 a[212:213], v246 offset:33280\n"
"v_max3_f32 v140, v54, v55, v140\n"
"MM v[64:67], a[164:167], v[192:195], v[64:67]\n"
"v_exp_f32 v36, v36\n"
"MM v[64:67], a[168:171], v[212:215], v[64:67]\n"
"ds_read_b64_tr_b16 a[214:215], v246 offset:37376\n"
"v_max3_f32 v140, v56, v57, v140\n"
"MM v[64:67], a[172:175], v[232:235], v[64:67]\n"
"buffer_load_dwordx4 v251, s[44:47], s71 offen offset:2048 nt lds\n"
"MM v[68:71], a[176:179], v[172:175], v[92:95]\n"
"v_exp_f32 v37, v37\n"
"MM v[68:71], a[180:183], v[192:195], v[68:71]\n"
"ds_read_b64_tr_b16 a[216:217], v243 offset:33536\n"
"v_max3_f32 v140, v58, v59, v140\n"
"MM v[68:71], a[184:187], v[212:215], v[68:71]\n"
"v_exp_f32 v38, v38\n"
"MM v[68:71], a[188:191], v[232:235], v[68:71]\n"
"ds_read_b64_tr_b16 a[218:219], v243 offset:37632\n"
"v_max3_f32 v140, v60, v61, v140\n"
"ds_read_b64_tr_b16 a[220:221], v246 offset:33536\n"
"ds_read_b64_tr_b16 a[222:223], v246 offset:37632\n"
"MM v[72:75], a[160:163], v[176:179], v[96:99]\n"
"v_exp_f32 v39, v39\n"
"MM v[72:75], a[164:167], v[196:199], v[72:75]\n"
"v_max3_f32 v140, v62, v63, v140\n"
"v_cvt_pk_bf16_f32 v112, v24, v25\n"
"MM v[72:75], a[168:171], v[216:219], v[72:75]\n"
"v_cvt_pk_bf16_f32 v113, v26, v27\n"
"v_max3_f32 v140, v64, v65, v140\n"
"MM v[72:75], a[172:175], v[236:239], v[72:75]\n"
"buffer_load_dwordx4 v250, s[44:47], s71 offen offset:3072 nt lds\n"
"MM v[76:79], a[176:179], v[176:179], v[96:99]\n"
"v_cvt_pk_bf16_f32 v114, v28, v29\n"
"v_cvt_pk_bf16_f32 v115, v30, v31\n"
"MM v[76:79], a[180:183], v[196:199], v[76:79]\n"
"v_max3_f32 v140, v66, v67, v140\n"
"v_cvt_pk_bf16_f32 v116, v32, v33\n"
"MM v[76:79], a[184:187], v[216:219], v[76:79]\n"
"v_cvt_pk_bf16_f32 v117, v34, v35\n"
"v_max3_f32 v140, v68, v69, v140\n"
"MM v[76:79], a[188:191], v[236:239], v[76:79]\n"
"v_cvt_pk_bf16_f32 v118, v36, v37\n"
"v_max3_f32 v140, v70, v71, v140\n"
"v_cvt_pk_bf16_f32 v119, v38, v39\n"
"s_waitcnt vmcnt(5)\n"
"s_waitcnt lgkmcnt(0)\n"
"v_mov_b32 v248, 35\n"
"ds_write_b32 v242, v248 offset:0\n"
"MM a[224:227], a[244:247], v[100:103], a[224:227]\n"
"v_max3_f32 v140, v72, v73, v140\n"
"v_max3_f32 v140, v74, v75, v140\n"
"MM a[0:3], a[192:195], v[100:103], a[0:3]\n"
"MM a[4:7], a[196:199], v[100:103], a[4:7]\n"
"v_max3_f32 v140, v76, v77, v140\n"
"v_max3_f32 v140, v78, v79, v140\n"
"MM a[8:11], a[200:203], v[100:103], a[8:11]\n"
"v_cmp_lt_f32 vcc, s76, v140\n"
"MM a[12:15], a[204:207], v[100:103], a[12:15]\n"
"s_nop 0\n"
"s_cbranch_vccnz K1_ESA13\n"
"K1_ENA13:\n"
"MM a[16:19], a[208:211], v[100:103], a[16:19]\n"
"v_exp_f32 v40, v40\n"
"MM a[20:23], a[212:215], v[100:103], a[20:23]\n"
"ds_read_b32 v248, v245 offset:0\n"
"MM a[24:27], a[216:219], v[100:103], a[24:27]\n"
"v_exp_f32 v41, v41\n"
"MM a[28:31], a[220:223], v[100:103], a[28:31]\n"
"v_exp_f32 v42, v42\n"
"MM a[228:231], a[244:247], v[104:107], a[228:231]\n"
"v_exp_f32 v43, v43\n"
"MM a[32:35], a[192:195], v[104:107], a[32:35]\n"
"s_waitcnt lgkmcnt(0)\n"
"v_cmp_gt_u32 vcc, 35, v248\n"
"MM a[36:39], a[196:199], v[104:107], a[36:39]\n"
"v_exp_f32 v44, v44\n"
"MM a[40:43], a[200:203], v[104:107], a[40:43]\n"
"s_cbranch_vccnz K1_A35_S\n"
"K1_A35_R:\n"
"s_add_u32 s71, s94, 114688\n"
"MM a[44:47], a[204:207], v[104:107], a[44:47]\n"
"v_exp_f32 v45, v45\n"
"MM a[48:51], a[208:211], v[104:107], a[48:51]\n"
"v_exp_f32 v46, v46\n"
"MM a[52:55], a[212:215], v[104:107], a[52:55]\n"
"v_exp_f32 v47, v47\n"
"MM a[56:59], a[216:219], v[104:107], a[56:59]\n"
"v_exp_f32 v48, v48\n"
"MM a[60:63], a[220:223], v[104:107], a[60:63]\n"
"s_and_b32 s71, s71, 0x1ffff\n"
"ds_read_b128 a[160:163], v240 offset:49152\n"
"MM a[232:235], a[244:247], v[108:111], a[232:235]\n"
"v_exp_f32 v49, v49\n"
"MM a[64:67], a[192:195], v[108:111], a[64:67]\n"
"s_add_u32 m0, s70, 81920\n"
"ds_read_b128 a[164:167], v240 offset:49408\n"
"MM a[68:71], a[196:199], v[108:111], a[68:71]\n"
"v_exp_f32 v50, v50\n"
"MM a[72:75], a[200:203], v[108:111], a[72:75]\n"
"v_exp_f32 v51, v51\n"
"MM a[76:79], a[204:207], v[108:111], a[76:79]\n"
"buffer_load_dwordx4 v249, s[48:51], s71 offen nt lds\n"
"MM a[80:83], a[208:211], v[108:111], a[80:83]\n"
"v_exp_f32 v52, v52\n"
"MM a[84:87], a[212:215], v[108:111], a[84:87]\n"
"v_exp_f32 v53, v53\n"
"MM a[88:91], a[216:219], v[108:111], a[88:91]\n"
"v_exp_f32 v54, v54\n"
"MM a[92:95], a[220:223], v[108:111], a[92:95]\n"
"ds_read_b128 a[168:171], v240 offset:49664\n"
"ds_read_b128 a[172:175], v240 offset:49920\n"
"MM a[236:239], a[244:247], v[112:115], a[236:239]\n"
"v_exp_f32 v55, v55\n"
"MM a[96:99], a[192:195], v[112:115], a[96:99]\n"
"v_cvt_pk_bf16_f32 v120, v40, v41\n"
"v_cvt_pk_bf16_f32 v121, v42, v43\n"
"MM a[100:103], a[196:199], v[112:115], a[100:103]\n"
"v_cvt_pk_bf16_f32 v122, v44, v45\n"
"v_cvt_pk_bf16_f32 v123, v46, v47\n"
"MM a[104:107], a[200:203], v[112:115], a[104:107]\n"
"buffer_load_dwordx4 v251, s[48:51], s71 offen offset:1024 nt lds\n"
"MM a[108:111], a[204:207], v[112:115], a[108:111]\n"
"v_exp_f32 v56, v56\n"
"MM a[112:115], a[208:211], v[112:115], a[112:115]\n"
"v_exp_f32 v57, v57\n"
"ds_read_b128 a[176:179], v240 offset:53248\n"
"MM a[116:119], a[212:215], v[112:115], a[116:119]\n"
"v_exp_f32 v58, v58\n"
"MM a[120:123], a[216:219], v[112:115], a[120:123]\n"
"v_exp_f32 v59, v59\n"
"ds_read_b128 a[180:183], v240 offset:53504\n"
"ds_read_b128 a[184:187], v240 offset:53760\n"
"ds_read_b128 a[188:191], v240 offset:54016\n"
"MM a[124:127], a[220:223], v[112:115], a[124:127]\n"
"buffer_load_dwordx4 v249, s[48:51], s71 offen offset:2048 nt lds\n"
"MM a[240:243], a[244:247], v[116:119], a[240:243]\n"
"v_exp_f32 v60, v60\n"
"MM a[128:131], a[192:195], v[116:119], a[128:131]\n"
"v_exp_f32 v61, v61\n"
"MM a[132:135], a[196:199], v[116:119], a[132:135]\n"
"v_exp_f32 v62, v62\n"
"MM a[136:139], a[200:203], v[116:119], a[136:139]\n"
"v_exp_f32 v63, v63\n"
"MM a[140:143], a[204:207], v[116:119], a[140:143]\n"
"v_cvt_pk_bf16_f32 v124, v48, v49\n"
"v_cvt_pk_bf16_f32 v125, v50, v51\n"
"MM a[144:147], a[208:211], v[116:119], a[144:147]\n"
"buffer_load_dwordx4 v251, s[48:51], s71 offen offset:3072 nt lds\n"
"MM a[148:151], a[212:215], v[116:119], a[148:151]\n"
"v_cvt_pk_bf16_f32 v126, v52, v53\n"
"v_cvt_pk_bf16_f32 v127, v54, v55\n"
"MM a[152:155], a[216:219], v[116:119], a[152:155]\n"
"v_exp_f32 v64, v64\n"
"MM a[156:159], a[220:223], v[116:119], a[156:159]\n"
"v_exp_f32 v65, v65\n"
"s_waitcnt lgkmcnt(0)\n"
"s_cmp_lg_u32 s75, 0\n"
"s_cbranch_scc1 K1_ESB13\n"
"K1_ENB13:\n"
"MM v[0:3], a[160:163], v[160:163], v[80:83]\n"
"v_exp_f32 v66, v66\n"
"MM v[0:3], a[164:167], v[180:183], v[0:3]\n"
"ds_read_b64_tr_b16 a[192:193], v244 offset:49152\n"
"s_add_u32 s71, s94, 122880\n"
"MM v[0:3], a[168:171], v[200:203], v[0:3]\n"
"v_exp_f32 v67, v67\n"
"MM v[0:3], a[172:175], v[220:223], v[0:3]\n"
"ds_read_b64_tr_b16 a[194:195], v244 offset:53248\n"
"s_and_b32 s71, s71, 0x1ffff\n"
"MM v[4:7], a[176:179], v[160:163], v[80:83]\n"
"v_exp_f32 v68, v68\n"
"MM v[4:7], a[180:183], v[180:183], v[4:7]\n"
"ds_read_b64_tr_b16 a[196:197], v247 offset:49152\n"
"s_add_u32 m0, s70, 98304\n"
"MM v[4:7], a[184:187], v[200:203], v[4:7]\n"
"v_exp_f32 v69, v69\n"
"MM v[4:7], a[188:191], v[220:223], v[4:7]\n"
"ds_read_b64_tr_b16 a[198:199], v247 offset:53248\n"
"v_max_f32 v140, v0, v1\n"
"MM v[8:11], a[160:163], v[164:167], v[84:87]\n"
"buffer_load_dwordx4 v249, s[48:51], s71 offen nt lds\n"
"MM v[8:11], a[164:167], v[184:187], v[8:11]\n"
"v_exp_f32 v70, v70\n"
"MM v[8:11], a[168:171], v[204:207], v[8:11]\n"
"ds_read_b64_tr_b16 a[200:201], v244 offset:49408\n"
"v_max3_f32 v140, v2, v3, v140\n"
"MM v[8:11], a[172:175], v[224:227], v[8:11]\n"
"v_exp_f32 v71, v71\n"
"MM v[12:15], a[176:179], v[164:167], v[84:87]\n"
"ds_read_b64_tr_b16 a[202:203], v244 offset:53504\n"
"v_cvt_pk_bf16_f32 v128, v56, v57\n"
"MM v[12:15], a[180:183], v[184:187], v[12:15]\n"
"v_max3_f32 v140, v4, v5, v140\n"
"v_cvt_pk_bf16_f32 v129, v58, v59\n"
"MM v[12:15], a[184:187], v[204:207], v[12:15]\n"
"ds_read_b64_tr_b16 a[204:205], v247 offset:49408\n"
"v_cvt_pk_bf16_f32 v130, v60, v61\n"
"MM v[12:15], a[188:191], v[224:227], v[12:15]\n"
"v_max3_f32 v140, v6, v7, v140\n"
"v_cvt_pk_bf16_f32 v131, v62, v63\n"
"MM v[16:19], a[160:163], v[168:171], v[88:91]\n"
"ds_read_b64_tr_b16 a[206:207], v247 offset:53504\n"
"v_max3_f32 v140, v8, v9, v140\n"
"MM v[16:19], a[164:167], v[188:191], v[16:19]\n"
"v_exp_f32 v72, v72\n"
"MM v[16:19], a[168:171], v[208:211], v[16:19]\n"
"buffer_load_dwordx4 v251, s[48:51], s71 offen offset:1024 nt lds\n"
"MM v[16:19], a[172:175], v[228:231], v[16:19]\n"
"v_exp_f32 v73, v73\n"
"MM v[20:23], a[176:179], v[168:171], v[88:91]\n"
"ds_read_b64_tr_b16 a[208:209], v244 offset:49664\n"
"v_max3_f32 v140, v10, v11, v140\n"
"MM v[20:23], a[180:183], v[188:191], v[20:23]\n"
"v_exp_f32 v74, v74\n"
"MM v[20:23], a[184:187], v[208:211], v[20:23]\n"
"ds_read_b64_tr_b16 a[210:211], v244 offset:53760\n"
"v_max3_f32 v140, v12, v13, v140\n"
"MM v[20:23], a[188:191], v[228:231], v[20:23]\n"
"v_exp_f32 v75, v75\n"
"MM v[24:27], a[160:163], v[172:175], v[92:95]\n"
"ds_read_b64_tr_b16 a[212:213], v247 offset:49664\n"
"v_max3_f32 v140, v14, v15, v140\n"
"MM v[24:27], a[164:167], v[192:195], v[24:27]\n"
"v_exp_f32 v76, v76\n"
"MM v[24:27], a[168:171], v[212:215], v[24:27]\n"
"ds_read_b64_tr_b16 a[214:215], v247 offset:53760\n"
"v_max3_f32 v140, v16, v17, v140\n"
"MM v[24:27], a[172:175], v[232:235], v[24:27]\n"
"buffer_load_dwordx4 v249, s[48:51], s71 offen offset:2048 nt lds\n"
"MM v[28:31], a[176:179], v[172:175], v[92:95]\n"
"v_exp_f32 v77, v77\n"
"MM v[28:31], a[180:183], v[192:195], v[28:31]\n"
"ds_read_b64_tr_b16 a[216:217], v244 offset:49920\n"
"v_max3_f32 v140, v18, v19, v140\n"
"MM v[28:31], a[184:187], v[212:215], v[28:31]\n"
"v_exp_f32 v78, v78\n"
"MM v[28:31], a[188:191], v[232:235], v[28:31]\n"
"ds_read_b64_tr_b16 a[218:219], v244 offset:54016\n"
"v_max3_f32 v140, v20, v21, v140\n"
"ds_read_b64_tr_b16 a[220:221], v247 offset:49920\n"
"ds_read_b64_tr_b16 a[222:223], v247 offset:54016\n"
"MM v[32:35], a[160:163], v[176:179], v[96:99]\n"
"v_exp_f32 v79, v79\n"
"MM v[32:35], a[164:167], v[196:199], v[32:35]\n"
"v_max3_f32 v140, v22, v23, v140\n"
"v_cvt_pk_bf16_f32 v132, v64, v65\n"
"MM v[32:35], a[168:171], v[216:219], v[32:35]\n"
"v_cvt_pk_bf16_f32 v133, v66, v67\n"
"v_max3_f32 v140, v24, v25, v140\n"
"MM v[32:35], a[172:175], v[236:239], v[32:35]\n"
"buffer_load_dwordx4 v251, s[48:51], s71 offen offset:3072 nt lds\n"
"MM v[36:39], a[176:179], v[176:179], v[96:99]\n"
"v_cvt_pk_bf16_f32 v134, v68, v69\n"
"v_cvt_pk_bf16_f32 v135, v70, v71\n"
"MM v[36:39], a[180:183], v[196:199], v[36:39]\n"
"v_max3_f32 v140, v26, v27, v140\n"
"v_cvt_pk_bf16_f32 v136, v72, v73\n"
"MM v[36:39], a[184:187], v[216:219], v[36:39]\n"
"v_cvt_pk_bf16_f32 v137, v74, v75\n"
"v_max3_f32 v140, v28, v29, v140\n"
"MM v[36:39], a[188:191], v[236:239], v[36:39]\n"
"v_cvt_pk_bf16_f32 v138, v76, v77\n"
"v_max3_f32 v140, v30, v31, v140\n"
"v_cvt_pk_bf16_f32 v139, v78, v79\n"
"s_waitcnt vmcnt(4)\n"
"s_waitcnt lgkmcnt(0)\n"
"v_mov_b32 v248, 36\n"
"ds_write_b32 v242, v248 offset:0\n"
"MM a[224:227], a[244:247], v[120:123], a[224:227]\n"
"v_max3_f32 v140, v32, v33, v140\n"
"v_max3_f32 v140, v34, v35, v140\n"
"MM a[0:3], a[192:195], v[120:123], a[0:3]\n"
"MM a[4:7], a[196:199], v[120:123], a[4:7]\n"
"v_max3_f32 v140, v36, v37, v140\n"
"v_max3_f32 v140, v38, v39, v140\n"
"MM a[8:11], a[200:203], v[120:123], a[8:11]\n"
"v_cmp_lt_f32 vcc, s76, v140\n"
"MM a[12:15], a[204:207], v[120:123], a[12:15]\n"
"s_nop 0\n"
"s_cbranch_vccnz K1_ESA14\n"
"K1_ENA14:\n"
"MM a[16:19], a[208:211], v[120:123], a[16:19]\n"
"v_exp_f32 v0, v0\n"
"MM a[20:23], a[212:215], v[120:123], a[20:23]\n"
"ds_read_b32 v248, v245 offset:0\n"
"MM a[24:27], a[216:219], v[120:123], a[24:27]\n"
"v_exp_f32 v1, v1\n"
"MM a[28:31], a[220:223], v[120:123], a[28:31]\n"
"v_exp_f32 v2, v2\n"
"MM a[228:231], a[244:247], v[124:127], a[228:231]\n"
"v_exp_f32 v3, v3\n"
"MM a[32:35], a[192:195], v[124:127], a[32:35]\n"
"s_waitcnt lgkmcnt(0)\n"
"v_cmp_gt_u32 vcc, 36, v248\n"
"MM a[36:39], a[196:199], v[124:127], a[36:39]\n"
"v_exp_f32 v4, v4\n"
"MM a[40:43], a[200:203], v[124:127], a[40:43]\n"
"s_cbranch_vccnz K1_A36_S\n"
"K1_A36_R:\n"
"ds_read_b128 a[160:163], v241 offset:0\n"
"MM a[44:47], a[204:207], v[124:127], a[44:47]\n"
"v_exp_f32 v5, v5\n"
"MM a[48:51], a[208:211], v[124:127], a[48:51]\n"
"v_exp_f32 v6, v6\n"
"MM a[52:55], a[212:215], v[124:127], a[52:55]\n"
"v_exp_f32 v7, v7\n"
"MM a[56:59], a[216:219], v[124:127], a[56:59]\n"
"v_exp_f32 v8, v8\n"
"MM a[60:63], a[220:223], v[124:127], a[60:63]\n"
"v_exp_f32 v9, v9\n"
"MM a[232:235], a[244:247], v[128:131], a[232:235]\n"
"v_exp_f32 v10, v10\n"
"MM a[64:67], a[192:195], v[128:131], a[64:67]\n"
"v_exp_f32 v11, v11\n"
"MM a[68:71], a[196:199], v[128:131], a[68:71]\n"
"ds_read_b128 a[164:167], v241 offset:256\n"
"ds_read_b128 a[168:171], v241 offset:512\n"
"MM a[72:75], a[200:203], v[128:131], a[72:75]\n"
"v_exp_f32 v12, v12\n"
"MM a[76:79], a[204:207], v[128:131], a[76:79]\n"
"v_exp_f32 v13, v13\n"
"MM a[80:83], a[208:211], v[128:131], a[80:83]\n"
"v_exp_f32 v14, v14\n"
"MM a[84:87], a[212:215], v[128:131], a[84:87]\n"
"v_exp_f32 v15, v15\n"
"MM a[88:91], a[216:219], v[128:131], a[88:91]\n"
"ds_read_b128 a[172:175], v241 offset:768\n"
"v_cvt_pk_bf16_f32 v100, v0, v1\n"
"MM a[92:95], a[220:223], v[128:131], a[92:95]\n"
"v_cvt_pk_bf16_f32 v101, v2, v3\n"
"ds_read_b128 a[176:179], v241 offset:4096\n"
"MM a[236:239], a[244:247], v[132:135], a[236:239]\n"
"v_cvt_pk_bf16_f32 v102, v4, v5\n"
"v_cvt_pk_bf16_f32 v103, v6, v7\n"
"MM a[96:99], a[192:195], v[132:135], a[96:99]\n"
"v_exp_f32 v16, v16\n"
"MM a[100:103], a[196:199], v[132:135], a[100:103]\n"
"v_exp_f32 v17, v17\n"
"MM a[104:107], a[200:203], v[132:135], a[104:107]\n"
"ds_read_b128 a[180:183], v241 offset:4352\n"
"ds_read_b128 a[184:187], v241 offset:4608\n"
"MM a[108:111], a[204:207], v[132:135], a[108:111]\n"
"v_exp_f32 v18, v18\n"
"MM a[112:115], a[208:211], v[132:135], a[112:115]\n"
"v_exp_f32 v19, v19\n"
"MM a[116:119], a[212:215], v[132:135], a[116:119]\n"
"v_exp_f32 v20, v20\n"
"MM a[120:123], a[216:219], v[132:135], a[120:123]\n"
"v_exp_f32 v21, v21\n"
"ds_read_b128 a[188:191], v241 offset:4864\n"
"MM a[124:127], a[220:223], v[132:135], a[124:127]\n"
"v_exp_f32 v22, v22\n"
"MM a[240:243], a[244:247], v[136:139], a[240:243]\n"
"v_exp_f32 v23, v23\n"
"MM a[128:131], a[192:195], v[136:139], a[128:131]\n"
"v_cvt_pk_bf16_f32 v104, v8, v9\n"
"v_cvt_pk_bf16_f32 v105, v10, v11\n"
"MM a[132:135], a[196:199], v[136:139], a[132:135]\n"
"v_cvt_pk_bf16_f32 v106, v12, v13\n"
"v_cvt_pk_bf16_f32 v107, v14, v15\n"
"MM a[136:139], a[200:203], v[136:139], a[136:139]\n"
"v_exp_f32 v24, v24\n"
"MM a[140:143], a[204:207], v[136:139], a[140:143]\n"
"v_exp_f32 v25, v25\n"
"MM a[144:147], a[208:211], v[136:139], a[144:147]\n"
"v_exp_f32 v26, v26\n"
"MM a[148:151], a[212:215], v[136:139], a[148:151]\n"
"v_exp_f32 v27, v27\n"
"MM a[152:155], a[216:219], v[136:139], a[152:155]\n"
"MM a[156:159], a[220:223], v[136:139], a[156:159]\n"
"s_waitcnt lgkmcnt(0)\n"
"s_cmp_lg_u32 s75, 0\n"
"s_cbranch_scc1 K1_ESB14\n"
"K1_ENB14:\n"
"MM v[40:43], a[160:163], v[160:163], v[80:83]\n"
"v_exp_f32 v28, v28\n"
"MM v[40:43], a[164:167], v[180:183], v[40:43]\n"
"ds_read_b64_tr_b16 a[192:193], v244 offset:16384\n"
"ds_read_b64_tr_b16 a[194:195], v244 offset:20480\n"
"MM v[40:43], a[168:171], v[200:203], v[40:43]\n"
"v_exp_f32 v29, v29\n"
"MM v[40:43], a[172:175], v[220:223], v[40:43]\n"
"ds_read_b64_tr_b16 a[196:197], v247 offset:16384\n"
"ds_read_b64_tr_b16 a[198:199], v247 offset:20480\n"
"MM v[44:47], a[176:179], v[160:163], v[80:83]\n"
"v_exp_f32 v30, v30\n"
"MM v[44:47], a[180:183], v[180:183], v[44:47]\n"
"v_exp_f32 v31, v31\n"
"MM v[44:47], a[184:187], v[200:203], v[44:47]\n"
"ds_read_b64_tr_b16 a[200:201], v244 offset:16640\n"
"ds_read_b64_tr_b16 a[202:203], v244 offset:20736\n"
"MM v[44:47], a[188:191], v[220:223], v[44:47]\n"
"v_max_f32 v140, v40, v41\n"
"v_max3_f32 v140, v42, v43, v140\n"
"MM v[48:51], a[160:163], v[164:167], v[84:87]\n"
"v_cvt_pk_bf16_f32 v108, v16, v17\n"
"v_cvt_pk_bf16_f32 v109, v18, v19\n"
"MM v[48:51], a[164:167], v[184:187], v[48:51]\n"
"v_cvt_pk_bf16_f32 v110, v20, v21\n"
"ds_read_b64_tr_b16 a[204:205], v247 offset:16640\n"
"MM v[48:51], a[168:171], v[204:207], v[48:51]\n"
"v_cvt_pk_bf16_f32 v111, v22, v23\n"
"ds_read_b64_tr_b16 a[206:207], v247 offset:20736\n"
"MM v[48:51], a[172:175], v[224:227], v[48:51]\n"
"v_max3_f32 v140, v44, v45, v140\n"
"v_max3_f32 v140, v46, v47, v140\n"
"MM v[52:55], a[176:179], v[164:167], v[84:87]\n"
"v_exp_f32 v32, v32\n"
"MM v[52:55], a[180:183], v[184:187], v[52:55]\n"
"ds_read_b64_tr_b16 a[208:209], v244 offset:16896\n"
"ds_read_b64_tr_b16 a[210:211], v244 offset:20992\n"
"MM v[52:55], a[184:187], v[204:207], v[52:55]\n"
"v_exp_f32 v33, v33\n"
"MM v[52:55], a[188:191], v[224:227], v[52:55]\n"
"v_max3_f32 v140, v48, v49, v140\n"
"v_max3_f32 v140, v50, v51, v140\n"
"MM v[56:59], a[160:163], v[168:171], v[88:91]\n"
"v_exp_f32 v34, v34\n"
"MM v[56:59], a[164:167], v[188:191], v[56:59]\n"
"ds_read_b64_tr_b16 a[212:213], v247 offset:16896\n"
"ds_read_b64_tr_b16 a[214:215], v247 offset:20992\n"
"MM v[56:59], a[168:171], v[208:211], v[56:59]\n"
"v_exp_f32 v35, v35\n"
"MM v[56:59], a[172:175], v[228:231], v[56:59]\n"
"v_max3_f32 v140, v52, v53, v140\n"
"v_max3_f32 v140, v54, v55, v140\n"
"MM v[60:63], a[176:179], v[168:171], v[88:91]\n"
"v_exp_f32 v36, v36\n"
"MM v[60:63], a[180:183], v[188:191], v[60:63]\n"
"ds_read_b64_tr_b16 a[216:217], v244 offset:17152\n"
"ds_read_b64_tr_b16 a[218:219], v244 offset:21248\n"
"MM v[60:63], a[184:187], v[208:211], v[60:63]\n"
"v_exp_f32 v37, v37\n"
"MM v[60:63], a[188:191], v[228:231], v[60:63]\n"
"v_max3_f32 v140, v56, v57, v140\n"
"v_max3_f32 v140, v58, v59, v140\n"
"MM v[64:67], a[160:163], v[172:175], v[92:95]\n"
"v_exp_f32 v38, v38\n"
"MM v[64:67], a[164:167], v[192:195], v[64:67]\n"
"ds_read_b64_tr_b16 a[220:221], v247 offset:17152\n"
"ds_read_b64_tr_b16 a[222:223], v247 offset:21248\n"
"MM v[64:67], a[168:171], v[212:215], v[64:67]\n"
"v_exp_f32 v39, v39\n"
"MM v[64:67], a[172:175], v[232:235], v[64:67]\n"
"v_max3_f32 v140, v60, v61, v140\n"
"v_max3_f32 v140, v62, v63, v140\n"
"MM v[68:71], a[176:179], v[172:175], v[92:95]\n"
"v_cvt_pk_bf16_f32 v112, v24, v25\n"
"v_cvt_pk_bf16_f32 v113, v26, v27\n"
"MM v[68:71], a[180:183], v[192:195], v[68:71]\n"
"v_cvt_pk_bf16_f32 v114, v28, v29\n"
"MM v[68:71], a[184:187], v[212:215], v[68:71]\n"
"MM v[68:71], a[188:191], v[232:235], v[68:71]\n"
"v_max3_f32 v140, v64, v65, v140\n"
"v_max3_f32 v140, v66, v67, v140\n"
"MM v[72:75], a[160:163], v[176:179], v[96:99]\n"
"v_cvt_pk_bf16_f32 v115, v30, v31\n"
"v_cvt_pk_bf16_f32 v116, v32, v33\n"
"MM v[72:75], a[164:167], v[196:199], v[72:75]\n"
"v_cvt_pk_bf16_f32 v117, v34, v35\n"
"MM v[72:75], a[168:171], v[216:219], v[72:75]\n"
"v_cvt_pk_bf16_f32 v118, v36, v37\n"
"MM v[72:75], a[172:175], v[236:239], v[72:75]\n"
"v_max3_f32 v140, v68, v69, v140\n"
"v_max3_f32 v140, v70, v71, v140\n"
"MM v[76:79], a[176:179], v[176:179], v[96:99]\n"
"v_cvt_pk_bf16_f32 v119, v38, v39\n"
"MM v[76:79], a[180:183], v[196:199], v[76:79]\n"
"MM v[76:79], a[184:187], v[216:219], v[76:79]\n"
"MM v[76:79], a[188:191], v[236:239], v[76:79]\n"
"s_waitcnt vmcnt(0)\n"
"s_waitcnt lgkmcnt(0)\n"
"v_mov_b32 v248, 37\n"
"ds_write_b32 v242, v248 offset:0\n"
"MM a[224:227], a[244:247], v[100:103], a[224:227]\n"
"v_max3_f32 v140, v72, v73, v140\n"
"v_max3_f32 v140, v74, v75, v140\n"
"MM a[0:3], a[192:195], v[100:103], a[0:3]\n"
"MM a[4:7], a[196:199], v[100:103], a[4:7]\n"
"v_max3_f32 v140, v76, v77, v140\n"
"v_max3_f32 v140, v78, v79, v140\n"
"MM a[8:11], a[200:203], v[100:103], a[8:11]\n"
"v_cmp_lt_f32 vcc, s76, v140\n"
"MM a[12:15], a[204:207], v[100:103], a[12:15]\n"
"s_nop 0\n"
"s_cbranch_vccnz K1_ESA15\n"
"K1_ENA15:\n"
"MM a[16:19], a[208:211], v[100:103], a[16:19]\n"
"v_exp_f32 v40, v40\n"
"MM a[20:23], a[212:215], v[100:103], a[20:23]\n"
"ds_read_b32 v248, v245 offset:0\n"
"MM a[24:27], a[216:219], v[100:103], a[24:27]\n"
"v_exp_f32 v41, v41\n"
"MM a[28:31], a[220:223], v[100:103], a[28:31]\n"
"v_exp_f32 v42, v42\n"
"MM a[228:231], a[244:247], v[104:107], a[228:231]\n"
"v_exp_f32 v43, v43\n"
"MM a[32:35], a[192:195], v[104:107], a[32:35]\n"
"s_waitcnt lgkmcnt(0)\n"
"v_cmp_gt_u32 vcc, 37, v248\n"
"MM a[36:39], a[196:199], v[104:107], a[36:39]\n"
"v_exp_f32 v44, v44\n"
"MM a[40:43], a[200:203], v[104:107], a[40:43]\n"
"s_cbranch_vccnz K1_A37_S\n"
"K1_A37_R:\n"
"s_mul_i32 s68, s60, 10240\n"
"MM a[44:47], a[204:207], v[104:107], a[44:47]\n"
"v_lshl_add_u32 v251, v253, 4, s68\n"
"v_add_u32 v252, 4096, v251\n"
"MM a[48:51], a[208:211], v[104:107], a[48:51]\n"
"s_mul_i32 s94, s64, 327680\n"
"s_add_u32 s94, s94, 10485760\n"
"MM a[52:55], a[212:215], v[104:107], a[52:55]\n"
"s_xor_b32 s95, s64, 1\n"
"s_mul_i32 s95, s95, 327680\n"
"MM a[56:59], a[216:219], v[104:107], a[56:59]\n"
"s_add_u32 s95, s95, 10485760\n"
"s_add_u32 s91, s94, 0\n"
"MM a[60:63], a[220:223], v[104:107], a[60:63]\n"
"v_exp_f32 v45, v45\n"
"MM a[232:235], a[244:247], v[108:111], a[232:235]\n"
"buffer_load_dwordx4 v[160:163], v251, s[56:59], s91 offen sc1\n"
"MM a[64:67], a[192:195], v[108:111], a[64:67]\n"
"v_exp_f32 v46, v46\n"
"MM a[68:71], a[196:199], v[108:111], a[68:71]\n"
"buffer_load_dwordx4 v[164:167], v251, s[56:59], s91 offen offset:1024 sc1\n"
"MM a[72:75], a[200:203], v[108:111], a[72:75]\n"
"v_exp_f32 v47, v47\n"
"MM a[76:79], a[204:207], v[108:111], a[76:79]\n"
"buffer_load_dwordx4 v[168:171], v251, s[56:59], s91 offen offset:2048 sc1\n"
"MM a[80:83], a[208:211], v[108:111], a[80:83]\n"
"v_exp_f32 v48, v48\n"
"MM a[84:87], a[212:215], v[108:111], a[84:87]\n"
"buffer_load_dwordx4 v[172:175], v251, s[56:59], s91 offen offset:3072 sc1\n"
"MM a[88:91], a[216:219], v[108:111], a[88:91]\n"
"buffer_load_dwordx4 v[176:179], v252, s[56:59], s91 offen sc1\n"
"v_exp_f32 v49, v49\n"
"MM a[92:95], a[220:223], v[108:111], a[92:95]\n"
"v_exp_f32 v50, v50\n"
"s_add_u32 s91, s94, 40960\n"
"MM a[236:239], a[244:247], v[112:115], a[236:239]\n"
"buffer_load_dwordx4 v[180:183], v251, s[56:59], s91 offen sc1\n"
"MM a[96:99], a[192:195], v[112:115], a[96:99]\n"
"v_exp_f32 v51, v51\n"
"MM a[100:103], a[196:199], v[112:115], a[100:103]\n"
"buffer_load_dwordx4 v[184:187], v251, s[56:59], s91 offen offset:1024 sc1\n"
"MM a[104:107], a[200:203], v[112:115], a[104:107]\n"
"v_exp_f32 v52, v52\n"
"MM a[108:111], a[204:207], v[112:115], a[108:111]\n"
"buffer_load_dwordx4 v[188:191], v251, s[56:59], s91 offen offset:2048 sc1\n"
"MM a[112:115], a[208:211], v[112:115], a[112:115]\n"
"v_exp_f32 v53, v53\n"
"MM a[116:119], a[212:215], v[112:115], a[116:119]\n"
"buffer_load_dwordx4 v[192:195], v251, s[56:59], s91 offen offset:3072 sc1\n"
"MM a[120:123], a[216:219], v[112:115], a[120:123]\n"
"v_exp_f32 v54, v54\n"
"MM a[124:127], a[220:223], v[112:115], a[124:127]\n"
"buffer_load_dwordx4 v[196:199], v252, s[56:59], s91 offen sc1\n"
"v_exp_f32 v55, v55\n"
"MM a[240:243], a[244:247], v[116:119], a[240:243]\n"
"s_add_u32 s91, s94, 5120\n"
"v_cvt_pk_bf16_f32 v120, v40, v41\n"
"MM a[128:131], a[192:195], v[116:119], a[128:131]\n"
"buffer_load_dwordx4 v[200:203], v251, s[56:59], s91 offen sc1\n"
"v_cvt_pk_bf16_f32 v121, v42, v43\n"
"MM a[132:135], a[196:199], v[116:119], a[132:135]\n"
"v_cvt_pk_bf16_f32 v122, v44, v45\n"
"v_cvt_pk_bf16_f32 v123, v46, v47\n"
"MM a[136:139], a[200:203], v[116:119], a[136:139]\n"
"buffer_load_dwordx4 v[204:207], v251, s[56:59], s91 offen offset:1024 sc1\n"
"MM a[140:143], a[204:207], v[116:119], a[140:143]\n"
"v_exp_f32 v56, v56\n"
"MM a[144:147], a[208:211], v[116:119], a[144:147]\n"
"buffer_load_dwordx4 v[208:211], v251, s[56:59], s91 offen offset:2048 sc1\n"
"MM a[148:151], a[212:215], v[116:119], a[148:151]\n"
"v_exp_f32 v57, v57\n"
"MM a[152:155], a[216:219], v[116:119], a[152:155]\n"
"buffer_load_dwordx4 v[212:215], v251, s[56:59], s91 offen offset:3072 sc1\n"
"MM a[156:159], a[220:223], v[116:119], a[156:159]\n"
"v_exp_f32 v58, v58\n"
"v_exp_f32 v59, v59\n"
"buffer_load_dwordx4 v[216:219], v252, s[56:59], s91 offen sc1\n"
"s_cmp_lg_u32 s75, 0\n"
"s_cbranch_scc1 K1_ESB15\n"
"K1_ENB15:\n"
"ds_read_b64_tr_b16 a[192:193], v244 offset:32768\n"
"ds_read_b64_tr_b16 a[194:195], v244 offset:36864\n"
"ds_read_b64_tr_b16 a[196:197], v247 offset:32768\n"
"ds_read_b64_tr_b16 a[198:199], v247 offset:36864\n"
"ds_read_b64_tr_b16 a[200:201], v244 offset:33024\n"
"ds_read_b64_tr_b16 a[202:203], v244 offset:37120\n"
"ds_read_b64_tr_b16 a[204:205], v247 offset:33024\n"
"ds_read_b64_tr_b16 a[206:207], v247 offset:37120\n"
"ds_read_b64_tr_b16 a[208:209], v244 offset:33280\n"
"ds_read_b64_tr_b16 a[210:211], v244 offset:37376\n"
"ds_read_b64_tr_b16 a[212:213], v247 offset:33280\n"
"ds_read_b64_tr_b16 a[214:215], v247 offset:37376\n"
"ds_read_b64_tr_b16 a[216:217], v244 offset:33536\n"
"ds_read_b64_tr_b16 a[218:219], v244 offset:37632\n"
"ds_read_b64_tr_b16 a[220:221], v247 offset:33536\n"
"ds_read_b64_tr_b16 a[222:223], v247 offset:37632\n"
"v_exp_f32 v60, v60\n"
"v_exp_f32 v61, v61\n"
"v_exp_f32 v62, v62\n"
"v_exp_f32 v63, v63\n"
"v_cvt_pk_bf16_f32 v124, v48, v49\n"
"v_cvt_pk_bf16_f32 v125, v50, v51\n"
"v_cvt_pk_bf16_f32 v126, v52, v53\n"
"v_cvt_pk_bf16_f32 v127, v54, v55\n"
"v_exp_f32 v64, v64\n"
"v_exp_f32 v65, v65\n"
"v_exp_f32 v66, v66\n"
"v_exp_f32 v67, v67\n"
"v_exp_f32 v68, v68\n"
"v_exp_f32 v69, v69\n"
"v_exp_f32 v70, v70\n"
"v_exp_f32 v71, v71\n"
"v_cvt_pk_bf16_f32 v128, v56, v57\n"
"v_cvt_pk_bf16_f32 v129, v58, v59\n"
"v_cvt_pk_bf16_f32 v130, v60, v61\n"
"v_cvt_pk_bf16_f32 v131, v62, v63\n"
"v_exp_f32 v72, v72\n"
"v_exp_f32 v73, v73\n"
"v_exp_f32 v74, v74\n"
"v_exp_f32 v75, v75\n"
"v_exp_f32 v76, v76\n"
"v_exp_f32 v77, v77\n"
"v_exp_f32 v78, v78\n"
"v_exp_f32 v79, v79\n"
"v_cvt_pk_bf16_f32 v132, v64, v65\n"
"v_cvt_pk_bf16_f32 v133, v66, v67\n"
"v_cvt_pk_bf16_f32 v134, v68, v69\n"
"v_cvt_pk_bf16_f32 v135, v70, v71\n"
"v_cvt_pk_bf16_f32 v136, v72, v73\n"
"v_cvt_pk_bf16_f32 v137, v74, v75\n"
"v_cvt_pk_bf16_f32 v138, v76, v77\n"
"v_cvt_pk_bf16_f32 v139, v78, v79\n"
"s_mul_i32 s68, s61, 20480\n"
"s_add_u32 s69, s68, s77\n"
"s_add_u32 s69, s69, 32768\n"
"v_lshl_add_u32 v248, v253, 4, s69\n"
"v_lshlrev_b32 v249, 4, v253\n"
"v_add_u32 v250, 4096, v249\n"
"s_mul_i32 s40, s65, 40960\n"
"s_add_u32 s40, s40, s68\n"
"s_add_u32 s41, s40, 5120\n"
"s_add_u32 s42, s40, 10240\n"
"s_add_u32 s43, s40, 15360\n"
"s_and_b32 s69, s64, 1\n"
"s_cmp_eq_u32 s69, s62\n"
"s_cbranch_scc1 K1_EPI_L\n"
"s_nop 1\n"
"MM a[224:227], a[244:247], v[120:123], a[224:227]\n"
"s_waitcnt lgkmcnt(14)\n"
"MM a[0:3], a[192:195], v[120:123], a[0:3]\n"
"s_waitcnt lgkmcnt(12)\n"
"MM a[4:7], a[196:199], v[120:123], a[4:7]\n"
"s_waitcnt lgkmcnt(10)\n"
"MM a[8:11], a[200:203], v[120:123], a[8:11]\n"
"s_waitcnt lgkmcnt(8)\n"
"MM a[12:15], a[204:207], v[120:123], a[12:15]\n"
"s_add_u32 s91, s94, 46080\n"
"s_waitcnt lgkmcnt(6)\n"
"MM a[16:19], a[208:211], v[120:123], a[16:19]\n"
"s_waitcnt lgkmcnt(4)\n"
"MM a[20:23], a[212:215], v[120:123], a[20:23]\n"
"s_waitcnt lgkmcnt(2)\n"
"MM a[24:27], a[216:219], v[120:123], a[24:27]\n"
"s_waitcnt lgkmcnt(0)\n"
"MM a[28:31], a[220:223], v[120:123], a[28:31]\n"
"MM a[228:231], a[244:247], v[124:127], a[228:231]\n"
"MM a[32:35], a[192:195], v[124:127], a[32:35]\n"
"v_accvgpr_read_b32 v140, a224\n"
"v_rcp_f32 v140, v140\n"
"MM a[36:39], a[196:199], v[124:127], a[36:39]\n"
"buffer_load_dwordx4 v[220:223], v251, s[56:59], s91 offen sc1\n"
"v_accvgpr_read_b32 v0, a0\n"
"v_accvgpr_read_b32 v1, a1\n"
"MM a[40:43], a[200:203], v[124:127], a[40:43]\n"
"v_accvgpr_read_b32 v2, a2\n"
"v_accvgpr_read_b32 v3, a3\n"
"MM a[44:47], a[204:207], v[124:127], a[44:47]\n"
"v_accvgpr_read_b32 v4, a4\n"
"v_accvgpr_read_b32 v5, a5\n"
"v_accvgpr_read_b32 v6, a6\n"
"MM a[48:51], a[208:211], v[124:127], a[48:51]\n"
"buffer_load_dwordx4 v[224:227], v251, s[56:59], s91 offen offset:1024 sc1\n"
"v_accvgpr_read_b32 v7, a7\n"
"v_mul_f32 v0, v140, v0\n"
"MM a[52:55], a[212:215], v[124:127], a[52:55]\n"
"v_mul_f32 v1, v140, v1\n"
"v_mul_f32 v2, v140, v2\n"
"MM a[56:59], a[216:219], v[124:127], a[56:59]\n"
"v_mul_f32 v3, v140, v3\n"
"v_mul_f32 v4, v140, v4\n"
"v_mul_f32 v5, v140, v5\n"
"buffer_load_dwordx4 v[228:231], v251, s[56:59], s91 offen offset:2048 sc1\n"
"MM a[60:63], a[220:223], v[124:127], a[60:63]\n"
"v_mul_f32 v6, v140, v6\n"
"v_mul_f32 v7, v140, v7\n"
"MM a[232:235], a[244:247], v[128:131], a[232:235]\n"
"v_cvt_pk_bf16_f32 v0, v0, v1\n"
"v_cvt_pk_bf16_f32 v1, v2, v3\n"
"MM a[64:67], a[192:195], v[128:131], a[64:67]\n"
"v_cvt_pk_bf16_f32 v2, v4, v5\n"
"v_cvt_pk_bf16_f32 v3, v6, v7\n"
"buffer_load_dwordx4 v[232:235], v251, s[56:59], s91 offen offset:3072 sc1\n"
"MM a[68:71], a[196:199], v[128:131], a[68:71]\n"
"buffer_store_dwordx4 v[0:3], v249, s[56:59], s40 offen sc1\n"
"MM a[72:75], a[200:203], v[128:131], a[72:75]\n"
"v_accvgpr_read_b32 v8, a8\n"
"MM a[76:79], a[204:207], v[128:131], a[76:79]\n"
"v_accvgpr_read_b32 v9, a9\n"
"v_accvgpr_read_b32 v10, a10\n"
"MM a[80:83], a[208:211], v[128:131], a[80:83]\n"
"v_accvgpr_read_b32 v11, a11\n"
"v_accvgpr_read_b32 v12, a12\n"
"buffer_load_dwordx4 v[236:239], v252, s[56:59], s91 offen sc1\n"
"v_accvgpr_read_b32 v13, a13\n"
"MM a[84:87], a[212:215], v[128:131], a[84:87]\n"
"v_accvgpr_read_b32 v14, a14\n"
"v_accvgpr_read_b32 v15, a15\n"
"MM a[88:91], a[216:219], v[128:131], a[88:91]\n"
"v_mul_f32 v8, v140, v8\n"
"v_mul_f32 v9, v140, v9\n"
"MM a[92:95], a[220:223], v[128:131], a[92:95]\n"
"v_mul_f32 v10, v140, v10\n"
"s_add_u32 s91, s94, 81920\n"
"v_mul_f32 v11, v140, v11\n"
"MM a[236:239], a[244:247], v[132:135], a[236:239]\n"
"v_mul_f32 v12, v140, v12\n"
"v_mul_f32 v13, v140, v13\n"
"v_mul_f32 v14, v140, v14\n"
"MM a[96:99], a[192:195], v[132:135], a[96:99]\n"
"v_mul_f32 v15, v140, v15\n"
"v_cvt_pk_bf16_f32 v8, v8, v9\n"
"MM a[100:103], a[196:199], v[132:135], a[100:103]\n"
"buffer_load_dwordx4 a[160:163], v251, s[56:59], s91 offen sc1\n"
"v_cvt_pk_bf16_f32 v9, v10, v11\n"
"v_cvt_pk_bf16_f32 v10, v12, v13\n"
"MM a[104:107], a[200:203], v[132:135], a[104:107]\n"
"v_cvt_pk_bf16_f32 v11, v14, v15\n"
"buffer_store_dwordx4 v[8:11], v249, s[56:59], s41 offen sc1\n"
"MM a[108:111], a[204:207], v[132:135], a[108:111]\n"
"MM a[112:115], a[208:211], v[132:135], a[112:115]\n"
"v_accvgpr_read_b32 v16, a16\n"
"v_accvgpr_read_b32 v17, a17\n"
"MM a[116:119], a[212:215], v[132:135], a[116:119]\n"
"buffer_load_dwordx4 a[164:167], v251, s[56:59], s91 offen offset:1024 sc1\n"
"v_accvgpr_read_b32 v18, a18\n"
"v_accvgpr_read_b32 v19, a19\n"
"MM a[120:123], a[216:219], v[132:135], a[120:123]\n"
"v_accvgpr_read_b32 v20, a20\n"
"v_accvgpr_read_b32 v21, a21\n"
"v_accvgpr_read_b32 v22, a22\n"
"MM a[124:127], a[220:223], v[132:135], a[124:127]\n"
"v_accvgpr_read_b32 v23, a23\n"
"v_mul_f32 v16, v140, v16\n"
"buffer_load_dwordx4 a[168:171], v251, s[56:59], s91 offen offset:2048 sc1\n"
"MM a[240:243], a[244:247], v[136:139], a[240:243]\n"
"v_mul_f32 v17, v140, v17\n"
"v_mul_f32 v18, v140, v18\n"
"MM a[128:131], a[192:195], v[136:139], a[128:131]\n"
"v_mul_f32 v19, v140, v19\n"
"v_mul_f32 v20, v140, v20\n"
"v_mul_f32 v21, v140, v21\n"
"MM a[132:135], a[196:199], v[136:139], a[132:135]\n"
"v_mul_f32 v22, v140, v22\n"
"buffer_load_dwordx4 a[172:175], v251, s[56:59], s91 offen offset:3072 sc1\n"
"v_mul_f32 v23, v140, v23\n"
"MM a[136:139], a[200:203], v[136:139], a[136:139]\n"
"v_cvt_pk_bf16_f32 v16, v16, v17\n"
"v_cvt_pk_bf16_f32 v17, v18, v19\n"
"MM a[140:143], a[204:207], v[136:139], a[140:143]\n"
"v_cvt_pk_bf16_f32 v18, v20, v21\n"
"v_cvt_pk_bf16_f32 v19, v22, v23\n"
"MM a[144:147], a[208:211], v[136:139], a[144:147]\n"
"buffer_store_dwordx4 v[16:19], v249, s[56:59], s42 offen sc1\n"
"MM a[148:151], a[212:215], v[136:139], a[148:151]\n"
"buffer_load_dwordx4 a[176:179], v252, s[56:59], s91 offen sc1\n"
"v_accvgpr_read_b32 v24, a24\n"
"MM a[152:155], a[216:219], v[136:139], a[152:155]\n"
"v_accvgpr_read_b32 v25, a25\n"
"v_accvgpr_read_b32 v26, a26\n"
"MM a[156:159], a[220:223], v[136:139], a[156:159]\n"
"v_accvgpr_read_b32 v27, a27\n"
"v_accvgpr_read_b32 v28, a28\n"
"v_accvgpr_read_b32 v29, a29\n"
"v_accvgpr_read_b32 v30, a30\n"
"v_accvgpr_read_b32 v31, a31\n"
"v_mul_f32 v24, v140, v24\n"
"v_mul_f32 v25, v140, v25\n"
"v_mul_f32 v26, v140, v26\n"
"v_mul_f32 v27, v140, v27\n"
"s_add_u32 s91, s94, 122880\n"
"v_mul_f32 v28, v140, v28\n"
"v_mul_f32 v29, v140, v29\n"
"v_mul_f32 v30, v140, v30\n"
"v_mul_f32 v31, v140, v31\n"
"v_cvt_pk_bf16_f32 v24, v24, v25\n"
"v_cvt_pk_bf16_f32 v25, v26, v27\n"
"v_cvt_pk_bf16_f32 v26, v28, v29\n"
"v_cvt_pk_bf16_f32 v27, v30, v31\n"
"buffer_store_dwordx4 v[24:27], v249, s[56:59], s43 offen sc1\n"
"v_accvgpr_read_b32 v142, a228\n"
"v_rcp_f32 v142, v142\n"
"s_nop 0\n"
"v_mov_b32 v143, v142\n"
"v_accvgpr_read_b32 v40, a32\n"
"v_accvgpr_read_b32 v41, a33\n"
"v_accvgpr_read_b32 v42, a34\n"
"v_accvgpr_read_b32 v43, a35\n"
"buffer_load_dwordx4 a[180:183], v251, s[56:59], s91 offen sc1\n"
"v_accvgpr_read_b32 v44, a36\n"
"v_accvgpr_read_b32 v45, a37\n"
"v_accvgpr_read_b32 v46, a38\n"
"v_accvgpr_read_b32 v47, a39\n"
"v_pk_mul_f32 v[40:41], v[40:41], v[142:143]\n"
"v_pk_mul_f32 v[42:43], v[42:43], v[142:143]\n"
"v_pk_mul_f32 v[44:45], v[44:45], v[142:143]\n"
"v_pk_mul_f32 v[46:47], v[46:47], v[142:143]\n"
"v_cvt_pk_bf16_f32 v40, v40, v41\n"
"v_cvt_pk_bf16_f32 v41, v42, v43\n"
"v_cvt_pk_bf16_f32 v42, v44, v45\n"
"v_cvt_pk_bf16_f32 v43, v46, v47\n"
"buffer_store_dwordx4 v[40:43], v249, s[56:59], s40 offen offset:1024 sc1\n"
"v_accvgpr_read_b32 v48, a40\n"
"v_accvgpr_read_b32 v49, a41\n"
"v_accvgpr_read_b32 v50, a42\n"
"buffer_load_dwordx4 a[184:187], v251, s[56:59], s91 offen offset:1024 sc1\n"
"v_accvgpr_read_b32 v51, a43\n"
"v_accvgpr_read_b32 v52, a44\n"
"v_accvgpr_read_b32 v53, a45\n"
"v_accvgpr_read_b32 v54, a46\n"
"v_accvgpr_read_b32 v55, a47\n"
"v_pk_mul_f32 v[48:49], v[48:49], v[142:143]\n"
"v_pk_mul_f32 v[50:51], v[50:51], v[142:143]\n"
"v_pk_mul_f32 v[52:53], v[52:53], v[142:143]\n"
"v_pk_mul_f32 v[54:55], v[54:55], v[142:143]\n"
"v_cvt_pk_bf16_f32 v48, v48, v49\n"
"v_cvt_pk_bf16_f32 v49, v50, v51\n"
"v_cvt_pk_bf16_f32 v50, v52, v53\n"
"v_cvt_pk_bf16_f32 v51, v54, v55\n"
"buffer_store_dwordx4 v[48:51], v249, s[56:59], s41 offen offset:1024 sc1\n"
"v_accvgpr_read_b32 v56, a48\n"
"v_accvgpr_read_b32 v57, a49\n"
"v_accvgpr_read_b32 v58, a50\n"
"buffer_load_dwordx4 a[188:191], v251, s[56:59], s91 offen offset:2048 sc1\n"
"v_accvgpr_read_b32 v59, a51\n"
"v_accvgpr_read_b32 v60, a52\n"
"v_accvgpr_read_b32 v61, a53\n"
"v_accvgpr_read_b32 v62, a54\n"
"v_accvgpr_read_b32 v63, a55\n"
"v_pk_mul_f32 v[56:57], v[56:57], v[142:143]\n"
"v_pk_mul_f32 v[58:59], v[58:59], v[142:143]\n"
"v_pk_mul_f32 v[60:61], v[60:61], v[142:143]\n"
"v_pk_mul_f32 v[62:63], v[62:63], v[142:143]\n"
"v_cvt_pk_bf16_f32 v56, v56, v57\n"
"v_cvt_pk_bf16_f32 v57, v58, v59\n"
"v_cvt_pk_bf16_f32 v58, v60, v61\n"
"v_cvt_pk_bf16_f32 v59, v62, v63\n"
"buffer_store_dwordx4 v[56:59], v249, s[56:59], s42 offen offset:1024 sc1\n"
"v_accvgpr_read_b32 v64, a56\n"
"v_accvgpr_read_b32 v65, a57\n"
"buffer_load_dwordx4 a[192:195], v251, s[56:59], s91 offen offset:3072 sc1\n"
"v_accvgpr_read_b32 v66, a58\n"
"v_accvgpr_read_b32 v67, a59\n"
"v_accvgpr_read_b32 v68, a60\n"
"v_accvgpr_read_b32 v69, a61\n"
"v_accvgpr_read_b32 v70, a62\n"
"v_accvgpr_read_b32 v71, a63\n"
"v_pk_mul_f32 v[64:65], v[64:65], v[142:143]\n"
"v_pk_mul_f32 v[66:67], v[66:67], v[142:143]\n"
"v_pk_mul_f32 v[68:69], v[68:69], v[142:143]\n"
"v_pk_mul_f32 v[70:71], v[70:71], v[142:143]\n"
"v_cvt_pk_bf16_f32 v64, v64, v65\n"
"v_cvt_pk_bf16_f32 v65, v66, v67\n"
"v_cvt_pk_bf16_f32 v66, v68, v69\n"
"v_cvt_pk_bf16_f32 v67, v70, v71\n"
"buffer_store_dwordx4 v[64:67], v249, s[56:59], s43 offen offset:1024 sc1\n"
"v_accvgpr_read_b32 v144, a232\n"
"buffer_load_dwordx4 a[196:199], v252, s[56:59], s91 offen sc1\n"
"v_rcp_f32 v144, v144\n"
"s_nop 0\n"
"v_mov_b32 v145, v144\n"
"v_accvgpr_read_b32 v146, a236\n"
"v_rcp_f32 v146, v146\n"
"s_nop 0\n"
"v_mov_b32 v147, v146\n"
"v_accvgpr_read_b32 v148, a240\n"
"v_rcp_f32 v148, v148\n"
"s_nop 0\n"
"v_mov_b32 v149, v148\n"
"v_accvgpr_read_b32 v0, a64\n"
"v_accvgpr_read_b32 v1, a65\n"
"v_accvgpr_read_b32 v2, a66\n"
"v_accvgpr_read_b32 v3, a67\n"
"v_accvgpr_read_b32 v4, a68\n"
"v_accvgpr_read_b32 v5, a69\n"
"v_accvgpr_read_b32 v6, a70\n"
"v_accvgpr_read_b32 v7, a71\n"
"v_pk_mul_f32 v[0:1], v[0:1], v[144:145]\n"
"s_add_u32 s91, s94, 87040\n"
"v_pk_mul_f32 v[2:3], v[2:3], v[144:145]\n"
"v_pk_mul_f32 v[4:5], v[4:5], v[144:145]\n"
"v_pk_mul_f32 v[6:7], v[6:7], v[144:145]\n"
"v_cvt_pk_bf16_f32 v0, v0, v1\n"
"v_cvt_pk_bf16_f32 v1, v2, v3\n"
"v_cvt_pk_bf16_f32 v2, v4, v5\n"
"v_cvt_pk_bf16_f32 v3, v6, v7\n"
"buffer_store_dwordx4 v[0:3], v249, s[56:59], s40 offen offset:2048 sc1\n"
"v_accvgpr_read_b32 v8, a72\n"
"v_accvgpr_read_b32 v9, a73\n"
"v_accvgpr_read_b32 v10, a74\n"
"v_accvgpr_read_b32 v11, a75\n"
"v_accvgpr_read_b32 v12, a76\n"
"v_accvgpr_read_b32 v13, a77\n"
"v_accvgpr_read_b32 v14, a78\n"
"v_accvgpr_read_b32 v15, a79\n"
"buffer_load_dwordx4 a[200:203], v251, s[56:59], s91 offen sc1\n"
"v_pk_mul_f32 v[8:9], v[8:9], v[144:145]\n"
"v_pk_mul_f32 v[10:11], v[10:11], v[144:145]\n"
"v_pk_mul_f32 v[12:13], v[12:13], v[144:145]\n"
"v_pk_mul_f32 v[14:15], v[14:15], v[144:145]\n"
"v_cvt_pk_bf16_f32 v8, v8, v9\n"
"v_cvt_pk_bf16_f32 v9, v10, v11\n"
"v_cvt_pk_bf16_f32 v10, v12, v13\n"
"v_cvt_pk_bf16_f32 v11, v14, v15\n"
"buffer_store_dwordx4 v[8:11], v249, s[56:59], s41 offen offset:2048 sc1\n"
"v_accvgpr_read_b32 v16, a80\n"
"v_accvgpr_read_b32 v17, a81\n"
"v_accvgpr_read_b32 v18, a82\n"
"v_accvgpr_read_b32 v19, a83\n"
"v_accvgpr_read_b32 v20, a84\n"
"v_accvgpr_read_b32 v21, a85\n"
"v_accvgpr_read_b32 v22, a86\n"
"buffer_load_dwordx4 a[204:207], v251, s[56:59], s91 offen offset:1024 sc1\n"
"v_accvgpr_read_b32 v23, a87\n"
"v_pk_mul_f32 v[16:17], v[16:17], v[144:145]\n"
"v_pk_mul_f32 v[18:19], v[18:19], v[144:145]\n"
"v_pk_mul_f32 v[20:21], v[20:21], v[144:145]\n"
"v_pk_mul_f32 v[22:23], v[22:23], v[144:145]\n"
"v_cvt_pk_bf16_f32 v16, v16, v17\n"
"v_cvt_pk_bf16_f32 v17, v18, v19\n"
"v_cvt_pk_bf16_f32 v18, v20, v21\n"
"v_cvt_pk_bf16_f32 v19, v22, v23\n"
"buffer_store_dwordx4 v[16:19], v249, s[56:59], s42 offen offset:2048 sc1\n"
"v_accvgpr_read_b32 v24, a88\n"
"v_accvgpr_read_b32 v25, a89\n"
"v_accvgpr_read_b32 v26, a90\n"
"v_accvgpr_read_b32 v27, a91\n"
"v_accvgpr_read_b32 v28, a92\n"
"v_accvgpr_read_b32 v29, a93\n"
"v_accvgpr_read_b32 v30, a94\n"
"buffer_load_dwordx4 a[208:211], v251, s[56:59], s91 offen offset:2048 sc1\n"
"v_accvgpr_read_b32 v31, a95\n"
"v_pk_mul_f32 v[24:25], v[24:25], v[144:145]\n"
"v_pk_mul_f32 v[26:27], v[26:27], v[144:145]\n"
"v_pk_mul_f32 v[28:29], v[28:29], v[144:145]\n"
"v_pk_mul_f32 v[30:31], v[30:31], v[144:145]\n"
"v_cvt_pk_bf16_f32 v24, v24, v25\n"
"v_cvt_pk_bf16_f32 v25, v26, v27\n"
"v_cvt_pk_bf16_f32 v26, v28, v29\n"
"v_cvt_pk_bf16_f32 v27, v30, v31\n"
"buffer_store_dwordx4 v[24:27], v249, s[56:59], s43 offen offset:2048 sc1\n"
"v_accvgpr_read_b32 v40, a96\n"
"v_accvgpr_read_b32 v41, a97\n"
"v_accvgpr_read_b32 v42, a98\n"
"v_accvgpr_read_b32 v43, a99\n"
"v_accvgpr_read_b32 v44, a100\n"
"v_accvgpr_read_b32 v45, a101\n"
"buffer_load_dwordx4 a[212:215], v251, s[56:59], s91 offen offset:3072 sc1\n"
"v_accvgpr_read_b32 v46, a102\n"
"v_accvgpr_read_b32 v47, a103\n"
"v_pk_mul_f32 v[40:41], v[40:41], v[146:147]\n"
"v_pk_mul_f32 v[42:43], v[42:43], v[146:147]\n"
"v_pk_mul_f32 v[44:45], v[44:45], v[146:147]\n"
"v_pk_mul_f32 v[46:47], v[46:47], v[146:147]\n"
"v_cvt_pk_bf16_f32 v40, v40, v41\n"
"v_cvt_pk_bf16_f32 v41, v42, v43\n"
"v_cvt_pk_bf16_f32 v42, v44, v45\n"
"v_cvt_pk_bf16_f32 v43, v46, v47\n"
"buffer_store_dwordx4 v[40:43], v249, s[56:59], s40 offen offset:3072 sc1\n"
"v_accvgpr_read_b32 v48, a104\n"
"v_accvgpr_read_b32 v49, a105\n"
"v_accvgpr_read_b32 v50, a106\n"
"v_accvgpr_read_b32 v51, a107\n"
"v_accvgpr_read_b32 v52, a108\n"
"buffer_load_dwordx4 a[216:219], v252, s[56:59], s91 offen sc1\n"
"v_accvgpr_read_b32 v53, a109\n"
"v_accvgpr_read_b32 v54, a110\n"
"v_accvgpr_read_b32 v55, a111\n"
"v_pk_mul_f32 v[48:49], v[48:49], v[146:147]\n"
"v_pk_mul_f32 v[50:51], v[50:51], v[146:147]\n"
"v_pk_mul_f32 v[52:53], v[52:53], v[146:147]\n"
"v_pk_mul_f32 v[54:55], v[54:55], v[146:147]\n"
"v_cvt_pk_bf16_f32 v48, v48, v49\n"
"v_cvt_pk_bf16_f32 v49, v50, v51\n"
"v_cvt_pk_bf16_f32 v50, v52, v53\n"
"v_cvt_pk_bf16_f32 v51, v54, v55\n"
"buffer_store_dwordx4 v[48:51], v249, s[56:59], s41 offen offset:3072 sc1\n"
"v_accvgpr_read_b32 v56, a112\n"
"v_accvgpr_read_b32 v57, a113\n"
"v_accvgpr_read_b32 v58, a114\n"
"v_accvgpr_read_b32 v59, a115\n"
"v_accvgpr_read_b32 v60, a116\n"
"s_add_u32 s91, s94, 128000\n"
"v_accvgpr_read_b32 v61, a117\n"
"v_accvgpr_read_b32 v62, a118\n"
"v_accvgpr_read_b32 v63, a119\n"
"v_pk_mul_f32 v[56:57], v[56:57], v[146:147]\n"
"v_pk_mul_f32 v[58:59], v[58:59], v[146:147]\n"
"v_pk_mul_f32 v[60:61], v[60:61], v[146:147]\n"
"v_pk_mul_f32 v[62:63], v[62:63], v[146:147]\n"
"v_cvt_pk_bf16_f32 v56, v56, v57\n"
"v_cvt_pk_bf16_f32 v57, v58, v59\n"
"v_cvt_pk_bf16_f32 v58, v60, v61\n"
"v_cvt_pk_bf16_f32 v59, v62, v63\n"
"buffer_store_dwordx4 v[56:59], v249, s[56:59], s42 offen offset:3072 sc1\n"
"v_accvgpr_read_b32 v64, a120\n"
"v_accvgpr_read_b32 v65, a121\n"
"v_accvgpr_read_b32 v66, a122\n"
"v_accvgpr_read_b32 v67, a123\n"
"buffer_load_dwordx4 a[220:223], v251, s[56:59], s91 offen sc1\n"
"v_accvgpr_read_b32 v68, a124\n"
"v_accvgpr_read_b32 v69, a125\n"
"v_accvgpr_read_b32 v70, a126\n"
"v_accvgpr_read_b32 v71, a127\n"
"v_pk_mul_f32 v[64:65], v[64:65], v[146:147]\n"
"v_pk_mul_f32 v[66:67], v[66:67], v[146:147]\n"
"v_pk_mul_f32 v[68:69], v[68:69], v[146:147]\n"
"v_pk_mul_f32 v[70:71], v[70:71], v[146:147]\n"
"v_cvt_pk_bf16_f32 v64, v64, v65\n"
"v_cvt_pk_bf16_f32 v65, v66, v67\n"
"v_cvt_pk_bf16_f32 v66, v68, v69\n"
"v_cvt_pk_bf16_f32 v67, v70, v71\n"
"buffer_store_dwordx4 v[64:67], v249, s[56:59], s43 offen offset:3072 sc1\n"
"v_accvgpr_read_b32 v0, a128\n"
"v_accvgpr_read_b32 v1, a129\n"
"v_accvgpr_read_b32 v2, a130\n"
"buffer_load_dwordx4 a[224:227], v251, s[56:59], s91 offen offset:1024 sc1\n"
"v_accvgpr_read_b32 v3, a131\n"
"v_accvgpr_read_b32 v4, a132\n"
"v_accvgpr_read_b32 v5, a133\n"
"v_accvgpr_read_b32 v6, a134\n"
"v_accvgpr_read_b32 v7, a135\n"
"v_pk_mul_f32 v[0:1], v[0:1], v[148:149]\n"
"v_pk_mul_f32 v[2:3], v[2:3], v[148:149]\n"
"v_pk_mul_f32 v[4:5], v[4:5], v[148:149]\n"
"v_pk_mul_f32 v[6:7], v[6:7], v[148:149]\n"
"v_cvt_pk_bf16_f32 v0, v0, v1\n"
"v_cvt_pk_bf16_f32 v1, v2, v3\n"
"v_cvt_pk_bf16_f32 v2, v4, v5\n"
"v_cvt_pk_bf16_f32 v3, v6, v7\n"
"buffer_store_dwordx4 v[0:3], v250, s[56:59], s40 offen sc1\n"
"v_accvgpr_read_b32 v8, a136\n"
"v_accvgpr_read_b32 v9, a137\n"
"v_accvgpr_read_b32 v10, a138\n"
"buffer_load_dwordx4 a[228:231], v251, s[56:59], s91 offen offset:2048 sc1\n"
"v_accvgpr_read_b32 v11, a139\n"
"v_accvgpr_read_b32 v12, a140\n"
"v_accvgpr_read_b32 v13, a141\n"
"v_accvgpr_read_b32 v14, a142\n"
"v_accvgpr_read_b32 v15, a143\n"
"v_pk_mul_f32 v[8:9], v[8:9], v[148:149]\n"
"v_pk_mul_f32 v[10:11], v[10:11], v[148:149]\n"
"v_pk_mul_f32 v[12:13], v[12:13], v[148:149]\n"
"v_pk_mul_f32 v[14:15], v[14:15], v[148:149]\n"
"v_cvt_pk_bf16_f32 v8, v8, v9\n"
"v_cvt_pk_bf16_f32 v9, v10, v11\n"
"v_cvt_pk_bf16_f32 v10, v12, v13\n"
"v_cvt_pk_bf16_f32 v11, v14, v15\n"
"buffer_store_dwordx4 v[8:11], v250, s[56:59], s41 offen sc1\n"
"v_accvgpr_read_b32 v16, a144\n"
"v_accvgpr_read_b32 v17, a145\n"
"buffer_load_dwordx4 a[232:235], v251, s[56:59], s91 offen offset:3072 sc1\n"
"v_accvgpr_read_b32 v18, a146\n"
"v_accvgpr_read_b32 v19, a147\n"
"v_accvgpr_read_b32 v20, a148\n"
"v_accvgpr_read_b32 v21, a149\n"
"v_accvgpr_read_b32 v22, a150\n"
"v_accvgpr_read_b32 v23, a151\n"
"v_pk_mul_f32 v[16:17], v[16:17], v[148:149]\n"
"v_pk_mul_f32 v[18:19], v[18:19], v[148:149]\n"
"v_pk_mul_f32 v[20:21], v[20:21], v[148:149]\n"
"v_pk_mul_f32 v[22:23], v[22:23], v[148:149]\n"
"v_cvt_pk_bf16_f32 v16, v16, v17\n"
"v_cvt_pk_bf16_f32 v17, v18, v19\n"
"v_cvt_pk_bf16_f32 v18, v20, v21\n"
"v_cvt_pk_bf16_f32 v19, v22, v23\n"
"buffer_store_dwordx4 v[16:19], v250, s[56:59], s42 offen sc1\n"
"v_accvgpr_read_b32 v24, a152\n"
"buffer_load_dwordx4 a[236:239], v252, s[56:59], s91 offen sc1\n"
"v_accvgpr_read_b32 v25, a153\n"
"v_accvgpr_read_b32 v26, a154\n"
"v_accvgpr_read_b32 v27, a155\n"
"v_accvgpr_read_b32 v28, a156\n"
"v_accvgpr_read_b32 v29, a157\n"
"v_accvgpr_read_b32 v30, a158\n"
"v_accvgpr_read_b32 v31, a159\n"
"v_pk_mul_f32 v[24:25], v[24:25], v[148:149]\n"
"v_pk_mul_f32 v[26:27], v[26:27], v[148:149]\n"
"v_pk_mul_f32 v[28:29], v[28:29], v[148:149]\n"
"v_pk_mul_f32 v[30:31], v[30:31], v[148:149]\n"
"v_cvt_pk_bf16_f32 v24, v24, v25\n"
"v_cvt_pk_bf16_f32 v25, v26, v27\n"
"v_cvt_pk_bf16_f32 v26, v28, v29\n"
"v_cvt_pk_bf16_f32 v27, v30, v31\n"
"buffer_store_dwordx4 v[24:27], v250, s[56:59], s43 offen sc1\n"
"s_add_u32 s68, s77, 32768\n"
"v_lshl_add_u32 v240, v253, 4, s68\n"
"v_add_u32 v241, 40960, v240\n"
"s_barrier\n"
"ds_read_b128 a[0:3], v240 offset:0\n"
"ds_read_b128 a[20:23], v240 offset:5120\n"
"ds_read_b128 a[4:7], v240 offset:1024\n"
"ds_read_b128 a[24:27], v240 offset:6144\n"
"ds_read_b128 a[8:11], v240 offset:2048\n"
"ds_read_b128 a[28:31], v240 offset:7168\n"
"ds_read_b128 a[12:15], v240 offset:3072\n"
"ds_read_b128 a[32:35], v240 offset:8192\n"
"ds_read_b128 a[16:19], v240 offset:4096\n"
"ds_read_b128 a[36:39], v240 offset:9216\n"
"s_waitcnt lgkmcnt(9)\n"
"s_waitcnt vmcnt(59)\n"
"MM v[0:3], v[160:163], a[0:3], 0\n"
"v_subrev_u32 v248, 16, v253\n"
"v_bfe_u32 v246, v248, 4, 1\n"
"s_waitcnt lgkmcnt(8)\n"
"s_waitcnt vmcnt(54)\n"
"MM v[0:3], v[180:183], a[20:23], v[0:3]\n"
"s_lshl_b32 s68, s87, 1\n"
"v_add_u32 v246, s68, v246\n"
"MM v[4:7], v[164:167], a[0:3], 0\n"
"v_and_b32 v247, 7, v248\n"
"s_waitcnt vmcnt(53)\n"
"MM v[4:7], v[184:187], a[20:23], v[4:7]\n"
"v_bfe_u32 v244, v248, 3, 1\n"
"MM v[8:11], v[168:171], a[0:3], 0\n"
"v_lshl_add_u32 v247, v247, 1, v244\n"
"s_waitcnt vmcnt(52)\n"
"MM v[8:11], v[188:191], a[20:23], v[8:11]\n"
"MM v[12:15], v[172:175], a[0:3], 0\n"
"v_cmp_gt_u32 vcc, 32, v248\n"
"s_waitcnt vmcnt(51)\n"
"MM v[12:15], v[192:195], a[20:23], v[12:15]\n"
"MM v[16:19], v[176:179], a[0:3], 0\n"
"v_mov_b32 v244, s63\n"
"s_waitcnt vmcnt(50)\n"
"MM v[16:19], v[196:199], a[20:23], v[16:19]\n"
"v_cndmask_b32 v246, v244, v246, vcc\n"
"s_waitcnt lgkmcnt(7)\n"
"MM v[20:23], v[160:163], a[4:7], 0\n"
"s_waitcnt lgkmcnt(6)\n"
"MM v[20:23], v[180:183], a[24:27], v[20:23]\n"
"v_mov_b32 v244, s64\n"
"MM v[24:27], v[164:167], a[4:7], 0\n"
"MM v[24:27], v[184:187], a[24:27], v[24:27]\n"
"v_cndmask_b32 v247, v244, v247, vcc\n"
"MM v[28:31], v[168:171], a[4:7], 0\n"
"s_and_b32 s68, s64, 1\n"
"MM v[28:31], v[188:191], a[24:27], v[28:31]\n"
"MM v[32:35], v[172:175], a[4:7], 0\n"
"v_and_b32 v244, 7, v253\n"
"MM v[32:35], v[192:195], a[24:27], v[32:35]\n"
"MM v[36:39], v[176:179], a[4:7], 0\n"
"v_lshl_add_u32 v244, v244, 1, s68\n"
"MM v[36:39], v[196:199], a[24:27], v[36:39]\n"
"s_mov_b32 vcc_lo, 0xff\n"
"s_waitcnt lgkmcnt(5)\n"
"MM v[40:43], v[160:163], a[8:11], 0\n"
"s_waitcnt lgkmcnt(4)\n"
"MM v[40:43], v[180:183], a[28:31], v[40:43]\n"
"s_mov_b32 vcc_hi, 0xff000000\n"
"MM v[44:47], v[164:167], a[8:11], 0\n"
"MM v[44:47], v[184:187], a[28:31], v[44:47]\n"
"v_cndmask_b32 v247, v247, v244, vcc\n"
"MM v[48:51], v[168:171], a[8:11], 0\n"
"s_xor_b32 s68, s64, 1\n"
"MM v[48:51], v[188:191], a[28:31], v[48:51]\n"
"MM v[52:55], v[172:175], a[8:11], 0\n"
"v_mov_b32 v244, s68\n"
"MM v[52:55], v[192:195], a[28:31], v[52:55]\n"
"MM v[56:59], v[176:179], a[8:11], 0\n"
"v_cmp_eq_u32 vcc, 8, v253\n"
"MM v[56:59], v[196:199], a[28:31], v[56:59]\n"
"s_nop 0\n"
"v_cndmask_b32 v247, v247, v244, vcc\n"
"s_waitcnt lgkmcnt(3)\n"
"MM v[60:63], v[160:163], a[12:15], 0\n"
"s_waitcnt lgkmcnt(2)\n"
"MM v[60:63], v[180:183], a[32:35], v[60:63]\n"
"v_and_b32 v244, 7, v246\n"
"MM v[64:67], v[164:167], a[12:15], 0\n"
"MM v[64:67], v[184:187], a[32:35], v[64:67]\n"
"v_lshlrev_b32 v244, 5, v244\n"
"MM v[68:71], v[168:171], a[12:15], 0\n"
"v_and_b32 v248, 3, v247\n"
"MM v[68:71], v[188:191], a[32:35], v[68:71]\n"
"MM v[72:75], v[172:175], a[12:15], 0\n"
"v_lshl_add_u32 v244, v248, 3, v244\n"
"MM v[72:75], v[192:195], a[32:35], v[72:75]\n"
"MM v[76:79], v[176:179], a[12:15], 0\n"
"v_lshrrev_b32 v248, 3, v246\n"
"MM v[76:79], v[196:199], a[32:35], v[76:79]\n"
"v_lshl_add_u32 v244, v248, 2, v244\n"
"s_waitcnt lgkmcnt(1)\n"
"MM v[80:83], v[160:163], a[16:19], 0\n"
"s_waitcnt lgkmcnt(0)\n"
"MM v[80:83], v[180:183], a[36:39], v[80:83]\n"
"v_lshrrev_b32 v248, 2, v247\n"
"MM v[84:87], v[164:167], a[16:19], 0\n"
"MM v[84:87], v[184:187], a[36:39], v[84:87]\n"
"v_add_u32 v244, v244, v248\n"
"MM v[88:91], v[168:171], a[16:19], 0\n"
"MM v[88:91], v[188:191], a[36:39], v[88:91]\n"
"MM v[92:95], v[172:175], a[16:19], 0\n"
"MM v[92:95], v[192:195], a[36:39], v[92:95]\n"
"MM v[96:99], v[176:179], a[16:19], 0\n"
"MM v[96:99], v[196:199], a[36:39], v[96:99]\n"
"s_waitcnt vmcnt(0)\n"
"K1_TAIL:\n"
"s_nop 7\n"
"s_barrier\n"
"s_cmp_lg_u32 s60, 3\n"
"s_cbranch_scc1 K1_NOFP\n"
"s_add_u32 s68, s74, 1\n"
"v_mov_b32 v246, s68\n"
"s_lshl_b32 s68, s65, 7\n"
"s_add_u32 s68, s68, 42991648\n"
"v_mov_b32 v247, 0\n"
"s_mov_b64 exec, 1\n"
"buffer_store_dword v246, v247, s[56:59], s68 offen sc1\n"
"s_mov_b64 exec, -1\n"
"K1_NOFP:\n"
"s_nop 3\n"
"MM v[100:103], v[200:203], a[0:3], 0\n"
"s_add_u32 s91, s94, 163840\n"
"v_and_b32 v246, 3, v253\n"
"MM v[100:103], v[220:223], a[20:23], v[100:103]\n"
"buffer_load_dwordx4 a[80:83], v251, s[56:59], s91 offen sc1\n"
"MM v[104:107], v[204:207], a[0:3], 0\n"
"ds_read_b128 a[40:43], v240 offset:10240\n"
"v_lshlrev_b32 v246, 5, v246\n"
"MM v[104:107], v[224:227], a[20:23], v[104:107]\n"
"buffer_load_dwordx4 a[84:87], v251, s[56:59], s91 offen offset:1024 sc1\n"
"MM v[108:111], v[208:211], a[0:3], 0\n"
"v_mov_b32 v243, 0\n"
"ds_read_b128 a[44:47], v240 offset:11264\n"
"MM v[108:111], v[228:231], a[20:23], v[108:111]\n"
"v_cmp_gt_u32 vcc, 48, v253\n"
"ds_read_b128 a[48:51], v240 offset:12288\n"
"MM v[112:115], v[212:215], a[0:3], 0\n"
"buffer_load_dwordx4 a[88:91], v251, s[56:59], s91 offen offset:2048 sc1\n"
"MM v[112:115], v[232:235], a[20:23], v[112:115]\n"
"v_cndmask_b32 v246, v246, v243, vcc\n"
"v_mov_b32 v243, 32\n"
"MM v[116:119], v[216:219], a[0:3], 0\n"
"ds_read_b128 a[52:55], v240 offset:13312\n"
"v_cmp_eq_u32 vcc, 8, v253\n"
"MM v[116:119], v[236:239], a[20:23], v[116:119]\n"
"buffer_load_dwordx4 a[92:95], v251, s[56:59], s91 offen offset:3072 sc1\n"
"MM v[120:123], v[200:203], a[4:7], 0\n"
"ds_read_b128 a[56:59], v240 offset:14336\n"
"v_cndmask_b32 v246, v246, v243, vcc\n"
"MM v[120:123], v[220:223], a[24:27], v[120:123]\n"
"buffer_load_dwordx4 a[96:99], v252, s[56:59], s91 offen sc1\n"
"MM v[124:127], v[204:207], a[4:7], 0\n"
"v_mov_b32 v243, 64\n"
"ds_read_b128 a[60:63], v240 offset:15360\n"
"MM v[124:127], v[224:227], a[24:27], v[124:127]\n"
"v_cmp_gt_u32 vcc, 8, v253\n"
"ds_read_b128 a[64:67], v240 offset:16384\n"
"MM v[128:131], v[208:211], a[4:7], 0\n"
"s_add_u32 s91, s94, 204800\n"
"v_cndmask_b32 v246, v246, v243, vcc\n"
"MM v[128:131], v[228:231], a[24:27], v[128:131]\n"
"buffer_load_dwordx4 a[100:103], v251, s[56:59], s91 offen sc1\n"
"MM v[132:135], v[212:215], a[4:7], 0\n"
"v_mov_b32 v243, 96\n"
"ds_read_b128 a[68:71], v240 offset:17408\n"
"MM v[132:135], v[232:235], a[24:27], v[132:135]\n"
"v_cmp_le_u32 vcc, 56, v253\n"
"MM v[136:139], v[216:219], a[4:7], 0\n"
"buffer_load_dwordx4 a[104:107], v251, s[56:59], s91 offen offset:1024 sc1\n"
"MM v[136:139], v[236:239], a[24:27], v[136:139]\n"
"v_cndmask_b32 v246, v246, v243, vcc\n"
"MM v[140:143], v[200:203], a[8:11], 0\n"
"ds_read_b128 a[72:75], v240 offset:18432\n"
"MM v[140:143], v[220:223], a[28:31], v[140:143]\n"
"buffer_load_dwordx4 a[108:111], v251, s[56:59], s91 offen offset:2048 sc1\n"
"MM v[144:147], v[204:207], a[8:11], 0\n"
"v_lshl_add_u32 v243, v244, 7, v246\n"
"MM v[144:147], v[224:227], a[28:31], v[144:147]\n"
"v_add_u32 v243, 42991616, v243\n"
"MM v[148:151], v[208:211], a[8:11], 0\n"
"buffer_load_dwordx4 a[112:115], v251, s[56:59], s91 offen offset:3072 sc1\n"
"MM v[148:151], v[228:231], a[28:31], v[148:151]\n"
"ds_read_b128 a[76:79], v240 offset:19456\n"
"v_mul_u32_u24 v246, 106496, v244\n"
"MM v[152:155], v[212:215], a[8:11], 0\n"
"MM v[152:155], v[232:235], a[28:31], v[152:155]\n"
"buffer_load_dwordx4 a[116:119], v252, s[56:59], s91 offen sc1\n"
"MM v[156:159], v[216:219], a[8:11], 0\n"
"v_readlane_b32 s44, v246, 0\n"
"MM v[156:159], v[236:239], a[28:31], v[156:159]\n"
"v_readlane_b32 s45, v246, 1\n"
"MM v[160:163], v[200:203], a[12:15], 0\n"
"s_add_u32 s91, s94, 168960\n"
"MM v[160:163], v[220:223], a[32:35], v[160:163]\n"
"buffer_load_dwordx4 a[120:123], v251, s[56:59], s91 offen sc1\n"
"MM v[164:167], v[204:207], a[12:15], 0\n"
"v_readlane_b32 s46, v246, 2\n"
"MM v[164:167], v[224:227], a[32:35], v[164:167]\n"
"v_readlane_b32 s47, v246, 3\n"
"MM v[168:171], v[208:211], a[12:15], 0\n"
"buffer_load_dwordx4 a[124:127], v251, s[56:59], s91 offen offset:1024 sc1\n"
"MM v[168:171], v[228:231], a[32:35], v[168:171]\n"
"v_readlane_b32 s48, v246, 4\n"
"MM v[172:175], v[212:215], a[12:15], 0\n"
"MM v[172:175], v[232:235], a[32:35], v[172:175]\n"
"v_readlane_b32 s49, v246, 5\n"
"MM v[176:179], v[216:219], a[12:15], 0\n"
"buffer_load_dwordx4 a[128:131], v251, s[56:59], s91 offen offset:2048 sc1\n"
"MM v[176:179], v[236:239], a[32:35], v[176:179]\n"
"v_readlane_b32 s50, v246, 6\n"
"MM v[180:183], v[200:203], a[16:19], 0\n"
"MM v[180:183], v[220:223], a[36:39], v[180:183]\n"
"buffer_load_dwordx4 a[132:135], v251, s[56:59], s91 offen offset:3072 sc1\n"
"MM v[184:187], v[204:207], a[16:19], 0\n"
"v_readlane_b32 s51, v246, 7\n"
"MM v[184:187], v[224:227], a[36:39], v[184:187]\n"
"MM v[188:191], v[208:211], a[16:19], 0\n"
"buffer_load_dwordx4 a[136:139], v252, s[56:59], s91 offen sc1\n"
"MM v[188:191], v[228:231], a[36:39], v[188:191]\n"
"MM v[192:195], v[212:215], a[16:19], 0\n"
"MM v[192:195], v[232:235], a[36:39], v[192:195]\n"
"MM v[196:199], v[216:219], a[16:19], 0\n"
"MM v[196:199], v[236:239], a[36:39], v[196:199]\n"
"s_waitcnt lgkmcnt(0)\n"
"MM v[0:3], a[160:163], a[40:43], v[0:3]\n"
"s_add_u32 s91, s94, 209920\n"
"MM v[0:3], a[180:183], a[60:63], v[0:3]\n"
"buffer_load_dwordx4 a[140:143], v251, s[56:59], s91 offen sc1\n"
"MM v[4:7], a[164:167], a[40:43], v[4:7]\n"
"buffer_load_dwordx4 a[144:147], v251, s[56:59], s91 offen offset:1024 sc1\n"
"MM v[4:7], a[184:187], a[60:63], v[4:7]\n"
"MM v[8:11], a[168:171], a[40:43], v[8:11]\n"
"MM v[8:11], a[188:191], a[60:63], v[8:11]\n"
"MM v[12:15], a[172:175], a[40:43], v[12:15]\n"
"MM v[12:15], a[192:195], a[60:63], v[12:15]\n"
"buffer_load_dwordx4 a[148:151], v251, s[56:59], s91 offen offset:2048 sc1\n"
"MM v[16:19], a[176:179], a[40:43], v[16:19]\n"
"MM v[16:19], a[196:199], a[60:63], v[16:19]\n"
"MM v[20:23], a[160:163], a[44:47], v[20:23]\n"
"MM v[20:23], a[180:183], a[64:67], v[20:23]\n"
"buffer_load_dwordx4 a[152:155], v251, s[56:59], s91 offen offset:3072 sc1\n"
"MM v[24:27], a[164:167], a[44:47], v[24:27]\n"
"MM v[24:27], a[184:187], a[64:67], v[24:27]\n"
"MM v[28:31], a[168:171], a[44:47], v[28:31]\n"
"MM v[28:31], a[188:191], a[64:67], v[28:31]\n"
"MM v[32:35], a[172:175], a[44:47], v[32:35]\n"
"buffer_load_dwordx4 a[156:159], v252, s[56:59], s91 offen sc1\n"
"MM v[32:35], a[192:195], a[64:67], v[32:35]\n"
"MM v[36:39], a[176:179], a[44:47], v[36:39]\n"
"MM v[36:39], a[196:199], a[64:67], v[36:39]\n"
"MM v[40:43], a[160:163], a[48:51], v[40:43]\n"
"MM v[40:43], a[180:183], a[68:71], v[40:43]\n"
"s_add_u32 s91, s94, 245760\n"
"MM v[44:47], a[164:167], a[48:51], v[44:47]\n"
"buffer_load_dwordx4 v[200:203], v251, s[56:59], s91 offen sc1\n"
"MM v[44:47], a[184:187], a[68:71], v[44:47]\n"
"MM v[48:51], a[168:171], a[48:51], v[48:51]\n"
"MM v[48:51], a[188:191], a[68:71], v[48:51]\n"
"MM v[52:55], a[172:175], a[48:51], v[52:55]\n"
"MM v[52:55], a[192:195], a[68:71], v[52:55]\n"
"buffer_load_dwordx4 v[204:207], v251, s[56:59], s91 offen offset:1024 sc1\n"
"MM v[56:59], a[176:179], a[48:51], v[56:59]\n"
"MM v[56:59], a[196:199], a[68:71], v[56:59]\n"
"MM v[60:63], a[160:163], a[52:55], v[60:63]\n"
"MM v[60:63], a[180:183], a[72:75], v[60:63]\n"
"MM v[64:67], a[164:167], a[52:55], v[64:67]\n"
"buffer_load_dwordx4 v[208:211], v251, s[56:59], s91 offen offset:2048 sc1\n"
"MM v[64:67], a[184:187], a[72:75], v[64:67]\n"
"MM v[68:71], a[168:171], a[52:55], v[68:71]\n"
"MM v[68:71], a[188:191], a[72:75], v[68:71]\n"
"MM v[72:75], a[172:175], a[52:55], v[72:75]\n"
"buffer_load_dwordx4 v[212:215], v251, s[56:59], s91 offen offset:3072 sc1\n"
"MM v[72:75], a[192:195], a[72:75], v[72:75]\n"
"MM v[76:79], a[176:179], a[52:55], v[76:79]\n"
"MM v[76:79], a[196:199], a[72:75], v[76:79]\n"
"MM v[80:83], a[160:163], a[56:59], v[80:83]\n"
"MM v[80:83], a[180:183], a[76:79], v[80:83]\n"
"buffer_load_dwordx4 v[216:219], v252, s[56:59], s91 offen sc1\n"
"MM v[84:87], a[164:167], a[56:59], v[84:87]\n"
"MM v[84:87], a[184:187], a[76:79], v[84:87]\n"
"MM v[88:91], a[168:171], a[56:59], v[88:91]\n"
"MM v[88:91], a[188:191], a[76:79], v[88:91]\n"
"MM v[92:95], a[172:175], a[56:59], v[92:95]\n"
"MM v[92:95], a[192:195], a[76:79], v[92:95]\n"
"MM v[96:99], a[176:179], a[56:59], v[96:99]\n"
"MM v[96:99], a[196:199], a[76:79], v[96:99]\n"
"MM v[100:103], a[200:203], a[40:43], v[100:103]\n"
"s_add_u32 s91, s94, 286720\n"
"MM v[100:103], a[220:223], a[60:63], v[100:103]\n"
"buffer_load_dwordx4 v[220:223], v251, s[56:59], s91 offen sc1\n"
"MM v[104:107], a[204:207], a[40:43], v[104:107]\n"
"buffer_load_dwordx4 v[224:227], v251, s[56:59], s91 offen offset:1024 sc1\n"
"MM v[104:107], a[224:227], a[60:63], v[104:107]\n"
"ds_read_b128 a[0:3], v240 offset:20480\n"
"ds_read_b128 a[4:7], v240 offset:21504\n"
"MM v[108:111], a[208:211], a[40:43], v[108:111]\n"
"buffer_load_dwordx4 v[228:231], v251, s[56:59], s91 offen offset:2048 sc1\n"
"MM v[108:111], a[228:231], a[60:63], v[108:111]\n"
"buffer_load_dwordx4 v[232:235], v251, s[56:59], s91 offen offset:3072 sc1\n"
"MM v[112:115], a[212:215], a[40:43], v[112:115]\n"
"ds_read_b128 a[8:11], v240 offset:22528\n"
"ds_read_b128 a[12:15], v240 offset:23552\n"
"MM v[112:115], a[232:235], a[60:63], v[112:115]\n"
"buffer_load_dwordx4 v[236:239], v252, s[56:59], s91 offen sc1\n"
"MM v[116:119], a[216:219], a[40:43], v[116:119]\n"
"s_add_u32 s91, s94, 250880\n"
"ds_read_b128 a[16:19], v240 offset:24576\n"
"MM v[116:119], a[236:239], a[60:63], v[116:119]\n"
"buffer_load_dwordx4 a[160:163], v251, s[56:59], s91 offen sc1\n"
"MM v[120:123], a[200:203], a[44:47], v[120:123]\n"
"buffer_load_dwordx4 a[164:167], v251, s[56:59], s91 offen offset:1024 sc1\n"
"MM v[120:123], a[220:223], a[64:67], v[120:123]\n"
"ds_read_b128 a[20:23], v240 offset:25600\n"
"MM v[124:127], a[204:207], a[44:47], v[124:127]\n"
"buffer_load_dwordx4 a[168:171], v251, s[56:59], s91 offen offset:2048 sc1\n"
"MM v[124:127], a[224:227], a[64:67], v[124:127]\n"
"ds_read_b128 a[24:27], v240 offset:26624\n"
"MM v[128:131], a[208:211], a[44:47], v[128:131]\n"
"buffer_load_dwordx4 a[172:175], v251, s[56:59], s91 offen offset:3072 sc1\n"
"MM v[128:131], a[228:231], a[64:67], v[128:131]\n"
"buffer_load_dwordx4 a[176:179], v252, s[56:59], s91 offen sc1\n"
"MM v[132:135], a[212:215], a[44:47], v[132:135]\n"
"ds_read_b128 a[28:31], v240 offset:27648\n"
"MM v[132:135], a[232:235], a[64:67], v[132:135]\n"
"s_add_u32 s91, s94, 291840\n"
"MM v[136:139], a[216:219], a[44:47], v[136:139]\n"
"buffer_load_dwordx4 a[180:183], v251, s[56:59], s91 offen sc1\n"
"MM v[136:139], a[236:239], a[64:67], v[136:139]\n"
"buffer_load_dwordx4 a[184:187], v251, s[56:59], s91 offen offset:1024 sc1\n"
"MM v[140:143], a[200:203], a[48:51], v[140:143]\n"
"ds_read_b128 a[32:35], v240 offset:28672\n"
"MM v[140:143], a[220:223], a[68:71], v[140:143]\n"
"buffer_load_dwordx4 a[188:191], v251, s[56:59], s91 offen offset:2048 sc1\n"
"MM v[144:147], a[204:207], a[48:51], v[144:147]\n"
"MM v[144:147], a[224:227], a[68:71], v[144:147]\n"
"buffer_load_dwordx4 a[192:195], v251, s[56:59], s91 offen offset:3072 sc1\n"
"MM v[148:151], a[208:211], a[48:51], v[148:151]\n"
"ds_read_b128 a[36:39], v240 offset:29696\n"
"MM v[148:151], a[228:231], a[68:71], v[148:151]\n"
"buffer_load_dwordx4 a[196:199], v252, s[56:59], s91 offen sc1\n"
"MM v[152:155], a[212:215], a[48:51], v[152:155]\n"
"buffer_load_dword v244, v243, s[56:59], 0 offen sc1\n"
"MM v[152:155], a[232:235], a[68:71], v[152:155]\n"
"MM v[156:159], a[216:219], a[48:51], v[156:159]\n"
"MM v[156:159], a[236:239], a[68:71], v[156:159]\n"
"MM v[160:163], a[200:203], a[52:55], v[160:163]\n"
"MM v[160:163], a[220:223], a[72:75], v[160:163]\n"
"MM v[164:167], a[204:207], a[52:55], v[164:167]\n"
"MM v[164:167], a[224:227], a[72:75], v[164:167]\n"
"MM v[168:171], a[208:211], a[52:55], v[168:171]\n"
"MM v[168:171], a[228:231], a[72:75], v[168:171]\n"
"MM v[172:175], a[212:215], a[52:55], v[172:175]\n"
"MM v[172:175], a[232:235], a[72:75], v[172:175]\n"
"MM v[176:179], a[216:219], a[52:55], v[176:179]\n"
"MM v[176:179], a[236:239], a[72:75], v[176:179]\n"
"MM v[180:183], a[200:203], a[56:59], v[180:183]\n"
"MM v[180:183], a[220:223], a[76:79], v[180:183]\n"
"MM v[184:187], a[204:207], a[56:59], v[184:187]\n"
"MM v[184:187], a[224:227], a[76:79], v[184:187]\n"
"MM v[188:191], a[208:211], a[56:59], v[188:191]\n"
"MM v[188:191], a[228:231], a[76:79], v[188:191]\n"
"MM v[192:195], a[212:215], a[56:59], v[192:195]\n"
"MM v[192:195], a[232:235], a[76:79], v[192:195]\n"
"MM v[196:199], a[216:219], a[56:59], v[196:199]\n"
"MM v[196:199], a[236:239], a[76:79], v[196:199]\n"
"s_mov_b32 s90, 262144\n"
"s_mov_b32 s96, 0x100\n"
"s_mov_b32 s97, 0x0\n"
"s_waitcnt vmcnt(0)\n"
"v_cmp_ne_u32 vcc, v244, v254\n"
"s_nop 1\n"
"s_andn2_b64 vcc, s[96:97], vcc\n"
"s_cbranch_scc0 K1_FP_OK\n"
"K1_FP_L:\n"
"s_nop 4\n"
"buffer_load_dword v244, v243, s[56:59], 0 offen sc1\n"
"s_waitcnt vmcnt(0)\n"
"v_cmp_ne_u32 vcc, v244, v254\n"
"s_nop 1\n"
"s_andn2_b64 vcc, s[96:97], vcc\n"
"s_cbranch_scc0 K1_FP_OK\n"
"s_sleep 2\n"
"s_sub_u32 s90, s90, 1\n"
"s_cmp_lg_u32 s90, 0\n"
"s_cbranch_scc1 K1_FP_L\n"
"K1_FP_OK:\n"
"s_nop 1\n"
"s_waitcnt lgkmcnt(0)\n"
"MM v[0:3], a[80:83], a[0:3], v[0:3]\n"
"s_xor_b32 s40, s65, 8\n"
"s_mul_i32 s40, s40, 40960\n"
"MM v[0:3], a[100:103], a[20:23], v[0:3]\n"
"s_mul_i32 s41, s60, 10240\n"
"s_add_u32 s40, s40, s41\n"
"MM v[4:7], a[84:87], a[0:3], v[4:7]\n"
"s_add_u32 s41, s41, s77\n"
"s_add_u32 m0, s41, 73728\n"
"MM v[4:7], a[104:107], a[20:23], v[4:7]\n"
"buffer_load_dwordx4 v249, s[56:59], s40 offen sc1 lds\n"
"MM v[8:11], a[88:91], a[0:3], v[8:11]\n"
"MM v[8:11], a[108:111], a[20:23], v[8:11]\n"
"MM v[12:15], a[92:95], a[0:3], v[12:15]\n"
"buffer_load_dwordx4 v249, s[56:59], s40 offen offset:1024 sc1 lds\n"
"MM v[12:15], a[112:115], a[20:23], v[12:15]\n"
"MM v[16:19], a[96:99], a[0:3], v[16:19]\n"
"MM v[16:19], a[116:119], a[20:23], v[16:19]\n"
"MM v[20:23], a[80:83], a[4:7], v[20:23]\n"
"buffer_load_dwordx4 v249, s[56:59], s40 offen offset:2048 sc1 lds\n"
"MM v[20:23], a[100:103], a[24:27], v[20:23]\n"
"MM v[24:27], a[84:87], a[4:7], v[24:27]\n"
"MM v[24:27], a[104:107], a[24:27], v[24:27]\n"
"MM v[28:31], a[88:91], a[4:7], v[28:31]\n"
"buffer_load_dwordx4 v249, s[56:59], s40 offen offset:3072 sc1 lds\n"
"MM v[28:31], a[108:111], a[24:27], v[28:31]\n"
"MM v[32:35], a[92:95], a[4:7], v[32:35]\n"
"MM v[32:35], a[112:115], a[24:27], v[32:35]\n"
"MM v[36:39], a[96:99], a[4:7], v[36:39]\n"
"s_add_u32 m0, s41, 77824\n"
"MM v[36:39], a[116:119], a[24:27], v[36:39]\n"
"s_add_u32 s40, s40, 4096\n"
"MM v[40:43], a[80:83], a[8:11], v[40:43]\n"
"buffer_load_dwordx4 v249, s[56:59], s40 offen sc1 lds\n"
"MM v[40:43], a[100:103], a[28:31], v[40:43]\n"
"MM v[44:47], a[84:87], a[8:11], v[44:47]\n"
"MM v[44:47], a[104:107], a[28:31], v[44:47]\n"
"MM v[48:51], a[88:91], a[8:11], v[48:51]\n"
"buffer_load_dwordx4 v249, s[56:59], s40 offen offset:1024 sc1 lds\n"
"MM v[48:51], a[108:111], a[28:31], v[48:51]\n"
"MM v[52:55], a[92:95], a[8:11], v[52:55]\n"
"MM v[52:55], a[112:115], a[28:31], v[52:55]\n"
"MM v[56:59], a[96:99], a[8:11], v[56:59]\n"
"MM v[56:59], a[116:119], a[28:31], v[56:59]\n"
"buffer_load_dwordx4 v249, s[56:59], s40 offen offset:2048 sc1 lds\n"
"MM v[60:63], a[80:83], a[12:15], v[60:63]\n"
"MM v[60:63], a[100:103], a[32:35], v[60:63]\n"
"MM v[64:67], a[84:87], a[12:15], v[64:67]\n"
"buffer_load_dwordx4 v249, s[56:59], s40 offen offset:3072 sc1 lds\n"
"MM v[64:67], a[104:107], a[32:35], v[64:67]\n"
"MM v[68:71], a[88:91], a[12:15], v[68:71]\n"
"MM v[68:71], a[108:111], a[32:35], v[68:71]\n"
"MM v[72:75], a[92:95], a[12:15], v[72:75]\n"
"s_add_u32 m0, s41, 81920\n"
"MM v[72:75], a[112:115], a[32:35], v[72:75]\n"
"s_add_u32 s40, s40, 4096\n"
"MM v[76:79], a[96:99], a[12:15], v[76:79]\n"
"buffer_load_dwordx4 v249, s[56:59], s40 offen sc1 lds\n"
"MM v[76:79], a[116:119], a[32:35], v[76:79]\n"
"MM v[80:83], a[80:83], a[16:19], v[80:83]\n"
"MM v[80:83], a[100:103], a[36:39], v[80:83]\n"
"MM v[84:87], a[84:87], a[16:19], v[84:87]\n"
"buffer_load_dwordx4 v249, s[56:59], s40 offen offset:1024 sc1 lds\n"
"MM v[84:87], a[104:107], a[36:39], v[84:87]\n"
"MM v[88:91], a[88:91], a[16:19], v[88:91]\n"
"MM v[88:91], a[108:111], a[36:39], v[88:91]\n"
"MM v[92:95], a[92:95], a[16:19], v[92:95]\n"
"MM v[92:95], a[112:115], a[36:39], v[92:95]\n"
"MM v[96:99], a[96:99], a[16:19], v[96:99]\n"
"MM v[96:99], a[116:119], a[36:39], v[96:99]\n"
"MM v[100:103], a[120:123], a[0:3], v[100:103]\n"
"s_add_u32 s91, s95, 0\n"
"s_mov_b64 s[36:37], s[92:93]\n"
"MM v[100:103], a[140:143], a[20:23], v[100:103]\n"
"buffer_load_dwordx4 a[200:203], v251, s[56:59], s91 offen sc1\n"
"MM v[104:107], a[124:127], a[0:3], v[104:107]\n"
"ds_read_b128 a[40:43], v240 offset:30720\n"
"s_and_b32 s37, s37, 0xffff\n"
"MM v[104:107], a[144:147], a[20:23], v[104:107]\n"
"buffer_load_dwordx4 a[204:207], v251, s[56:59], s91 offen offset:1024 sc1\n"
"MM v[108:111], a[128:131], a[0:3], v[108:111]\n"
"ds_read_b128 a[44:47], v240 offset:31744\n"
"ds_read_b128 a[48:51], v240 offset:32768\n"
"MM v[108:111], a[148:151], a[20:23], v[108:111]\n"
"s_mov_b32 s38, 3276800\n"
"ds_read_b128 a[52:55], v240 offset:33792\n"
"MM v[112:115], a[132:135], a[0:3], v[112:115]\n"
"buffer_load_dwordx4 a[208:211], v251, s[56:59], s91 offen offset:2048 sc1\n"
"MM v[112:115], a[152:155], a[20:23], v[112:115]\n"
"s_mov_b32 s39, 0x20000\n"
"ds_read_b128 a[56:59], v240 offset:34816\n"
"MM v[116:119], a[136:139], a[0:3], v[116:119]\n"
"buffer_load_dwordx4 a[212:215], v251, s[56:59], s91 offen offset:3072 sc1\n"
"MM v[116:119], a[156:159], a[20:23], v[116:119]\n"
"v_and_b32 v246, 15, v253\n"
"ds_read_b128 a[60:63], v240 offset:35840\n"
"MM v[120:123], a[120:123], a[4:7], v[120:123]\n"
"MM v[120:123], a[140:143], a[24:27], v[120:123]\n"
"buffer_load_dwordx4 a[216:219], v252, s[56:59], s91 offen sc1\n"
"MM v[124:127], a[124:127], a[4:7], v[124:127]\n"
"v_mul_u32_u24 v246, 160, v246\n"
"ds_read_b128 a[64:67], v240 offset:36864\n"
"MM v[124:127], a[144:147], a[24:27], v[124:127]\n"
"MM v[128:131], a[128:131], a[4:7], v[128:131]\n"
"s_add_u32 s91, s95, 40960\n"
"MM v[128:131], a[148:151], a[24:27], v[128:131]\n"
"buffer_load_dwordx4 a[220:223], v251, s[56:59], s91 offen sc1\n"
"MM v[132:135], a[132:135], a[4:7], v[132:135]\n"
"ds_read_b128 a[68:71], v240 offset:37888\n"
"v_lshrrev_b32 v247, 4, v253\n"
"MM v[132:135], a[152:155], a[24:27], v[132:135]\n"
"MM v[136:139], a[136:139], a[4:7], v[136:139]\n"
"buffer_load_dwordx4 a[224:227], v251, s[56:59], s91 offen offset:1024 sc1\n"
"MM v[136:139], a[156:159], a[24:27], v[136:139]\n"
"MM v[140:143], a[120:123], a[8:11], v[140:143]\n"
"ds_read_b128 a[72:75], v240 offset:38912\n"
"MM v[140:143], a[140:143], a[28:31], v[140:143]\n"
"buffer_load_dwordx4 a[228:231], v251, s[56:59], s91 offen offset:2048 sc1\n"
"MM v[144:147], a[124:127], a[8:11], v[144:147]\n"
"v_lshl_add_u32 v246, v247, 3, v246\n"
"MM v[144:147], a[144:147], a[28:31], v[144:147]\n"
"MM v[148:151], a[128:131], a[8:11], v[148:151]\n"
"buffer_load_dwordx4 a[232:235], v251, s[56:59], s91 offen offset:3072 sc1\n"
"MM v[148:151], a[148:151], a[28:31], v[148:151]\n"
"ds_read_b128 a[76:79], v240 offset:39936\n"
"MM v[152:155], a[132:135], a[8:11], v[152:155]\n"
"MM v[152:155], a[152:155], a[28:31], v[152:155]\n"
"buffer_load_dwordx4 a[236:239], v252, s[56:59], s91 offen sc1\n"
"MM v[156:159], a[136:139], a[8:11], v[156:159]\n"
"v_add_u32 v250, s77, v246\n"
"MM v[156:159], a[156:159], a[28:31], v[156:159]\n"
"MM v[160:163], a[120:123], a[12:15], v[160:163]\n"
"s_add_u32 s91, s95, 81920\n"
"MM v[160:163], a[140:143], a[32:35], v[160:163]\n"
"buffer_load_dwordx4 a[80:83], v251, s[56:59], s91 offen sc1\n"
"MM v[164:167], a[124:127], a[12:15], v[164:167]\n"
"MM v[164:167], a[144:147], a[32:35], v[164:167]\n"
"MM v[168:171], a[128:131], a[12:15], v[168:171]\n"
"buffer_load_dwordx4 a[84:87], v251, s[56:59], s91 offen offset:1024 sc1\n"
"MM v[168:171], a[148:151], a[32:35], v[168:171]\n"
"MM v[172:175], a[132:135], a[12:15], v[172:175]\n"
"MM v[172:175], a[152:155], a[32:35], v[172:175]\n"
"MM v[176:179], a[136:139], a[12:15], v[176:179]\n"
"buffer_load_dwordx4 a[88:91], v251, s[56:59], s91 offen offset:2048 sc1\n"
"MM v[176:179], a[156:159], a[32:35], v[176:179]\n"
"MM v[180:183], a[120:123], a[16:19], v[180:183]\n"
"MM v[180:183], a[140:143], a[36:39], v[180:183]\n"
"buffer_load_dwordx4 a[92:95], v251, s[56:59], s91 offen offset:3072 sc1\n"
"MM v[184:187], a[124:127], a[16:19], v[184:187]\n"
"MM v[184:187], a[144:147], a[36:39], v[184:187]\n"
"MM v[188:191], a[128:131], a[16:19], v[188:191]\n"
"buffer_load_dwordx4 a[96:99], v252, s[56:59], s91 offen sc1\n"
"MM v[188:191], a[148:151], a[36:39], v[188:191]\n"
"MM v[192:195], a[132:135], a[16:19], v[192:195]\n"
"MM v[192:195], a[152:155], a[36:39], v[192:195]\n"
"MM v[196:199], a[136:139], a[16:19], v[196:199]\n"
"MM v[196:199], a[156:159], a[36:39], v[196:199]\n"
"s_waitcnt lgkmcnt(0)\n"
"MM v[0:3], v[200:203], a[40:43], v[0:3]\n"
"s_add_u32 s91, s95, 122880\n"
"v_and_b32 v246, 15, v253\n"
"MM v[0:3], v[220:223], a[60:63], v[0:3]\n"
"buffer_load_dwordx4 a[100:103], v251, s[56:59], s91 offen sc1\n"
"MM v[4:7], v[204:207], a[40:43], v[4:7]\n"
"v_lshrrev_b32 v247, 2, v246\n"
"v_lshrrev_b32 v248, 4, v253\n"
"MM v[4:7], v[224:227], a[60:63], v[4:7]\n"
"buffer_load_dwordx4 a[104:107], v251, s[56:59], s91 offen offset:1024 sc1\n"
"MM v[8:11], v[208:211], a[40:43], v[8:11]\n"
"v_cmp_eq_u32 vcc, v247, v248\n"
"v_and_b32 v247, 1, v246\n"
"MM v[8:11], v[228:231], a[60:63], v[8:11]\n"
"buffer_load_dwordx4 a[108:111], v251, s[56:59], s91 offen offset:2048 sc1\n"
"MM v[12:15], v[212:215], a[40:43], v[12:15]\n"
"v_lshlrev_b32 v247, 4, v247\n"
"v_mov_b32 v248, 0x3f80\n"
"MM v[12:15], v[232:235], a[60:63], v[12:15]\n"
"buffer_load_dwordx4 a[112:115], v251, s[56:59], s91 offen offset:3072 sc1\n"
"MM v[16:19], v[216:219], a[40:43], v[16:19]\n"
"MM v[16:19], v[236:239], a[60:63], v[16:19]\n"
"v_lshlrev_b32 v247, v247, v248\n"
"MM v[20:23], v[200:203], a[44:47], v[20:23]\n"
"MM v[20:23], v[220:223], a[64:67], v[20:23]\n"
"buffer_load_dwordx4 a[116:119], v252, s[56:59], s91 offen sc1\n"
"MM v[24:27], v[204:207], a[44:47], v[24:27]\n"
"v_mov_b32 v248, 0\n"
"MM v[24:27], v[224:227], a[64:67], v[24:27]\n"
"v_cndmask_b32 v247, v248, v247, vcc\n"
"MM v[28:31], v[208:211], a[44:47], v[28:31]\n"
"s_add_u32 s91, s95, 163840\n"
"MM v[28:31], v[228:231], a[64:67], v[28:31]\n"
"buffer_load_dwordx4 a[120:123], v251, s[56:59], s91 offen sc1\n"
"MM v[32:35], v[212:215], a[44:47], v[32:35]\n"
"v_and_b32 v246, 2, v246\n"
"MM v[32:35], v[232:235], a[64:67], v[32:35]\n"
"MM v[36:39], v[216:219], a[44:47], v[36:39]\n"
"buffer_load_dwordx4 a[124:127], v251, s[56:59], s91 offen offset:1024 sc1\n"
"MM v[36:39], v[236:239], a[64:67], v[36:39]\n"
"v_cmp_eq_u32 vcc, 0, v246\n"
"MM v[40:43], v[200:203], a[48:51], v[40:43]\n"
"s_nop 0\n"
"v_cndmask_b32 v246, v248, v247, vcc\n"
"MM v[40:43], v[220:223], a[68:71], v[40:43]\n"
"buffer_load_dwordx4 a[128:131], v251, s[56:59], s91 offen offset:2048 sc1\n"
"MM v[44:47], v[204:207], a[48:51], v[44:47]\n"
"v_cndmask_b32 v247, v247, v248, vcc\n"
"MM v[44:47], v[224:227], a[68:71], v[44:47]\n"
"MM v[48:51], v[208:211], a[48:51], v[48:51]\n"
"buffer_load_dwordx4 a[132:135], v251, s[56:59], s91 offen offset:3072 sc1\n"
"MM v[48:51], v[228:231], a[68:71], v[48:51]\n"
"v_accvgpr_write_b32 a248, v246\n"
"MM v[52:55], v[212:215], a[48:51], v[52:55]\n"
"MM v[52:55], v[232:235], a[68:71], v[52:55]\n"
"v_accvgpr_write_b32 a249, v247\n"
"MM v[56:59], v[216:219], a[48:51], v[56:59]\n"
"buffer_load_dwordx4 a[136:139], v252, s[56:59], s91 offen sc1\n"
"MM v[56:59], v[236:239], a[68:71], v[56:59]\n"
"v_accvgpr_write_b32 a250, v248\n"
"MM v[60:63], v[200:203], a[52:55], v[60:63]\n"
"s_add_u32 s91, s95, 204800\n"
"MM v[60:63], v[220:223], a[72:75], v[60:63]\n"
"buffer_load_dwordx4 a[140:143], v251, s[56:59], s91 offen sc1\n"
"MM v[64:67], v[204:207], a[52:55], v[64:67]\n"
"v_accvgpr_write_b32 a251, v248\n"
"MM v[64:67], v[224:227], a[72:75], v[64:67]\n"
"MM v[68:71], v[208:211], a[52:55], v[68:71]\n"
"v_accvgpr_write_b32 a252, v248\n"
"MM v[68:71], v[228:231], a[72:75], v[68:71]\n"
"buffer_load_dwordx4 a[144:147], v251, s[56:59], s91 offen offset:1024 sc1\n"
"MM v[72:75], v[212:215], a[52:55], v[72:75]\n"
"v_accvgpr_write_b32 a253, v248\n"
"MM v[72:75], v[232:235], a[72:75], v[72:75]\n"
"MM v[76:79], v[216:219], a[52:55], v[76:79]\n"
"buffer_load_dwordx4 a[148:151], v251, s[56:59], s91 offen offset:2048 sc1\n"
"MM v[76:79], v[236:239], a[72:75], v[76:79]\n"
"v_accvgpr_write_b32 a254, v246\n"
"MM v[80:83], v[200:203], a[56:59], v[80:83]\n"
"v_accvgpr_write_b32 a255, v247\n"
"MM v[80:83], v[220:223], a[76:79], v[80:83]\n"
"buffer_load_dwordx4 a[152:155], v251, s[56:59], s91 offen offset:3072 sc1\n"
"MM v[84:87], v[204:207], a[56:59], v[84:87]\n"
"MM v[84:87], v[224:227], a[76:79], v[84:87]\n"
"MM v[88:91], v[208:211], a[56:59], v[88:91]\n"
"buffer_load_dwordx4 a[156:159], v252, s[56:59], s91 offen sc1\n"
"MM v[88:91], v[228:231], a[76:79], v[88:91]\n"
"MM v[92:95], v[212:215], a[56:59], v[92:95]\n"
"MM v[92:95], v[232:235], a[76:79], v[92:95]\n"
"MM v[96:99], v[216:219], a[56:59], v[96:99]\n"
"MM v[96:99], v[236:239], a[76:79], v[96:99]\n"
"s_waitcnt vmcnt(30)\n"
"v_mov_b32 v248, 40\n"
"ds_write_b32 v242, v248 offset:0\n"
"MM v[100:103], a[160:163], a[40:43], v[100:103]\n"
"s_add_u32 s91, s95, 245760\n"
"MM v[100:103], a[180:183], a[60:63], v[100:103]\n"
"buffer_load_dwordx4 v[200:203], v251, s[56:59], s91 offen sc1\n"
"MM v[104:107], a[164:167], a[40:43], v[104:107]\n"
"buffer_load_dwordx4 v[204:207], v251, s[56:59], s91 offen offset:1024 sc1\n"
"MM v[104:107], a[184:187], a[60:63], v[104:107]\n"
"MM v[108:111], a[168:171], a[40:43], v[108:111]\n"
"MM v[108:111], a[188:191], a[60:63], v[108:111]\n"
"MM v[112:115], a[172:175], a[40:43], v[112:115]\n"
"ds_read_b32 v248, v245 offset:0\n"
"MM v[112:115], a[192:195], a[60:63], v[112:115]\n"
"buffer_load_dwordx4 v[208:211], v251, s[56:59], s91 offen offset:2048 sc1\n"
"MM v[116:119], a[176:179], a[40:43], v[116:119]\n"
"MM v[116:119], a[196:199], a[60:63], v[116:119]\n"
"MM v[120:123], a[160:163], a[44:47], v[120:123]\n"
"MM v[120:123], a[180:183], a[64:67], v[120:123]\n"
"buffer_load_dwordx4 v[212:215], v251, s[56:59], s91 offen offset:3072 sc1\n"
"MM v[124:127], a[164:167], a[44:47], v[124:127]\n"
"s_waitcnt lgkmcnt(0)\n"
"v_cmp_gt_u32 vcc, 40, v248\n"
"MM v[124:127], a[184:187], a[64:67], v[124:127]\n"
"MM v[128:131], a[168:171], a[44:47], v[128:131]\n"
"s_cbranch_vccnz K1_XG_S\n"
"K1_XG_R:\n"
"MM v[128:131], a[188:191], a[64:67], v[128:131]\n"
"ds_read_b128 a[0:3], v241 offset:0\n"
"ds_read_b128 a[4:7], v241 offset:1024\n"
"MM v[132:135], a[172:175], a[44:47], v[132:135]\n"
"ds_read_b128 a[8:11], v241 offset:2048\n"
"ds_read_b128 a[12:15], v241 offset:3072\n"
"MM v[132:135], a[192:195], a[64:67], v[132:135]\n"
"buffer_load_dwordx4 v[216:219], v252, s[56:59], s91 offen sc1\n"
"MM v[136:139], a[176:179], a[44:47], v[136:139]\n"
"ds_read_b128 a[16:19], v241 offset:4096\n"
"MM v[136:139], a[196:199], a[64:67], v[136:139]\n"
"ds_read_b128 a[20:23], v241 offset:5120\n"
"MM v[140:143], a[160:163], a[48:51], v[140:143]\n"
"MM v[140:143], a[180:183], a[68:71], v[140:143]\n"
"s_add_u32 s91, s95, 286720\n"
"MM v[144:147], a[164:167], a[48:51], v[144:147]\n"
"buffer_load_dwordx4 v[220:223], v251, s[56:59], s91 offen sc1\n"
"MM v[144:147], a[184:187], a[68:71], v[144:147]\n"
"ds_read_b128 a[24:27], v241 offset:6144\n"
"MM v[148:151], a[168:171], a[48:51], v[148:151]\n"
"ds_read_b128 a[28:31], v241 offset:7168\n"
"MM v[148:151], a[188:191], a[68:71], v[148:151]\n"
"MM v[152:155], a[172:175], a[48:51], v[152:155]\n"
"MM v[152:155], a[192:195], a[68:71], v[152:155]\n"
"buffer_load_dwordx4 v[224:227], v251, s[56:59], s91 offen offset:1024 sc1\n"
"MM v[156:159], a[176:179], a[48:51], v[156:159]\n"
"ds_read_b128 a[32:35], v241 offset:8192\n"
"MM v[156:159], a[196:199], a[68:71], v[156:159]\n"
"ds_read_b128 a[36:39], v241 offset:9216\n"
"MM v[160:163], a[160:163], a[52:55], v[160:163]\n"
"MM v[160:163], a[180:183], a[72:75], v[160:163]\n"
"MM v[164:167], a[164:167], a[52:55], v[164:167]\n"
"buffer_load_dwordx4 v[228:231], v251, s[56:59], s91 offen offset:2048 sc1\n"
"MM v[164:167], a[184:187], a[72:75], v[164:167]\n"
"MM v[168:171], a[168:171], a[52:55], v[168:171]\n"
"MM v[168:171], a[188:191], a[72:75], v[168:171]\n"
"MM v[172:175], a[172:175], a[52:55], v[172:175]\n"
"buffer_load_dwordx4 v[232:235], v251, s[56:59], s91 offen offset:3072 sc1\n"
"MM v[172:175], a[192:195], a[72:75], v[172:175]\n"
"MM v[176:179], a[176:179], a[52:55], v[176:179]\n"
"MM v[176:179], a[196:199], a[72:75], v[176:179]\n"
"MM v[180:183], a[160:163], a[56:59], v[180:183]\n"
"MM v[180:183], a[180:183], a[76:79], v[180:183]\n"
"buffer_load_dwordx4 v[236:239], v252, s[56:59], s91 offen sc1\n"
"MM v[184:187], a[164:167], a[56:59], v[184:187]\n"
"MM v[184:187], a[184:187], a[76:79], v[184:187]\n"
"MM v[188:191], a[168:171], a[56:59], v[188:191]\n"
"MM v[188:191], a[188:191], a[76:79], v[188:191]\n"
"MM v[192:195], a[172:175], a[56:59], v[192:195]\n"
"MM v[192:195], a[192:195], a[76:79], v[192:195]\n"
"MM v[196:199], a[176:179], a[56:59], v[196:199]\n"
"MM v[196:199], a[196:199], a[76:79], v[196:199]\n"
"s_waitcnt lgkmcnt(0)\n"
"s_waitcnt vmcnt(30)\n"
"MM v[0:3], a[200:203], a[0:3], v[0:3]\n"
"s_add_u32 s91, s95, 5120\n"
"s_mul_i32 s40, s65, 106496\n"
"MM v[0:3], a[220:223], a[20:23], v[0:3]\n"
"buffer_load_dwordx4 a[160:163], v251, s[56:59], s91 offen sc1\n"
"MM v[4:7], a[204:207], a[0:3], v[4:7]\n"
"ds_read_b128 a[40:43], v241 offset:10240\n"
"ds_read_b128 a[44:47], v241 offset:11264\n"
"MM v[4:7], a[224:227], a[20:23], v[4:7]\n"
"s_mul_i32 s68, s60, 26624\n"
"ds_read_b128 a[48:51], v241 offset:12288\n"
"MM v[8:11], a[208:211], a[0:3], v[8:11]\n"
"buffer_load_dwordx4 a[164:167], v251, s[56:59], s91 offen offset:1024 sc1\n"
"MM v[8:11], a[228:231], a[20:23], v[8:11]\n"
"s_add_u32 s40, s40, s68\n"
"ds_read_b128 a[52:55], v241 offset:13312\n"
"MM v[12:15], a[212:215], a[0:3], v[12:15]\n"
"s_add_u32 s40, s40, 15728640\n"
"ds_read_b128 a[56:59], v241 offset:14336\n"
"MM v[12:15], a[232:235], a[20:23], v[12:15]\n"
"buffer_load_dwordx4 a[168:171], v251, s[56:59], s91 offen offset:2048 sc1\n"
"MM v[16:19], a[216:219], a[0:3], v[16:19]\n"
"ds_read_b128 a[60:63], v241 offset:15360\n"
"MM v[16:19], a[236:239], a[20:23], v[16:19]\n"
"MM v[20:23], a[200:203], a[4:7], v[20:23]\n"
"MM v[20:23], a[220:223], a[24:27], v[20:23]\n"
"buffer_load_dwordx4 a[172:175], v251, s[56:59], s91 offen offset:3072 sc1\n"
"MM v[24:27], a[204:207], a[4:7], v[24:27]\n"
"ds_read_b128 a[64:67], v241 offset:16384\n"
"MM v[24:27], a[224:227], a[24:27], v[24:27]\n"
"MM v[28:31], a[208:211], a[4:7], v[28:31]\n"
"MM v[28:31], a[228:231], a[24:27], v[28:31]\n"
"MM v[32:35], a[212:215], a[4:7], v[32:35]\n"
"buffer_load_dwordx4 a[176:179], v252, s[56:59], s91 offen sc1\n"
"MM v[32:35], a[232:235], a[24:27], v[32:35]\n"
"ds_read_b128 a[68:71], v241 offset:17408\n"
"MM v[36:39], a[216:219], a[4:7], v[36:39]\n"
"MM v[36:39], a[236:239], a[24:27], v[36:39]\n"
"MM v[40:43], a[200:203], a[8:11], v[40:43]\n"
"ds_read_b128 a[72:75], v241 offset:18432\n"
"MM v[40:43], a[220:223], a[28:31], v[40:43]\n"
"s_add_u32 s91, s95, 46080\n"
"MM v[44:47], a[204:207], a[8:11], v[44:47]\n"
"buffer_load_dwordx4 a[180:183], v251, s[56:59], s91 offen sc1\n"
"MM v[44:47], a[224:227], a[28:31], v[44:47]\n"
"MM v[48:51], a[208:211], a[8:11], v[48:51]\n"
"ds_read_b128 a[76:79], v241 offset:19456\n"
"MM v[48:51], a[228:231], a[28:31], v[48:51]\n"
"MM v[52:55], a[212:215], a[8:11], v[52:55]\n"
"MM v[52:55], a[232:235], a[28:31], v[52:55]\n"
"buffer_load_dwordx4 a[184:187], v251, s[56:59], s91 offen offset:1024 sc1\n"
"MM v[56:59], a[216:219], a[8:11], v[56:59]\n"
"MM v[56:59], a[236:239], a[28:31], v[56:59]\n"
"MM v[60:63], a[200:203], a[12:15], v[60:63]\n"
"MM v[60:63], a[220:223], a[32:35], v[60:63]\n"
"MM v[64:67], a[204:207], a[12:15], v[64:67]\n"
"buffer_load_dwordx4 a[188:191], v251, s[56:59], s91 offen offset:2048 sc1\n"
"MM v[64:67], a[224:227], a[32:35], v[64:67]\n"
"MM v[68:71], a[208:211], a[12:15], v[68:71]\n"
"MM v[68:71], a[228:231], a[32:35], v[68:71]\n"
"MM v[72:75], a[212:215], a[12:15], v[72:75]\n"
"buffer_load_dwordx4 a[192:195], v251, s[56:59], s91 offen offset:3072 sc1\n"
"MM v[72:75], a[232:235], a[32:35], v[72:75]\n"
"MM v[76:79], a[216:219], a[12:15], v[76:79]\n"
"MM v[76:79], a[236:239], a[32:35], v[76:79]\n"
"MM v[80:83], a[200:203], a[16:19], v[80:83]\n"
"MM v[80:83], a[220:223], a[36:39], v[80:83]\n"
"buffer_load_dwordx4 a[196:199], v252, s[56:59], s91 offen sc1\n"
"MM v[84:87], a[204:207], a[16:19], v[84:87]\n"
"MM v[84:87], a[224:227], a[36:39], v[84:87]\n"
"MM v[88:91], a[208:211], a[16:19], v[88:91]\n"
"MM v[88:91], a[228:231], a[36:39], v[88:91]\n"
"MM v[92:95], a[212:215], a[16:19], v[92:95]\n"
"MM v[92:95], a[232:235], a[36:39], v[92:95]\n"
"MM v[96:99], a[216:219], a[16:19], v[96:99]\n"
"MM v[96:99], a[236:239], a[36:39], v[96:99]\n"
"s_waitcnt lgkmcnt(0)\n"
"s_waitcnt vmcnt(30)\n"
"MM v[0:3], a[80:83], a[40:43], v[0:3]\n"
"s_add_u32 s91, s95, 87040\n"
"v_bfe_u32 v246, v253, 2, 1\n"
"MM v[0:3], a[100:103], a[60:63], v[0:3]\n"
"buffer_load_dwordx4 a[200:203], v251, s[56:59], s91 offen sc1\n"
"MM v[4:7], a[84:87], a[40:43], v[4:7]\n"
"s_and_b32 s68, s87, 1\n"
"ds_read_b128 a[0:3], v241 offset:20480\n"
"MM v[4:7], a[104:107], a[60:63], v[4:7]\n"
"v_xor_b32 v246, s68, v246\n"
"ds_read_b128 a[4:7], v241 offset:21504\n"
"MM v[8:11], a[88:91], a[40:43], v[8:11]\n"
"v_bfe_u32 v247, v253, 1, 1\n"
"ds_read_b128 a[8:11], v241 offset:22528\n"
"MM v[8:11], a[108:111], a[60:63], v[8:11]\n"
"buffer_load_dwordx4 a[204:207], v251, s[56:59], s91 offen offset:1024 sc1\n"
"MM v[12:15], a[92:95], a[40:43], v[12:15]\n"
"v_and_b32 v248, 1, v253\n"
"v_lshl_add_u32 v247, v247, 3, v248\n"
"MM v[12:15], a[112:115], a[60:63], v[12:15]\n"
"ds_read_b128 a[12:15], v241 offset:23552\n"
"s_lshl_b32 s68, s60, 1\n"
"MM v[16:19], a[96:99], a[40:43], v[16:19]\n"
"buffer_load_dwordx4 a[208:211], v251, s[56:59], s91 offen offset:2048 sc1\n"
"MM v[16:19], a[116:119], a[60:63], v[16:19]\n"
"v_add_u32 v247, s68, v247\n"
"ds_read_b128 a[16:19], v241 offset:24576\n"
"MM v[20:23], a[80:83], a[44:47], v[20:23]\n"
"v_mad_u32_u24 v247, v246, 15, v247\n"
"ds_read_b128 a[20:23], v241 offset:25600\n"
"MM v[20:23], a[100:103], a[64:67], v[20:23]\n"
"v_mad_u32_u24 v246, v246, 10, 15\n"
"MM v[24:27], a[84:87], a[44:47], v[24:27]\n"
"buffer_load_dwordx4 a[212:215], v251, s[56:59], s91 offen offset:3072 sc1\n"
"MM v[24:27], a[104:107], a[64:67], v[24:27]\n"
"v_cmp_lt_u32 vcc, v247, v246\n"
"ds_read_b128 a[24:27], v241 offset:26624\n"
"MM v[28:31], a[88:91], a[44:47], v[28:31]\n"
"v_mul_u32_u24 v246, 52, v247\n"
"MM v[28:31], a[108:111], a[64:67], v[28:31]\n"
"MM v[32:35], a[92:95], a[44:47], v[32:35]\n"
"buffer_load_dwordx4 a[216:219], v252, s[56:59], s91 offen sc1\n"
"MM v[32:35], a[112:115], a[64:67], v[32:35]\n"
"v_lshrrev_b32 v246, 8, v246\n"
"ds_read_b128 a[28:31], v241 offset:27648\n"
"MM v[36:39], a[96:99], a[44:47], v[36:39]\n"
"v_mul_u32_u24 v248, 5, v246\n"
"MM v[36:39], a[116:119], a[64:67], v[36:39]\n"
"MM v[40:43], a[80:83], a[48:51], v[40:43]\n"
"v_sub_u32 v247, v247, v248\n"
"ds_read_b128 a[32:35], v241 offset:28672\n"
"MM v[40:43], a[100:103], a[68:71], v[40:43]\n"
"s_add_u32 s91, s95, 128000\n"
"MM v[44:47], a[84:87], a[48:51], v[44:47]\n"
"v_mul_u32_u24 v246, 2560, v246\n"
"MM v[44:47], a[104:107], a[68:71], v[44:47]\n"
"buffer_load_dwordx4 a[220:223], v251, s[56:59], s91 offen sc1\n"
"MM v[48:51], a[88:91], a[48:51], v[48:51]\n"
"v_lshl_add_u32 v246, v247, 5, v246\n"
"ds_read_b128 a[36:39], v241 offset:29696\n"
"MM v[48:51], a[108:111], a[68:71], v[48:51]\n"
"v_mov_b32 v247, 16384\n"
"MM v[52:55], a[92:95], a[48:51], v[52:55]\n"
"MM v[52:55], a[112:115], a[68:71], v[52:55]\n"
"buffer_load_dwordx4 a[224:227], v251, s[56:59], s91 offen offset:1024 sc1\n"
"MM v[56:59], a[96:99], a[48:51], v[56:59]\n"
"v_cndmask_b32 v246, v247, v246, vcc\n"
"MM v[56:59], a[116:119], a[68:71], v[56:59]\n"
"v_readlane_b32 s98, v246, 0\n"
"MM v[60:63], a[80:83], a[52:55], v[60:63]\n"
"MM v[60:63], a[100:103], a[72:75], v[60:63]\n"
"v_readlane_b32 s99, v246, 1\n"
"MM v[64:67], a[84:87], a[52:55], v[64:67]\n"
"buffer_load_dwordx4 a[228:231], v251, s[56:59], s91 offen offset:2048 sc1\n"
"MM v[64:67], a[104:107], a[72:75], v[64:67]\n"
"v_readlane_b32 s82, v246, 2\n"
"MM v[68:71], a[88:91], a[52:55], v[68:71]\n"
"MM v[68:71], a[108:111], a[72:75], v[68:71]\n"
"v_readlane_b32 s83, v246, 3\n"
"MM v[72:75], a[92:95], a[52:55], v[72:75]\n"
"buffer_load_dwordx4 a[232:235], v251, s[56:59], s91 offen offset:3072 sc1\n"
"MM v[72:75], a[112:115], a[72:75], v[72:75]\n"
"v_readlane_b32 s78, v246, 4\n"
"MM v[76:79], a[96:99], a[52:55], v[76:79]\n"
"v_readlane_b32 s79, v246, 5\n"
"MM v[76:79], a[116:119], a[72:75], v[76:79]\n"
"MM v[80:83], a[80:83], a[56:59], v[80:83]\n"
"v_readlane_b32 s76, v246, 6\n"
"MM v[80:83], a[100:103], a[76:79], v[80:83]\n"
"buffer_load_dwordx4 a[236:239], v252, s[56:59], s91 offen sc1\n"
"MM v[84:87], a[84:87], a[56:59], v[84:87]\n"
"v_readlane_b32 s75, v246, 7\n"
"MM v[84:87], a[104:107], a[76:79], v[84:87]\n"
"MM v[88:91], a[88:91], a[56:59], v[88:91]\n"
"MM v[88:91], a[108:111], a[76:79], v[88:91]\n"
"MM v[92:95], a[92:95], a[56:59], v[92:95]\n"
"MM v[92:95], a[112:115], a[76:79], v[92:95]\n"
"MM v[96:99], a[96:99], a[56:59], v[96:99]\n"
"MM v[96:99], a[116:119], a[76:79], v[96:99]\n"
"s_waitcnt lgkmcnt(0)\n"
"s_waitcnt vmcnt(30)\n"
"MM v[0:3], a[120:123], a[0:3], v[0:3]\n"
"s_add_u32 s91, s95, 168960\n"
"MM v[0:3], a[140:143], a[20:23], v[0:3]\n"
"buffer_load_dwordx4 a[80:83], v251, s[56:59], s91 offen sc1\n"
"MM v[4:7], a[124:127], a[0:3], v[4:7]\n"
"ds_read_b128 a[40:43], v241 offset:30720\n"
"ds_read_b128 a[44:47], v241 offset:31744\n"
"MM v[4:7], a[144:147], a[20:23], v[4:7]\n"
"ds_read_b128 a[48:51], v241 offset:32768\n"
"ds_read_b128 a[52:55], v241 offset:33792\n"
"MM v[8:11], a[128:131], a[0:3], v[8:11]\n"
"buffer_load_dwordx4 a[84:87], v251, s[56:59], s91 offen offset:1024 sc1\n"
"MM v[8:11], a[148:151], a[20:23], v[8:11]\n"
"ds_read_b128 a[56:59], v241 offset:34816\n"
"MM v[12:15], a[132:135], a[0:3], v[12:15]\n"
"MM v[12:15], a[152:155], a[20:23], v[12:15]\n"
"buffer_load_dwordx4 a[88:91], v251, s[56:59], s91 offen offset:2048 sc1\n"
"MM v[16:19], a[136:139], a[0:3], v[16:19]\n"
"ds_read_b128 a[60:63], v241 offset:35840\n"
"MM v[16:19], a[156:159], a[20:23], v[16:19]\n"
"MM v[20:23], a[120:123], a[4:7], v[20:23]\n"
"MM v[20:23], a[140:143], a[24:27], v[20:23]\n"
"buffer_load_dwordx4 a[92:95], v251, s[56:59], s91 offen offset:3072 sc1\n"
"MM v[24:27], a[124:127], a[4:7], v[24:27]\n"
"ds_read_b128 a[64:67], v241 offset:36864\n"
"MM v[24:27], a[144:147], a[24:27], v[24:27]\n"
"MM v[28:31], a[128:131], a[4:7], v[28:31]\n"
"MM v[28:31], a[148:151], a[24:27], v[28:31]\n"
"MM v[32:35], a[132:135], a[4:7], v[32:35]\n"
"buffer_load_dwordx4 a[96:99], v252, s[56:59], s91 offen sc1\n"
"MM v[32:35], a[152:155], a[24:27], v[32:35]\n"
"ds_read_b128 a[68:71], v241 offset:37888\n"
"MM v[36:39], a[136:139], a[4:7], v[36:39]\n"
"MM v[36:39], a[156:159], a[24:27], v[36:39]\n"
"MM v[40:43], a[120:123], a[8:11], v[40:43]\n"
"ds_read_b128 a[72:75], v241 offset:38912\n"
"MM v[40:43], a[140:143], a[28:31], v[40:43]\n"
"s_add_u32 s91, s95, 209920\n"
"MM v[44:47], a[124:127], a[8:11], v[44:47]\n"
"buffer_load_dwordx4 a[100:103], v251, s[56:59], s91 offen sc1\n"
"MM v[44:47], a[144:147], a[28:31], v[44:47]\n"
"MM v[48:51], a[128:131], a[8:11], v[48:51]\n"
"ds_read_b128 a[76:79], v241 offset:39936\n"
"MM v[48:51], a[148:151], a[28:31], v[48:51]\n"
"MM v[52:55], a[132:135], a[8:11], v[52:55]\n"
"MM v[52:55], a[152:155], a[28:31], v[52:55]\n"
"buffer_load_dwordx4 a[104:107], v251, s[56:59], s91 offen offset:1024 sc1\n"
"MM v[56:59], a[136:139], a[8:11], v[56:59]\n"
"MM v[56:59], a[156:159], a[28:31], v[56:59]\n"
"MM v[60:63], a[120:123], a[12:15], v[60:63]\n"
"MM v[60:63], a[140:143], a[32:35], v[60:63]\n"
"MM v[64:67], a[124:127], a[12:15], v[64:67]\n"
"buffer_load_dwordx4 a[108:111], v251, s[56:59], s91 offen offset:2048 sc1\n"
"MM v[64:67], a[144:147], a[32:35], v[64:67]\n"
"MM v[68:71], a[128:131], a[12:15], v[68:71]\n"
"MM v[68:71], a[148:151], a[32:35], v[68:71]\n"
"MM v[72:75], a[132:135], a[12:15], v[72:75]\n"
"buffer_load_dwordx4 a[112:115], v251, s[56:59], s91 offen offset:3072 sc1\n"
"MM v[72:75], a[152:155], a[32:35], v[72:75]\n"
"MM v[76:79], a[136:139], a[12:15], v[76:79]\n"
"MM v[76:79], a[156:159], a[32:35], v[76:79]\n"
"MM v[80:83], a[120:123], a[16:19], v[80:83]\n"
"MM v[80:83], a[140:143], a[36:39], v[80:83]\n"
"buffer_load_dwordx4 a[116:119], v252, s[56:59], s91 offen sc1\n"
"MM v[84:87], a[124:127], a[16:19], v[84:87]\n"
"MM v[84:87], a[144:147], a[36:39], v[84:87]\n"
"MM v[88:91], a[128:131], a[16:19], v[88:91]\n"
"MM v[88:91], a[148:151], a[36:39], v[88:91]\n"
"MM v[92:95], a[132:135], a[16:19], v[92:95]\n"
"MM v[92:95], a[152:155], a[36:39], v[92:95]\n"
"MM v[96:99], a[136:139], a[16:19], v[96:99]\n"
"MM v[96:99], a[156:159], a[36:39], v[96:99]\n"
"s_waitcnt lgkmcnt(0)\n"
"s_waitcnt vmcnt(30)\n"
"MM v[0:3], v[200:203], a[40:43], v[0:3]\n"
"s_add_u32 s91, s95, 250880\n"
"MM v[0:3], v[220:223], a[60:63], v[0:3]\n"
"buffer_load_dwordx4 a[120:123], v251, s[56:59], s91 offen sc1\n"
"MM v[4:7], v[204:207], a[40:43], v[4:7]\n"
"ds_read_b128 a[0:3], v241 offset:0\n"
"ds_read_b128 a[4:7], v241 offset:1024\n"
"MM v[4:7], v[224:227], a[60:63], v[4:7]\n"
"ds_read_b128 a[8:11], v241 offset:2048\n"
"ds_read_b128 a[12:15], v241 offset:3072\n"
"MM v[8:11], v[208:211], a[40:43], v[8:11]\n"
"buffer_load_dwordx4 a[124:127], v251, s[56:59], s91 offen offset:1024 sc1\n"
"MM v[8:11], v[228:231], a[60:63], v[8:11]\n"
"ds_read_b128 a[16:19], v241 offset:4096\n"
"MM v[12:15], v[212:215], a[40:43], v[12:15]\n"
"MM v[12:15], v[232:235], a[60:63], v[12:15]\n"
"buffer_load_dwordx4 a[128:131], v251, s[56:59], s91 offen offset:2048 sc1\n"
"MM v[16:19], v[216:219], a[40:43], v[16:19]\n"
"ds_read_b128 a[20:23], v241 offset:5120\n"
"MM v[16:19], v[236:239], a[60:63], v[16:19]\n"
"MM v[20:23], v[200:203], a[44:47], v[20:23]\n"
"MM v[20:23], v[220:223], a[64:67], v[20:23]\n"
"buffer_load_dwordx4 a[132:135], v251, s[56:59], s91 offen offset:3072 sc1\n"
"MM v[24:27], v[204:207], a[44:47], v[24:27]\n"
"ds_read_b128 a[24:27], v241 offset:6144\n"
"MM v[24:27], v[224:227], a[64:67], v[24:27]\n"
"MM v[28:31], v[208:211], a[44:47], v[28:31]\n"
"MM v[28:31], v[228:231], a[64:67], v[28:31]\n"
"MM v[32:35], v[212:215], a[44:47], v[32:35]\n"
"buffer_load_dwordx4 a[136:139], v252, s[56:59], s91 offen sc1\n"
"MM v[32:35], v[232:235], a[64:67], v[32:35]\n"
"ds_read_b128 a[28:31], v241 offset:7168\n"
"MM v[36:39], v[216:219], a[44:47], v[36:39]\n"
"MM v[36:39], v[236:239], a[64:67], v[36:39]\n"
"MM v[40:43], v[200:203], a[48:51], v[40:43]\n"
"ds_read_b128 a[32:35], v241 offset:8192\n"
"MM v[40:43], v[220:223], a[68:71], v[40:43]\n"
"s_add_u32 s91, s95, 291840\n"
"MM v[44:47], v[204:207], a[48:51], v[44:47]\n"
"buffer_load_dwordx4 a[140:143], v251, s[56:59], s91 offen sc1\n"
"MM v[44:47], v[224:227], a[68:71], v[44:47]\n"
"MM v[48:51], v[208:211], a[48:51], v[48:51]\n"
"ds_read_b128 a[36:39], v241 offset:9216\n"
"MM v[48:51], v[228:231], a[68:71], v[48:51]\n"
"MM v[52:55], v[212:215], a[48:51], v[52:55]\n"
"MM v[52:55], v[232:235], a[68:71], v[52:55]\n"
"buffer_load_dwordx4 a[144:147], v251, s[56:59], s91 offen offset:1024 sc1\n"
"MM v[56:59], v[216:219], a[48:51], v[56:59]\n"
"MM v[56:59], v[236:239], a[68:71], v[56:59]\n"
"MM v[60:63], v[200:203], a[52:55], v[60:63]\n"
"MM v[60:63], v[220:223], a[72:75], v[60:63]\n"
"MM v[64:67], v[204:207], a[52:55], v[64:67]\n"
"buffer_load_dwordx4 a[148:151], v251, s[56:59], s91 offen offset:2048 sc1\n"
"MM v[64:67], v[224:227], a[72:75], v[64:67]\n"
"MM v[68:71], v[208:211], a[52:55], v[68:71]\n"
"MM v[68:71], v[228:231], a[72:75], v[68:71]\n"
"MM v[72:75], v[212:215], a[52:55], v[72:75]\n"
"buffer_load_dwordx4 a[152:155], v251, s[56:59], s91 offen offset:3072 sc1\n"
"MM v[72:75], v[232:235], a[72:75], v[72:75]\n"
"MM v[76:79], v[216:219], a[52:55], v[76:79]\n"
"MM v[76:79], v[236:239], a[72:75], v[76:79]\n"
"MM v[80:83], v[200:203], a[56:59], v[80:83]\n"
"MM v[80:83], v[220:223], a[76:79], v[80:83]\n"
"buffer_load_dwordx4 a[156:159], v252, s[56:59], s91 offen sc1\n"
"MM v[84:87], v[204:207], a[56:59], v[84:87]\n"
"MM v[84:87], v[224:227], a[76:79], v[84:87]\n"
"MM v[88:91], v[208:211], a[56:59], v[88:91]\n"
"MM v[88:91], v[228:231], a[76:79], v[88:91]\n"
"MM v[92:95], v[212:215], a[56:59], v[92:95]\n"
"MM v[92:95], v[232:235], a[76:79], v[92:95]\n"
"MM v[96:99], v[216:219], a[56:59], v[96:99]\n"
"MM v[96:99], v[236:239], a[76:79], v[96:99]\n"
"s_waitcnt lgkmcnt(0)\n"
"s_waitcnt vmcnt(30)\n"
"MM v[100:103], a[160:163], a[0:3], v[100:103]\n"
"MM v[100:103], a[180:183], a[20:23], v[100:103]\n"
"ds_read_b128 a[40:43], v241 offset:10240\n"
"ds_read_b128 a[44:47], v241 offset:11264\n"
"MM v[104:107], a[164:167], a[0:3], v[104:107]\n"
"v_cvt_pk_bf16_f32 v0, v0, v1\n"
"v_cvt_pk_bf16_f32 v1, v2, v3\n"
"MM v[104:107], a[184:187], a[20:23], v[104:107]\n"
"v_cvt_pk_bf16_f32 v2, v4, v5\n"
"v_cvt_pk_bf16_f32 v3, v6, v7\n"
"MM v[108:111], a[168:171], a[0:3], v[108:111]\n"
"s_add_u32 s41, s40, 0\n"
"ds_read_b128 a[48:51], v241 offset:12288\n"
"MM v[108:111], a[188:191], a[20:23], v[108:111]\n"
"buffer_store_dwordx4 v[0:3], v249, s[56:59], s41 offen sc1\n"
"MM v[112:115], a[172:175], a[0:3], v[112:115]\n"
"ds_read_b128 a[52:55], v241 offset:13312\n"
"ds_read_b128 a[56:59], v241 offset:14336\n"
"MM v[112:115], a[192:195], a[20:23], v[112:115]\n"
"v_cvt_pk_bf16_f32 v4, v8, v9\n"
"v_cvt_pk_bf16_f32 v5, v10, v11\n"
"MM v[116:119], a[176:179], a[0:3], v[116:119]\n"
"v_cvt_pk_bf16_f32 v6, v12, v13\n"
"ds_read_b128 a[60:63], v241 offset:15360\n"
"MM v[116:119], a[196:199], a[20:23], v[116:119]\n"
"v_cvt_pk_bf16_f32 v7, v14, v15\n"
"MM v[120:123], a[160:163], a[4:7], v[120:123]\n"
"buffer_store_dwordx4 v[4:7], v249, s[56:59], s41 offen offset:1024 sc1\n"
"MM v[120:123], a[180:183], a[24:27], v[120:123]\n"
"MM v[124:127], a[164:167], a[4:7], v[124:127]\n"
"ds_read_b128 a[64:67], v241 offset:16384\n"
"MM v[124:127], a[184:187], a[24:27], v[124:127]\n"
"v_cvt_pk_bf16_f32 v8, v16, v17\n"
"MM v[128:131], a[168:171], a[4:7], v[128:131]\n"
"v_cvt_pk_bf16_f32 v9, v18, v19\n"
"MM v[128:131], a[188:191], a[24:27], v[128:131]\n"
"v_cvt_pk_bf16_f32 v10, v20, v21\n"
"MM v[132:135], a[172:175], a[4:7], v[132:135]\n"
"v_cvt_pk_bf16_f32 v11, v22, v23\n"
"ds_read_b128 a[68:71], v241 offset:17408\n"
"MM v[132:135], a[192:195], a[24:27], v[132:135]\n"
"buffer_store_dwordx4 v[8:11], v249, s[56:59], s41 offen offset:2048 sc1\n"
"MM v[136:139], a[176:179], a[4:7], v[136:139]\n"
"MM v[136:139], a[196:199], a[24:27], v[136:139]\n"
"MM v[140:143], a[160:163], a[8:11], v[140:143]\n"
"v_cvt_pk_bf16_f32 v12, v24, v25\n"
"ds_read_b128 a[72:75], v241 offset:18432\n"
"MM v[140:143], a[180:183], a[28:31], v[140:143]\n"
"v_cvt_pk_bf16_f32 v13, v26, v27\n"
"v_cvt_pk_bf16_f32 v14, v28, v29\n"
"MM v[144:147], a[164:167], a[8:11], v[144:147]\n"
"v_cvt_pk_bf16_f32 v15, v30, v31\n"
"MM v[144:147], a[184:187], a[28:31], v[144:147]\n"
"buffer_store_dwordx4 v[12:15], v249, s[56:59], s41 offen offset:3072 sc1\n"
"MM v[148:151], a[168:171], a[8:11], v[148:151]\n"
"ds_read_b128 a[76:79], v241 offset:19456\n"
"MM v[148:151], a[188:191], a[28:31], v[148:151]\n"
"MM v[152:155], a[172:175], a[8:11], v[152:155]\n"
"v_cvt_pk_bf16_f32 v16, v32, v33\n"
"MM v[152:155], a[192:195], a[28:31], v[152:155]\n"
"v_cvt_pk_bf16_f32 v17, v34, v35\n"
"MM v[156:159], a[176:179], a[8:11], v[156:159]\n"
"v_cvt_pk_bf16_f32 v18, v36, v37\n"
"MM v[156:159], a[196:199], a[28:31], v[156:159]\n"
"v_cvt_pk_bf16_f32 v19, v38, v39\n"
"MM v[160:163], a[160:163], a[12:15], v[160:163]\n"
"s_add_u32 s41, s40, 4096\n"
"MM v[160:163], a[180:183], a[32:35], v[160:163]\n"
"buffer_store_dwordx4 v[16:19], v249, s[56:59], s41 offen sc1\n"
"MM v[164:167], a[164:167], a[12:15], v[164:167]\n"
"MM v[164:167], a[184:187], a[32:35], v[164:167]\n"
"MM v[168:171], a[168:171], a[12:15], v[168:171]\n"
"v_cvt_pk_bf16_f32 v20, v40, v41\n"
"MM v[168:171], a[188:191], a[32:35], v[168:171]\n"
"v_cvt_pk_bf16_f32 v21, v42, v43\n"
"v_cvt_pk_bf16_f32 v22, v44, v45\n"
"MM v[172:175], a[172:175], a[12:15], v[172:175]\n"
"v_cvt_pk_bf16_f32 v23, v46, v47\n"
"MM v[172:175], a[192:195], a[32:35], v[172:175]\n"
"buffer_store_dwordx4 v[20:23], v249, s[56:59], s41 offen offset:1024 sc1\n"
"MM v[176:179], a[176:179], a[12:15], v[176:179]\n"
"MM v[176:179], a[196:199], a[32:35], v[176:179]\n"
"MM v[180:183], a[160:163], a[16:19], v[180:183]\n"
"v_cvt_pk_bf16_f32 v24, v48, v49\n"
"MM v[180:183], a[180:183], a[36:39], v[180:183]\n"
"v_cvt_pk_bf16_f32 v25, v50, v51\n"
"MM v[184:187], a[164:167], a[16:19], v[184:187]\n"
"v_cvt_pk_bf16_f32 v26, v52, v53\n"
"MM v[184:187], a[184:187], a[36:39], v[184:187]\n"
"v_cvt_pk_bf16_f32 v27, v54, v55\n"
"MM v[188:191], a[168:171], a[16:19], v[188:191]\n"
"buffer_store_dwordx4 v[24:27], v249, s[56:59], s41 offen offset:2048 sc1\n"
"MM v[188:191], a[188:191], a[36:39], v[188:191]\n"
"MM v[192:195], a[172:175], a[16:19], v[192:195]\n"
"MM v[192:195], a[192:195], a[36:39], v[192:195]\n"
"MM v[196:199], a[176:179], a[16:19], v[196:199]\n"
"MM v[196:199], a[196:199], a[36:39], v[196:199]\n"
"s_waitcnt lgkmcnt(0)\n"
"s_waitcnt vmcnt(27)\n"
"MM v[100:103], a[200:203], a[40:43], v[100:103]\n"
"v_cvt_pk_bf16_f32 v28, v56, v57\n"
"v_cvt_pk_bf16_f32 v29, v58, v59\n"
"MM v[100:103], a[220:223], a[60:63], v[100:103]\n"
"v_mov_b32 v30, 0\n"
"ds_read_b128 a[0:3], v241 offset:20480\n"
"MM v[104:107], a[204:207], a[40:43], v[104:107]\n"
"v_mov_b32 v31, 0\n"
"ds_read_b128 a[4:7], v241 offset:21504\n"
"MM v[104:107], a[224:227], a[60:63], v[104:107]\n"
"buffer_store_dwordx4 v[28:31], v249, s[56:59], s41 offen offset:3072 sc1\n"
"MM v[108:111], a[208:211], a[40:43], v[108:111]\n"
"ds_read_b128 a[8:11], v241 offset:22528\n"
"v_cvt_pk_bf16_f32 v32, v60, v61\n"
"MM v[108:111], a[228:231], a[60:63], v[108:111]\n"
"v_cvt_pk_bf16_f32 v33, v62, v63\n"
"v_cvt_pk_bf16_f32 v34, v64, v65\n"
"MM v[112:115], a[212:215], a[40:43], v[112:115]\n"
"v_cvt_pk_bf16_f32 v35, v66, v67\n"
"ds_read_b128 a[12:15], v241 offset:23552\n"
"MM v[112:115], a[232:235], a[60:63], v[112:115]\n"
"s_add_u32 s41, s40, 8192\n"
"ds_read_b128 a[16:19], v241 offset:24576\n"
"MM v[116:119], a[216:219], a[40:43], v[116:119]\n"
"buffer_store_dwordx4 v[32:35], v249, s[56:59], s41 offen sc1\n"
"MM v[116:119], a[236:239], a[60:63], v[116:119]\n"
"ds_read_b128 a[20:23], v241 offset:25600\n"
"MM v[120:123], a[200:203], a[44:47], v[120:123]\n"
"v_cvt_pk_bf16_f32 v36, v68, v69\n"
"v_cvt_pk_bf16_f32 v37, v70, v71\n"
"MM v[120:123], a[220:223], a[64:67], v[120:123]\n"
"v_cvt_pk_bf16_f32 v38, v72, v73\n"
"MM v[124:127], a[204:207], a[44:47], v[124:127]\n"
"v_cvt_pk_bf16_f32 v39, v74, v75\n"
"ds_read_b128 a[24:27], v241 offset:26624\n"
"MM v[124:127], a[224:227], a[64:67], v[124:127]\n"
"buffer_store_dwordx4 v[36:39], v249, s[56:59], s41 offen offset:1024 sc1\n"
"MM v[128:131], a[208:211], a[44:47], v[128:131]\n"
"MM v[128:131], a[228:231], a[64:67], v[128:131]\n"
"v_cvt_pk_bf16_f32 v40, v76, v77\n"
"MM v[132:135], a[212:215], a[44:47], v[132:135]\n"
"v_cvt_pk_bf16_f32 v41, v78, v79\n"
"ds_read_b128 a[28:31], v241 offset:27648\n"
"MM v[132:135], a[232:235], a[64:67], v[132:135]\n"
"v_cvt_pk_bf16_f32 v42, v80, v81\n"
"v_cvt_pk_bf16_f32 v43, v82, v83\n"
"MM v[136:139], a[216:219], a[44:47], v[136:139]\n"
"buffer_store_dwordx4 v[40:43], v249, s[56:59], s41 offen offset:2048 sc1\n"
"MM v[136:139], a[236:239], a[64:67], v[136:139]\n"
"MM v[140:143], a[200:203], a[48:51], v[140:143]\n"
"ds_read_b128 a[32:35], v241 offset:28672\n"
"MM v[140:143], a[220:223], a[68:71], v[140:143]\n"
"v_cvt_pk_bf16_f32 v44, v84, v85\n"
"MM v[144:147], a[204:207], a[48:51], v[144:147]\n"
"v_cvt_pk_bf16_f32 v45, v86, v87\n"
"MM v[144:147], a[224:227], a[68:71], v[144:147]\n"
"v_cvt_pk_bf16_f32 v46, v88, v89\n"
"v_cvt_pk_bf16_f32 v47, v90, v91\n"
"MM v[148:151], a[208:211], a[48:51], v[148:151]\n"
"buffer_store_dwordx4 v[44:47], v249, s[56:59], s41 offen offset:3072 sc1\n"
"MM v[148:151], a[228:231], a[68:71], v[148:151]\n"
"ds_read_b128 a[36:39], v241 offset:29696\n"
"MM v[152:155], a[212:215], a[48:51], v[152:155]\n"
"MM v[152:155], a[232:235], a[68:71], v[152:155]\n"
"v_cvt_pk_bf16_f32 v48, v92, v93\n"
"MM v[156:159], a[216:219], a[48:51], v[156:159]\n"
"v_cvt_pk_bf16_f32 v49, v94, v95\n"
"v_cvt_pk_bf16_f32 v50, v96, v97\n"
"MM v[156:159], a[236:239], a[68:71], v[156:159]\n"
"v_cvt_pk_bf16_f32 v51, v98, v99\n"
"MM v[160:163], a[200:203], a[52:55], v[160:163]\n"
"s_add_u32 s41, s40, 12288\n"
"MM v[160:163], a[220:223], a[72:75], v[160:163]\n"
"buffer_store_dwordx4 v[48:51], v249, s[56:59], s41 offen sc1\n"
"MM v[164:167], a[204:207], a[52:55], v[164:167]\n"
"MM v[164:167], a[224:227], a[72:75], v[164:167]\n"
"MM v[168:171], a[208:211], a[52:55], v[168:171]\n"
"MM v[168:171], a[228:231], a[72:75], v[168:171]\n"
"MM v[172:175], a[212:215], a[52:55], v[172:175]\n"
"MM v[172:175], a[232:235], a[72:75], v[172:175]\n"
"MM v[176:179], a[216:219], a[52:55], v[176:179]\n"
"MM v[176:179], a[236:239], a[72:75], v[176:179]\n"
"MM v[180:183], a[200:203], a[56:59], v[180:183]\n"
"MM v[180:183], a[220:223], a[76:79], v[180:183]\n"
"MM v[184:187], a[204:207], a[56:59], v[184:187]\n"
"MM v[184:187], a[224:227], a[76:79], v[184:187]\n"
"MM v[188:191], a[208:211], a[56:59], v[188:191]\n"
"MM v[188:191], a[228:231], a[76:79], v[188:191]\n"
"MM v[192:195], a[212:215], a[56:59], v[192:195]\n"
"MM v[192:195], a[232:235], a[76:79], v[192:195]\n"
"MM v[196:199], a[216:219], a[56:59], v[196:199]\n"
"MM v[196:199], a[236:239], a[76:79], v[196:199]\n"
"s_waitcnt lgkmcnt(0)\n"
"s_waitcnt vmcnt(23)\n"
"MM v[100:103], a[80:83], a[0:3], v[100:103]\n"
"s_and_b32 s68, s87, 6\n"
"s_add_u32 s68, s68, 0\n"
"MM v[100:103], a[100:103], a[20:23], v[100:103]\n"
"s_mul_i32 s68, s68, 13312\n"
"s_add_u32 s68, s68, 15728640\n"
"MM v[104:107], a[84:87], a[0:3], v[104:107]\n"
"s_and_b32 s69, s87, 1\n"
"s_xor_b32 s69, s69, 0\n"
"MM v[104:107], a[104:107], a[20:23], v[104:107]\n"
"s_mul_i32 s90, s69, s60\n"
"s_lshl_b32 s69, s69, 3\n"
"MM v[108:111], a[88:91], a[0:3], v[108:111]\n"
"s_add_u32 s69, s69, s60\n"
"ds_read_b128 a[40:43], v241 offset:30720\n"
"MM v[108:111], a[108:111], a[20:23], v[108:111]\n"
"s_lshl_b32 s69, s69, 10\n"
"s_add_u32 s68, s68, s69\n"
"MM v[112:115], a[92:95], a[0:3], v[112:115]\n"
"v_add_u32 v60, s68, v249\n"
"ds_read_b128 a[44:47], v241 offset:31744\n"
"MM v[112:115], a[112:115], a[20:23], v[112:115]\n"
"s_cmp_eq_u32 s90, 0\n"
"s_cselect_b32 s69, 0, 2080374528\n"
"MM v[116:119], a[96:99], a[0:3], v[116:119]\n"
"v_add_u32 v61, 4096, v60\n"
"v_add_u32 v61, s69, v61\n"
"MM v[116:119], a[116:119], a[20:23], v[116:119]\n"
"s_and_b32 s68, s87, 6\n"
"s_add_u32 s68, s68, 1\n"
"MM v[120:123], a[80:83], a[4:7], v[120:123]\n"
"s_mul_i32 s68, s68, 13312\n"
"s_add_u32 s68, s68, 15728640\n"
"MM v[120:123], a[100:103], a[24:27], v[120:123]\n"
"s_and_b32 s69, s87, 1\n"
"s_xor_b32 s69, s69, 1\n"
"MM v[124:127], a[84:87], a[4:7], v[124:127]\n"
"s_mul_i32 s90, s69, s60\n"
"s_lshl_b32 s69, s69, 3\n"
"MM v[124:127], a[104:107], a[24:27], v[124:127]\n"
"s_add_u32 s69, s69, s60\n"
"s_lshl_b32 s69, s69, 10\n"
"MM v[128:131], a[88:91], a[4:7], v[128:131]\n"
"s_add_u32 s68, s68, s69\n"
"v_add_u32 v62, s68, v249\n"
"MM v[128:131], a[108:111], a[24:27], v[128:131]\n"
"s_cmp_eq_u32 s90, 0\n"
"s_cselect_b32 s69, 0, 2080374528\n"
"MM v[132:135], a[92:95], a[4:7], v[132:135]\n"
"v_add_u32 v63, 4096, v62\n"
"v_add_u32 v63, s69, v63\n"
"MM v[132:135], a[112:115], a[24:27], v[132:135]\n"
"v_add_u32 v64, s98, v250\n"
"v_add_u32 v65, s99, v250\n"
"MM v[136:139], a[96:99], a[4:7], v[136:139]\n"
"v_add_u32 v66, s82, v250\n"
"v_add_u32 v67, s83, v250\n"
"MM v[136:139], a[116:119], a[24:27], v[136:139]\n"
"v_add_u32 v68, s78, v250\n"
"ds_read_b128 a[48:51], v241 offset:32768\n"
"MM v[140:143], a[80:83], a[8:11], v[140:143]\n"
"v_add_u32 v69, s79, v250\n"
"v_add_u32 v70, s76, v250\n"
"MM v[140:143], a[100:103], a[28:31], v[140:143]\n"
"v_add_u32 v71, s75, v250\n"
"s_and_b32 s68, s87, 1\n"
"MM v[144:147], a[84:87], a[8:11], v[144:147]\n"
"s_mul_i32 s68, s68, 160\n"
"v_mov_b32 v80, s68\n"
"MM v[144:147], a[104:107], a[28:31], v[144:147]\n"
"v_sub_u32 v81, 160, v80\n"
"s_lshl_b32 s68, s60, 6\n"
"MM v[148:151], a[88:91], a[8:11], v[148:151]\n"
"s_add_u32 s68, s68, 0\n"
"v_add_u32 v246, s68, v253\n"
"ds_read_b128 a[52:55], v241 offset:33792\n"
"MM v[148:151], a[108:111], a[28:31], v[148:151]\n"
"v_mul_u32_u24 v247, 6554, v246\n"
"v_lshrrev_b32 v247, 16, v247\n"
"MM v[152:155], a[92:95], a[8:11], v[152:155]\n"
"v_mul_u32_u24 v248, 10, v247\n"
"v_sub_u32 v248, v246, v248\n"
"MM v[152:155], a[112:115], a[28:31], v[152:155]\n"
"v_lshl_add_u32 v72, v246, 4, s77\n"
"v_cmp_gt_u32 vcc, 48, v247\n"
"MM v[156:159], a[96:99], a[8:11], v[156:159]\n"
"v_mul_u32_u24 v247, 1280, v247\n"
"v_lshl_add_u32 v76, v248, 4, v247\n"
"ds_read_b128 a[56:59], v241 offset:34816\n"
"MM v[156:159], a[116:119], a[28:31], v[156:159]\n"
"v_cndmask_b32 v247, v81, v80, vcc\n"
"v_add_u32 v76, v76, v247\n"
"MM v[160:163], a[80:83], a[12:15], v[160:163]\n"
"s_lshl_b32 s68, s60, 6\n"
"s_add_u32 s68, s68, 256\n"
"MM v[160:163], a[100:103], a[32:35], v[160:163]\n"
"v_add_u32 v246, s68, v253\n"
"v_mul_u32_u24 v247, 6554, v246\n"
"MM v[164:167], a[84:87], a[12:15], v[164:167]\n"
"v_lshrrev_b32 v247, 16, v247\n"
"v_mul_u32_u24 v248, 10, v247\n"
"ds_read_b128 a[60:63], v241 offset:35840\n"
"MM v[164:167], a[104:107], a[32:35], v[164:167]\n"
"v_sub_u32 v248, v246, v248\n"
"v_lshl_add_u32 v73, v246, 4, s77\n"
"MM v[168:171], a[88:91], a[12:15], v[168:171]\n"
"v_cmp_gt_u32 vcc, 48, v247\n"
"v_mul_u32_u24 v247, 1280, v247\n"
"MM v[168:171], a[108:111], a[32:35], v[168:171]\n"
"v_lshl_add_u32 v77, v248, 4, v247\n"
"v_cndmask_b32 v247, v81, v80, vcc\n"
"MM v[172:175], a[92:95], a[12:15], v[172:175]\n"
"v_add_u32 v77, v77, v247\n"
"s_lshl_b32 s68, s60, 6\n"
"ds_read_b128 a[64:67], v241 offset:36864\n"
"MM v[172:175], a[112:115], a[32:35], v[172:175]\n"
"s_add_u32 s68, s68, 512\n"
"v_add_u32 v246, s68, v253\n"
"MM v[176:179], a[96:99], a[12:15], v[176:179]\n"
"v_mul_u32_u24 v247, 6554, v246\n"
"v_lshrrev_b32 v247, 16, v247\n"
"MM v[176:179], a[116:119], a[32:35], v[176:179]\n"
"v_mul_u32_u24 v248, 10, v247\n"
"v_sub_u32 v248, v246, v248\n"
"ds_read_b128 a[68:71], v241 offset:37888\n"
"ds_read_b128 a[72:75], v241 offset:38912\n"
"ds_read_b128 a[76:79], v241 offset:39936\n"
"MM v[180:183], a[80:83], a[16:19], v[180:183]\n"
"v_lshl_add_u32 v74, v246, 4, s77\n"
"v_cmp_gt_u32 vcc, 48, v247\n"
"MM v[180:183], a[100:103], a[36:39], v[180:183]\n"
"v_mul_u32_u24 v247, 1280, v247\n"
"v_lshl_add_u32 v78, v248, 4, v247\n"
"MM v[184:187], a[84:87], a[16:19], v[184:187]\n"
"v_cndmask_b32 v247, v81, v80, vcc\n"
"v_add_u32 v78, v78, v247\n"
"MM v[184:187], a[104:107], a[36:39], v[184:187]\n"
"s_lshl_b32 s68, s60, 6\n"
"s_add_u32 s68, s68, 768\n"
"MM v[188:191], a[88:91], a[16:19], v[188:191]\n"
"v_add_u32 v246, s68, v253\n"
"v_mul_u32_u24 v247, 6554, v246\n"
"MM v[188:191], a[108:111], a[36:39], v[188:191]\n"
"v_lshrrev_b32 v247, 16, v247\n"
"v_mul_u32_u24 v248, 10, v247\n"
"MM v[192:195], a[92:95], a[16:19], v[192:195]\n"
"v_sub_u32 v248, v246, v248\n"
"v_lshl_add_u32 v75, v246, 4, s77\n"
"MM v[192:195], a[112:115], a[36:39], v[192:195]\n"
"v_cmp_gt_u32 vcc, 48, v247\n"
"v_mul_u32_u24 v247, 1280, v247\n"
"MM v[196:199], a[96:99], a[16:19], v[196:199]\n"
"v_lshl_add_u32 v79, v248, 4, v247\n"
"v_cndmask_b32 v247, v81, v80, vcc\n"
"MM v[196:199], a[116:119], a[36:39], v[196:199]\n"
"v_add_u32 v79, v79, v247\n"
"v_cmp_gt_u32 vcc, 800, v246\n"
"v_mov_b32 v247, 2147483392\n"
"s_nop 0\n"
"v_cndmask_b32 v79, v247, v79, vcc\n"
"s_waitcnt lgkmcnt(0)\n"
"s_waitcnt vmcnt(13)\n"
"MM v[100:103], a[120:123], a[40:43], v[100:103]\n"
"MM v[100:103], a[140:143], a[60:63], v[100:103]\n"
"MM v[104:107], a[124:127], a[40:43], v[104:107]\n"
"MM v[104:107], a[144:147], a[60:63], v[104:107]\n"
"MM v[108:111], a[128:131], a[40:43], v[108:111]\n"
"MM v[108:111], a[148:151], a[60:63], v[108:111]\n"
"MM v[112:115], a[132:135], a[40:43], v[112:115]\n"
"MM v[112:115], a[152:155], a[60:63], v[112:115]\n"
"MM v[116:119], a[136:139], a[40:43], v[116:119]\n"
"MM v[116:119], a[156:159], a[60:63], v[116:119]\n"
"MM v[120:123], a[120:123], a[44:47], v[120:123]\n"
"MM v[120:123], a[140:143], a[64:67], v[120:123]\n"
"MM v[124:127], a[124:127], a[44:47], v[124:127]\n"
"MM v[124:127], a[144:147], a[64:67], v[124:127]\n"
"MM v[128:131], a[128:131], a[44:47], v[128:131]\n"
"MM v[128:131], a[148:151], a[64:67], v[128:131]\n"
"MM v[132:135], a[132:135], a[44:47], v[132:135]\n"
"MM v[132:135], a[152:155], a[64:67], v[132:135]\n"
"MM v[136:139], a[136:139], a[44:47], v[136:139]\n"
"MM v[136:139], a[156:159], a[64:67], v[136:139]\n"
"MM v[140:143], a[120:123], a[48:51], v[140:143]\n"
"MM v[140:143], a[140:143], a[68:71], v[140:143]\n"
"MM v[144:147], a[124:127], a[48:51], v[144:147]\n"
"MM v[144:147], a[144:147], a[68:71], v[144:147]\n"
"MM v[148:151], a[128:131], a[48:51], v[148:151]\n"
"MM v[148:151], a[148:151], a[68:71], v[148:151]\n"
"MM v[152:155], a[132:135], a[48:51], v[152:155]\n"
"MM v[152:155], a[152:155], a[68:71], v[152:155]\n"
"MM v[156:159], a[136:139], a[48:51], v[156:159]\n"
"MM v[156:159], a[156:159], a[68:71], v[156:159]\n"
"MM v[160:163], a[120:123], a[52:55], v[160:163]\n"
"MM v[160:163], a[140:143], a[72:75], v[160:163]\n"
"MM v[164:167], a[124:127], a[52:55], v[164:167]\n"
"MM v[164:167], a[144:147], a[72:75], v[164:167]\n"
"MM v[168:171], a[128:131], a[52:55], v[168:171]\n"
"MM v[168:171], a[148:151], a[72:75], v[168:171]\n"
"MM v[172:175], a[132:135], a[52:55], v[172:175]\n"
"MM v[172:175], a[152:155], a[72:75], v[172:175]\n"
"MM v[176:179], a[136:139], a[52:55], v[176:179]\n"
"MM v[176:179], a[156:159], a[72:75], v[176:179]\n"
"MM v[180:183], a[120:123], a[56:59], v[180:183]\n"
"MM v[180:183], a[140:143], a[76:79], v[180:183]\n"
"MM v[184:187], a[124:127], a[56:59], v[184:187]\n"
"MM v[184:187], a[144:147], a[76:79], v[184:187]\n"
"MM v[188:191], a[128:131], a[56:59], v[188:191]\n"
"MM v[188:191], a[148:151], a[76:79], v[188:191]\n"
"MM v[192:195], a[132:135], a[56:59], v[192:195]\n"
"MM v[192:195], a[152:155], a[76:79], v[192:195]\n"
"MM v[196:199], a[136:139], a[56:59], v[196:199]\n"
"MM v[196:199], a[156:159], a[76:79], v[196:199]\n"
"s_waitcnt vmcnt(0)\n"
"v_mov_b32 v248, 41\n"
"ds_write_b32 v242, v248 offset:0\n"
"v_cvt_pk_bf16_f32 v100, v100, v101\n"
"v_cvt_pk_bf16_f32 v101, v102, v103\n"
"v_cvt_pk_bf16_f32 v102, v104, v105\n"
"v_cvt_pk_bf16_f32 v103, v106, v107\n"
"s_add_u32 s41, s40, 13312\n"
"buffer_store_dwordx4 v[100:103], v249, s[56:59], s41 offen sc1\n"
"v_cvt_pk_bf16_f32 v104, v108, v109\n"
"v_cvt_pk_bf16_f32 v105, v110, v111\n"
"v_cvt_pk_bf16_f32 v106, v112, v113\n"
"v_cvt_pk_bf16_f32 v107, v114, v115\n"
"buffer_store_dwordx4 v[104:107], v249, s[56:59], s41 offen offset:1024 sc1\n"
"v_cvt_pk_bf16_f32 v108, v116, v117\n"
"v_cvt_pk_bf16_f32 v109, v118, v119\n"
"v_cvt_pk_bf16_f32 v110, v120, v121\n"
"v_cvt_pk_bf16_f32 v111, v122, v123\n"
"buffer_store_dwordx4 v[108:111], v249, s[56:59], s41 offen offset:2048 sc1\n"
"ds_read_b32 v248, v245 offset:0\n"
"v_cvt_pk_bf16_f32 v112, v124, v125\n"
"v_cvt_pk_bf16_f32 v113, v126, v127\n"
"v_cvt_pk_bf16_f32 v114, v128, v129\n"
"v_cvt_pk_bf16_f32 v115, v130, v131\n"
"buffer_store_dwordx4 v[112:115], v249, s[56:59], s41 offen offset:3072 sc1\n"
"v_cvt_pk_bf16_f32 v116, v132, v133\n"
"v_cvt_pk_bf16_f32 v117, v134, v135\n"
"v_cvt_pk_bf16_f32 v118, v136, v137\n"
"v_cvt_pk_bf16_f32 v119, v138, v139\n"
"s_add_u32 s41, s40, 17408\n"
"buffer_store_dwordx4 v[116:119], v249, s[56:59], s41 offen sc1\n"
"v_cvt_pk_bf16_f32 v120, v140, v141\n"
"v_cvt_pk_bf16_f32 v121, v142, v143\n"
"v_cvt_pk_bf16_f32 v122, v144, v145\n"
"v_cvt_pk_bf16_f32 v123, v146, v147\n"
"buffer_store_dwordx4 v[120:123], v249, s[56:59], s41 offen offset:1024 sc1\n"
"s_waitcnt lgkmcnt(0)\n"
"v_cmp_gt_u32 vcc, 41, v248\n"
"v_cvt_pk_bf16_f32 v124, v148, v149\n"
"v_cvt_pk_bf16_f32 v125, v150, v151\n"
"s_cbranch_vccnz K1_YG_S\n"
"K1_YG_R:\n"
"s_cmp_lg_u32 s60, 3\n"
"s_cbranch_scc1 K1_NOF0\n"
"s_add_u32 s68, s88, 1\n"
"v_mov_b32 v246, s68\n"
"s_lshl_b32 s68, s65, 7\n"
"s_add_u32 s68, s68, 42991680\n"
"v_mov_b32 v247, 0\n"
"s_mov_b64 exec, 1\n"
"buffer_store_dword v246, v247, s[56:59], s68 offen sc1\n"
"s_mov_b64 exec, -1\n"
"K1_NOF0:\n"
"s_nop 3\n"
"v_cvt_pk_bf16_f32 v126, v152, v153\n"
"v_cvt_pk_bf16_f32 v127, v154, v155\n"
"buffer_store_dwordx4 v[124:127], v249, s[56:59], s41 offen offset:2048 sc1\n"
"v_cvt_pk_bf16_f32 v128, v156, v157\n"
"v_cvt_pk_bf16_f32 v129, v158, v159\n"
"v_mov_b32 v130, 0\n"
"v_mov_b32 v131, 0\n"
"buffer_store_dwordx4 v[128:131], v249, s[56:59], s41 offen offset:3072 sc1\n"
"v_cvt_pk_bf16_f32 v132, v160, v161\n"
"v_cvt_pk_bf16_f32 v133, v162, v163\n"
"v_cvt_pk_bf16_f32 v134, v164, v165\n"
"v_cvt_pk_bf16_f32 v135, v166, v167\n"
"s_add_u32 s41, s40, 21504\n"
"buffer_store_dwordx4 v[132:135], v249, s[56:59], s41 offen sc1\n"
"v_cvt_pk_bf16_f32 v136, v168, v169\n"
"v_cvt_pk_bf16_f32 v137, v170, v171\n"
"v_cvt_pk_bf16_f32 v138, v172, v173\n"
"v_cvt_pk_bf16_f32 v139, v174, v175\n"
"buffer_store_dwordx4 v[136:139], v249, s[56:59], s41 offen offset:1024 sc1\n"
"v_cvt_pk_bf16_f32 v140, v176, v177\n"
"v_cvt_pk_bf16_f32 v141, v178, v179\n"
"v_cvt_pk_bf16_f32 v142, v180, v181\n"
"v_cvt_pk_bf16_f32 v143, v182, v183\n"
"buffer_store_dwordx4 v[140:143], v249, s[56:59], s41 offen offset:2048 sc1\n"
"v_cvt_pk_bf16_f32 v144, v184, v185\n"
"v_cvt_pk_bf16_f32 v145, v186, v187\n"
"v_cvt_pk_bf16_f32 v146, v188, v189\n"
"v_cvt_pk_bf16_f32 v147, v190, v191\n"
"buffer_store_dwordx4 v[144:147], v249, s[56:59], s41 offen offset:3072 sc1\n"
"v_cvt_pk_bf16_f32 v148, v192, v193\n"
"v_cvt_pk_bf16_f32 v149, v194, v195\n"
"v_cvt_pk_bf16_f32 v150, v196, v197\n"
"v_cvt_pk_bf16_f32 v151, v198, v199\n"
"s_add_u32 s41, s40, 25600\n"
"buffer_store_dwordx4 v[148:151], v249, s[56:59], s41 offen sc1\n"
"s_waitcnt vmcnt(0)\n"
"s_barrier\n"
"s_cmp_lg_u32 s60, 3\n"
"s_cbranch_scc1 K1_NOF1\n"
"s_add_u32 s68, s73, 1\n"
"v_mov_b32 v246, s68\n"
"s_lshl_b32 s68, s65, 7\n"
"s_add_u32 s68, s68, 42991712\n"
"v_mov_b32 v247, 0\n"
"s_mov_b64 exec, 1\n"
"buffer_store_dword v246, v247, s[56:59], s68 offen sc1\n"
"s_mov_b64 exec, -1\n"
"K1_NOF1:\n"
"s_nop 3\n"
"s_mul_i32 s40, s86, 102400\n"
"s_and_b32 s69, s87, 6\n"
"s_mul_i32 s69, s69, 160\n"
"s_add_u32 s40, s40, s69\n"
"s_mov_b32 s90, 262144\n"
"s_mov_b32 s96, 0xff\n"
"s_mov_b32 s97, 0x0\n"
"K1_FT0_L:\n"
"s_nop 4\n"
"buffer_load_dword v244, v243, s[56:59], 0 offen sc1\n"
"s_waitcnt vmcnt(0)\n"
"v_cmp_ne_u32 vcc, v244, v254\n"
"s_nop 1\n"
"s_andn2_b64 vcc, s[96:97], vcc\n"
"s_cbranch_scc0 K1_FT0_OK\n"
"s_sleep 2\n"
"s_sub_u32 s90, s90, 1\n"
"s_cmp_lg_u32 s90, 0\n"
"s_cbranch_scc1 K1_FT0_L\n"
"K1_FT0_OK:\n"
"s_nop 1\n"
"buffer_load_dwordx4 a[0:3], v60, s[56:59], s44 offen sc1\n"
"buffer_load_dwordx4 a[4:7], v61, s[56:59], s44 offen sc1\n"
"buffer_load_dwordx4 a[8:11], v60, s[56:59], s45 offen sc1\n"
"buffer_load_dwordx4 a[12:15], v61, s[56:59], s45 offen sc1\n"
"buffer_load_dwordx4 a[16:19], v60, s[56:59], s46 offen sc1\n"
"buffer_load_dwordx4 a[20:23], v61, s[56:59], s46 offen sc1\n"
"buffer_load_dwordx4 a[24:27], v60, s[56:59], s47 offen sc1\n"
"buffer_load_dwordx4 a[28:31], v61, s[56:59], s47 offen sc1\n"
"buffer_load_dwordx4 a[32:35], v60, s[56:59], s48 offen sc1\n"
"buffer_load_dwordx4 a[36:39], v61, s[56:59], s48 offen sc1\n"
"buffer_load_dwordx4 a[40:43], v60, s[56:59], s49 offen sc1\n"
"buffer_load_dwordx4 a[44:47], v61, s[56:59], s49 offen sc1\n"
"buffer_load_dwordx4 a[48:51], v60, s[56:59], s50 offen sc1\n"
"buffer_load_dwordx4 a[52:55], v61, s[56:59], s50 offen sc1\n"
"buffer_load_dwordx4 a[56:59], v60, s[56:59], s51 offen sc1\n"
"buffer_load_dwordx4 a[60:63], v61, s[56:59], s51 offen sc1\n"
"s_mov_b32 s90, 262144\n"
"s_mov_b32 s96, 0x0\n"
"s_mov_b32 s97, 0xff000000\n"
"v_cmp_ne_u32 vcc, v244, v254\n"
"s_nop 1\n"
"s_andn2_b64 vcc, s[96:97], vcc\n"
"s_cbranch_scc0 K1_FT1_OK\n"
"K1_FT1_L:\n"
"s_nop 4\n"
"buffer_load_dword v244, v243, s[56:59], 0 offen sc1\n"
"s_waitcnt vmcnt(0)\n"
"v_cmp_ne_u32 vcc, v244, v254\n"
"s_nop 1\n"
"s_andn2_b64 vcc, s[96:97], vcc\n"
"s_cbranch_scc0 K1_FT1_OK\n"
"s_sleep 2\n"
"s_sub_u32 s90, s90, 1\n"
"s_cmp_lg_u32 s90, 0\n"
"s_cbranch_scc1 K1_FT1_L\n"
"K1_FT1_OK:\n"
"s_nop 1\n"
"buffer_load_dwordx4 a[64:67], v62, s[56:59], s44 offen sc1\n"
"buffer_load_dwordx4 a[68:71], v63, s[56:59], s44 offen sc1\n"
"buffer_load_dwordx4 a[72:75], v62, s[56:59], s45 offen sc1\n"
"buffer_load_dwordx4 a[76:79], v63, s[56:59], s45 offen sc1\n"
"buffer_load_dwordx4 a[80:83], v62, s[56:59], s46 offen sc1\n"
"buffer_load_dwordx4 a[84:87], v63, s[56:59], s46 offen sc1\n"
"buffer_load_dwordx4 a[88:91], v62, s[56:59], s47 offen sc1\n"
"buffer_load_dwordx4 a[92:95], v63, s[56:59], s47 offen sc1\n"
"buffer_load_dwordx4 a[96:99], v62, s[56:59], s48 offen sc1\n"
"buffer_load_dwordx4 a[100:103], v63, s[56:59], s48 offen sc1\n"
"buffer_load_dwordx4 a[104:107], v62, s[56:59], s49 offen sc1\n"
"buffer_load_dwordx4 a[108:111], v63, s[56:59], s49 offen sc1\n"
"buffer_load_dwordx4 a[112:115], v62, s[56:59], s50 offen sc1\n"
"buffer_load_dwordx4 a[116:119], v63, s[56:59], s50 offen sc1\n"
"buffer_load_dwordx4 a[120:123], v62, s[56:59], s51 offen sc1\n"
"buffer_load_dwordx4 a[124:127], v63, s[56:59], s51 offen sc1\n"
"s_waitcnt vmcnt(31)\n"
"MM v[0:3], a[248:251], a[0:3], 0\n"
"MM v[4:7], a[252:255], a[0:3], 0\n"
"s_waitcnt vmcnt(30)\n"
"MM v[8:11], a[248:251], a[4:7], 0\n"
"MM v[12:15], a[252:255], a[4:7], 0\n"
"s_waitcnt vmcnt(29)\n"
"MM v[0:3], a[248:251], a[8:11], v[0:3]\n"
"MM v[4:7], a[252:255], a[8:11], v[4:7]\n"
"s_waitcnt vmcnt(28)\n"
"MM v[8:11], a[248:251], a[12:15], v[8:11]\n"
"MM v[12:15], a[252:255], a[12:15], v[12:15]\n"
"s_waitcnt vmcnt(27)\n"
"MM v[0:3], a[248:251], a[16:19], v[0:3]\n"
"MM v[4:7], a[252:255], a[16:19], v[4:7]\n"
"s_waitcnt vmcnt(26)\n"
"MM v[8:11], a[248:251], a[20:23], v[8:11]\n"
"MM v[12:15], a[252:255], a[20:23], v[12:15]\n"
"s_waitcnt vmcnt(25)\n"
"MM v[0:3], a[248:251], a[24:27], v[0:3]\n"
"MM v[4:7], a[252:255], a[24:27], v[4:7]\n"
"s_waitcnt vmcnt(24)\n"
"MM v[8:11], a[248:251], a[28:31], v[8:11]\n"
"MM v[12:15], a[252:255], a[28:31], v[12:15]\n"
"s_waitcnt vmcnt(23)\n"
"MM v[0:3], a[248:251], a[32:35], v[0:3]\n"
"MM v[4:7], a[252:255], a[32:35], v[4:7]\n"
"s_waitcnt vmcnt(22)\n"
"MM v[8:11], a[248:251], a[36:39], v[8:11]\n"
"MM v[12:15], a[252:255], a[36:39], v[12:15]\n"
"s_waitcnt vmcnt(21)\n"
"MM v[0:3], a[248:251], a[40:43], v[0:3]\n"
"MM v[4:7], a[252:255], a[40:43], v[4:7]\n"
"s_waitcnt vmcnt(20)\n"
"MM v[8:11], a[248:251], a[44:47], v[8:11]\n"
"MM v[12:15], a[252:255], a[44:47], v[12:15]\n"
"s_waitcnt vmcnt(19)\n"
"MM v[0:3], a[248:251], a[48:51], v[0:3]\n"
"MM v[4:7], a[252:255], a[48:51], v[4:7]\n"
"s_waitcnt vmcnt(18)\n"
"MM v[8:11], a[248:251], a[52:55], v[8:11]\n"
"MM v[12:15], a[252:255], a[52:55], v[12:15]\n"
"s_waitcnt vmcnt(17)\n"
"MM v[0:3], a[248:251], a[56:59], v[0:3]\n"
"MM v[4:7], a[252:255], a[56:59], v[4:7]\n"
"s_waitcnt vmcnt(16)\n"
"MM v[8:11], a[248:251], a[60:63], v[8:11]\n"
"MM v[12:15], a[252:255], a[60:63], v[12:15]\n"
"s_nop 3\n"
"v_cvt_pk_bf16_f32 v0, v0, v1\n"
"v_cvt_pk_bf16_f32 v1, v2, v3\n"
"ds_write_b64 v64, v[0:1] offset:0\n"
"v_cvt_pk_bf16_f32 v4, v4, v5\n"
"v_cvt_pk_bf16_f32 v5, v6, v7\n"
"ds_write_b64 v65, v[4:5] offset:0\n"
"v_cvt_pk_bf16_f32 v8, v8, v9\n"
"v_cvt_pk_bf16_f32 v9, v10, v11\n"
"ds_write_b64 v66, v[8:9] offset:0\n"
"v_cvt_pk_bf16_f32 v12, v12, v13\n"
"v_cvt_pk_bf16_f32 v13, v14, v15\n"
"ds_write_b64 v67, v[12:13] offset:0\n"
"s_waitcnt vmcnt(15)\n"
"MM v[16:19], a[248:251], a[64:67], 0\n"
"MM v[20:23], a[252:255], a[64:67], 0\n"
"s_waitcnt vmcnt(14)\n"
"MM v[24:27], a[248:251], a[68:71], 0\n"
"MM v[28:31], a[252:255], a[68:71], 0\n"
"s_waitcnt vmcnt(13)\n"
"MM v[16:19], a[248:251], a[72:75], v[16:19]\n"
"MM v[20:23], a[252:255], a[72:75], v[20:23]\n"
"s_waitcnt vmcnt(12)\n"
"MM v[24:27], a[248:251], a[76:79], v[24:27]\n"
"MM v[28:31], a[252:255], a[76:79], v[28:31]\n"
"s_waitcnt vmcnt(11)\n"
"MM v[16:19], a[248:251], a[80:83], v[16:19]\n"
"MM v[20:23], a[252:255], a[80:83], v[20:23]\n"
"s_waitcnt vmcnt(10)\n"
"MM v[24:27], a[248:251], a[84:87], v[24:27]\n"
"MM v[28:31], a[252:255], a[84:87], v[28:31]\n"
"s_waitcnt vmcnt(9)\n"
"MM v[16:19], a[248:251], a[88:91], v[16:19]\n"
"MM v[20:23], a[252:255], a[88:91], v[20:23]\n"
"s_waitcnt vmcnt(8)\n"
"MM v[24:27], a[248:251], a[92:95], v[24:27]\n"
"MM v[28:31], a[252:255], a[92:95], v[28:31]\n"
"s_waitcnt vmcnt(7)\n"
"MM v[16:19], a[248:251], a[96:99], v[16:19]\n"
"MM v[20:23], a[252:255], a[96:99], v[20:23]\n"
"s_waitcnt vmcnt(6)\n"
"MM v[24:27], a[248:251], a[100:103], v[24:27]\n"
"MM v[28:31], a[252:255], a[100:103], v[28:31]\n"
"s_waitcnt vmcnt(5)\n"
"MM v[16:19], a[248:251], a[104:107], v[16:19]\n"
"MM v[20:23], a[252:255], a[104:107], v[20:23]\n"
"s_waitcnt vmcnt(4)\n"
"MM v[24:27], a[248:251], a[108:111], v[24:27]\n"
"MM v[28:31], a[252:255], a[108:111], v[28:31]\n"
"s_waitcnt vmcnt(3)\n"
"MM v[16:19], a[248:251], a[112:115], v[16:19]\n"
"MM v[20:23], a[252:255], a[112:115], v[20:23]\n"
"s_waitcnt vmcnt(2)\n"
"MM v[24:27], a[248:251], a[116:119], v[24:27]\n"
"MM v[28:31], a[252:255], a[116:119], v[28:31]\n"
"s_waitcnt vmcnt(1)\n"
"MM v[16:19], a[248:251], a[120:123], v[16:19]\n"
"MM v[20:23], a[252:255], a[120:123], v[20:23]\n"
"s_waitcnt vmcnt(0)\n"
"MM v[24:27], a[248:251], a[124:127], v[24:27]\n"
"MM v[28:31], a[252:255], a[124:127], v[28:31]\n"
"s_nop 3\n"
"v_cvt_pk_bf16_f32 v16, v16, v17\n"
"v_cvt_pk_bf16_f32 v17, v18, v19\n"
"ds_write_b64 v68, v[16:17] offset:0\n"
"v_cvt_pk_bf16_f32 v20, v20, v21\n"
"v_cvt_pk_bf16_f32 v21, v22, v23\n"
"ds_write_b64 v69, v[20:21] offset:0\n"
"v_cvt_pk_bf16_f32 v24, v24, v25\n"
"v_cvt_pk_bf16_f32 v25, v26, v27\n"
"ds_write_b64 v70, v[24:25] offset:0\n"
"v_cvt_pk_bf16_f32 v28, v28, v29\n"
"v_cvt_pk_bf16_f32 v29, v30, v31\n"
"ds_write_b64 v71, v[28:29] offset:0\n"
"s_waitcnt lgkmcnt(0)\n"
"s_barrier\n"
"ds_read_b128 v[40:43], v72 offset:0\n"
"ds_read_b128 v[44:47], v73 offset:0\n"
"ds_read_b128 v[48:51], v74 offset:0\n"
"ds_read_b128 v[52:55], v75 offset:0\n"
"s_waitcnt lgkmcnt(3)\n"
"buffer_store_dwordx4 v[40:43], v76, s[36:39], s40 offen\n"
"s_waitcnt lgkmcnt(2)\n"
"buffer_store_dwordx4 v[44:47], v77, s[36:39], s40 offen\n"
"s_waitcnt lgkmcnt(1)\n"
"buffer_store_dwordx4 v[48:51], v78, s[36:39], s40 offen\n"
"s_waitcnt lgkmcnt(0)\n"
"buffer_store_dwordx4 v[52:55], v79, s[36:39], s40 offen\n"
"s_endpgm\n"
"K1_EPI_L:\n"
"s_nop 1\n"
"MM a[224:227], a[244:247], v[120:123], a[224:227]\n"
"s_waitcnt lgkmcnt(14)\n"
"MM a[0:3], a[192:195], v[120:123], a[0:3]\n"
"s_waitcnt lgkmcnt(12)\n"
"MM a[4:7], a[196:199], v[120:123], a[4:7]\n"
"s_waitcnt lgkmcnt(10)\n"
"MM a[8:11], a[200:203], v[120:123], a[8:11]\n"
"s_waitcnt lgkmcnt(8)\n"
"MM a[12:15], a[204:207], v[120:123], a[12:15]\n"
"s_add_u32 s91, s94, 46080\n"
"s_waitcnt lgkmcnt(6)\n"
"MM a[16:19], a[208:211], v[120:123], a[16:19]\n"
"s_waitcnt lgkmcnt(4)\n"
"MM a[20:23], a[212:215], v[120:123], a[20:23]\n"
"s_waitcnt lgkmcnt(2)\n"
"MM a[24:27], a[216:219], v[120:123], a[24:27]\n"
"s_waitcnt lgkmcnt(0)\n"
"MM a[28:31], a[220:223], v[120:123], a[28:31]\n"
"MM a[228:231], a[244:247], v[124:127], a[228:231]\n"
"MM a[32:35], a[192:195], v[124:127], a[32:35]\n"
"v_accvgpr_read_b32 v140, a224\n"
"v_rcp_f32 v140, v140\n"
"MM a[36:39], a[196:199], v[124:127], a[36:39]\n"
"buffer_load_dwordx4 v[220:223], v251, s[56:59], s91 offen sc1\n"
"v_accvgpr_read_b32 v0, a0\n"
"MM a[40:43], a[200:203], v[124:127], a[40:43]\n"
"v_accvgpr_read_b32 v1, a1\n"
"v_accvgpr_read_b32 v2, a2\n"
"MM a[44:47], a[204:207], v[124:127], a[44:47]\n"
"v_accvgpr_read_b32 v3, a3\n"
"v_accvgpr_read_b32 v4, a4\n"
"MM a[48:51], a[208:211], v[124:127], a[48:51]\n"
"v_accvgpr_read_b32 v5, a5\n"
"v_accvgpr_read_b32 v6, a6\n"
"buffer_load_dwordx4 v[224:227], v251, s[56:59], s91 offen offset:1024 sc1\n"
"MM a[52:55], a[212:215], v[124:127], a[52:55]\n"
"v_accvgpr_read_b32 v7, a7\n"
"v_mul_f32 v0, v140, v0\n"
"MM a[56:59], a[216:219], v[124:127], a[56:59]\n"
"v_mul_f32 v1, v140, v1\n"
"v_mul_f32 v2, v140, v2\n"
"MM a[60:63], a[220:223], v[124:127], a[60:63]\n"
"v_mul_f32 v3, v140, v3\n"
"v_mul_f32 v4, v140, v4\n"
"buffer_load_dwordx4 v[228:231], v251, s[56:59], s91 offen offset:2048 sc1\n"
"MM a[232:235], a[244:247], v[128:131], a[232:235]\n"
"v_mul_f32 v5, v140, v5\n"
"v_mul_f32 v6, v140, v6\n"
"MM a[64:67], a[192:195], v[128:131], a[64:67]\n"
"v_mul_f32 v7, v140, v7\n"
"v_cvt_pk_bf16_f32 v0, v0, v1\n"
"MM a[68:71], a[196:199], v[128:131], a[68:71]\n"
"v_cvt_pk_bf16_f32 v1, v2, v3\n"
"v_cvt_pk_bf16_f32 v2, v4, v5\n"
"buffer_load_dwordx4 v[232:235], v251, s[56:59], s91 offen offset:3072 sc1\n"
"MM a[72:75], a[200:203], v[128:131], a[72:75]\n"
"v_cvt_pk_bf16_f32 v3, v6, v7\n"
"ds_write_b128 v248, v[0:3] offset:0\n"
"MM a[76:79], a[204:207], v[128:131], a[76:79]\n"
"v_accvgpr_read_b32 v8, a8\n"
"v_accvgpr_read_b32 v9, a9\n"
"MM a[80:83], a[208:211], v[128:131], a[80:83]\n"
"v_accvgpr_read_b32 v10, a10\n"
"v_accvgpr_read_b32 v11, a11\n"
"buffer_load_dwordx4 v[236:239], v252, s[56:59], s91 offen sc1\n"
"MM a[84:87], a[212:215], v[128:131], a[84:87]\n"
"v_accvgpr_read_b32 v12, a12\n"
"v_accvgpr_read_b32 v13, a13\n"
"MM a[88:91], a[216:219], v[128:131], a[88:91]\n"
"v_accvgpr_read_b32 v14, a14\n"
"v_accvgpr_read_b32 v15, a15\n"
"MM a[92:95], a[220:223], v[128:131], a[92:95]\n"
"v_mul_f32 v8, v140, v8\n"
"v_mul_f32 v9, v140, v9\n"
"MM a[236:239], a[244:247], v[132:135], a[236:239]\n"
"s_add_u32 s91, s94, 81920\n"
"v_mul_f32 v10, v140, v10\n"
"v_mul_f32 v11, v140, v11\n"
"MM a[96:99], a[192:195], v[132:135], a[96:99]\n"
"v_mul_f32 v12, v140, v12\n"
"v_mul_f32 v13, v140, v13\n"
"MM a[100:103], a[196:199], v[132:135], a[100:103]\n"
"v_mul_f32 v14, v140, v14\n"
"v_mul_f32 v15, v140, v15\n"
"MM a[104:107], a[200:203], v[132:135], a[104:107]\n"
"buffer_load_dwordx4 a[160:163], v251, s[56:59], s91 offen sc1\n"
"v_cvt_pk_bf16_f32 v8, v8, v9\n"
"v_cvt_pk_bf16_f32 v9, v10, v11\n"
"MM a[108:111], a[204:207], v[132:135], a[108:111]\n"
"v_cvt_pk_bf16_f32 v10, v12, v13\n"
"v_cvt_pk_bf16_f32 v11, v14, v15\n"
"MM a[112:115], a[208:211], v[132:135], a[112:115]\n"
"ds_write_b128 v248, v[8:11] offset:5120\n"
"v_accvgpr_read_b32 v16, a16\n"
"MM a[116:119], a[212:215], v[132:135], a[116:119]\n"
"buffer_load_dwordx4 a[164:167], v251, s[56:59], s91 offen offset:1024 sc1\n"
"v_accvgpr_read_b32 v17, a17\n"
"v_accvgpr_read_b32 v18, a18\n"
"MM a[120:123], a[216:219], v[132:135], a[120:123]\n"
"v_accvgpr_read_b32 v19, a19\n"
"v_accvgpr_read_b32 v20, a20\n"
"MM a[124:127], a[220:223], v[132:135], a[124:127]\n"
"v_accvgpr_read_b32 v21, a21\n"
"v_accvgpr_read_b32 v22, a22\n"
"MM a[240:243], a[244:247], v[136:139], a[240:243]\n"
"buffer_load_dwordx4 a[168:171], v251, s[56:59], s91 offen offset:2048 sc1\n"
"v_accvgpr_read_b32 v23, a23\n"
"v_mul_f32 v16, v140, v16\n"
"MM a[128:131], a[192:195], v[136:139], a[128:131]\n"
"v_mul_f32 v17, v140, v17\n"
"v_mul_f32 v18, v140, v18\n"
"MM a[132:135], a[196:199], v[136:139], a[132:135]\n"
"v_mul_f32 v19, v140, v19\n"
"v_mul_f32 v20, v140, v20\n"
"MM a[136:139], a[200:203], v[136:139], a[136:139]\n"
"v_mul_f32 v21, v140, v21\n"
"buffer_load_dwordx4 a[172:175], v251, s[56:59], s91 offen offset:3072 sc1\n"
"v_mul_f32 v22, v140, v22\n"
"MM a[140:143], a[204:207], v[136:139], a[140:143]\n"
"v_mul_f32 v23, v140, v23\n"
"v_cvt_pk_bf16_f32 v16, v16, v17\n"
"MM a[144:147], a[208:211], v[136:139], a[144:147]\n"
"v_cvt_pk_bf16_f32 v17, v18, v19\n"
"v_cvt_pk_bf16_f32 v18, v20, v21\n"
"MM a[148:151], a[212:215], v[136:139], a[148:151]\n"
"v_cvt_pk_bf16_f32 v19, v22, v23\n"
"buffer_load_dwordx4 a[176:179], v252, s[56:59], s91 offen sc1\n"
"ds_write_b128 v248, v[16:19] offset:10240\n"
"MM a[152:155], a[216:219], v[136:139], a[152:155]\n"
"v_accvgpr_read_b32 v24, a24\n"
"v_accvgpr_read_b32 v25, a25\n"
"MM a[156:159], a[220:223], v[136:139], a[156:159]\n"
"v_accvgpr_read_b32 v26, a26\n"
"v_accvgpr_read_b32 v27, a27\n"
"v_accvgpr_read_b32 v28, a28\n"
"v_accvgpr_read_b32 v29, a29\n"
"v_accvgpr_read_b32 v30, a30\n"
"v_accvgpr_read_b32 v31, a31\n"
"v_mul_f32 v24, v140, v24\n"
"v_mul_f32 v25, v140, v25\n"
"v_mul_f32 v26, v140, v26\n"
"s_add_u32 s91, s94, 122880\n"
"v_mul_f32 v27, v140, v27\n"
"v_mul_f32 v28, v140, v28\n"
"v_mul_f32 v29, v140, v29\n"
"v_mul_f32 v30, v140, v30\n"
"v_mul_f32 v31, v140, v31\n"
"v_cvt_pk_bf16_f32 v24, v24, v25\n"
"v_cvt_pk_bf16_f32 v25, v26, v27\n"
"v_cvt_pk_bf16_f32 v26, v28, v29\n"
"v_cvt_pk_bf16_f32 v27, v30, v31\n"
"ds_write_b128 v248, v[24:27] offset:15360\n"
"v_accvgpr_read_b32 v142, a228\n"
"v_rcp_f32 v142, v142\n"
"s_nop 0\n"
"v_mov_b32 v143, v142\n"
"v_accvgpr_read_b32 v40, a32\n"
"v_accvgpr_read_b32 v41, a33\n"
"v_accvgpr_read_b32 v42, a34\n"
"buffer_load_dwordx4 a[180:183], v251, s[56:59], s91 offen sc1\n"
"v_accvgpr_read_b32 v43, a35\n"
"v_accvgpr_read_b32 v44, a36\n"
"v_accvgpr_read_b32 v45, a37\n"
"v_accvgpr_read_b32 v46, a38\n"
"v_accvgpr_read_b32 v47, a39\n"
"v_pk_mul_f32 v[40:41], v[40:41], v[142:143]\n"
"v_pk_mul_f32 v[42:43], v[42:43], v[142:143]\n"
"v_pk_mul_f32 v[44:45], v[44:45], v[142:143]\n"
"v_pk_mul_f32 v[46:47], v[46:47], v[142:143]\n"
"v_cvt_pk_bf16_f32 v40, v40, v41\n"
"v_cvt_pk_bf16_f32 v41, v42, v43\n"
"v_cvt_pk_bf16_f32 v42, v44, v45\n"
"v_cvt_pk_bf16_f32 v43, v46, v47\n"
"ds_write_b128 v248, v[40:43] offset:1024\n"
"v_accvgpr_read_b32 v48, a40\n"
"v_accvgpr_read_b32 v49, a41\n"
"buffer_load_dwordx4 a[184:187], v251, s[56:59], s91 offen offset:1024 sc1\n"
"v_accvgpr_read_b32 v50, a42\n"
"v_accvgpr_read_b32 v51, a43\n"
"v_accvgpr_read_b32 v52, a44\n"
"v_accvgpr_read_b32 v53, a45\n"
"v_accvgpr_read_b32 v54, a46\n"
"v_accvgpr_read_b32 v55, a47\n"
"v_pk_mul_f32 v[48:49], v[48:49], v[142:143]\n"
"v_pk_mul_f32 v[50:51], v[50:51], v[142:143]\n"
"v_pk_mul_f32 v[52:53], v[52:53], v[142:143]\n"
"v_pk_mul_f32 v[54:55], v[54:55], v[142:143]\n"
"v_cvt_pk_bf16_f32 v48, v48, v49\n"
"v_cvt_pk_bf16_f32 v49, v50, v51\n"
"v_cvt_pk_bf16_f32 v50, v52, v53\n"
"v_cvt_pk_bf16_f32 v51, v54, v55\n"
"ds_write_b128 v248, v[48:51] offset:6144\n"
"v_accvgpr_read_b32 v56, a48\n"
"v_accvgpr_read_b32 v57, a49\n"
"buffer_load_dwordx4 a[188:191], v251, s[56:59], s91 offen offset:2048 sc1\n"
"v_accvgpr_read_b32 v58, a50\n"
"v_accvgpr_read_b32 v59, a51\n"
"v_accvgpr_read_b32 v60, a52\n"
"v_accvgpr_read_b32 v61, a53\n"
"v_accvgpr_read_b32 v62, a54\n"
"v_accvgpr_read_b32 v63, a55\n"
"v_pk_mul_f32 v[56:57], v[56:57], v[142:143]\n"
"v_pk_mul_f32 v[58:59], v[58:59], v[142:143]\n"
"v_pk_mul_f32 v[60:61], v[60:61], v[142:143]\n"
"v_pk_mul_f32 v[62:63], v[62:63], v[142:143]\n"
"v_cvt_pk_bf16_f32 v56, v56, v57\n"
"v_cvt_pk_bf16_f32 v57, v58, v59\n"
"v_cvt_pk_bf16_f32 v58, v60, v61\n"
"v_cvt_pk_bf16_f32 v59, v62, v63\n"
"ds_write_b128 v248, v[56:59] offset:11264\n"
"v_accvgpr_read_b32 v64, a56\n"
"buffer_load_dwordx4 a[192:195], v251, s[56:59], s91 offen offset:3072 sc1\n"
"v_accvgpr_read_b32 v65, a57\n"
"v_accvgpr_read_b32 v66, a58\n"
"v_accvgpr_read_b32 v67, a59\n"
"v_accvgpr_read_b32 v68, a60\n"
"v_accvgpr_read_b32 v69, a61\n"
"v_accvgpr_read_b32 v70, a62\n"
"v_accvgpr_read_b32 v71, a63\n"
"v_pk_mul_f32 v[64:65], v[64:65], v[142:143]\n"
"v_pk_mul_f32 v[66:67], v[66:67], v[142:143]\n"
"v_pk_mul_f32 v[68:69], v[68:69], v[142:143]\n"
"v_pk_mul_f32 v[70:71], v[70:71], v[142:143]\n"
"v_cvt_pk_bf16_f32 v64, v64, v65\n"
"v_cvt_pk_bf16_f32 v65, v66, v67\n"
"v_cvt_pk_bf16_f32 v66, v68, v69\n"
"v_cvt_pk_bf16_f32 v67, v70, v71\n"
"ds_write_b128 v248, v[64:67] offset:16384\n"
"v_accvgpr_read_b32 v144, a232\n"
"buffer_load_dwordx4 a[196:199], v252, s[56:59], s91 offen sc1\n"
"v_rcp_f32 v144, v144\n"
"s_nop 0\n"
"v_mov_b32 v145, v144\n"
"v_accvgpr_read_b32 v146, a236\n"
"v_rcp_f32 v146, v146\n"
"s_nop 0\n"
"v_mov_b32 v147, v146\n"
"v_accvgpr_read_b32 v148, a240\n"
"v_rcp_f32 v148, v148\n"
"s_nop 0\n"
"v_mov_b32 v149, v148\n"
"v_accvgpr_read_b32 v0, a64\n"
"v_accvgpr_read_b32 v1, a65\n"
"v_accvgpr_read_b32 v2, a66\n"
"v_accvgpr_read_b32 v3, a67\n"
"v_accvgpr_read_b32 v4, a68\n"
"v_accvgpr_read_b32 v5, a69\n"
"v_accvgpr_read_b32 v6, a70\n"
"v_accvgpr_read_b32 v7, a71\n"
"s_add_u32 s91, s94, 87040\n"
"v_pk_mul_f32 v[0:1], v[0:1], v[144:145]\n"
"v_pk_mul_f32 v[2:3], v[2:3], v[144:145]\n"
"v_pk_mul_f32 v[4:5], v[4:5], v[144:145]\n"
"v_pk_mul_f32 v[6:7], v[6:7], v[144:145]\n"
"v_cvt_pk_bf16_f32 v0, v0, v1\n"
"v_cvt_pk_bf16_f32 v1, v2, v3\n"
"v_cvt_pk_bf16_f32 v2, v4, v5\n"
"v_cvt_pk_bf16_f32 v3, v6, v7\n"
"ds_write_b128 v248, v[0:3] offset:2048\n"
"v_accvgpr_read_b32 v8, a72\n"
"v_accvgpr_read_b32 v9, a73\n"
"v_accvgpr_read_b32 v10, a74\n"
"v_accvgpr_read_b32 v11, a75\n"
"v_accvgpr_read_b32 v12, a76\n"
"v_accvgpr_read_b32 v13, a77\n"
"v_accvgpr_read_b32 v14, a78\n"
"buffer_load_dwordx4 a[200:203], v251, s[56:59], s91 offen sc1\n"
"v_accvgpr_read_b32 v15, a79\n"
"v_pk_mul_f32 v[8:9], v[8:9], v[144:145]\n"
"v_pk_mul_f32 v[10:11], v[10:11], v[144:145]\n"
"v_pk_mul_f32 v[12:13], v[12:13], v[144:145]\n"
"v_pk_mul_f32 v[14:15], v[14:15], v[144:145]\n"
"v_cvt_pk_bf16_f32 v8, v8, v9\n"
"v_cvt_pk_bf16_f32 v9, v10, v11\n"
"v_cvt_pk_bf16_f32 v10, v12, v13\n"
"v_cvt_pk_bf16_f32 v11, v14, v15\n"
"ds_write_b128 v248, v[8:11] offset:7168\n"
"v_accvgpr_read_b32 v16, a80\n"
"v_accvgpr_read_b32 v17, a81\n"
"v_accvgpr_read_b32 v18, a82\n"
"v_accvgpr_read_b32 v19, a83\n"
"v_accvgpr_read_b32 v20, a84\n"
"v_accvgpr_read_b32 v21, a85\n"
"v_accvgpr_read_b32 v22, a86\n"
"buffer_load_dwordx4 a[204:207], v251, s[56:59], s91 offen offset:1024 sc1\n"
"v_accvgpr_read_b32 v23, a87\n"
"v_pk_mul_f32 v[16:17], v[16:17], v[144:145]\n"
"v_pk_mul_f32 v[18:19], v[18:19], v[144:145]\n"
"v_pk_mul_f32 v[20:21], v[20:21], v[144:145]\n"
"v_pk_mul_f32 v[22:23], v[22:23], v[144:145]\n"
"v_cvt_pk_bf16_f32 v16, v16, v17\n"
"v_cvt_pk_bf16_f32 v17, v18, v19\n"
"v_cvt_pk_bf16_f32 v18, v20, v21\n"
"v_cvt_pk_bf16_f32 v19, v22, v23\n"
"ds_write_b128 v248, v[16:19] offset:12288\n"
"v_accvgpr_read_b32 v24, a88\n"
"v_accvgpr_read_b32 v25, a89\n"
"v_accvgpr_read_b32 v26, a90\n"
"v_accvgpr_read_b32 v27, a91\n"
"v_accvgpr_read_b32 v28, a92\n"
"v_accvgpr_read_b32 v29, a93\n"
"buffer_load_dwordx4 a[208:211], v251, s[56:59], s91 offen offset:2048 sc1\n"
"v_accvgpr_read_b32 v30, a94\n"
"v_accvgpr_read_b32 v31, a95\n"
"v_pk_mul_f32 v[24:25], v[24:25], v[144:145]\n"
"v_pk_mul_f32 v[26:27], v[26:27], v[144:145]\n"
"v_pk_mul_f32 v[28:29], v[28:29], v[144:145]\n"
"v_pk_mul_f32 v[30:31], v[30:31], v[144:145]\n"
"v_cvt_pk_bf16_f32 v24, v24, v25\n"
"v_cvt_pk_bf16_f32 v25, v26, v27\n"
"v_cvt_pk_bf16_f32 v26, v28, v29\n"
"v_cvt_pk_bf16_f32 v27, v30, v31\n"
"ds_write_b128 v248, v[24:27] offset:17408\n"
"v_accvgpr_read_b32 v40, a96\n"
"v_accvgpr_read_b32 v41, a97\n"
"v_accvgpr_read_b32 v42, a98\n"
"v_accvgpr_read_b32 v43, a99\n"
"v_accvgpr_read_b32 v44, a100\n"
"v_accvgpr_read_b32 v45, a101\n"
"buffer_load_dwordx4 a[212:215], v251, s[56:59], s91 offen offset:3072 sc1\n"
"v_accvgpr_read_b32 v46, a102\n"
"v_accvgpr_read_b32 v47, a103\n"
"v_pk_mul_f32 v[40:41], v[40:41], v[146:147]\n"
"v_pk_mul_f32 v[42:43], v[42:43], v[146:147]\n"
"v_pk_mul_f32 v[44:45], v[44:45], v[146:147]\n"
"v_pk_mul_f32 v[46:47], v[46:47], v[146:147]\n"
"v_cvt_pk_bf16_f32 v40, v40, v41\n"
"v_cvt_pk_bf16_f32 v41, v42, v43\n"
"v_cvt_pk_bf16_f32 v42, v44, v45\n"
"v_cvt_pk_bf16_f32 v43, v46, v47\n"
"ds_write_b128 v248, v[40:43] offset:3072\n"
"v_accvgpr_read_b32 v48, a104\n"
"v_accvgpr_read_b32 v49, a105\n"
"v_accvgpr_read_b32 v50, a106\n"
"v_accvgpr_read_b32 v51, a107\n"
"v_accvgpr_read_b32 v52, a108\n"
"buffer_load_dwordx4 a[216:219], v252, s[56:59], s91 offen sc1\n"
"v_accvgpr_read_b32 v53, a109\n"
"v_accvgpr_read_b32 v54, a110\n"
"v_accvgpr_read_b32 v55, a111\n"
"v_pk_mul_f32 v[48:49], v[48:49], v[146:147]\n"
"v_pk_mul_f32 v[50:51], v[50:51], v[146:147]\n"
"v_pk_mul_f32 v[52:53], v[52:53], v[146:147]\n"
"v_pk_mul_f32 v[54:55], v[54:55], v[146:147]\n"
"v_cvt_pk_bf16_f32 v48, v48, v49\n"
"v_cvt_pk_bf16_f32 v49, v50, v51\n"
"v_cvt_pk_bf16_f32 v50, v52, v53\n"
"v_cvt_pk_bf16_f32 v51, v54, v55\n"
"ds_write_b128 v248, v[48:51] offset:8192\n"
"v_accvgpr_read_b32 v56, a112\n"
"v_accvgpr_read_b32 v57, a113\n"
"v_accvgpr_read_b32 v58, a114\n"
"v_accvgpr_read_b32 v59, a115\n"
"s_add_u32 s91, s94, 128000\n"
"v_accvgpr_read_b32 v60, a116\n"
"v_accvgpr_read_b32 v61, a117\n"
"v_accvgpr_read_b32 v62, a118\n"
"v_accvgpr_read_b32 v63, a119\n"
"v_pk_mul_f32 v[56:57], v[56:57], v[146:147]\n"
"v_pk_mul_f32 v[58:59], v[58:59], v[146:147]\n"
"v_pk_mul_f32 v[60:61], v[60:61], v[146:147]\n"
"v_pk_mul_f32 v[62:63], v[62:63], v[146:147]\n"
"v_cvt_pk_bf16_f32 v56, v56, v57\n"
"v_cvt_pk_bf16_f32 v57, v58, v59\n"
"v_cvt_pk_bf16_f32 v58, v60, v61\n"
"v_cvt_pk_bf16_f32 v59, v62, v63\n"
"ds_write_b128 v248, v[56:59] offset:13312\n"
"v_accvgpr_read_b32 v64, a120\n"
"v_accvgpr_read_b32 v65, a121\n"
"v_accvgpr_read_b32 v66, a122\n"
"v_accvgpr_read_b32 v67, a123\n"
"buffer_load_dwordx4 a[220:223], v251, s[56:59], s91 offen sc1\n"
"v_accvgpr_read_b32 v68, a124\n"
"v_accvgpr_read_b32 v69, a125\n"
"v_accvgpr_read_b32 v70, a126\n"
"v_accvgpr_read_b32 v71, a127\n"
"v_pk_mul_f32 v[64:65], v[64:65], v[146:147]\n"
"v_pk_mul_f32 v[66:67], v[66:67], v[146:147]\n"
"v_pk_mul_f32 v[68:69], v[68:69], v[146:147]\n"
"v_pk_mul_f32 v[70:71], v[70:71], v[146:147]\n"
"v_cvt_pk_bf16_f32 v64, v64, v65\n"
"v_cvt_pk_bf16_f32 v65, v66, v67\n"
"v_cvt_pk_bf16_f32 v66, v68, v69\n"
"v_cvt_pk_bf16_f32 v67, v70, v71\n"
"ds_write_b128 v248, v[64:67] offset:18432\n"
"v_accvgpr_read_b32 v0, a128\n"
"v_accvgpr_read_b32 v1, a129\n"
"v_accvgpr_read_b32 v2, a130\n"
"buffer_load_dwordx4 a[224:227], v251, s[56:59], s91 offen offset:1024 sc1\n"
"v_accvgpr_read_b32 v3, a131\n"
"v_accvgpr_read_b32 v4, a132\n"
"v_accvgpr_read_b32 v5, a133\n"
"v_accvgpr_read_b32 v6, a134\n"
"v_accvgpr_read_b32 v7, a135\n"
"v_pk_mul_f32 v[0:1], v[0:1], v[148:149]\n"
"v_pk_mul_f32 v[2:3], v[2:3], v[148:149]\n"
"v_pk_mul_f32 v[4:5], v[4:5], v[148:149]\n"
"v_pk_mul_f32 v[6:7], v[6:7], v[148:149]\n"
"v_cvt_pk_bf16_f32 v0, v0, v1\n"
"v_cvt_pk_bf16_f32 v1, v2, v3\n"
"v_cvt_pk_bf16_f32 v2, v4, v5\n"
"v_cvt_pk_bf16_f32 v3, v6, v7\n"
"ds_write_b128 v248, v[0:3] offset:4096\n"
"v_accvgpr_read_b32 v8, a136\n"
"v_accvgpr_read_b32 v9, a137\n"
"v_accvgpr_read_b32 v10, a138\n"
"buffer_load_dwordx4 a[228:231], v251, s[56:59], s91 offen offset:2048 sc1\n"
"v_accvgpr_read_b32 v11, a139\n"
"v_accvgpr_read_b32 v12, a140\n"
"v_accvgpr_read_b32 v13, a141\n"
"v_accvgpr_read_b32 v14, a142\n"
"v_accvgpr_read_b32 v15, a143\n"
"v_pk_mul_f32 v[8:9], v[8:9], v[148:149]\n"
"v_pk_mul_f32 v[10:11], v[10:11], v[148:149]\n"
"v_pk_mul_f32 v[12:13], v[12:13], v[148:149]\n"
"v_pk_mul_f32 v[14:15], v[14:15], v[148:149]\n"
"v_cvt_pk_bf16_f32 v8, v8, v9\n"
"v_cvt_pk_bf16_f32 v9, v10, v11\n"
"v_cvt_pk_bf16_f32 v10, v12, v13\n"
"v_cvt_pk_bf16_f32 v11, v14, v15\n"
"ds_write_b128 v248, v[8:11] offset:9216\n"
"v_accvgpr_read_b32 v16, a144\n"
"v_accvgpr_read_b32 v17, a145\n"
"buffer_load_dwordx4 a[232:235], v251, s[56:59], s91 offen offset:3072 sc1\n"
"v_accvgpr_read_b32 v18, a146\n"
"v_accvgpr_read_b32 v19, a147\n"
"v_accvgpr_read_b32 v20, a148\n"
"v_accvgpr_read_b32 v21, a149\n"
"v_accvgpr_read_b32 v22, a150\n"
"v_accvgpr_read_b32 v23, a151\n"
"v_pk_mul_f32 v[16:17], v[16:17], v[148:149]\n"
"v_pk_mul_f32 v[18:19], v[18:19], v[148:149]\n"
"v_pk_mul_f32 v[20:21], v[20:21], v[148:149]\n"
"v_pk_mul_f32 v[22:23], v[22:23], v[148:149]\n"
"v_cvt_pk_bf16_f32 v16, v16, v17\n"
"v_cvt_pk_bf16_f32 v17, v18, v19\n"
"v_cvt_pk_bf16_f32 v18, v20, v21\n"
"v_cvt_pk_bf16_f32 v19, v22, v23\n"
"ds_write_b128 v248, v[16:19] offset:14336\n"
"v_accvgpr_read_b32 v24, a152\n"
"buffer_load_dwordx4 a[236:239], v252, s[56:59], s91 offen sc1\n"
"v_accvgpr_read_b32 v25, a153\n"
"v_accvgpr_read_b32 v26, a154\n"
"v_accvgpr_read_b32 v27, a155\n"
"v_accvgpr_read_b32 v28, a156\n"
"v_accvgpr_read_b32 v29, a157\n"
"v_accvgpr_read_b32 v30, a158\n"
"v_accvgpr_read_b32 v31, a159\n"
"v_pk_mul_f32 v[24:25], v[24:25], v[148:149]\n"
"v_pk_mul_f32 v[26:27], v[26:27], v[148:149]\n"
"v_pk_mul_f32 v[28:29], v[28:29], v[148:149]\n"
"v_pk_mul_f32 v[30:31], v[30:31], v[148:149]\n"
"v_cvt_pk_bf16_f32 v24, v24, v25\n"
"v_cvt_pk_bf16_f32 v25, v26, v27\n"
"v_cvt_pk_bf16_f32 v26, v28, v29\n"
"v_cvt_pk_bf16_f32 v27, v30, v31\n"
"ds_write_b128 v248, v[24:27] offset:19456\n"
"s_add_u32 s68, s77, 32768\n"
"v_lshl_add_u32 v240, v253, 4, s68\n"
"v_add_u32 v241, 40960, v240\n"
"s_waitcnt lgkmcnt(0)\n"
"s_barrier\n"
"ds_read_b128 a[0:3], v240 offset:0\n"
"ds_read_b128 a[20:23], v240 offset:5120\n"
"ds_read_b128 a[4:7], v240 offset:1024\n"
"ds_read_b128 a[24:27], v240 offset:6144\n"
"ds_read_b128 a[8:11], v240 offset:2048\n"
"ds_read_b128 a[28:31], v240 offset:7168\n"
"ds_read_b128 a[12:15], v240 offset:3072\n"
"ds_read_b128 a[32:35], v240 offset:8192\n"
"ds_read_b128 a[16:19], v240 offset:4096\n"
"ds_read_b128 a[36:39], v240 offset:9216\n"
"s_waitcnt lgkmcnt(9)\n"
"s_waitcnt vmcnt(39)\n"
"MM v[0:3], v[160:163], a[0:3], 0\n"
"v_subrev_u32 v248, 16, v253\n"
"v_bfe_u32 v246, v248, 4, 1\n"
"s_waitcnt lgkmcnt(8)\n"
"s_waitcnt vmcnt(34)\n"
"MM v[0:3], v[180:183], a[20:23], v[0:3]\n"
"s_lshl_b32 s68, s87, 1\n"
"v_add_u32 v246, s68, v246\n"
"MM v[4:7], v[164:167], a[0:3], 0\n"
"v_and_b32 v247, 7, v248\n"
"s_waitcnt vmcnt(33)\n"
"MM v[4:7], v[184:187], a[20:23], v[4:7]\n"
"v_bfe_u32 v244, v248, 3, 1\n"
"MM v[8:11], v[168:171], a[0:3], 0\n"
"v_lshl_add_u32 v247, v247, 1, v244\n"
"s_waitcnt vmcnt(32)\n"
"MM v[8:11], v[188:191], a[20:23], v[8:11]\n"
"MM v[12:15], v[172:175], a[0:3], 0\n"
"v_cmp_gt_u32 vcc, 32, v248\n"
"s_waitcnt vmcnt(31)\n"
"MM v[12:15], v[192:195], a[20:23], v[12:15]\n"
"MM v[16:19], v[176:179], a[0:3], 0\n"
"v_mov_b32 v244, s63\n"
"s_waitcnt vmcnt(30)\n"
"MM v[16:19], v[196:199], a[20:23], v[16:19]\n"
"v_cndmask_b32 v246, v244, v246, vcc\n"
"s_waitcnt lgkmcnt(7)\n"
"MM v[20:23], v[160:163], a[4:7], 0\n"
"s_waitcnt lgkmcnt(6)\n"
"MM v[20:23], v[180:183], a[24:27], v[20:23]\n"
"v_mov_b32 v244, s64\n"
"MM v[24:27], v[164:167], a[4:7], 0\n"
"MM v[24:27], v[184:187], a[24:27], v[24:27]\n"
"v_cndmask_b32 v247, v244, v247, vcc\n"
"MM v[28:31], v[168:171], a[4:7], 0\n"
"s_and_b32 s68, s64, 1\n"
"MM v[28:31], v[188:191], a[24:27], v[28:31]\n"
"MM v[32:35], v[172:175], a[4:7], 0\n"
"v_and_b32 v244, 7, v253\n"
"MM v[32:35], v[192:195], a[24:27], v[32:35]\n"
"MM v[36:39], v[176:179], a[4:7], 0\n"
"v_lshl_add_u32 v244, v244, 1, s68\n"
"MM v[36:39], v[196:199], a[24:27], v[36:39]\n"
"s_mov_b32 vcc_lo, 0xff\n"
"s_waitcnt lgkmcnt(5)\n"
"MM v[40:43], v[160:163], a[8:11], 0\n"
"s_waitcnt lgkmcnt(4)\n"
"MM v[40:43], v[180:183], a[28:31], v[40:43]\n"
"s_mov_b32 vcc_hi, 0xff000000\n"
"MM v[44:47], v[164:167], a[8:11], 0\n"
"MM v[44:47], v[184:187], a[28:31], v[44:47]\n"
"v_cndmask_b32 v247, v247, v244, vcc\n"
"MM v[48:51], v[168:171], a[8:11], 0\n"
"s_xor_b32 s68, s64, 1\n"
"MM v[48:51], v[188:191], a[28:31], v[48:51]\n"
"MM v[52:55], v[172:175], a[8:11], 0\n"
"v_mov_b32 v244, s68\n"
"MM v[52:55], v[192:195], a[28:31], v[52:55]\n"
"MM v[56:59], v[176:179], a[8:11], 0\n"
"v_cmp_eq_u32 vcc, 8, v253\n"
"MM v[56:59], v[196:199], a[28:31], v[56:59]\n"
"s_nop 0\n"
"v_cndmask_b32 v247, v247, v244, vcc\n"
"s_waitcnt lgkmcnt(3)\n"
"MM v[60:63], v[160:163], a[12:15], 0\n"
"s_waitcnt lgkmcnt(2)\n"
"MM v[60:63], v[180:183], a[32:35], v[60:63]\n"
"v_and_b32 v244, 7, v246\n"
"MM v[64:67], v[164:167], a[12:15], 0\n"
"MM v[64:67], v[184:187], a[32:35], v[64:67]\n"
"v_lshlrev_b32 v244, 5, v244\n"
"MM v[68:71], v[168:171], a[12:15], 0\n"
"v_and_b32 v248, 3, v247\n"
"MM v[68:71], v[188:191], a[32:35], v[68:71]\n"
"MM v[72:75], v[172:175], a[12:15], 0\n"
"v_lshl_add_u32 v244, v248, 3, v244\n"
"MM v[72:75], v[192:195], a[32:35], v[72:75]\n"
"MM v[76:79], v[176:179], a[12:15], 0\n"
"v_lshrrev_b32 v248, 3, v246\n"
"MM v[76:79], v[196:199], a[32:35], v[76:79]\n"
"v_lshl_add_u32 v244, v248, 2, v244\n"
"s_waitcnt lgkmcnt(1)\n"
"MM v[80:83], v[160:163], a[16:19], 0\n"
"s_waitcnt lgkmcnt(0)\n"
"MM v[80:83], v[180:183], a[36:39], v[80:83]\n"
"v_lshrrev_b32 v248, 2, v247\n"
"MM v[84:87], v[164:167], a[16:19], 0\n"
"MM v[84:87], v[184:187], a[36:39], v[84:87]\n"
"v_add_u32 v244, v244, v248\n"
"MM v[88:91], v[168:171], a[16:19], 0\n"
"MM v[88:91], v[188:191], a[36:39], v[88:91]\n"
"MM v[92:95], v[172:175], a[16:19], 0\n"
"MM v[92:95], v[192:195], a[36:39], v[92:95]\n"
"MM v[96:99], v[176:179], a[16:19], 0\n"
"MM v[96:99], v[196:199], a[36:39], v[96:99]\n"
"s_waitcnt vmcnt(0)\n"
"s_branch K1_TAIL\n"
"K1_ESA1:\n"
"s_call_b64 s[80:81], K1_RAREA1\n"
"s_branch K1_ENA1\n"
"K1_ESB1:\n"
"s_call_b64 s[80:81], K1_RAREB\n"
"s_branch K1_ENB1\n"
"K1_ESA2:\n"
"s_call_b64 s[80:81], K1_RAREA0\n"
"s_branch K1_ENA2\n"
"K1_ESB2:\n"
"s_call_b64 s[80:81], K1_RAREB\n"
"s_branch K1_ENB2\n"
"K1_ESA3:\n"
"s_call_b64 s[80:81], K1_RAREA1\n"
"s_branch K1_ENA3\n"
"K1_ESB3:\n"
"s_call_b64 s[80:81], K1_RAREB\n"
"s_branch K1_ENB3\n"
"K1_ESA4:\n"
"s_call_b64 s[80:81], K1_RAREA0\n"
"s_branch K1_ENA4\n"
"K1_ESB4:\n"
"s_call_b64 s[80:81], K1_RAREB\n"
"s_branch K1_ENB4\n"
"K1_ESA5:\n"
"s_call_b64 s[80:81], K1_RAREA1\n"
"s_branch K1_ENA5\n"
"K1_ESB5:\n"
"s_call_b64 s[80:81], K1_RAREB\n"
"s_branch K1_ENB5\n"
"K1_ESA6:\n"
"s_call_b64 s[80:81], K1_RAREA0\n"
"s_branch K1_ENA6\n"
"K1_ESB6:\n"
"s_call_b64 s[80:81], K1_RAREB\n"
"s_branch K1_ENB6\n"
"K1_ESA7:\n"
"s_call_b64 s[80:81], K1_RAREA1\n"
"s_branch K1_ENA7\n"
"K1_ESB7:\n"
"s_call_b64 s[80:81], K1_RAREB\n"
"s_branch K1_ENB7\n"
"K1_ESA8:\n"
"s_call_b64 s[80:81], K1_RAREA0\n"
"s_branch K1_ENA8\n"
"K1_ESB8:\n"
"s_call_b64 s[80:81], K1_RAREB\n"
"s_branch K1_ENB8\n"
"K1_ESA9:\n"
"s_call_b64 s[80:81], K1_RAREA1\n"
"s_branch K1_ENA9\n"
"K1_ESB9:\n"
"s_call_b64 s[80:81], K1_RAREB\n"
"s_branch K1_ENB9\n"
"K1_ESA10:\n"
"s_call_b64 s[80:81], K1_RAREA0\n"
"s_branch K1_ENA10\n"
"K1_ESB10:\n"
"s_call_b64 s[80:81], K1_RAREB\n"
"s_branch K1_ENB10\n"
"K1_ESA11:\n"
"s_call_b64 s[80:81], K1_RAREA1\n"
"s_branch K1_ENA11\n"
"K1_ESB11:\n"
"s_call_b64 s[80:81], K1_RAREB\n"
"s_branch K1_ENB11\n"
"K1_ESA12:\n"
"s_call_b64 s[80:81], K1_RAREA0\n"
"s_branch K1_ENA12\n"
"K1_ESB12:\n"
"s_call_b64 s[80:81], K1_RAREB\n"
"s_branch K1_ENB12\n"
"K1_ESA13:\n"
"s_call_b64 s[80:81], K1_RAREA1\n"
"s_branch K1_ENA13\n"
"K1_ESB13:\n"
"s_call_b64 s[80:81], K1_RAREB\n"
"s_branch K1_ENB13\n"
"K1_ESA14:\n"
"s_call_b64 s[80:81], K1_RAREA0\n"
"s_branch K1_ENA14\n"
"K1_ESB14:\n"
"s_call_b64 s[80:81], K1_RAREB\n"
"s_branch K1_ENB14\n"
"K1_ESA15:\n"
"s_call_b64 s[80:81], K1_RAREA1\n"
"s_branch K1_ENA15\n"
"K1_ESB15:\n"
"s_call_b64 s[80:81], K1_RAREB\n"
"s_branch K1_ENB15\n"
"K1_A21_S:\n"
"s_mov_b32 s90, 4194304\n"
"K1_A21_L:\n"
"ds_read_b32 v248, v245\n"
"s_waitcnt lgkmcnt(0)\n"
"v_cmp_gt_u32 vcc, 21, v248\n"
"s_nop 1\n"
"s_cbranch_vccz K1_A21_R\n"
"s_sub_u32 s90, s90, 1\n"
"s_cmp_lg_u32 s90, 0\n"
"s_cbranch_scc1 K1_A21_L\n"
"s_branch K1_A21_R\n"
"K1_A22_S:\n"
"s_mov_b32 s90, 4194304\n"
"K1_A22_L:\n"
"ds_read_b32 v248, v245\n"
"s_waitcnt lgkmcnt(0)\n"
"v_cmp_gt_u32 vcc, 22, v248\n"
"s_nop 1\n"
"s_cbranch_vccz K1_A22_R\n"
"s_sub_u32 s90, s90, 1\n"
"s_cmp_lg_u32 s90, 0\n"
"s_cbranch_scc1 K1_A22_L\n"
"s_branch K1_A22_R\n"
"K1_A23_S:\n"
"s_mov_b32 s90, 4194304\n"
"K1_A23_L:\n"
"ds_read_b32 v248, v245\n"
"s_waitcnt lgkmcnt(0)\n"
"v_cmp_gt_u32 vcc, 23, v248\n"
"s_nop 1\n"
"s_cbranch_vccz K1_A23_R\n"
"s_sub_u32 s90, s90, 1\n"
"s_cmp_lg_u32 s90, 0\n"
"s_cbranch_scc1 K1_A23_L\n"
"s_branch K1_A23_R\n"
"K1_A24_S:\n"
"s_mov_b32 s90, 4194304\n"
"K1_A24_L:\n"
"ds_read_b32 v248, v245\n"
"s_waitcnt lgkmcnt(0)\n"
"v_cmp_gt_u32 vcc, 24, v248\n"
"s_nop 1\n"
"s_cbranch_vccz K1_A24_R\n"
"s_sub_u32 s90, s90, 1\n"
"s_cmp_lg_u32 s90, 0\n"
"s_cbranch_scc1 K1_A24_L\n"
"s_branch K1_A24_R\n"
"K1_A25_S:\n"
"s_mov_b32 s90, 4194304\n"
"K1_A25_L:\n"
"ds_read_b32 v248, v245\n"
"s_waitcnt lgkmcnt(0)\n"
"v_cmp_gt_u32 vcc, 25, v248\n"
"s_nop 1\n"
"s_cbranch_vccz K1_A25_R\n"
"s_sub_u32 s90, s90, 1\n"
"s_cmp_lg_u32 s90, 0\n"
"s_cbranch_scc1 K1_A25_L\n"
"s_branch K1_A25_R\n"
"K1_A26_S:\n"
"s_mov_b32 s90, 4194304\n"
"K1_A26_L:\n"
"ds_read_b32 v248, v245\n"
"s_waitcnt lgkmcnt(0)\n"
"v_cmp_gt_u32 vcc, 26, v248\n"
"s_nop 1\n"
"s_cbranch_vccz K1_A26_R\n"
"s_sub_u32 s90, s90, 1\n"
"s_cmp_lg_u32 s90, 0\n"
"s_cbranch_scc1 K1_A26_L\n"
"s_branch K1_A26_R\n"
"K1_A27_S:\n"
"s_mov_b32 s90, 4194304\n"
"K1_A27_L:\n"
"ds_read_b32 v248, v245\n"
"s_waitcnt lgkmcnt(0)\n"
"v_cmp_gt_u32 vcc, 27, v248\n"
"s_nop 1\n"
"s_cbranch_vccz K1_A27_R\n"
"s_sub_u32 s90, s90, 1\n"
"s_cmp_lg_u32 s90, 0\n"
"s_cbranch_scc1 K1_A27_L\n"
"s_branch K1_A27_R\n"
"K1_A28_S:\n"
"s_mov_b32 s90, 4194304\n"
"K1_A28_L:\n"
"ds_read_b32 v248, v245\n"
"s_waitcnt lgkmcnt(0)\n"
"v_cmp_gt_u32 vcc, 28, v248\n"
"s_nop 1\n"
"s_cbranch_vccz K1_A28_R\n"
"s_sub_u32 s90, s90, 1\n"
"s_cmp_lg_u32 s90, 0\n"
"s_cbranch_scc1 K1_A28_L\n"
"s_branch K1_A28_R\n"
"K1_A29_S:\n"
"s_mov_b32 s90, 4194304\n"
"K1_A29_L:\n"
"ds_read_b32 v248, v245\n"
"s_waitcnt lgkmcnt(0)\n"
"v_cmp_gt_u32 vcc, 29, v248\n"
"s_nop 1\n"
"s_cbranch_vccz K1_A29_R\n"
"s_sub_u32 s90, s90, 1\n"
"s_cmp_lg_u32 s90, 0\n"
"s_cbranch_scc1 K1_A29_L\n"
"s_branch K1_A29_R\n"
"K1_A30_S:\n"
"s_mov_b32 s90, 4194304\n"
"K1_A30_L:\n"
"ds_read_b32 v248, v245\n"
"s_waitcnt lgkmcnt(0)\n"
"v_cmp_gt_u32 vcc, 30, v248\n"
"s_nop 1\n"
"s_cbranch_vccz K1_A30_R\n"
"s_sub_u32 s90, s90, 1\n"
"s_cmp_lg_u32 s90, 0\n"
"s_cbranch_scc1 K1_A30_L\n"
"s_branch K1_A30_R\n"
"K1_A31_S:\n"
"s_mov_b32 s90, 4194304\n"
"K1_A31_L:\n"
"ds_read_b32 v248, v245\n"
"s_waitcnt lgkmcnt(0)\n"
"v_cmp_gt_u32 vcc, 31, v248\n"
"s_nop 1\n"
"s_cbranch_vccz K1_A31_R\n"
"s_sub_u32 s90, s90, 1\n"
"s_cmp_lg_u32 s90, 0\n"
"s_cbranch_scc1 K1_A31_L\n"
"s_branch K1_A31_R\n"
"K1_A32_S:\n"
"s_mov_b32 s90, 4194304\n"
"K1_A32_L:\n"
"ds_read_b32 v248, v245\n"
"s_waitcnt lgkmcnt(0)\n"
"v_cmp_gt_u32 vcc, 32, v248\n"
"s_nop 1\n"
"s_cbranch_vccz K1_A32_R\n"
"s_sub_u32 s90, s90, 1\n"
"s_cmp_lg_u32 s90, 0\n"
"s_cbranch_scc1 K1_A32_L\n"
"s_branch K1_A32_R\n"
"K1_A33_S:\n"
"s_mov_b32 s90, 4194304\n"
"K1_A33_L:\n"
"ds_read_b32 v248, v245\n"
"s_waitcnt lgkmcnt(0)\n"
"v_cmp_gt_u32 vcc, 33, v248\n"
"s_nop 1\n"
"s_cbranch_vccz K1_A33_R\n"
"s_sub_u32 s90, s90, 1\n"
"s_cmp_lg_u32 s90, 0\n"
"s_cbranch_scc1 K1_A33_L\n"
"s_branch K1_A33_R\n"
"K1_A34_S:\n"
"s_mov_b32 s90, 4194304\n"
"K1_A34_L:\n"
"ds_read_b32 v248, v245\n"
"s_waitcnt lgkmcnt(0)\n"
"v_cmp_gt_u32 vcc, 34, v248\n"
"s_nop 1\n"
"s_cbranch_vccz K1_A34_R\n"
"s_sub_u32 s90, s90, 1\n"
"s_cmp_lg_u32 s90, 0\n"
"s_cbranch_scc1 K1_A34_L\n"
"s_branch K1_A34_R\n"
"K1_A35_S:\n"
"s_mov_b32 s90, 4194304\n"
"K1_A35_L:\n"
"ds_read_b32 v248, v245\n"
"s_waitcnt lgkmcnt(0)\n"
"v_cmp_gt_u32 vcc, 35, v248\n"
"s_nop 1\n"
"s_cbranch_vccz K1_A35_R\n"
"s_sub_u32 s90, s90, 1\n"
"s_cmp_lg_u32 s90, 0\n"
"s_cbranch_scc1 K1_A35_L\n"
"s_branch K1_A35_R\n"
"K1_A36_S:\n"
"s_mov_b32 s90, 4194304\n"
"K1_A36_L:\n"
"ds_read_b32 v248, v245\n"
"s_waitcnt lgkmcnt(0)\n"
"v_cmp_gt_u32 vcc, 36, v248\n"
"s_nop 1\n"
"s_cbranch_vccz K1_A36_R\n"
"s_sub_u32 s90, s90, 1\n"
"s_cmp_lg_u32 s90, 0\n"
"s_cbranch_scc1 K1_A36_L\n"
"s_branch K1_A36_R\n"
"K1_A37_S:\n"
"s_mov_b32 s90, 4194304\n"
"K1_A37_L:\n"
"ds_read_b32 v248, v245\n"
"s_waitcnt lgkmcnt(0)\n"
"v_cmp_gt_u32 vcc, 37, v248\n"
"s_nop 1\n"
"s_cbranch_vccz K1_A37_R\n"
"s_sub_u32 s90, s90, 1\n"
"s_cmp_lg_u32 s90, 0\n"
"s_cbranch_scc1 K1_A37_L\n"
"s_branch K1_A37_R\n"
"K1_XG_S:\n"
"s_mov_b32 s90, 4194304\n"
"K1_XG_L:\n"
"ds_read_b32 v248, v245\n"
"s_waitcnt lgkmcnt(0)\n"
"v_cmp_gt_u32 vcc, 40, v248\n"
"s_nop 1\n"
"s_cbranch_vccz K1_XG_R\n"
"s_sub_u32 s90, s90, 1\n"
"s_cmp_lg_u32 s90, 0\n"
"s_cbranch_scc1 K1_XG_L\n"
"s_branch K1_XG_R\n"
"K1_YG_S:\n"
"s_mov_b32 s90, 4194304\n"
"K1_YG_L:\n"
"ds_read_b32 v248, v245\n"
"s_waitcnt lgkmcnt(0)\n"
"v_cmp_gt_u32 vcc, 41, v248\n"
"s_nop 1\n"
"s_cbranch_vccz K1_YG_R\n"
"s_sub_u32 s90, s90, 1\n"
"s_cmp_lg_u32 s90, 0\n"
"s_cbranch_scc1 K1_YG_L\n"
"s_branch K1_YG_R\n"
"K1_RAREA0:\n"
"s_nop 7\n"
"v_max3_f32 v140, v0, v1, v2\n"
"v_max3_f32 v140, v3, v4, v140\n"
"v_max3_f32 v140, v5, v6, v140\n"
"v_max_f32 v140, v7, v140\n"
"v_max3_f32 v141, v8, v9, v10\n"
"v_max3_f32 v141, v11, v12, v141\n"
"v_max3_f32 v141, v13, v14, v141\n"
"v_max_f32 v141, v15, v141\n"
"v_max3_f32 v142, v16, v17, v18\n"
"v_max3_f32 v142, v19, v20, v142\n"
"v_max3_f32 v142, v21, v22, v142\n"
"v_max_f32 v142, v23, v142\n"
"v_max3_f32 v143, v24, v25, v26\n"
"v_max3_f32 v143, v27, v28, v143\n"
"v_max3_f32 v143, v29, v30, v143\n"
"v_max_f32 v143, v31, v143\n"
"v_max3_f32 v144, v32, v33, v34\n"
"v_max3_f32 v144, v35, v36, v144\n"
"v_max3_f32 v144, v37, v38, v144\n"
"v_max_f32 v144, v39, v144\n"
"v_mov_b32 v145, v140\n"
"s_nop 1\n"
"v_permlane32_swap_b32 v140, v145\n"
"v_max_f32 v140, v140, v145\n"
"v_mov_b32 v145, v140\n"
"s_nop 1\n"
"v_permlane16_swap_b32 v140, v145\n"
"v_max_f32 v140, v140, v145\n"
"v_mov_b32 v146, v141\n"
"s_nop 1\n"
"v_permlane32_swap_b32 v141, v146\n"
"v_max_f32 v141, v141, v146\n"
"v_mov_b32 v146, v141\n"
"s_nop 1\n"
"v_permlane16_swap_b32 v141, v146\n"
"v_max_f32 v141, v141, v146\n"
"v_mov_b32 v147, v142\n"
"s_nop 1\n"
"v_permlane32_swap_b32 v142, v147\n"
"v_max_f32 v142, v142, v147\n"
"v_mov_b32 v147, v142\n"
"s_nop 1\n"
"v_permlane16_swap_b32 v142, v147\n"
"v_max_f32 v142, v142, v147\n"
"v_mov_b32 v148, v143\n"
"s_nop 1\n"
"v_permlane32_swap_b32 v143, v148\n"
"v_max_f32 v143, v143, v148\n"
"v_mov_b32 v148, v143\n"
"s_nop 1\n"
"v_permlane16_swap_b32 v143, v148\n"
"v_max_f32 v143, v143, v148\n"
"v_mov_b32 v149, v144\n"
"s_nop 1\n"
"v_permlane32_swap_b32 v144, v149\n"
"v_max_f32 v144, v144, v149\n"
"v_mov_b32 v149, v144\n"
"s_nop 1\n"
"v_permlane16_swap_b32 v144, v149\n"
"v_max_f32 v144, v144, v149\n"
"v_max_f32 v140, 0, v140\n"
"v_sub_f32 v0, v0, v140\n"
"v_sub_f32 v1, v1, v140\n"
"v_sub_f32 v2, v2, v140\n"
"v_sub_f32 v3, v3, v140\n"
"v_sub_f32 v4, v4, v140\n"
"v_sub_f32 v5, v5, v140\n"
"v_sub_f32 v6, v6, v140\n"
"v_sub_f32 v7, v7, v140\n"
"v_sub_f32 v80, v80, v140\n"
"v_sub_f32 v81, v81, v140\n"
"v_sub_f32 v82, v82, v140\n"
"v_sub_f32 v83, v83, v140\n"
"v_exp_f32 v150, -v140\n"
"v_max_f32 v141, 0, v141\n"
"v_sub_f32 v8, v8, v141\n"
"v_sub_f32 v9, v9, v141\n"
"v_sub_f32 v10, v10, v141\n"
"v_sub_f32 v11, v11, v141\n"
"v_sub_f32 v12, v12, v141\n"
"v_sub_f32 v13, v13, v141\n"
"v_sub_f32 v14, v14, v141\n"
"v_sub_f32 v15, v15, v141\n"
"v_sub_f32 v84, v84, v141\n"
"v_sub_f32 v85, v85, v141\n"
"v_sub_f32 v86, v86, v141\n"
"v_sub_f32 v87, v87, v141\n"
"v_exp_f32 v151, -v141\n"
"v_max_f32 v142, 0, v142\n"
"v_sub_f32 v16, v16, v142\n"
"v_sub_f32 v17, v17, v142\n"
"v_sub_f32 v18, v18, v142\n"
"v_sub_f32 v19, v19, v142\n"
"v_sub_f32 v20, v20, v142\n"
"v_sub_f32 v21, v21, v142\n"
"v_sub_f32 v22, v22, v142\n"
"v_sub_f32 v23, v23, v142\n"
"v_sub_f32 v88, v88, v142\n"
"v_sub_f32 v89, v89, v142\n"
"v_sub_f32 v90, v90, v142\n"
"v_sub_f32 v91, v91, v142\n"
"v_exp_f32 v152, -v142\n"
"v_max_f32 v143, 0, v143\n"
"v_sub_f32 v24, v24, v143\n"
"v_sub_f32 v25, v25, v143\n"
"v_sub_f32 v26, v26, v143\n"
"v_sub_f32 v27, v27, v143\n"
"v_sub_f32 v28, v28, v143\n"
"v_sub_f32 v29, v29, v143\n"
"v_sub_f32 v30, v30, v143\n"
"v_sub_f32 v31, v31, v143\n"
"v_sub_f32 v92, v92, v143\n"
"v_sub_f32 v93, v93, v143\n"
"v_sub_f32 v94, v94, v143\n"
"v_sub_f32 v95, v95, v143\n"
"v_exp_f32 v153, -v143\n"
"v_max_f32 v144, 0, v144\n"
"v_sub_f32 v32, v32, v144\n"
"v_sub_f32 v33, v33, v144\n"
"v_sub_f32 v34, v34, v144\n"
"v_sub_f32 v35, v35, v144\n"
"v_sub_f32 v36, v36, v144\n"
"v_sub_f32 v37, v37, v144\n"
"v_sub_f32 v38, v38, v144\n"
"v_sub_f32 v39, v39, v144\n"
"v_sub_f32 v96, v96, v144\n"
"v_sub_f32 v97, v97, v144\n"
"v_sub_f32 v98, v98, v144\n"
"v_sub_f32 v99, v99, v144\n"
"v_exp_f32 v154, -v144\n"
"s_mov_b32 s75, 1\n"
"s_nop 3\n"
"s_setpc_b64 s[80:81]\n"
"K1_RAREA1:\n"
"s_nop 7\n"
"v_max3_f32 v140, v40, v41, v42\n"
"v_max3_f32 v140, v43, v44, v140\n"
"v_max3_f32 v140, v45, v46, v140\n"
"v_max_f32 v140, v47, v140\n"
"v_max3_f32 v141, v48, v49, v50\n"
"v_max3_f32 v141, v51, v52, v141\n"
"v_max3_f32 v141, v53, v54, v141\n"
"v_max_f32 v141, v55, v141\n"
"v_max3_f32 v142, v56, v57, v58\n"
"v_max3_f32 v142, v59, v60, v142\n"
"v_max3_f32 v142, v61, v62, v142\n"
"v_max_f32 v142, v63, v142\n"
"v_max3_f32 v143, v64, v65, v66\n"
"v_max3_f32 v143, v67, v68, v143\n"
"v_max3_f32 v143, v69, v70, v143\n"
"v_max_f32 v143, v71, v143\n"
"v_max3_f32 v144, v72, v73, v74\n"
"v_max3_f32 v144, v75, v76, v144\n"
"v_max3_f32 v144, v77, v78, v144\n"
"v_max_f32 v144, v79, v144\n"
"v_mov_b32 v145, v140\n"
"s_nop 1\n"
"v_permlane32_swap_b32 v140, v145\n"
"v_max_f32 v140, v140, v145\n"
"v_mov_b32 v145, v140\n"
"s_nop 1\n"
"v_permlane16_swap_b32 v140, v145\n"
"v_max_f32 v140, v140, v145\n"
"v_mov_b32 v146, v141\n"
"s_nop 1\n"
"v_permlane32_swap_b32 v141, v146\n"
"v_max_f32 v141, v141, v146\n"
"v_mov_b32 v146, v141\n"
"s_nop 1\n"
"v_permlane16_swap_b32 v141, v146\n"
"v_max_f32 v141, v141, v146\n"
"v_mov_b32 v147, v142\n"
"s_nop 1\n"
"v_permlane32_swap_b32 v142, v147\n"
"v_max_f32 v142, v142, v147\n"
"v_mov_b32 v147, v142\n"
"s_nop 1\n"
"v_permlane16_swap_b32 v142, v147\n"
"v_max_f32 v142, v142, v147\n"
"v_mov_b32 v148, v143\n"
"s_nop 1\n"
"v_permlane32_swap_b32 v143, v148\n"
"v_max_f32 v143, v143, v148\n"
"v_mov_b32 v148, v143\n"
"s_nop 1\n"
"v_permlane16_swap_b32 v143, v148\n"
"v_max_f32 v143, v143, v148\n"
"v_mov_b32 v149, v144\n"
"s_nop 1\n"
"v_permlane32_swap_b32 v144, v149\n"
"v_max_f32 v144, v144, v149\n"
"v_mov_b32 v149, v144\n"
"s_nop 1\n"
"v_permlane16_swap_b32 v144, v149\n"
"v_max_f32 v144, v144, v149\n"
"v_max_f32 v140, 0, v140\n"
"v_sub_f32 v40, v40, v140\n"
"v_sub_f32 v41, v41, v140\n"
"v_sub_f32 v42, v42, v140\n"
"v_sub_f32 v43, v43, v140\n"
"v_sub_f32 v44, v44, v140\n"
"v_sub_f32 v45, v45, v140\n"
"v_sub_f32 v46, v46, v140\n"
"v_sub_f32 v47, v47, v140\n"
"v_sub_f32 v80, v80, v140\n"
"v_sub_f32 v81, v81, v140\n"
"v_sub_f32 v82, v82, v140\n"
"v_sub_f32 v83, v83, v140\n"
"v_exp_f32 v150, -v140\n"
"v_max_f32 v141, 0, v141\n"
"v_sub_f32 v48, v48, v141\n"
"v_sub_f32 v49, v49, v141\n"
"v_sub_f32 v50, v50, v141\n"
"v_sub_f32 v51, v51, v141\n"
"v_sub_f32 v52, v52, v141\n"
"v_sub_f32 v53, v53, v141\n"
"v_sub_f32 v54, v54, v141\n"
"v_sub_f32 v55, v55, v141\n"
"v_sub_f32 v84, v84, v141\n"
"v_sub_f32 v85, v85, v141\n"
"v_sub_f32 v86, v86, v141\n"
"v_sub_f32 v87, v87, v141\n"
"v_exp_f32 v151, -v141\n"
"v_max_f32 v142, 0, v142\n"
"v_sub_f32 v56, v56, v142\n"
"v_sub_f32 v57, v57, v142\n"
"v_sub_f32 v58, v58, v142\n"
"v_sub_f32 v59, v59, v142\n"
"v_sub_f32 v60, v60, v142\n"
"v_sub_f32 v61, v61, v142\n"
"v_sub_f32 v62, v62, v142\n"
"v_sub_f32 v63, v63, v142\n"
"v_sub_f32 v88, v88, v142\n"
"v_sub_f32 v89, v89, v142\n"
"v_sub_f32 v90, v90, v142\n"
"v_sub_f32 v91, v91, v142\n"
"v_exp_f32 v152, -v142\n"
"v_max_f32 v143, 0, v143\n"
"v_sub_f32 v64, v64, v143\n"
"v_sub_f32 v65, v65, v143\n"
"v_sub_f32 v66, v66, v143\n"
"v_sub_f32 v67, v67, v143\n"
"v_sub_f32 v68, v68, v143\n"
"v_sub_f32 v69, v69, v143\n"
"v_sub_f32 v70, v70, v143\n"
"v_sub_f32 v71, v71, v143\n"
"v_sub_f32 v92, v92, v143\n"
"v_sub_f32 v93, v93, v143\n"
"v_sub_f32 v94, v94, v143\n"
"v_sub_f32 v95, v95, v143\n"
"v_exp_f32 v153, -v143\n"
"v_max_f32 v144, 0, v144\n"
"v_sub_f32 v72, v72, v144\n"
"v_sub_f32 v73, v73, v144\n"
"v_sub_f32 v74, v74, v144\n"
"v_sub_f32 v75, v75, v144\n"
"v_sub_f32 v76, v76, v144\n"
"v_sub_f32 v77, v77, v144\n"
"v_sub_f32 v78, v78, v144\n"
"v_sub_f32 v79, v79, v144\n"
"v_sub_f32 v96, v96, v144\n"
"v_sub_f32 v97, v97, v144\n"
"v_sub_f32 v98, v98, v144\n"
"v_sub_f32 v99, v99, v144\n"
"v_exp_f32 v154, -v144\n"
"s_mov_b32 s75, 1\n"
"s_nop 3\n"
"s_setpc_b64 s[80:81]\n"
"K1_RAREB:\n"
"s_nop 7\n"
"s_nop 3\n"
"v_accvgpr_read_b32 v145, a0\n"
"v_mul_f32 v145, v150, v145\n"
"v_accvgpr_write_b32 a0, v145\n"
"v_accvgpr_read_b32 v146, a1\n"
"v_mul_f32 v146, v150, v146\n"
"v_accvgpr_write_b32 a1, v146\n"
"v_accvgpr_read_b32 v147, a2\n"
"v_mul_f32 v147, v150, v147\n"
"v_accvgpr_write_b32 a2, v147\n"
"v_accvgpr_read_b32 v148, a3\n"
"v_mul_f32 v148, v150, v148\n"
"v_accvgpr_write_b32 a3, v148\n"
"v_accvgpr_read_b32 v145, a4\n"
"v_mul_f32 v145, v150, v145\n"
"v_accvgpr_write_b32 a4, v145\n"
"v_accvgpr_read_b32 v146, a5\n"
"v_mul_f32 v146, v150, v146\n"
"v_accvgpr_write_b32 a5, v146\n"
"v_accvgpr_read_b32 v147, a6\n"
"v_mul_f32 v147, v150, v147\n"
"v_accvgpr_write_b32 a6, v147\n"
"v_accvgpr_read_b32 v148, a7\n"
"v_mul_f32 v148, v150, v148\n"
"v_accvgpr_write_b32 a7, v148\n"
"v_accvgpr_read_b32 v145, a8\n"
"v_mul_f32 v145, v150, v145\n"
"v_accvgpr_write_b32 a8, v145\n"
"v_accvgpr_read_b32 v146, a9\n"
"v_mul_f32 v146, v150, v146\n"
"v_accvgpr_write_b32 a9, v146\n"
"v_accvgpr_read_b32 v147, a10\n"
"v_mul_f32 v147, v150, v147\n"
"v_accvgpr_write_b32 a10, v147\n"
"v_accvgpr_read_b32 v148, a11\n"
"v_mul_f32 v148, v150, v148\n"
"v_accvgpr_write_b32 a11, v148\n"
"v_accvgpr_read_b32 v145, a12\n"
"v_mul_f32 v145, v150, v145\n"
"v_accvgpr_write_b32 a12, v145\n"
"v_accvgpr_read_b32 v146, a13\n"
"v_mul_f32 v146, v150, v146\n"
"v_accvgpr_write_b32 a13, v146\n"
"v_accvgpr_read_b32 v147, a14\n"
"v_mul_f32 v147, v150, v147\n"
"v_accvgpr_write_b32 a14, v147\n"
"v_accvgpr_read_b32 v148, a15\n"
"v_mul_f32 v148, v150, v148\n"
"v_accvgpr_write_b32 a15, v148\n"
"v_accvgpr_read_b32 v145, a16\n"
"v_mul_f32 v145, v150, v145\n"
"v_accvgpr_write_b32 a16, v145\n"
"v_accvgpr_read_b32 v146, a17\n"
"v_mul_f32 v146, v150, v146\n"
"v_accvgpr_write_b32 a17, v146\n"
"v_accvgpr_read_b32 v147, a18\n"
"v_mul_f32 v147, v150, v147\n"
"v_accvgpr_write_b32 a18, v147\n"
"v_accvgpr_read_b32 v148, a19\n"
"v_mul_f32 v148, v150, v148\n"
"v_accvgpr_write_b32 a19, v148\n"
"v_accvgpr_read_b32 v145, a20\n"
"v_mul_f32 v145, v150, v145\n"
"v_accvgpr_write_b32 a20, v145\n"
"v_accvgpr_read_b32 v146, a21\n"
"v_mul_f32 v146, v150, v146\n"
"v_accvgpr_write_b32 a21, v146\n"
"v_accvgpr_read_b32 v147, a22\n"
"v_mul_f32 v147, v150, v147\n"
"v_accvgpr_write_b32 a22, v147\n"
"v_accvgpr_read_b32 v148, a23\n"
"v_mul_f32 v148, v150, v148\n"
"v_accvgpr_write_b32 a23, v148\n"
"v_accvgpr_read_b32 v145, a24\n"
"v_mul_f32 v145, v150, v145\n"
"v_accvgpr_write_b32 a24, v145\n"
"v_accvgpr_read_b32 v146, a25\n"
"v_mul_f32 v146, v150, v146\n"
"v_accvgpr_write_b32 a25, v146\n"
"v_accvgpr_read_b32 v147, a26\n"
"v_mul_f32 v147, v150, v147\n"
"v_accvgpr_write_b32 a26, v147\n"
"v_accvgpr_read_b32 v148, a27\n"
"v_mul_f32 v148, v150, v148\n"
"v_accvgpr_write_b32 a27, v148\n"
"v_accvgpr_read_b32 v145, a28\n"
"v_mul_f32 v145, v150, v145\n"
"v_accvgpr_write_b32 a28, v145\n"
"v_accvgpr_read_b32 v146, a29\n"
"v_mul_f32 v146, v150, v146\n"
"v_accvgpr_write_b32 a29, v146\n"
"v_accvgpr_read_b32 v147, a30\n"
"v_mul_f32 v147, v150, v147\n"
"v_accvgpr_write_b32 a30, v147\n"
"v_accvgpr_read_b32 v148, a31\n"
"v_mul_f32 v148, v150, v148\n"
"v_accvgpr_write_b32 a31, v148\n"
"v_accvgpr_read_b32 v145, a32\n"
"v_mul_f32 v145, v151, v145\n"
"v_accvgpr_write_b32 a32, v145\n"
"v_accvgpr_read_b32 v146, a33\n"
"v_mul_f32 v146, v151, v146\n"
"v_accvgpr_write_b32 a33, v146\n"
"v_accvgpr_read_b32 v147, a34\n"
"v_mul_f32 v147, v151, v147\n"
"v_accvgpr_write_b32 a34, v147\n"
"v_accvgpr_read_b32 v148, a35\n"
"v_mul_f32 v148, v151, v148\n"
"v_accvgpr_write_b32 a35, v148\n"
"v_accvgpr_read_b32 v145, a36\n"
"v_mul_f32 v145, v151, v145\n"
"v_accvgpr_write_b32 a36, v145\n"
"v_accvgpr_read_b32 v146, a37\n"
"v_mul_f32 v146, v151, v146\n"
"v_accvgpr_write_b32 a37, v146\n"
"v_accvgpr_read_b32 v147, a38\n"
"v_mul_f32 v147, v151, v147\n"
"v_accvgpr_write_b32 a38, v147\n"
"v_accvgpr_read_b32 v148, a39\n"
"v_mul_f32 v148, v151, v148\n"
"v_accvgpr_write_b32 a39, v148\n"
"v_accvgpr_read_b32 v145, a40\n"
"v_mul_f32 v145, v151, v145\n"
"v_accvgpr_write_b32 a40, v145\n"
"v_accvgpr_read_b32 v146, a41\n"
"v_mul_f32 v146, v151, v146\n"
"v_accvgpr_write_b32 a41, v146\n"
"v_accvgpr_read_b32 v147, a42\n"
"v_mul_f32 v147, v151, v147\n"
"v_accvgpr_write_b32 a42, v147\n"
"v_accvgpr_read_b32 v148, a43\n"
"v_mul_f32 v148, v151, v148\n"
"v_accvgpr_write_b32 a43, v148\n"
"v_accvgpr_read_b32 v145, a44\n"
"v_mul_f32 v145, v151, v145\n"
"v_accvgpr_write_b32 a44, v145\n"
"v_accvgpr_read_b32 v146, a45\n"
"v_mul_f32 v146, v151, v146\n"
"v_accvgpr_write_b32 a45, v146\n"
"v_accvgpr_read_b32 v147, a46\n"
"v_mul_f32 v147, v151, v147\n"
"v_accvgpr_write_b32 a46, v147\n"
"v_accvgpr_read_b32 v148, a47\n"
"v_mul_f32 v148, v151, v148\n"
"v_accvgpr_write_b32 a47, v148\n"
"v_accvgpr_read_b32 v145, a48\n"
"v_mul_f32 v145, v151, v145\n"
"v_accvgpr_write_b32 a48, v145\n"
"v_accvgpr_read_b32 v146, a49\n"
"v_mul_f32 v146, v151, v146\n"
"v_accvgpr_write_b32 a49, v146\n"
"v_accvgpr_read_b32 v147, a50\n"
"v_mul_f32 v147, v151, v147\n"
"v_accvgpr_write_b32 a50, v147\n"
"v_accvgpr_read_b32 v148, a51\n"
"v_mul_f32 v148, v151, v148\n"
"v_accvgpr_write_b32 a51, v148\n"
"v_accvgpr_read_b32 v145, a52\n"
"v_mul_f32 v145, v151, v145\n"
"v_accvgpr_write_b32 a52, v145\n"
"v_accvgpr_read_b32 v146, a53\n"
"v_mul_f32 v146, v151, v146\n"
"v_accvgpr_write_b32 a53, v146\n"
"v_accvgpr_read_b32 v147, a54\n"
"v_mul_f32 v147, v151, v147\n"
"v_accvgpr_write_b32 a54, v147\n"
"v_accvgpr_read_b32 v148, a55\n"
"v_mul_f32 v148, v151, v148\n"
"v_accvgpr_write_b32 a55, v148\n"
"v_accvgpr_read_b32 v145, a56\n"
"v_mul_f32 v145, v151, v145\n"
"v_accvgpr_write_b32 a56, v145\n"
"v_accvgpr_read_b32 v146, a57\n"
"v_mul_f32 v146, v151, v146\n"
"v_accvgpr_write_b32 a57, v146\n"
"v_accvgpr_read_b32 v147, a58\n"
"v_mul_f32 v147, v151, v147\n"
"v_accvgpr_write_b32 a58, v147\n"
"v_accvgpr_read_b32 v148, a59\n"
"v_mul_f32 v148, v151, v148\n"
"v_accvgpr_write_b32 a59, v148\n"
"v_accvgpr_read_b32 v145, a60\n"
"v_mul_f32 v145, v151, v145\n"
"v_accvgpr_write_b32 a60, v145\n"
"v_accvgpr_read_b32 v146, a61\n"
"v_mul_f32 v146, v151, v146\n"
"v_accvgpr_write_b32 a61, v146\n"
"v_accvgpr_read_b32 v147, a62\n"
"v_mul_f32 v147, v151, v147\n"
"v_accvgpr_write_b32 a62, v147\n"
"v_accvgpr_read_b32 v148, a63\n"
"v_mul_f32 v148, v151, v148\n"
"v_accvgpr_write_b32 a63, v148\n"
"v_accvgpr_read_b32 v145, a64\n"
"v_mul_f32 v145, v152, v145\n"
"v_accvgpr_write_b32 a64, v145\n"
"v_accvgpr_read_b32 v146, a65\n"
"v_mul_f32 v146, v152, v146\n"
"v_accvgpr_write_b32 a65, v146\n"
"v_accvgpr_read_b32 v147, a66\n"
"v_mul_f32 v147, v152, v147\n"
"v_accvgpr_write_b32 a66, v147\n"
"v_accvgpr_read_b32 v148, a67\n"
"v_mul_f32 v148, v152, v148\n"
"v_accvgpr_write_b32 a67, v148\n"
"v_accvgpr_read_b32 v145, a68\n"
"v_mul_f32 v145, v152, v145\n"
"v_accvgpr_write_b32 a68, v145\n"
"v_accvgpr_read_b32 v146, a69\n"
"v_mul_f32 v146, v152, v146\n"
"v_accvgpr_write_b32 a69, v146\n"
"v_accvgpr_read_b32 v147, a70\n"
"v_mul_f32 v147, v152, v147\n"
"v_accvgpr_write_b32 a70, v147\n"
"v_accvgpr_read_b32 v148, a71\n"
"v_mul_f32 v148, v152, v148\n"
"v_accvgpr_write_b32 a71, v148\n"
"v_accvgpr_read_b32 v145, a72\n"
"v_mul_f32 v145, v152, v145\n"
"v_accvgpr_write_b32 a72, v145\n"
"v_accvgpr_read_b32 v146, a73\n"
"v_mul_f32 v146, v152, v146\n"
"v_accvgpr_write_b32 a73, v146\n"
"v_accvgpr_read_b32 v147, a74\n"
"v_mul_f32 v147, v152, v147\n"
"v_accvgpr_write_b32 a74, v147\n"
"v_accvgpr_read_b32 v148, a75\n"
"v_mul_f32 v148, v152, v148\n"
"v_accvgpr_write_b32 a75, v148\n"
"v_accvgpr_read_b32 v145, a76\n"
"v_mul_f32 v145, v152, v145\n"
"v_accvgpr_write_b32 a76, v145\n"
"v_accvgpr_read_b32 v146, a77\n"
"v_mul_f32 v146, v152, v146\n"
"v_accvgpr_write_b32 a77, v146\n"
"v_accvgpr_read_b32 v147, a78\n"
"v_mul_f32 v147, v152, v147\n"
"v_accvgpr_write_b32 a78, v147\n"
"v_accvgpr_read_b32 v148, a79\n"
"v_mul_f32 v148, v152, v148\n"
"v_accvgpr_write_b32 a79, v148\n"
"v_accvgpr_read_b32 v145, a80\n"
"v_mul_f32 v145, v152, v145\n"
"v_accvgpr_write_b32 a80, v145\n"
"v_accvgpr_read_b32 v146, a81\n"
"v_mul_f32 v146, v152, v146\n"
"v_accvgpr_write_b32 a81, v146\n"
"v_accvgpr_read_b32 v147, a82\n"
"v_mul_f32 v147, v152, v147\n"
"v_accvgpr_write_b32 a82, v147\n"
"v_accvgpr_read_b32 v148, a83\n"
"v_mul_f32 v148, v152, v148\n"
"v_accvgpr_write_b32 a83, v148\n"
"v_accvgpr_read_b32 v145, a84\n"
"v_mul_f32 v145, v152, v145\n"
"v_accvgpr_write_b32 a84, v145\n"
"v_accvgpr_read_b32 v146, a85\n"
"v_mul_f32 v146, v152, v146\n"
"v_accvgpr_write_b32 a85, v146\n"
"v_accvgpr_read_b32 v147, a86\n"
"v_mul_f32 v147, v152, v147\n"
"v_accvgpr_write_b32 a86, v147\n"
"v_accvgpr_read_b32 v148, a87\n"
"v_mul_f32 v148, v152, v148\n"
"v_accvgpr_write_b32 a87, v148\n"
"v_accvgpr_read_b32 v145, a88\n"
"v_mul_f32 v145, v152, v145\n"
"v_accvgpr_write_b32 a88, v145\n"
"v_accvgpr_read_b32 v146, a89\n"
"v_mul_f32 v146, v152, v146\n"
"v_accvgpr_write_b32 a89, v146\n"
"v_accvgpr_read_b32 v147, a90\n"
"v_mul_f32 v147, v152, v147\n"
"v_accvgpr_write_b32 a90, v147\n"
"v_accvgpr_read_b32 v148, a91\n"
"v_mul_f32 v148, v152, v148\n"
"v_accvgpr_write_b32 a91, v148\n"
"v_accvgpr_read_b32 v145, a92\n"
"v_mul_f32 v145, v152, v145\n"
"v_accvgpr_write_b32 a92, v145\n"
"v_accvgpr_read_b32 v146, a93\n"
"v_mul_f32 v146, v152, v146\n"
"v_accvgpr_write_b32 a93, v146\n"
"v_accvgpr_read_b32 v147, a94\n"
"v_mul_f32 v147, v152, v147\n"
"v_accvgpr_write_b32 a94, v147\n"
"v_accvgpr_read_b32 v148, a95\n"
"v_mul_f32 v148, v152, v148\n"
"v_accvgpr_write_b32 a95, v148\n"
"v_accvgpr_read_b32 v145, a96\n"
"v_mul_f32 v145, v153, v145\n"
"v_accvgpr_write_b32 a96, v145\n"
"v_accvgpr_read_b32 v146, a97\n"
"v_mul_f32 v146, v153, v146\n"
"v_accvgpr_write_b32 a97, v146\n"
"v_accvgpr_read_b32 v147, a98\n"
"v_mul_f32 v147, v153, v147\n"
"v_accvgpr_write_b32 a98, v147\n"
"v_accvgpr_read_b32 v148, a99\n"
"v_mul_f32 v148, v153, v148\n"
"v_accvgpr_write_b32 a99, v148\n"
"v_accvgpr_read_b32 v145, a100\n"
"v_mul_f32 v145, v153, v145\n"
"v_accvgpr_write_b32 a100, v145\n"
"v_accvgpr_read_b32 v146, a101\n"
"v_mul_f32 v146, v153, v146\n"
"v_accvgpr_write_b32 a101, v146\n"
"v_accvgpr_read_b32 v147, a102\n"
"v_mul_f32 v147, v153, v147\n"
"v_accvgpr_write_b32 a102, v147\n"
"v_accvgpr_read_b32 v148, a103\n"
"v_mul_f32 v148, v153, v148\n"
"v_accvgpr_write_b32 a103, v148\n"
"v_accvgpr_read_b32 v145, a104\n"
"v_mul_f32 v145, v153, v145\n"
"v_accvgpr_write_b32 a104, v145\n"
"v_accvgpr_read_b32 v146, a105\n"
"v_mul_f32 v146, v153, v146\n"
"v_accvgpr_write_b32 a105, v146\n"
"v_accvgpr_read_b32 v147, a106\n"
"v_mul_f32 v147, v153, v147\n"
"v_accvgpr_write_b32 a106, v147\n"
"v_accvgpr_read_b32 v148, a107\n"
"v_mul_f32 v148, v153, v148\n"
"v_accvgpr_write_b32 a107, v148\n"
"v_accvgpr_read_b32 v145, a108\n"
"v_mul_f32 v145, v153, v145\n"
"v_accvgpr_write_b32 a108, v145\n"
"v_accvgpr_read_b32 v146, a109\n"
"v_mul_f32 v146, v153, v146\n"
"v_accvgpr_write_b32 a109, v146\n"
"v_accvgpr_read_b32 v147, a110\n"
"v_mul_f32 v147, v153, v147\n"
"v_accvgpr_write_b32 a110, v147\n"
"v_accvgpr_read_b32 v148, a111\n"
"v_mul_f32 v148, v153, v148\n"
"v_accvgpr_write_b32 a111, v148\n"
"v_accvgpr_read_b32 v145, a112\n"
"v_mul_f32 v145, v153, v145\n"
"v_accvgpr_write_b32 a112, v145\n"
"v_accvgpr_read_b32 v146, a113\n"
"v_mul_f32 v146, v153, v146\n"
"v_accvgpr_write_b32 a113, v146\n"
"v_accvgpr_read_b32 v147, a114\n"
"v_mul_f32 v147, v153, v147\n"
"v_accvgpr_write_b32 a114, v147\n"
"v_accvgpr_read_b32 v148, a115\n"
"v_mul_f32 v148, v153, v148\n"
"v_accvgpr_write_b32 a115, v148\n"
"v_accvgpr_read_b32 v145, a116\n"
"v_mul_f32 v145, v153, v145\n"
"v_accvgpr_write_b32 a116, v145\n"
"v_accvgpr_read_b32 v146, a117\n"
"v_mul_f32 v146, v153, v146\n"
"v_accvgpr_write_b32 a117, v146\n"
"v_accvgpr_read_b32 v147, a118\n"
"v_mul_f32 v147, v153, v147\n"
"v_accvgpr_write_b32 a118, v147\n"
"v_accvgpr_read_b32 v148, a119\n"
"v_mul_f32 v148, v153, v148\n"
"v_accvgpr_write_b32 a119, v148\n"
"v_accvgpr_read_b32 v145, a120\n"
"v_mul_f32 v145, v153, v145\n"
"v_accvgpr_write_b32 a120, v145\n"
"v_accvgpr_read_b32 v146, a121\n"
"v_mul_f32 v146, v153, v146\n"
"v_accvgpr_write_b32 a121, v146\n"
"v_accvgpr_read_b32 v147, a122\n"
"v_mul_f32 v147, v153, v147\n"
"v_accvgpr_write_b32 a122, v147\n"
"v_accvgpr_read_b32 v148, a123\n"
"v_mul_f32 v148, v153, v148\n"
"v_accvgpr_write_b32 a123, v148\n"
"v_accvgpr_read_b32 v145, a124\n"
"v_mul_f32 v145, v153, v145\n"
"v_accvgpr_write_b32 a124, v145\n"
"v_accvgpr_read_b32 v146, a125\n"
"v_mul_f32 v146, v153, v146\n"
"v_accvgpr_write_b32 a125, v146\n"
"v_accvgpr_read_b32 v147, a126\n"
"v_mul_f32 v147, v153, v147\n"
"v_accvgpr_write_b32 a126, v147\n"
"v_accvgpr_read_b32 v148, a127\n"
"v_mul_f32 v148, v153, v148\n"
"v_accvgpr_write_b32 a127, v148\n"
"v_accvgpr_read_b32 v145, a128\n"
"v_mul_f32 v145, v154, v145\n"
"v_accvgpr_write_b32 a128, v145\n"
"v_accvgpr_read_b32 v146, a129\n"
"v_mul_f32 v146, v154, v146\n"
"v_accvgpr_write_b32 a129, v146\n"
"v_accvgpr_read_b32 v147, a130\n"
"v_mul_f32 v147, v154, v147\n"
"v_accvgpr_write_b32 a130, v147\n"
"v_accvgpr_read_b32 v148, a131\n"
"v_mul_f32 v148, v154, v148\n"
"v_accvgpr_write_b32 a131, v148\n"
"v_accvgpr_read_b32 v145, a132\n"
"v_mul_f32 v145, v154, v145\n"
"v_accvgpr_write_b32 a132, v145\n"
"v_accvgpr_read_b32 v146, a133\n"
"v_mul_f32 v146, v154, v146\n"
"v_accvgpr_write_b32 a133, v146\n"
"v_accvgpr_read_b32 v147, a134\n"
"v_mul_f32 v147, v154, v147\n"
"v_accvgpr_write_b32 a134, v147\n"
"v_accvgpr_read_b32 v148, a135\n"
"v_mul_f32 v148, v154, v148\n"
"v_accvgpr_write_b32 a135, v148\n"
"v_accvgpr_read_b32 v145, a136\n"
"v_mul_f32 v145, v154, v145\n"
"v_accvgpr_write_b32 a136, v145\n"
"v_accvgpr_read_b32 v146, a137\n"
"v_mul_f32 v146, v154, v146\n"
"v_accvgpr_write_b32 a137, v146\n"
"v_accvgpr_read_b32 v147, a138\n"
"v_mul_f32 v147, v154, v147\n"
"v_accvgpr_write_b32 a138, v147\n"
"v_accvgpr_read_b32 v148, a139\n"
"v_mul_f32 v148, v154, v148\n"
"v_accvgpr_write_b32 a139, v148\n"
"v_accvgpr_read_b32 v145, a140\n"
"v_mul_f32 v145, v154, v145\n"
"v_accvgpr_write_b32 a140, v145\n"
"v_accvgpr_read_b32 v146, a141\n"
"v_mul_f32 v146, v154, v146\n"
"v_accvgpr_write_b32 a141, v146\n"
"v_accvgpr_read_b32 v147, a142\n"
"v_mul_f32 v147, v154, v147\n"
"v_accvgpr_write_b32 a142, v147\n"
"v_accvgpr_read_b32 v148, a143\n"
"v_mul_f32 v148, v154, v148\n"
"v_accvgpr_write_b32 a143, v148\n"
"v_accvgpr_read_b32 v145, a144\n"
"v_mul_f32 v145, v154, v145\n"
"v_accvgpr_write_b32 a144, v145\n"
"v_accvgpr_read_b32 v146, a145\n"
"v_mul_f32 v146, v154, v146\n"
"v_accvgpr_write_b32 a145, v146\n"
"v_accvgpr_read_b32 v147, a146\n"
"v_mul_f32 v147, v154, v147\n"
"v_accvgpr_write_b32 a146, v147\n"
"v_accvgpr_read_b32 v148, a147\n"
"v_mul_f32 v148, v154, v148\n"
"v_accvgpr_write_b32 a147, v148\n"
"v_accvgpr_read_b32 v145, a148\n"
"v_mul_f32 v145, v154, v145\n"
"v_accvgpr_write_b32 a148, v145\n"
"v_accvgpr_read_b32 v146, a149\n"
"v_mul_f32 v146, v154, v146\n"
"v_accvgpr_write_b32 a149, v146\n"
"v_accvgpr_read_b32 v147, a150\n"
"v_mul_f32 v147, v154, v147\n"
"v_accvgpr_write_b32 a150, v147\n"
"v_accvgpr_read_b32 v148, a151\n"
"v_mul_f32 v148, v154, v148\n"
"v_accvgpr_write_b32 a151, v148\n"
"v_accvgpr_read_b32 v145, a152\n"
"v_mul_f32 v145, v154, v145\n"
"v_accvgpr_write_b32 a152, v145\n"
"v_accvgpr_read_b32 v146, a153\n"
"v_mul_f32 v146, v154, v146\n"
"v_accvgpr_write_b32 a153, v146\n"
"v_accvgpr_read_b32 v147, a154\n"
"v_mul_f32 v147, v154, v147\n"
"v_accvgpr_write_b32 a154, v147\n"
"v_accvgpr_read_b32 v148, a155\n"
"v_mul_f32 v148, v154, v148\n"
"v_accvgpr_write_b32 a155, v148\n"
"v_accvgpr_read_b32 v145, a156\n"
"v_mul_f32 v145, v154, v145\n"
"v_accvgpr_write_b32 a156, v145\n"
"v_accvgpr_read_b32 v146, a157\n"
"v_mul_f32 v146, v154, v146\n"
"v_accvgpr_write_b32 a157, v146\n"
"v_accvgpr_read_b32 v147, a158\n"
"v_mul_f32 v147, v154, v147\n"
"v_accvgpr_write_b32 a158, v147\n"
"v_accvgpr_read_b32 v148, a159\n"
"v_mul_f32 v148, v154, v148\n"
"v_accvgpr_write_b32 a159, v148\n"
"v_accvgpr_read_b32 v145, a224\n"
"v_mul_f32 v145, v150, v145\n"
"v_accvgpr_write_b32 a224, v145\n"
"v_accvgpr_read_b32 v146, a225\n"
"v_mul_f32 v146, v150, v146\n"
"v_accvgpr_write_b32 a225, v146\n"
"v_accvgpr_read_b32 v147, a226\n"
"v_mul_f32 v147, v150, v147\n"
"v_accvgpr_write_b32 a226, v147\n"
"v_accvgpr_read_b32 v148, a227\n"
"v_mul_f32 v148, v150, v148\n"
"v_accvgpr_write_b32 a227, v148\n"
"v_accvgpr_read_b32 v145, a228\n"
"v_mul_f32 v145, v151, v145\n"
"v_accvgpr_write_b32 a228, v145\n"
"v_accvgpr_read_b32 v146, a229\n"
"v_mul_f32 v146, v151, v146\n"
"v_accvgpr_write_b32 a229, v146\n"
"v_accvgpr_read_b32 v147, a230\n"
"v_mul_f32 v147, v151, v147\n"
"v_accvgpr_write_b32 a230, v147\n"
"v_accvgpr_read_b32 v148, a231\n"
"v_mul_f32 v148, v151, v148\n"
"v_accvgpr_write_b32 a231, v148\n"
"v_accvgpr_read_b32 v145, a232\n"
"v_mul_f32 v145, v152, v145\n"
"v_accvgpr_write_b32 a232, v145\n"
"v_accvgpr_read_b32 v146, a233\n"
"v_mul_f32 v146, v152, v146\n"
"v_accvgpr_write_b32 a233, v146\n"
"v_accvgpr_read_b32 v147, a234\n"
"v_mul_f32 v147, v152, v147\n"
"v_accvgpr_write_b32 a234, v147\n"
"v_accvgpr_read_b32 v148, a235\n"
"v_mul_f32 v148, v152, v148\n"
"v_accvgpr_write_b32 a235, v148\n"
"v_accvgpr_read_b32 v145, a236\n"
"v_mul_f32 v145, v153, v145\n"
"v_accvgpr_write_b32 a236, v145\n"
"v_accvgpr_read_b32 v146, a237\n"
"v_mul_f32 v146, v153, v146\n"
"v_accvgpr_write_b32 a237, v146\n"
"v_accvgpr_read_b32 v147, a238\n"
"v_mul_f32 v147, v153, v147\n"
"v_accvgpr_write_b32 a238, v147\n"
"v_accvgpr_read_b32 v148, a239\n"
"v_mul_f32 v148, v153, v148\n"
"v_accvgpr_write_b32 a239, v148\n"
"v_accvgpr_read_b32 v145, a240\n"
"v_mul_f32 v145, v154, v145\n"
"v_accvgpr_write_b32 a240, v145\n"
"v_accvgpr_read_b32 v146, a241\n"
"v_mul_f32 v146, v154, v146\n"
"v_accvgpr_write_b32 a241, v146\n"
"v_accvgpr_read_b32 v147, a242\n"
"v_mul_f32 v147, v154, v147\n"
"v_accvgpr_write_b32 a242, v147\n"
"v_accvgpr_read_b32 v148, a243\n"
"v_mul_f32 v148, v154, v148\n"
"v_accvgpr_write_b32 a243, v148\n"
"s_mov_b32 s75, 0\n"
"s_nop 3\n"
"s_setpc_b64 s[80:81]\n"
"K1_CODE_END:\n"
".purgem MM\n"
    :: "s"(x), "s"(kc), "s"(vc), "s"(wq), "s"(wo), "s"(ws), "s"(out), "s"(blockIdx.x), "s"(wave), "s"((unsigned)(size_t)lds) : CLOB);
}

extern "C" void kernel_launch(void* const* d_in, const int* in_sizes, int n_in, void* d_out, int out_size, void* d_ws, size_t ws_size, hipStream_t stream) {
  (void)in_sizes; (void)n_in; (void)out_size; (void)ws_size;
  mha_fused<<<256, 256, 0, stream>>>(d_in[0], d_in[1], d_in[2], d_in[3], d_in[4], d_ws, d_out);
}
```
